```python
import torch

def _softmax(x, axis=-1):
    return torch.softmax(x, dim=axis)

def _gelu(x):
    return torch.nn.functional.gelu(x, approximate='tanh')

def _silu(x):
    return torch.nn.functional.silu(x)

def _sigmoid(x):
    return torch.sigmoid(x)

def _layernorm(x, axis=-1, eps=1e-05):
    m = x.mean(dim=axis, keepdim=True)
    v = x.var(dim=axis, keepdim=True, unbiased=False)
    return (x - m) / torch.sqrt(v + eps)

def _scatter_add(tgt, idx, src, axis=0):
    return tgt.clone().index_add_(axis, idx, src)

def _segment_sum(src, seg, n):
    out = torch.zeros((n,) + src.shape[1:], dtype=src.dtype)
    return out.index_add_(0, seg, src)

def _pad(x, pw):
    flat = tuple((v for pair in reversed(pw) for v in pair))
    return torch.nn.functional.pad(x, flat)
kernel_name = 'parallel_sum__memory_aug_recurrent_mem_tr__swiglu_ffn__prompt'

def setup_inputs():

    _w = lambda a: (lambda t: t.to(torch.bfloat16) if t.is_floating_point() else t)(torch.as_tensor(a))

    def _A_setup():
        B, S, Dm = (1, 2048, 640)
        H, Dh = (32, 192)
        M, seg = (8, 32)
        seg = min(seg, S)
        S = S // seg * seg
        return {'x': _w((lambda *s: torch.randn(*s, dtype=torch.bfloat16) * 0.05)(B, S, Dm)), 'mem0': _w((lambda *s: torch.randn(*s, dtype=torch.bfloat16) * 1.6)(B, M, Dm)), 'Wq': _w((lambda *s: torch.randn(*s, dtype=torch.bfloat16) * 0.8)(Dm, H, Dh) * Dm ** (-0.5)), 'Wk': _w((lambda *s: torch.randn(*s, dtype=torch.bfloat16) * 0.8)(Dm, H, Dh) * Dm ** (-0.5)), 'Wv': _w((lambda *s: torch.randn(*s, dtype=torch.bfloat16) * 1.6)(Dm, H, Dh) * Dm ** (-0.5)), 'Wo': _w((lambda *s: torch.randn(*s, dtype=torch.bfloat16) * 1.6)(H, Dh, Dm) * (H * Dh) ** (-0.5)), 'seg_len': seg}
    _A_in = _A_setup()

    def _B_setup():
        B, S, D, F = (1, 2048, 640, 1536)
        return {'x': _w((lambda *s: torch.randn(*s, dtype=torch.bfloat16) * 0.05)(B, S, D)), 'w_gate': _w((lambda *s: torch.randn(*s, dtype=torch.bfloat16) * 0.05)(D, F)), 'w_up': _w((lambda *s: torch.randn(*s, dtype=torch.bfloat16) * 0.05)(D, F)), 'w_down': _w((lambda *s: torch.randn(*s, dtype=torch.bfloat16) * 0.05)(F, D))}
    _B_in = _B_setup()
    return {'A_x': _A_in['x'], 'A_mem0': _A_in['mem0'], 'A_Wq': _A_in['Wq'], 'A_Wk': _A_in['Wk'], 'A_Wv': _A_in['Wv'], 'A_Wo': _A_in['Wo'], 'A_seg_len': _A_in['seg_len'], 'B_x': _B_in['x'], 'B_w_gate': _B_in['w_gate'], 'B_w_up': _B_in['w_up'], 'B_w_down': _B_in['w_down']}

def reference(A_x, A_mem0, A_Wq, A_Wk, A_Wv, A_Wo, A_seg_len, B_x, B_w_gate, B_w_up, B_w_down):

    def _A_ref(x, mem0, Wq, Wk, Wv, Wo, seg_len):
        B, S, Dm = x.shape
        H, Dh = (Wq.shape[1], Wq.shape[2])
        M = mem0.shape[1]
        scale = Dh ** (-0.5)
        n_seg = S // seg_len
        L = M + seg_len
        idx = torch.arange(L)
        mask = (idx[:, None] < M) | (idx[:, None] >= idx[None, :])
        mem = mem0
        outs = []
        for s in range(n_seg):
            xs = x[:, s * seg_len:(s + 1) * seg_len, :].clone()
            h = torch.concatenate([mem, xs], axis=1)
            q = torch.einsum('btd,dhe->bhte', h, Wq)
            k = torch.einsum('btd,dhe->bhte', h, Wk)
            v = torch.einsum('btd,dhe->bhte', h, Wv)
            scores = torch.einsum('bhqe,bhke->bhqk', q, k) * scale
            scores = torch.where(mask[None, None], scores, -1000000000.0)
            p = _softmax(scores, axis=-1)
            o = torch.einsum('bhqk,bhke->bhqe', p, v)
            o = torch.einsum('bhte,hed->btd', o, Wo)
            mem = o[:, :M, :].clone()
            outs.append(o[:, M:, :])
        return torch.concatenate(outs, axis=1)

    def _B_ref(x, w_gate, w_up, w_down):
        g = _silu(x @ w_gate)
        u = x @ w_up
        return g * u @ w_down
    x = A_x
    xn = _layernorm(x, axis=-1)
    return x + _A_ref(xn, A_mem0, A_Wq, A_Wk, A_Wv, A_Wo, A_seg_len) + _B_ref(xn, B_w_gate, B_w_up, B_w_down)
```

```cpp
#include <hip/hip_runtime.h>
#include <stdint.h>

typedef __bf16 bf16;
typedef __bf16 bf16x2 __attribute__((ext_vector_type(2)));
typedef __bf16 bf16x4 __attribute__((ext_vector_type(4)));
typedef __bf16 bf16x8 __attribute__((ext_vector_type(8)));
typedef __bf16 bf16x16 __attribute__((ext_vector_type(16)));
typedef float f32x2 __attribute__((ext_vector_type(2)));
typedef float f32x4 __attribute__((ext_vector_type(4)));
typedef unsigned u32x2 __attribute__((ext_vector_type(2)));
typedef unsigned u32x4 __attribute__((ext_vector_type(4)));
typedef unsigned u32x8 __attribute__((ext_vector_type(8)));
#define LAS __attribute__((address_space(3)))
#define DI __device__ __forceinline__
typedef __amdgpu_buffer_rsrc_t rsrc_t;

constexpr int NSEG = 64, DM = 640, NH = 32, HD = 192, FF = 1536;
constexpr unsigned SENT = 0xFFFFFFFFu;
constexpr unsigned SPIN_CAP = 1u << 17;

constexpr unsigned MB = 1u << 20;
constexpr unsigned O_COMM = 0;
constexpr unsigned O_U = 4 * MB;
constexpr unsigned O_FLAG = 44 * MB;
constexpr unsigned O_XN = 45 * MB;
constexpr unsigned O_KX = 48 * MB;
constexpr unsigned O_VX = 72 * MB;
constexpr unsigned O_VW = 96 * MB;
constexpr unsigned O_HF = 176 * MB;
constexpr unsigned O_KM = 182 * MB;
constexpr unsigned O_PTX = 188 * MB;
constexpr unsigned O_PTM = 192 * MB;
constexpr unsigned O_DBG = 194 * MB;
constexpr unsigned WS_SPAN = 200 * MB;
constexpr unsigned F_K = O_FLAG + 0 * 512, F_V = O_FLAG + 1 * 512, F_W = O_FLAG + 2 * 512, F_KM = O_FLAG + 3 * 512,
                   F_U = O_FLAG + 4 * 512, F_PT = O_FLAG + 5 * 512, F_F = O_FLAG + 6 * 512, F_PAR = O_FLAG + 7 * 512, F_ABORT = O_FLAG + 7 * 512 + 128;
constexpr int LDS_BYTES = 160 * 1024;

struct Params {
    const bf16 *x, *mem0, *Wq, *Wk, *Wv, *Wo, *wg, *wu, *wd;
    bf16* out;
    unsigned char* ws;
    unsigned mask, pad;
};

extern __shared__ __attribute__((aligned(16))) unsigned char smem[];

DI rsrc_t mkrsrc(const void* p, unsigned bytes) { return __builtin_amdgcn_make_buffer_rsrc((void*)p, 0, (int)bytes, 0x00020000); }
DI u32x4 mksrd(const void* p, unsigned bytes) {
    const uint64_t a = (uint64_t)p;
    u32x4 r = {(unsigned)a, (unsigned)(a >> 32) & 0xffffu, bytes, 0x00020000u};
    return r;
}
DI u32x4 ld16(rsrc_t r, unsigned v, unsigned s) { return __builtin_amdgcn_raw_buffer_load_b128(r, v, s, 16); }
DI unsigned ld4(rsrc_t r, unsigned v, unsigned s) { return __builtin_amdgcn_raw_buffer_load_b32(r, v, s, 16); }
DI void st16(u32x4 d, rsrc_t r, unsigned v, unsigned s) { __builtin_amdgcn_raw_buffer_store_b128(d, r, v, s, 16); }
DI void st4(unsigned d, rsrc_t r, unsigned v, unsigned s) { __builtin_amdgcn_raw_buffer_store_b32(d, r, v, s, 16); }
DI void dma16(u32x4 srd, unsigned voff, unsigned soff, unsigned dst) {
    asm volatile("s_mov_b32 m0, %0\n\ts_nop 0\n\tbuffer_load_dwordx4 %1, %2, %3 offen lds" ::"s"(dst), "v"(voff), "s"(srd), "s"(soff) : "memory");
}
DI void dma16_sc1(u32x4 srd, unsigned voff, unsigned soff, unsigned dst) {
    asm volatile("s_mov_b32 m0, %0\n\ts_nop 0\n\tbuffer_load_dwordx4 %1, %2, %3 offen sc1 lds" ::"s"(dst), "v"(voff), "s"(srd), "s"(soff) : "memory");
}
#define VMWAIT(n) asm volatile("s_waitcnt vmcnt(" #n ")" ::: "memory")
template <int N> DI void vmwait() { asm volatile("s_waitcnt vmcnt(%0)" ::"i"(N) : "memory"); }
DI void bar() { asm volatile("s_waitcnt lgkmcnt(0)\n\ts_barrier" ::: "memory"); }
#define FENCE asm volatile("" ::: "memory")
DI u32x4 lds16(unsigned a) { return *(LAS u32x4*)(uintptr_t)(a); }
DI u32x2 lds8(unsigned a) { return *(LAS u32x2*)(uintptr_t)(a); }
DI void ldsw16(unsigned a, u32x4 v) { *(LAS u32x4*)(uintptr_t)(a) = v; }
DI void ldsw8(unsigned a, u32x2 v) { *(LAS u32x2*)(uintptr_t)(a) = v; }
DI void ldsw4(unsigned a, unsigned v) { *(LAS unsigned*)(uintptr_t)(a) = v; }
DI u32x2 ldstr(unsigned a) { return __builtin_bit_cast(u32x2, __builtin_amdgcn_ds_read_tr16_b64_v4bf16((LAS bf16x4*)(uintptr_t)(a))); }
DI unsigned pk(float a, float b) { f32x2 v = {a, b}; return __builtin_bit_cast(unsigned, __builtin_convertvector(v, bf16x2)); }
DI float bflo(unsigned u) { return __builtin_bit_cast(float, u << 16); }
DI float bfhi(unsigned u) { return __builtin_bit_cast(float, u & 0xffff0000u); }
DI f32x4 mfma(u32x4 a, u32x4 b, f32x4 c) { return __builtin_amdgcn_mfma_f32_16x16x32_bf16(__builtin_bit_cast(bf16x8, a), __builtin_bit_cast(bf16x8, b), c, 0, 0, 0); }
DI f32x4 smf(u32x4 a, u32x8 b, f32x4 c, int idx) { return __builtin_amdgcn_smfmac_f32_16x16x64_bf16(__builtin_bit_cast(bf16x8, a), __builtin_bit_cast(bf16x16, b), c, idx, 0, 0); }
DI unsigned umax3(unsigned a, unsigned b, unsigned c) { return max(max(a, b), c); }
DI unsigned mx4(u32x4 v, unsigned m) { return umax3(umax3(v.x, v.y, m), v.z, v.w); }
DI bool none(bool c) { return __builtin_amdgcn_ballot_w64(c) == 0; }
DI u32x4 unsent(u32x4 v) { v.x = min(v.x, SENT - 1); v.y = min(v.y, SENT - 1); v.z = min(v.z, SENT - 1); v.w = min(v.w, SENT - 1); return v; }
DI void swap32(unsigned& a, unsigned& b) { auto r = __builtin_amdgcn_permlane32_swap(a, b, false, false); a = r[0]; b = r[1]; }
DI void swap16(unsigned& a, unsigned& b) { auto r = __builtin_amdgcn_permlane16_swap(a, b, false, false); a = r[0]; b = r[1]; }
DI float fswapadd32(float a, float b) { unsigned x = __builtin_bit_cast(unsigned, a), y = __builtin_bit_cast(unsigned, b); swap32(x, y); return __builtin_bit_cast(float, x) + __builtin_bit_cast(float, y); }
template <int CTRL> DI float dppf(float v) { return __builtin_bit_cast(float, __builtin_amdgcn_update_dpp(0, __builtin_bit_cast(int, v), CTRL, 0xf, 0xf, true)); }
template <int CTRL> DI unsigned dppu(unsigned v) { return (unsigned)__builtin_amdgcn_update_dpp(0, (int)v, CTRL, 0xf, 0xf, true); }
constexpr int ROR(int n) { return 0x120 + n; }
constexpr int SHL(int n) { return 0x100 + n; }
DI int kperm32(int g, int j) { return (j < 4) ? 4 * g + j : 16 + 4 * g + (j - 4); }

struct Ctx { rsrc_t ws; u32x4 wsd; int lane, w, par; bool dead; };

DI bool spin_over(Ctx& c, unsigned& spin) {
    ++spin;
    if ((spin & 1023u) == 0u) {
        if (spin >= SPIN_CAP) { st4(1u, c.ws, F_ABORT, 0); return true; }
        if (__builtin_amdgcn_readfirstlane(ld4(c.ws, F_ABORT, 0)) != 0u) return true;
    }
    return false;
}
DI void wait_flags(Ctx& c, unsigned voff, unsigned need) {
    unsigned spin = 0;
#pragma unroll 1
    while (!c.dead) {
        FENCE;
        const unsigned v = ld4(c.ws, voff, 0);
        if (none(v < need)) break;
        if (spin_over(c, spin)) c.dead = true;
    }
    FENCE;
}
DI void put_flag(Ctx& c, unsigned base, int prod, unsigned val) { if (c.lane == 0) st4(val, c.ws, base + prod * 16 + c.w * 4, 0); }
DI unsigned comm_base(const Ctx& c, int s) { return O_COMM + s * 65536u + (unsigned)((c.par ^ (s >> 5)) << 13); }
DI unsigned mem_item(int item) { const int ch = item >> 6; return (ch / 3) * 16384 + 5120 + (ch % 3) * 1024 + (item & 63) * 16; }
DI unsigned u_base(const Ctx& c, int s, int ct) { const unsigned b = s * 40 + ct; return O_U + b * 16384u + (((unsigned)c.par ^ (b >> 7)) & 1u) * 8192u; }

template <int NSLAB, int NI, int NB, class Issue, class Consume>
DI void stage_weights(Issue issue, Consume consume) {
    constexpr int PW = NI / 4;
#pragma unroll
    for (int t = 0; t < NB - 1 && t < NSLAB; ++t) issue(t, (t % NB) * NI * 1024);
#pragma unroll
    for (int t = 0; t < NSLAB; ++t) {
        constexpr int dummy = 0; (void)dummy;
        const int ahead = (NSLAB - 1 - t < NB - 2) ? NSLAB - 1 - t : NB - 2;
        if (ahead == 0) vmwait<0>(); else if (ahead == 1) vmwait<PW>(); else vmwait<2 * PW>();
        bar();
        if (t + NB - 1 < NSLAB) issue(t + NB - 1, ((t + NB - 1) % NB) * NI * 1024);
        consume(t, (t % NB) * NI * 1024);
    }
    bar();
}

template <int NT, class Epi>
DI void token_gemm(Ctx& c, const u32x4 (&W)[20][NT], int s0, int sstep, Epi epi, unsigned flagbase, int prod) {
    const unsigned v = c.lane * 16;
    auto issue = [&](int s, int it) {
        const int buf = it % 3;
#pragma unroll
        for (int i = 0; i < 10; ++i) dma16(c.wsd, v, O_XN + s * 40960 + (c.w * 10 + i) * 1024, buf * 40960 + (c.w * 10 + i) * 1024);
    };
    issue(s0, 0);
    if (s0 + sstep < NSEG) issue(s0 + sstep, 1);
    vmwait<0>();
    int it = 0;
#pragma unroll 1
    for (int s = s0; s < NSEG; s += sstep, ++it) {
        bar();
        const unsigned xb = (it % 3) * 40960 + v;
        f32x4 acc[NT][2];
#pragma unroll
        for (int t = 0; t < NT; ++t) { acc[t][0] = (f32x4){0, 0, 0, 0}; acc[t][1] = (f32x4){0, 0, 0, 0}; }
#pragma unroll
        for (int ks = 0; ks < 20; ++ks) {
            if (ks == 10) {
                vmwait<0>();
                if (flagbase && it > 0) put_flag(c, flagbase, prod, (unsigned)(s - sstep + 1));
                if (s + 2 * sstep < NSEG) issue(s + 2 * sstep, it + 2);
            }
            const u32x4 x0 = lds16(xb + (ks * 2) * 1024), x1 = lds16(xb + (ks * 2 + 1) * 1024);
#pragma unroll
            for (int t = 0; t < NT; ++t) { acc[t][0] = mfma(W[ks][t], x0, acc[t][0]); acc[t][1] = mfma(W[ks][t], x1, acc[t][1]); }
        }
        epi(s, acc);
        if (flagbase && it == 0) { vmwait<0>(); put_flag(c, flagbase, prod, (unsigned)(s + 1)); }
        if (c.dead) break;
    }
    vmwait<0>();
    if (flagbase) put_flag(c, flagbase, prod, (unsigned)NSEG);
    bar();
}

DI void load_w192(Ctx& c, const bf16* Wp, int h, u32x4 (&W)[20][3]) {
    const u32x4 srd = mksrd(Wp, DM * NH * HD * 2);
    const int r = c.lane >> 3, ch = c.lane & 7;
    const unsigned v = r * (NH * HD * 2) + ((ch ^ (((r >> 1) & 3) << 1)) * 16);
    const int g = c.lane >> 4, q = (c.lane >> 2) & 3, p = c.lane & 3;
    const int rr = (4 * g + q) & 7, rb = (4 * g + q) >> 3, key = (rr >> 1) & 3;
    auto issue = [&](int t, unsigned buf) {
#pragma unroll
        for (int i = 0; i < 12; ++i) { const int pi = c.w * 12 + i, pn = pi >> 4, blk = pi & 15;
            dma16(srd, v, (t * 128 + blk * 8) * (NH * HD * 2) + (h * HD + pn * 64) * 2, buf + pi * 1024); }
    };
    unsigned a[3];
#pragma unroll
    for (int t = 0; t < 3; ++t) { const int T = 3 * c.w + t; a[t] = (T >> 2) * 16384 + rb * 1024 + rr * 128 + (((2 * (T & 3) + (p >> 1)) ^ (key << 1)) * 16) + (p & 1) * 8; }
    auto consume = [&](int t, unsigned buf) {
#pragma unroll
        for (int k = 0; k < 4; ++k)
#pragma unroll
            for (int tl = 0; tl < 3; ++tl) {
                const u32x2 lo = ldstr(buf + a[tl] + (k * 4) * 1024), hi = ldstr(buf + a[tl] + (k * 4 + 2) * 1024);
                W[t * 4 + k][tl] = (u32x4){lo.x, lo.y, hi.x, hi.y};
            }
    };
    stage_weights<5, 48, 3>(issue, consume);
}
DI void pieces192(const f32x4 (&acc)[3][2], u32x4& p0, u32x4& p1, u32x4& p2) {
    p0 = (u32x4){pk(acc[0][0][0], acc[0][0][1]), pk(acc[0][0][2], acc[0][0][3]), pk(acc[1][0][0], acc[1][0][1]), pk(acc[1][0][2], acc[1][0][3])};
    p1 = (u32x4){pk(acc[0][1][0], acc[0][1][1]), pk(acc[0][1][2], acc[0][1][3]), pk(acc[1][1][0], acc[1][1][1]), pk(acc[1][1][2], acc[1][1][3])};
    unsigned a0 = pk(acc[2][0][0], acc[2][0][1]), a1 = pk(acc[2][0][2], acc[2][0][3]), b0 = pk(acc[2][1][0], acc[2][1][1]), b1 = pk(acc[2][1][2], acc[2][1][3]);
    swap16(a0, b0); swap16(a1, b1);
    p2 = (u32x4){a0, a1, b0, b1};
}

DI void role_kv(Ctx& c, const bf16* Wp, int h, unsigned obase, unsigned flag) {
    u32x4 W[20][3];
    load_w192(c, Wp, h, W);
    const int tok = c.lane & 15, g = c.lane >> 4;
    const unsigned o01 = (6 * c.w + g) * 512 + tok * 16, o2 = (6 * c.w + 4 + (g >> 1)) * 512 + ((g & 1) * 16 + tok) * 16;
    auto epi = [&](int s, const f32x4 (&acc)[3][2]) {
        u32x4 p0, p1, p2; pieces192(acc, p0, p1, p2);
        const unsigned b = obase + (s * NH + h) * 12288;
        st16(p0, c.ws, o01, b); st16(p1, c.ws, o01 + 256, b); st16(p2, c.ws, o2, b);
    };
    token_gemm<3>(c, W, 0, 1, epi, flag, h);
}

DI void role_q(Ctx& c, const Params& P, int h) {
    u32x4 W[20][3];
    load_w192(c, P.Wq, h, W);
    const int tok = c.lane & 15, g = c.lane >> 4;
    constexpr unsigned QI = 3 * 40960;
    const unsigned o01 = (6 * c.w + g) * 512 + tok * 16, o2 = (6 * c.w + 4 + (g >> 1)) * 512 + ((g & 1) * 16 + tok) * 16;
    const float sc = 0.07216878364870322f * 1.4426950408889634f;
    auto epi = [&](int s, const f32x4 (&acc)[3][2]) {
        u32x4 p0, p1, p2; pieces192(acc, p0, p1, p2);
        ldsw16(QI + o01, p0); ldsw16(QI + o01 + 256, p1); ldsw16(QI + o2, p2);
        bar();
        if (c.w < 2) {
            const int tt = c.w;
            wait_flags(c, F_K + h * 16 + (c.lane & 3) * 4, (unsigned)(s + 1));
            u32x4 k0[6], k1[6], km[6], qf[6];
            const unsigned kb = O_KX + (s * NH + h) * 12288;
#pragma unroll
            for (int es = 0; es < 6; ++es) { k0[es] = ld16(c.ws, g * 512 + tok * 16 + es * 2048, kb); k1[es] = ld16(c.ws, g * 512 + 256 + tok * 16 + es * 2048, kb); }
#pragma unroll
            for (int es = 0; es < 6; ++es) qf[es] = lds16(QI + (4 * es + g) * 512 + (16 * tt + tok) * 16);
            f32x4 x0 = {0, 0, 0, 0}, x1 = {0, 0, 0, 0}, xm = {0, 0, 0, 0};
#pragma unroll
            for (int es = 0; es < 6; ++es) { x0 = mfma(k0[es], qf[es], x0); x1 = mfma(k1[es], qf[es], x1); }
            wait_flags(c, F_KM + h * 16 + (c.lane & 3) * 4, (unsigned)(s + 1));
            const unsigned mb = O_KM + (s * NH + h) * 3072;
#pragma unroll
            for (int es = 0; es < 6; ++es) km[es] = ld16(c.ws, g * 128 + (tok & 7) * 16 + es * 512, mb);
#pragma unroll
            for (int es = 0; es < 6; ++es) xm = mfma(km[es], qf[es], xm);
            const float NEG = -__builtin_inff();
            const int T = 16 * tt + tok;
            float mx = NEG;
#pragma unroll
            for (int r = 0; r < 4; ++r) {
                if (4 * g + r > T) x0[r] = NEG;
                if (16 + 4 * g + r > T) x1[r] = NEG;
                if (g >= 2) xm[r] = NEG;
                mx = fmaxf(mx, fmaxf(x0[r], fmaxf(x1[r], xm[r])));
            }
            mx = fmaxf(mx, __shfl_xor(mx, 16)); mx = fmaxf(mx, __shfl_xor(mx, 32));
            float sum = 0;
#pragma unroll
            for (int r = 0; r < 4; ++r) {
                x0[r] = __builtin_amdgcn_exp2f((x0[r] - mx) * sc); x1[r] = __builtin_amdgcn_exp2f((x1[r] - mx) * sc); xm[r] = __builtin_amdgcn_exp2f((xm[r] - mx) * sc);
                sum += x0[r] + x1[r] + xm[r];
            }
            sum += __shfl_xor(sum, 16); sum += __shfl_xor(sum, 32);
            const float inv = __builtin_amdgcn_rcpf(sum);
            const u32x4 px = {pk(x0[0] * inv, x0[1] * inv), pk(x0[2] * inv, x0[3] * inv), pk(x1[0] * inv, x1[1] * inv), pk(x1[2] * inv, x1[3] * inv)};
            st16(px, c.ws, c.lane * 16, O_PTX + ((s * NH + h) * 2 + tt) * 1024);
            unsigned a0 = pk(xm[0] * inv, xm[1] * inv), a1 = pk(xm[2] * inv, xm[3] * inv), b0 = a0, b1 = a1;
            swap16(a0, b0); swap16(a1, b1);
            if (c.lane < 16) st16((u32x4){a0, a1, b0, b1}, c.ws, (h & 3) * 256 + tok * 16, O_PTM + ((s * 8 + (h >> 2)) * 2 + tt) * 1024);
            vmwait<0>();
        }
        put_flag(c, F_PT, h, (unsigned)(s + 1));
    };
    token_gemm<3>(c, W, 0, 1, epi, 0, h);
}

DI void role_f(Ctx& c, const Params& P, int id) {
    const int fb = id % 12, rep = id / 12;
    u32x4 W[20][4];
    {
        const u32x4 sg = mksrd(P.wg, DM * FF * 2), su = mksrd(P.wu, DM * FF * 2);
        const int r = c.lane >> 3, ch = c.lane & 7;
        const unsigned v = r * (FF * 2) + ((ch ^ (((r >> 1) & 3) << 1)) * 16);
        const int g = c.lane >> 4, q = (c.lane >> 2) & 3, p = c.lane & 3;
        const int rr = (4 * g + q) & 7, rb = (4 * g + q) >> 3, key = (rr >> 1) & 3;
        const u32x4 sw = (c.w < 2) ? sg : su;
        auto issue = [&](int t, unsigned buf) {
#pragma unroll
            for (int i = 0; i < 8; ++i) dma16(sw, v, (t * 64 + i * 8) * (FF * 2) + (fb * 128 + (c.w & 1) * 64) * 2, buf + (c.w * 8 + i) * 1024);
        };
        unsigned a[2];
#pragma unroll
        for (int t = 0; t < 2; ++t) { const int T = 2 * c.w + t; a[t] = (T >> 2) * 8192 + rb * 1024 + rr * 128 + (((2 * (T & 3) + (p >> 1)) ^ (key << 1)) * 16) + (p & 1) * 8; }
        auto consume = [&](int t, unsigned buf) {
#pragma unroll
            for (int k = 0; k < 2; ++k)
#pragma unroll
                for (int tl = 0; tl < 4; ++tl) {
                    const unsigned b = buf + a[tl & 1] + (tl >> 1) * 16384;
                    const u32x2 lo = ldstr(b + (k * 4) * 1024), hi = ldstr(b + (k * 4 + 2) * 1024);
                    W[t * 2 + k][tl] = (u32x4){lo.x, lo.y, hi.x, hi.y};
                }
        };
        stage_weights<10, 32, 4>(issue, consume);
    }
    auto epi = [&](int s, const f32x4 (&acc)[4][2]) {
#pragma unroll
        for (int tt = 0; tt < 2; ++tt) {
            float hv[8];
#pragma unroll
            for (int j = 0; j < 8; ++j) { const float gt = acc[j >> 2][tt][j & 3], up = acc[2 + (j >> 2)][tt][j & 3];
                hv[j] = gt * __builtin_amdgcn_rcpf(1.f + __builtin_amdgcn_exp2f(-1.4426950408889634f * gt)) * up; }
            st16((u32x4){pk(hv[0], hv[1]), pk(hv[2], hv[3]), pk(hv[4], hv[5]), pk(hv[6], hv[7])}, c.ws, c.lane * 16, O_HF + ((s * 48 + fb * 4 + c.w) * 2 + tt) * 1024);
        }
    };
    token_gemm<4>(c, W, rep, 2, epi, F_F, id);
}

DI int epiece(int p, int j) { const int w = p / 6, u = p % 6; return (u < 4) ? 48 * w + kperm32(u, j) : 48 * w + 32 + 8 * (u - 4) + j; }

DI void role_w(Ctx& c, const Params& P, int h) {
    u32x4 W[10][6];
    const int g = c.lane >> 4, tok = c.lane & 15;
    {
        const u32x4 srd = mksrd(P.Wo, NH * HD * DM * 2);
        const int r = c.lane >> 3, ch = c.lane & 7;
        const unsigned v = r * (DM * 2) + ((ch ^ (((r >> 1) & 3) << 1)) * 16);
        const int q = (c.lane >> 2) & 3, p = c.lane & 3;
        auto issue = [&](int t, unsigned buf) {
#pragma unroll
            for (int i = 0; i < 12; ++i) { const int pi = c.w * 12 + i, pn = pi / 24, blk = pi % 24;
                dma16(srd, v, (h * HD + blk * 8) * (DM * 2) + (t * 128 + pn * 64) * 2, buf + pi * 1024); }
        };
        unsigned a[6][2];
#pragma unroll
        for (int es = 0; es < 6; ++es)
#pragma unroll
            for (int hf = 0; hf < 2; ++hf) { const int row = epiece(4 * es + g, 4 * hf + q), key = (row >> 1) & 3;
                a[es][hf] = (c.w >> 1) * 24576 + row * 128 + (((4 * (c.w & 1) + (p >> 1)) ^ (key << 1)) * 16) + (p & 1) * 8; }
        auto consume = [&](int t, unsigned buf) {
#pragma unroll
            for (int tl = 0; tl < 2; ++tl)
#pragma unroll
                for (int es = 0; es < 6; ++es) {
                    const u32x2 lo = ldstr(buf + (a[es][0] ^ (tl * 32))), hi = ldstr(buf + (a[es][1] ^ (tl * 32)));
                    W[t * 2 + tl][es] = (u32x4){lo.x, lo.y, hi.x, hi.y};
                }
        };
        stage_weights<5, 48, 3>(issue, consume);
    }
    const unsigned v = c.lane * 16;
    auto issue = [&](int s) {
        wait_flags(c, F_V + h * 16 + (c.lane & 3) * 4, (unsigned)(s + 1));
#pragma unroll
        for (int i = 0; i < 3; ++i) dma16_sc1(c.wsd, v, O_VX + (s * NH + h) * 12288 + (c.w * 3 + i) * 1024, (s & 1) * 12288 + (c.w * 3 + i) * 1024);
    };
    issue(0);
#pragma unroll 1
    for (int s = 0; s < NSEG; ++s) {
        vmwait<0>();
        if (s > 0) put_flag(c, F_W, h, (unsigned)s);
        bar();
        u32x4 vf[2][6];
#pragma unroll
        for (int es = 0; es < 6; ++es)
#pragma unroll
            for (int tt = 0; tt < 2; ++tt) vf[tt][es] = lds16((s & 1) * 12288 + (4 * es + g) * 512 + (16 * tt + tok) * 16);
        if (s + 1 < NSEG) issue(s + 1);
#pragma unroll
        for (int t = 0; t < 10; ++t) {
            f32x4 a0 = {0, 0, 0, 0}, a1 = {0, 0, 0, 0};
#pragma unroll
            for (int es = 0; es < 6; ++es) { a0 = mfma(vf[0][es], W[t][es], a0); a1 = mfma(vf[1][es], W[t][es], a1); }
            const int ct = (t >> 1) * 8 + 2 * c.w + (t & 1);
            st16((u32x4){pk(a0[0], a0[1]), pk(a0[2], a0[3]), pk(a1[0], a1[1]), pk(a1[2], a1[3])}, c.ws, v, O_VW + ((s * 40 + ct) * NH + h) * 1024);
        }
        if (c.dead) break;
    }
    vmwait<0>();
    put_flag(c, F_W, h, (unsigned)NSEG);
    bar();
}

DI void role_out(Ctx& c, const Params& P, int oi) {
    const int cg = oi % 10, rep = oi / 10;
    const int g = c.lane >> 4, n = c.lane & 15;
    u32x4 W[12][4];
    {
        const u32x4 srd = mksrd(P.wd, FF * DM * 2);
        const int r = c.lane >> 3, ch = c.lane & 7;
        const unsigned v = r * (DM * 2) + ((ch ^ (((r >> 1) & 3) << 1)) * 16);
        const int q = (c.lane >> 2) & 3, p = c.lane & 3;
        const int rr = (4 * g + q) & 7, rb = (4 * g + q) >> 3, key = (rr >> 1) & 3;
        auto issue = [&](int t, unsigned buf) {
#pragma unroll
            for (int i = 0; i < 12; ++i) dma16(srd, v, (c.w * 384 + t * 96 + i * 8) * (DM * 2) + cg * 128, buf + (c.w * 12 + i) * 1024);
        };
        const unsigned a = c.w * 12288 + rb * 1024 + rr * 128 + (((p >> 1) ^ (key << 1)) * 16) + (p & 1) * 8;
        auto consume = [&](int t, unsigned buf) {
#pragma unroll
            for (int k = 0; k < 3; ++k)
#pragma unroll
                for (int tl = 0; tl < 4; ++tl) {
                    const u32x2 lo = ldstr(buf + (a ^ (tl * 32)) + (k * 4) * 1024), hi = ldstr(buf + (a ^ (tl * 32)) + (k * 4 + 2) * 1024);
                    W[t * 3 + k][tl] = (u32x4){lo.x, lo.y, hi.x, hi.y};
                }
        };
        stage_weights<4, 48, 3>(issue, consume);
    }
    const rsrc_t rx = mkrsrc(P.x, NSEG * 32 * DM * 2), ro = mkrsrc(P.out, NSEG * 32 * DM * 2);
    const unsigned v = c.lane * 16, fl = (c.w * 8 + ((c.lane >> 2) & 7)) * 16 + (c.lane & 3) * 4;
#pragma unroll 1
    for (int s = rep; s < NSEG; s += 12) {
        f32x4 acc[4][2];
#pragma unroll
        for (int t = 0; t < 4; ++t) { acc[t][0] = (f32x4){0, 0, 0, 0}; acc[t][1] = (f32x4){0, 0, 0, 0}; }
        wait_flags(c, F_F + ((s & 1) * 12 + 3 * c.w + ((c.lane >> 2) % 3)) * 16 + (c.lane & 3) * 4, (unsigned)(s + 1));
        {
            u32x4 hf[12][2];
#pragma unroll
            for (int k = 0; k < 12; ++k) { hf[k][0] = ld16(c.ws, v + k * 2048, O_HF + (s * 48 + c.w * 12) * 2048); hf[k][1] = ld16(c.ws, v + k * 2048 + 1024, O_HF + (s * 48 + c.w * 12) * 2048); }
#pragma unroll
            for (int k = 0; k < 12; ++k)
#pragma unroll
                for (int t = 0; t < 4; ++t) { acc[t][0] = mfma(W[k][t], hf[k][0], acc[t][0]); acc[t][1] = mfma(W[k][t], hf[k][1], acc[t][1]); }
        }
        wait_flags(c, F_W + fl, (unsigned)(s + 1));
        u32x4 z[8][4];
#pragma unroll
        for (int hh = 0; hh < 8; ++hh)
#pragma unroll
            for (int t = 0; t < 4; ++t) z[hh][t] = ld16(c.ws, v + hh * 1024, O_VW + ((s * 40 + cg * 4 + t) * NH + c.w * 8) * 1024);
        const int tokT = 16 * (g & 1) + n;
        const unsigned oo = (s * 32 + tokT) * (DM * 2) + (cg * 64 + c.w * 16 + (g >> 1) * 8) * 2;
        const u32x4 xr = __builtin_amdgcn_raw_buffer_load_b128(rx, oo, 0, 0);
        wait_flags(c, F_U + fl, (unsigned)(s + 1));
        u32x4 zu[2][4];
#pragma unroll
        for (int m = 0; m < 2; ++m)
#pragma unroll
            for (int t = 0; t < 4; ++t) zu[m][t] = ld16(c.ws, v + m * 1024, u_base(c, s, cg * 4 + t) + c.w * 2048);
        wait_flags(c, F_PT + fl, (unsigned)(s + 1));
#pragma unroll
        for (int hh = 0; hh < 8; ++hh) {
            const u32x4 p0 = ld16(c.ws, v + hh * 2048, O_PTX + (s * NH + c.w * 8) * 2048), p1 = ld16(c.ws, v + hh * 2048 + 1024, O_PTX + (s * NH + c.w * 8) * 2048);
#pragma unroll
            for (int t = 0; t < 4; ++t) { acc[t][0] = mfma(z[hh][t], p0, acc[t][0]); acc[t][1] = mfma(z[hh][t], p1, acc[t][1]); }
        }
#pragma unroll
        for (int m = 0; m < 2; ++m) {
            const u32x4 p0 = ld16(c.ws, v + m * 2048, O_PTM + (s * 8 + c.w * 2) * 2048), p1 = ld16(c.ws, v + m * 2048 + 1024, O_PTM + (s * 8 + c.w * 2) * 2048);
#pragma unroll
            for (int t = 0; t < 4; ++t) { acc[t][0] = mfma(zu[m][t], p0, acc[t][0]); acc[t][1] = mfma(zu[m][t], p1, acc[t][1]); }
        }
        bar();
#pragma unroll
        for (int t = 0; t < 4; ++t)
#pragma unroll
            for (int tt = 0; tt < 2; ++tt) ldsw16(((c.w * 4 + t) * 2 + tt) * 1024 + v, __builtin_bit_cast(u32x4, acc[t][tt]));
        bar();
        f32x4 lo = {bflo(xr.x), bfhi(xr.x), bflo(xr.y), bfhi(xr.y)}, hi = {bflo(xr.z), bfhi(xr.z), bflo(xr.w), bfhi(xr.w)};
#pragma unroll
        for (int ww = 0; ww < 4; ++ww) {
            const unsigned a = ((ww * 4 + c.w) * 2 + (g & 1)) * 1024 + ((g >> 1) * 32 + n) * 16;
            lo += __builtin_bit_cast(f32x4, lds16(a)); hi += __builtin_bit_cast(f32x4, lds16(a + 256));
        }
        __builtin_amdgcn_raw_buffer_store_b128((u32x4){pk(lo[0], lo[1]), pk(lo[2], lo[3]), pk(hi[0], hi[1]), pk(hi[2], hi[3])}, ro, oo, 0, 0);
        if (c.dead) break;
    }
}

constexpr unsigned L_MEM = 0, L_QK = 10240, L_KX = 10240 + 6400, L_RES = 32768;
constexpr int NL = 12;

DI void poll_mem(Ctx& c, int s, const unsigned (&mo)[3]) {
    const unsigned b = comm_base(c, s);
    u32x4 a0, a1, a2; unsigned spin = 0;
#pragma unroll 1
    for (;;) {
        FENCE;
        a0 = ld16(c.ws, mo[0], b); a1 = ld16(c.ws, mo[1], b); a2 = ld16(c.ws, mo[2], b);
        if (none(mx4(a2, mx4(a1, mx4(a0, 0))) == SENT)) break;
        if (c.dead || spin_over(c, spin)) { c.dead = true; break; }
    }
    const unsigned l = L_MEM + (c.w * 160 + c.lane) * 16;
    ldsw16(l, a0); ldsw16(l + 1024, a1);
    if (c.lane < 32) ldsw16(l + 2048, a2);
}
DI u32x8 cat(u32x2 a, u32x2 b, u32x2 c, u32x2 d) { return (u32x8){a.x, a.y, b.x, b.y, c.x, c.y, d.x, d.y}; }
DI u32x8 cat(u32x4 a, u32x4 b) { return (u32x8){a.x, a.y, a.z, a.w, b.x, b.y, b.z, b.w}; }

DI void role_s(Ctx& c, const Params& P, int h) {
    u32x8 W[60 - NL];
    const int g = c.lane >> 4, n = c.lane & 15, q = (c.lane >> 2) & 3, p = c.lane & 3;
    const unsigned res = L_RES + c.w * (NL * 2048) + c.lane * 16;
    {
        u32x8 T[NL];
        const u32x4 sq = mksrd(P.Wq, DM * NH * HD * 2), sk = mksrd(P.Wk, DM * NH * HD * 2);
        const u32x4 sw = (c.w < 2) ? sq : sk;
        const int r = c.lane >> 3, ch = c.lane & 7;
        const unsigned v0 = r * (NH * HD * 2) + ((ch ^ ((r >> 2) << 1)) * 16), v1 = v0 ^ 64;
        auto issue = [&](int t, unsigned buf) {
#pragma unroll
            for (int i = 0; i < 12; ++i) { const int pi = (c.w & 1) * 12 + i, pn = pi >> 3, blk = pi & 7;
                dma16(sw, (i & 1) ? v1 : v0, (t * 64 + blk * 8) * (NH * HD * 2) + (h * HD + pn * 64) * 2, buf + (c.w * 12 + i) * 1024); }
        };
        const int row0 = 16 * (g >> 1) + 8 * (q >> 1) + 4 * (g & 1) + (q & 1), key = (row0 >> 2) & 3;
        unsigned a[3];
#pragma unroll
        for (int t = 0; t < 3; ++t) { const int TT = 3 * c.w + t; a[t] = (TT >> 2) * 8192 + row0 * 128 + (((2 * (TT & 3) + (p >> 1)) ^ (key << 1)) * 16) + (p & 1) * 8; }
        auto consume = [&](int t, unsigned buf) {
#pragma unroll
            for (int f = 0; f < 6; ++f) {
                const unsigned b = buf + a[f % 3] + (f / 3) * 24576;
                const u32x8 x = cat(ldstr(b), ldstr(b + 256), ldstr(b + 4096), ldstr(b + 4096 + 256));
                if (t * 6 + f < 60 - NL) W[t * 6 + f] = x; else T[t * 6 + f - (60 - NL)] = x;
            }
        };
        stage_weights<10, 48, 3>(issue, consume);
#pragma unroll
        for (int f = 0; f < NL; ++f) { ldsw16(res + f * 2048, (u32x4){T[f][0], T[f][1], T[f][2], T[f][3]}); ldsw16(res + f * 2048 + 1024, (u32x4){T[f][4], T[f][5], T[f][6], T[f][7]}); }
    }
    unsigned mo[3];
    mo[0] = mem_item(c.w * 160 + c.lane); mo[1] = mem_item(c.w * 160 + 64 + c.lane); mo[2] = mem_item(c.w * 160 + 128 + (c.lane & 31));
    const int idx = (c.lane & 8) ? 0xEEEE : 0x4444;
    const unsigned kv = g * 512 + kperm32(2 * (c.w >> 1) + (n >> 3), n & 7) * 16;
    auto issue_kx = [&](int s) {
        wait_flags(c, F_K + h * 16 + (c.lane & 3) * 4, (unsigned)(s + 1));
#pragma unroll
        for (int i = 0; i < 3; ++i) dma16_sc1(c.wsd, kv, O_KX + (s * NH + h) * 12288 + ((c.w & 1) * 3 + i) * 2048, L_KX + (c.w * 3 + i) * 1024);
    };
    issue_kx(0);
    unsigned wq[3];
    wq[0] = L_QK + g * 1600 + (8 * (6 * c.w + (n >> 2)) + (n & 3)) * 8; wq[1] = wq[0] + 32; wq[2] = L_QK + g * 1600 + (8 * (6 * c.w + 4) + n) * 8;
    const unsigned rq = L_QK + (p & 1) * 1600 + (16 * g + 4 * (q >> 1) + 2 * (p >> 1) + (q & 1)) * 8;
    const unsigned rk = L_QK + (2 + (p & 1)) * 1600 + (8 * (8 * c.w + 2 * g + (p >> 1)) + q) * 8;
    const float sc = 0.07216878364870322f * 1.4426950408889634f;
    const int qi = c.w + 4 * (g & 1);
    const unsigned po = (h >> 3) * 16384 + (h & 7) * 640 + ((n == 1) ? 512 + qi * 16 : qi * 64 + (2 * (g >> 1) + (n >> 3)) * 16);
#pragma unroll 1
    for (int s = 0; s < NSEG; ++s) {
        poll_mem(c, s, mo);
        bar();
        f32x4 acc[6];
#pragma unroll
        for (int t = 0; t < 6; ++t) acc[t] = (f32x4){0, 0, 0, 0};
        {
            const u32x4 al0 = lds16(L_MEM + 8 * 1024 + c.lane * 16), al1 = lds16(L_MEM + 9 * 1024 + c.lane * 16);
#pragma unroll
            for (int ch = 0; ch < 8; ++ch) {
                const u32x4 a = lds16(L_MEM + ch * 1024 + c.lane * 16);
#pragma unroll
                for (int t = 0; t < 6; ++t) {
                    acc[t] = smf(a, W[ch * 6 + t], acc[t], idx);
                    if ((ch * 6 + t) % 4 == 3) { const int f = (ch * 6 + t) / 4; acc[f % 6] = smf(f < 6 ? al0 : al1, cat(lds16(res + f * 2048), lds16(res + f * 2048 + 1024)), acc[f % 6], idx); }
                }
            }
        }
#pragma unroll
        for (int t = 0; t < 3; ++t) {
            float y[4];
#pragma unroll
            for (int r = 0; r < 4; ++r) y[r] = fswapadd32(acc[t][r], acc[3 + t][r]);
            ldsw8(wq[t], (u32x2){pk(y[0], y[1]), pk(y[2], y[3])});
        }
        u32x8 kx[2][3];
        {
#pragma unroll
            for (int t = 0; t < 2; ++t)
#pragma unroll
                for (int E = 0; E < 3; ++E) kx[t][E] = cat(lds16(L_KX + ((t * 3 + E) * 2) * 1024 + c.lane * 16), lds16(L_KX + ((t * 3 + E) * 2 + 1) * 1024 + c.lane * 16));
        }
        bar();
        if (s < NSEG - 1) {
            f32x4 x0 = {0, 0, 0, 0}, x1 = {0, 0, 0, 0}, xm = {0, 0, 0, 0};
#pragma unroll
            for (int E = 0; E < 3; ++E) {
                const u32x2 q0 = ldstr(rq + E * 512), q1 = ldstr(rq + E * 512 + 64), k0 = ldstr(rq + 3200 + E * 512), k1 = ldstr(rq + 3200 + E * 512 + 64);
                const u32x4 qf = {q0.x, q0.y, q1.x, q1.y}, kf = {k0.x, k0.y, k1.x, k1.y};
                x0 = smf(qf, kx[0][E], x0, idx); x1 = smf(qf, kx[1][E], x1, idx); xm = mfma(qf, kf, xm);
            }
            float a0, a1, am;
            if (c.w == 0) { a0 = x0[0]; a1 = x1[0]; am = xm[0]; } else if (c.w == 1) { a0 = x0[1]; a1 = x1[1]; am = xm[1]; }
            else if (c.w == 2) { a0 = x0[2]; a1 = x1[2]; am = xm[2]; } else { a0 = x0[3]; a1 = x1[3]; am = xm[3]; }
            const float vx = fswapadd32(a0, a1);
            const float amr = dppf<ROR(8)>(am);
            unsigned u0 = __builtin_bit_cast(unsigned, amr), u1 = u0; swap32(u0, u1);
            float vm = am + __builtin_bit_cast(float, u1);
            if (c.lane >= 32 || n >= 8) vm = -__builtin_inff();
            float mx = fmaxf(vx, vm);
            mx = fmaxf(mx, dppf<ROR(8)>(mx)); mx = fmaxf(mx, dppf<ROR(4)>(mx)); mx = fmaxf(mx, dppf<ROR(2)>(mx)); mx = fmaxf(mx, dppf<ROR(1)>(mx));
            { unsigned m0 = __builtin_bit_cast(unsigned, mx), m1 = m0; swap32(m0, m1); mx = fmaxf(__builtin_bit_cast(float, m0), __builtin_bit_cast(float, m1)); }
            const float ex = __builtin_amdgcn_exp2f((vx - mx) * sc), em = __builtin_amdgcn_exp2f((vm - mx) * sc);
            float sum = ex + em;
            sum += dppf<ROR(8)>(sum); sum += dppf<ROR(4)>(sum); sum += dppf<ROR(2)>(sum); sum += dppf<ROR(1)>(sum);
            sum = fswapadd32(sum, sum);
            const float inv = __builtin_amdgcn_rcpf(sum);
            const float px = ex * inv, pm = em * inv;
            const float pmr = dppf<ROR(1)>(pm), pxl = dppf<SHL(1)>(px);
            const float lo = (n & 1) ? pmr : px, hi = (n & 1) ? pm : pxl;
            const unsigned d = min(pk(lo, hi), SENT - 1);
            const u32x4 o = {d, dppu<SHL(2)>(d), dppu<SHL(4)>(d), dppu<SHL(6)>(d)};
            if ((n & 7) == 0 || (n == 1 && c.lane < 32)) st16(o, c.ws, po, comm_base(c, s));
        }
        if (c.w < 3) {
            const u32x2 k0 = ldstr(rk), k1 = ldstr(rk + 32);
            st16((u32x4){k0.x, k0.y, k1.x, k1.y}, c.ws, c.lane * 16, O_KM + (s * NH + h) * 3072 + c.w * 1024);
        }
        vmwait<0>();
        put_flag(c, F_KM, h, (unsigned)(s + 1));
        if (s + 1 < NSEG - 1) issue_kx(s + 1);
        if (c.dead) break;
    }
}

DI void role_v(Ctx& c, const Params& P, int h) {
    u32x8 W[60 - NL];
    const int g = c.lane >> 4, n = c.lane & 15, q = (c.lane >> 2) & 3, p = c.lane & 3;
    const unsigned res = L_RES + c.w * (NL * 2048) + c.lane * 16;
    {
        u32x8 T[NL];
        const int r = c.lane >> 3, ch = c.lane & 7;
        {
            const u32x4 srd = mksrd(P.Wv, DM * NH * HD * 2);
            const unsigned v0 = r * (NH * HD * 2) + ((ch ^ ((r >> 2) << 1)) * 16), v1 = v0 ^ 64;
            auto issue = [&](int t, unsigned buf) {
#pragma unroll
                for (int i = 0; i < 12; ++i) { const int pi = c.w * 12 + i, pn = pi >> 4, blk = pi & 15;
                    dma16(srd, (i & 1) ? v1 : v0, (t * 128 + blk * 8) * (NH * HD * 2) + (h * HD + pn * 64) * 2, buf + pi * 1024); }
            };
            const int row0 = 16 * (g >> 1) + 8 * (q >> 1) + 4 * (g & 1) + (q & 1), key = (row0 >> 2) & 3;
            unsigned a[3];
#pragma unroll
            for (int t = 0; t < 3; ++t) { const int TT = 3 * c.w + t; a[t] = (TT >> 2) * 16384 + row0 * 128 + (((2 * (TT & 3) + (p >> 1)) ^ (key << 1)) * 16) + (p & 1) * 8; }
            auto consume = [&](int t, unsigned buf) {
#pragma unroll
                for (int f = 0; f < 6; ++f) {
                    const unsigned b = buf + a[f % 3] + (f / 3) * 8192;
                    W[t * 6 + f] = cat(ldstr(b), ldstr(b + 256), ldstr(b + 4096), ldstr(b + 4096 + 256));
                }
            };
            stage_weights<5, 48, 3>(issue, consume);
        }
        {
            const u32x4 srd = mksrd(P.Wo, NH * HD * DM * 2);
            const unsigned v = r * (DM * 2) + ((ch ^ (((r >> 1) & 3) << 1)) * 16);
            auto issue = [&](int t, unsigned buf) {
#pragma unroll
                for (int i = 0; i < 12; ++i) { const int pi = c.w * 12 + i, pn = pi / 24, blk = pi % 24;
                    dma16(srd, v, (h * HD + blk * 8) * (DM * 2) + (t * 128 + pn * 64) * 2, buf + pi * 1024); }
            };
            unsigned a[2];
#pragma unroll
            for (int o = 0; o < 2; ++o) { const int row = 8 * (g >> 1) + 4 * (g & 1) + 2 * o + (q & 1), key = (row >> 1) & 3;
                a[o] = (c.w >> 1) * 24576 + row * 128 + (((4 * (c.w & 1) + 2 * (q >> 1) + (p >> 1)) ^ (key << 1)) * 16) + (p & 1) * 8; }
            auto consume = [&](int t, unsigned buf) {
#pragma unroll
                for (int es = 0; es < 6; ++es) {
                    const unsigned b = buf + es * 4096;
                    const u32x8 x = cat(ldstr(b + a[0]), ldstr(b + a[1]), ldstr(b + a[0] + 2048), ldstr(b + a[1] + 2048));
                    if (30 + t * 6 + es < 60 - NL) W[30 + t * 6 + es] = x; else T[30 + t * 6 + es - (60 - NL)] = x;
                }
            };
            stage_weights<5, 48, 3>(issue, consume);
        }
#pragma unroll
        for (int f = 0; f < NL; ++f) { ldsw16(res + f * 2048, (u32x4){T[f][0], T[f][1], T[f][2], T[f][3]}); ldsw16(res + f * 2048 + 1024, (u32x4){T[f][4], T[f][5], T[f][6], T[f][7]}); }
    }
    unsigned mo[3];
    mo[0] = mem_item(c.w * 160 + c.lane); mo[1] = mem_item(c.w * 160 + 64 + c.lane); mo[2] = mem_item(c.w * 160 + 128 + (c.lane & 31));
    const int idx = (c.lane & 8) ? 0xEEEE : 0x4444;
    const unsigned w01 = L_QK + (g & 1) * 1568 + (48 * c.w + 16 * (g >> 1) + n) * 8, w2 = L_QK + (g & 1) * 1568 + (48 * c.w + 32 + n) * 8 + (g >> 1) * 4;
    const unsigned rv = L_QK + (p & 1) * 1568 + (8 * g + q) * 8;
#pragma unroll 1
    for (int s = 0; s < NSEG; ++s) {
        poll_mem(c, s, mo);
        if (s > 0) put_flag(c, F_U, h, (unsigned)s);
        bar();
        f32x4 acc[3];
#pragma unroll
        for (int t = 0; t < 3; ++t) acc[t] = (f32x4){0, 0, 0, 0};
#pragma unroll
        for (int ch = 0; ch < 10; ++ch) {
            const u32x4 a = lds16(L_MEM + ch * 1024 + c.lane * 16);
#pragma unroll
            for (int t = 0; t < 3; ++t) acc[t] = smf(a, W[ch * 3 + t], acc[t], idx);
        }
        {
            float y[4];
#pragma unroll
            for (int r = 0; r < 4; ++r) y[r] = fswapadd32(acc[0][r], acc[1][r]);
            ldsw8(w01, (u32x2){pk(y[0], y[1]), pk(y[2], y[3])});
            ldsw4(w2, pk(fswapadd32(acc[2][0], acc[2][2]), fswapadd32(acc[2][1], acc[2][3])));
        }
        bar();
        u32x4 vf[6];
#pragma unroll
        for (int es = 0; es < 6; ++es) { const u32x2 lo = ldstr(rv + es * 256), hi = ldstr(rv + es * 256 + 32); vf[es] = (u32x4){lo.x, lo.y, hi.x, hi.y}; }
        unsigned d[5][2];
#pragma unroll
        for (int t = 0; t < 5; ++t) {
            f32x4 u = {0, 0, 0, 0};
#pragma unroll
            for (int es = 0; es < 6; ++es) {
                const int f = 30 + t * 6 + es - (60 - NL);
                if (f < 0) u = smf(vf[es], W[30 + t * 6 + es], u, idx);
                else u = smf(vf[es], cat(lds16(res + f * 2048), lds16(res + f * 2048 + 1024)), u, idx);
            }
            d[t][0] = pk(u[0], u[1]); d[t][1] = pk(u[2], u[3]);
            if (t & 1) {
                swap16(d[t - 1][0], d[t][0]); swap16(d[t - 1][1], d[t][1]);
                const int ct = (t - 1 + (g & 1)) * 8 + 2 * c.w + (g >> 1);
                st16(unsent((u32x4){d[t - 1][0], d[t - 1][1], d[t][0], d[t][1]}), c.ws, u_base(c, s, ct) + n * 16, h * 256);
            } else if (t == 4) {
                unsigned b0 = d[4][0], b1 = d[4][1];
                swap16(d[4][0], b0); swap16(d[4][1], b1);
                const int ct = 32 + 2 * c.w + (g >> 1);
                if (!(g & 1)) st16(unsent((u32x4){d[4][0], d[4][1], b0, b1}), c.ws, u_base(c, s, ct) + n * 16, h * 256);
            }
        }
        if (c.dead) break;
    }
    vmwait<0>();
    put_flag(c, F_U, h, (unsigned)NSEG);
}

DI void role_c(Ctx& c, int j) {
    const int g = c.lane >> 4, kh = (c.lane >> 3) & 1, i8 = c.lane & 7;
    const unsigned zv = kh * 1024 + g * 256 + i8 * 16, uv = (c.w * 8 + 4 * kh + g) * 256 + i8 * 16;
    const unsigned pv = c.w * 16384 + kh * 640 + i8 * 64 + g * 16, pmv = c.w * 16384 + (4 * kh + g) * 640 + 512 + i8 * 16;
    const unsigned fl = F_W + (c.w * 8 + ((c.lane >> 2) & 7)) * 16 + (c.lane & 3) * 4;
    const unsigned rw = (2 * c.w + kh) * 512 + i8 * 64 + (g & 1) * 16;
    const unsigned mo = mem_item((2 * j + ((c.lane >> 2) & 1)) * 8 + (c.lane >> 3));
    const u32x4 sent = {SENT, SENT, SENT, SENT};
#pragma unroll 1
    for (int s = 0; s < NSEG - 1; ++s) {
        wait_flags(c, fl, (unsigned)(s + 1));
        u32x4 z[4][2];
#pragma unroll
        for (int m = 0; m < 4; ++m)
#pragma unroll
            for (int u = 0; u < 2; ++u) z[m][u] = ld16(c.ws, zv + m * 2048 + u * 128, O_VW + ((s * 40 + j) * NH + c.w * 8) * 1024);
        u32x4 zu0, zu1; unsigned spin = 0;
        const unsigned ub = u_base(c, s, j), cb = comm_base(c, s);
#pragma unroll 1
        for (;;) {
            FENCE;
            zu0 = ld16(c.ws, uv, ub); zu1 = ld16(c.ws, uv + 128, ub);
            if (none(mx4(zu1, mx4(zu0, 0)) == SENT)) break;
            if (c.dead || spin_over(c, spin)) { c.dead = true; break; }
        }
        u32x4 pf[4], pm;
#pragma unroll 1
        for (;;) {
            FENCE;
#pragma unroll
            for (int m = 0; m < 4; ++m) pf[m] = ld16(c.ws, pv + m * 1280, cb);
            pm = ld16(c.ws, pmv, cb);
            if (none(mx4(pm, mx4(pf[3], mx4(pf[2], mx4(pf[1], mx4(pf[0], 0))))) == SENT)) break;
            if (c.dead || spin_over(c, spin)) { c.dead = true; break; }
        }
        f32x4 a0 = {0, 0, 0, 0}, a1 = {0, 0, 0, 0};
#pragma unroll
        for (int m = 0; m < 4; ++m) { a0 = mfma(z[m][0], pf[m], a0); a1 = mfma(z[m][1], pf[m], a1); }
        a0 = mfma(zu0, pm, a0); a1 = mfma(zu1, pm, a1);
        const unsigned rb = (s & 1) * 4096;
        if (kh == (g >> 1)) { ldsw16(rb + rw, __builtin_bit_cast(u32x4, a0)); ldsw16(rb + rw + 32, __builtin_bit_cast(u32x4, a1)); }
        bar();
        if (c.w == 0) {
            f32x2 t[8];
#pragma unroll
            for (int pp = 0; pp < 8; ++pp) t[pp] = __builtin_bit_cast(f32x2, lds8(rb + pp * 512 + c.lane * 8));
            const f32x2 sum = ((t[0] + t[1]) + (t[2] + t[3])) + ((t[4] + t[5]) + (t[6] + t[7]));
            const unsigned d = min(pk(sum[0], sum[1]), SENT - 1);
            const u32x4 o = {d, dppu<SHL(1)>(d), dppu<SHL(2)>(d), dppu<SHL(3)>(d)};
            if ((c.lane & 3) == 0) st16(o, c.ws, mo, comm_base(c, s + 1));
        }
        if (s + 2 < NSEG - 1) {
            const unsigned b = u_base(c, s + 2, j) + c.w * 2048;
            st16(sent, c.ws, c.lane * 16, b); st16(sent, c.ws, c.lane * 16 + 1024, b);
        }
        if (c.dead) break;
    }
}

DI unsigned char* ws_base(unsigned char* ws) { return (unsigned char*)(((uint64_t)ws + (2 * MB - 1)) & ~(uint64_t)(2 * MB - 1)); }

__global__ void __launch_bounds__(256, 1) main_kernel(Params P) {
    Ctx c;
    unsigned char* wb = ws_base(P.ws);
    c.ws = mkrsrc(wb, WS_SPAN); c.wsd = mksrd(wb, WS_SPAN);
    c.lane = threadIdx.x & 63; c.w = __builtin_amdgcn_readfirstlane(threadIdx.x >> 6); c.dead = false;
    c.par = __builtin_amdgcn_readfirstlane(ld4(c.ws, F_PAR, 0)) & 1;
    const int x = blockIdx.x & 7, r = blockIdx.x >> 3;
    int oi = -1;
    if (x == 0) { if (P.mask & 1) role_s(c, P, r); }
    else if (x == 1) { if (P.mask & 1) role_v(c, P, r); }
    else if (x == 2 || x == 3) {
        if (r < 20) { if (P.mask & 1) role_c(c, (x - 2) * 20 + r); }
        else { const int id = (x - 2) * 12 + (r - 20); if (P.mask & 2) role_f(c, P, id); oi = id; }
    }
    else if (x == 4 || x == 5) { if (P.mask & 4) role_kv(c, x == 4 ? P.Wk : P.Wv, r, x == 4 ? O_KX : O_VX, x == 4 ? F_K : F_V); oi = 24 + (x - 4) * 32 + r; }
    else if (x == 6) { if (P.mask & 8) role_w(c, P, r); oi = 88 + r; }
    else { if (P.mask & 16) role_q(c, P, r); }
    if (oi >= 0 && (P.mask & 32)) role_out(c, P, oi);
}

__global__ void __launch_bounds__(256) prep_kernel(Params P) {
    __shared__ __attribute__((aligned(16))) bf16 rows[8][648];
    unsigned char* wb = ws_base(P.ws);
    const int tid = threadIdx.x, b = blockIdx.x;
    if (b == 256) {
        if (tid < 64) {
            unsigned best[2] = {~0u, ~0u};
            unsigned* fl = (unsigned*)(wb + O_FLAG);
            for (int i = tid; i < 7 * 128; i += 64) fl[i] = 0u;
            if (tid == 0) {
                fl[(F_ABORT - O_FLAG) / 4] = 0u;
                for (int it = 0; it < 8; ++it) {
                    unsigned* p = (unsigned*)(wb + O_COMM + 63 * 65536 + (it >> 1) * 16384 + (it & 1) * 8192);
                    unsigned long long t0, t1; const unsigned z = 0;
                    asm volatile("s_waitcnt vmcnt(0)\n\ts_memtime %0\n\ts_waitcnt lgkmcnt(0)\n\tglobal_store_dword %2, %3, off sc1\n\ts_waitcnt vmcnt(0)\n\ts_memtime %1\n\ts_waitcnt lgkmcnt(0)"
                                 : "=&s"(t0), "=&s"(t1) : "v"(p), "v"(z) : "memory");
                    best[it & 1] = min(best[it & 1], (unsigned)(t1 - t0));
                }
                fl[(F_PAR - O_FLAG) / 4] = (best[1] < best[0] ? 1u : 0u) ^ 1u;
            }
        }
        return;
    }
    {
        const u32x4 sent = {SENT, SENT, SENT, SENT};
        u32x4* cm = (u32x4*)(wb + O_COMM) + b * 1024;
        const int s = b >> 2;
#pragma unroll
        for (int i = 0; i < 4; ++i) {
            const int e = i * 256 + tid, o = (e * 16) & 8191;
            if (s == 0 && o >= 5120) {
                const int ch = (b & 3) * 3 + ((o - 5120) >> 10), l = (o >> 4) & 63;
                if (ch < 10) cm[e] = unsent(*(const u32x4*)(P.mem0 + (l & 7) * DM + ch * 64 + (l >> 3) * 8));
            } else if (!(s == 63 && o < 5120)) cm[e] = sent;
        }
        u32x4* um = (u32x4*)(wb + O_U);
        for (int e = b * 256 + tid; e < 80 * 1024; e += 65536) um[e] = sent;
    }
    const int row = tid >> 5, l = tid & 31;
    const bf16* xr = P.x + (size_t)(b * 8 + row) * DM;
    float v[20]; float sum = 0;
#pragma unroll
    for (int i = 0; i < 5; ++i) { const u32x2 u = *(const u32x2*)(xr + i * 128 + l * 4); v[4 * i] = bflo(u.x); v[4 * i + 1] = bfhi(u.x); v[4 * i + 2] = bflo(u.y); v[4 * i + 3] = bfhi(u.y); }
#pragma unroll
    for (int i = 0; i < 20; ++i) sum += v[i];
#pragma unroll
    for (int m = 16; m >= 1; m >>= 1) sum += __shfl_xor(sum, m);
    const float mean = sum * (1.f / DM);
    float var = 0;
#pragma unroll
    for (int i = 0; i < 20; ++i) { v[i] -= mean; var += v[i] * v[i]; }
#pragma unroll
    for (int m = 16; m >= 1; m >>= 1) var += __shfl_xor(var, m);
    const float rs = 1.f / sqrtf(var * (1.f / DM) + 1e-5f);
#pragma unroll
    for (int i = 0; i < 5; ++i) *(u32x2*)(&rows[row][i * 128 + l * 4]) = (u32x2){pk(v[4 * i] * rs, v[4 * i + 1] * rs), pk(v[4 * i + 2] * rs, v[4 * i + 3] * rs)};
    __syncthreads();
    const int seg = b >> 2, tt = (b >> 1) & 1, t8 = (b & 1) * 8;
    for (int e = tid; e < 640; e += 256) {
        const int t = e & 7, g = (e >> 3) & 3, ks = e >> 5;
        const u32x2 lo = *(const u32x2*)(&rows[t][32 * ks + 4 * g]), hi = *(const u32x2*)(&rows[t][32 * ks + 16 + 4 * g]);
        *(u32x4*)(wb + O_XN + seg * 40960 + (ks * 2 + tt) * 1024 + (g * 16 + t8 + t) * 16) = (u32x4){lo.x, lo.y, hi.x, hi.y};
    }
}

static void launch(void* const* d_in, void* d_out, void* d_ws, hipStream_t stream, unsigned mask, bool prep) {
    Params P;
    P.x = (const bf16*)d_in[0]; P.mem0 = (const bf16*)d_in[1]; P.Wq = (const bf16*)d_in[2]; P.Wk = (const bf16*)d_in[3];
    P.Wv = (const bf16*)d_in[4]; P.Wo = (const bf16*)d_in[5]; P.wg = (const bf16*)d_in[8]; P.wu = (const bf16*)d_in[9]; P.wd = (const bf16*)d_in[10];
    P.out = (bf16*)d_out; P.ws = (unsigned char*)d_ws; P.mask = mask; P.pad = 0;
    (void)hipFuncSetAttribute((const void*)main_kernel, hipFuncAttributeMaxDynamicSharedMemorySize, LDS_BYTES);
    if (prep) prep_kernel<<<257, 256, 0, stream>>>(P);
    if (mask) main_kernel<<<256, 256, LDS_BYTES, stream>>>(P);
}

extern "C" void kernel_launch(void* const* d_in, const int* in_sizes, int n_in, void* d_out, int out_size, void* d_ws, size_t ws_size, hipStream_t stream) {
    launch(d_in, d_out, d_ws, stream, 63u, true);
}
```

```cpp
#include <hip/hip_runtime.h>
#include <stdint.h>

typedef __bf16 bf16;
typedef __bf16 bf16x2 __attribute__((ext_vector_type(2)));
typedef __bf16 bf16x8 __attribute__((ext_vector_type(8)));
typedef float f32x2 __attribute__((ext_vector_type(2)));
typedef float f32x16 __attribute__((ext_vector_type(16)));
typedef unsigned u32x2 __attribute__((ext_vector_type(2)));
typedef unsigned u32x4 __attribute__((ext_vector_type(4)));
typedef unsigned u32x16 __attribute__((ext_vector_type(16)));
#define DI __device__ __forceinline__
typedef __amdgpu_buffer_rsrc_t rsrc_t;

constexpr int NSEG = 64, DM = 640, NH = 32, HD = 192, FF = 1536;
constexpr unsigned SENT = 0xFFFFFFFFu;
constexpr unsigned SPIN_CAP = 1u << 17;

constexpr unsigned MB = 1u << 20;
constexpr unsigned O_COMM = 0, O_U = 4 * MB, O_FLAG = 44 * MB, O_XN = 45 * MB, O_KX = 48 * MB, O_VX = 72 * MB, O_QX = 96 * MB, O_VW = 120 * MB,
                   O_HF = 200 * MB, O_KM = 206 * MB, O_PTX = 212 * MB, O_PTM = 216 * MB, O_DBG = 218 * MB, WS_SPAN = 224 * MB;
constexpr unsigned F_PAR = O_FLAG + 8 * 16384, F_ABORT = F_PAR + 128;
constexpr unsigned H_MYC = O_FLAG + 10 * 16384, H_ACK = H_MYC + 16384, H_DONE = H_ACK + 16384, H_REQ = H_DONE + 16384, H_CHAL = H_REQ + 4096;
#define FREG(k) (O_FLAG + (k) * 16384 + ((c.par ^ ((FSIDE >> (k)) & 1)) << 13))
#define F_K FREG(0)
#define F_V FREG(1)
#define F_F FREG(3)
#define F_W FREG(4)
#define F_KM FREG(5)
#define F_U FREG(6)
#define F_PT FREG(7)
#define FSIDE 27
#define MDEL 70
#define LDEL 60
#define HELP 5
#define NREP 2
#define RHO ((blockIdx.x ^ (P->mask >> 8)) & (NREP - 1))
constexpr unsigned F_X = O_FLAG + 2 * 16384;
constexpr int LDS_BYTES = 160 * 1024;
constexpr unsigned L_QK = 10240, L_RES = 49152;

struct Params {
    const bf16 *x, *mem0, *Wq, *Wk, *Wv, *Wo, *wg, *wu, *wd;
    bf16* out;
    unsigned char* ws;
    unsigned mask, tune;
};

DI rsrc_t mkrsrc(const void* p, unsigned bytes) { return __builtin_amdgcn_make_buffer_rsrc((void*)p, 0, (int)bytes, 0x00020000); }
DI void setsrd(u32x16& s, int i, const void* p, unsigned bytes) {
    const uint64_t a = (uint64_t)p;
    s[i] = (unsigned)a; s[i + 1] = (unsigned)(a >> 32) & 0xffffu; s[i + 2] = bytes; s[i + 3] = 0x00020000u;
}
DI u32x4 ld16(rsrc_t r, unsigned v, unsigned s) { return __builtin_amdgcn_raw_buffer_load_b128(r, v, s, 16); }
DI unsigned ld4(rsrc_t r, unsigned v, unsigned s) { return __builtin_amdgcn_raw_buffer_load_b32(r, v, s, 16); }
DI void st16(u32x4 d, rsrc_t r, unsigned v, unsigned s) { unsigned a = v + s; asm("" : "+v"(a)); __builtin_amdgcn_raw_buffer_store_b128(d, r, a, 0, 16); }
DI void st4(unsigned d, rsrc_t r, unsigned v, unsigned s) { __builtin_amdgcn_raw_buffer_store_b32(d, r, v, s, 16); }
#define FENCE asm volatile("" ::: "memory")
DI unsigned pk(float a, float b) { f32x2 v = {a, b}; return __builtin_bit_cast(unsigned, __builtin_convertvector(v, bf16x2)); }
DI float bflo(unsigned u) { return __builtin_bit_cast(float, u << 16); }
DI float bfhi(unsigned u) { return __builtin_bit_cast(float, u & 0xffff0000u); }
DI f32x16 mfma32(u32x4 a, u32x4 b, f32x16 c) { return __builtin_amdgcn_mfma_f32_32x32x16_bf16(__builtin_bit_cast(bf16x8, a), __builtin_bit_cast(bf16x8, b), c, 0, 0, 0); }
DI u32x4 unsent(u32x4 v) { v.x = min(v.x, SENT - 1); v.y = min(v.y, SENT - 1); v.z = min(v.z, SENT - 1); v.w = min(v.w, SENT - 1); return v; }
DI void swap32(unsigned& a, unsigned& b) { auto r = __builtin_amdgcn_permlane32_swap(a, b, false, false); a = r[0]; b = r[1]; }
DI int lane_id() { int l; asm volatile("v_mbcnt_lo_u32_b32 %0, -1, 0\n\tv_mbcnt_hi_u32_b32 %0, -1, %0" : "=v"(l)); return l; }
DI int row32(int G, int j) { return 16 * (G >> 1) + 8 * (j >> 2) + 4 * (G & 1) + (j & 3); }
DI unsigned fw(int i) { return i * 64; }
DI unsigned one(int lane, unsigned a) { return lane < 16 ? a + lane * 4 : 0x7ffffff0u; }
DI unsigned mem_item(int item) { const int ch = item >> 6; return (ch / 3) * (NREP * 16384) + 5120 + (ch % 3) * 1024 + (item & 63) * 16; }

struct Ctx { int w, par; };
typedef const __attribute__((address_space(4))) Params* kparams_t;
DI kparams_t kparams() { kparams_t p = (kparams_t)__builtin_amdgcn_kernarg_segment_ptr(); asm volatile("" : "+s"(p)); return p; }
DI unsigned char* ws_base(unsigned char* ws) { return (unsigned char*)(((uint64_t)ws + (2 * MB - 1)) & ~(uint64_t)(2 * MB - 1)); }
DI void uniform(u32x16& s) {
#pragma unroll
    for (int i = 0; i < 16; ++i) s[i] = __builtin_amdgcn_readfirstlane(s[i]);
}
#define CLOB "v255", "a255", "s99", "vcc", "scc", "memory"
#define RUN(TEXT) uniform(sa); uniform(sb); asm volatile(TEXT : "+{s[36:51]}"(sa), "+{s[52:67]}"(sb), "+{v[0:15]}"(va), "+{v[16:31]}"(vb) : : CLOB); __builtin_amdgcn_endpgm()
#define RUN_RETURN(TEXT) uniform(sa); uniform(sb); asm volatile(TEXT : "+{s[36:51]}"(sa), "+{s[52:67]}"(sb), "+{v[0:15]}"(va), "+{v[16:31]}"(vb) : : "s22", "s23", "s68", "s69", "s70", "s71", "s72", "s73", "s74", "s75", "s76", "s77", "s79", "v32", "v33", "v34", "v35", "v36", "v37", "v38", "v39", "v40", "v41", "v42", "v43", "v44", "v45", "v46", "v47", "v48", "v49", "v50", "v51", "v52", "v53", "v54", "v55", "v56", "v57", "v58", "v59", "v60", "v61", "v62", "v63", "v64", "v65", "v66", "v67", "v68", "v69", "v70", "v71", "v72", "v73", "v74", "v75", "v76", "v77", "v78", "v79", "v80", "v81", "v82", "v83", "v84", "v85", "v86", "v87", "v88", "v89", "v90", "v91", "v92", "v93", "v94", "v95", "v96", "v97", "v98", "v99", "v100", "v101", "v102", "v103", "v104", "v105", "v106", "v107", "v108", "v109", "v110", "v111", "v112", "v113", "v114", "v115", "v116", "v117", "v118", "v119", "v120", "v121", "v122", "v123", "v124", "v125", "v126", "v127", "v128", "v129", "v130", "v131", "v132", "v133", "v134", "v135", "v136", "v137", "v138", "v139", "v140", "v141", "v142", "v143", "v144", "v145", "v146", "v147", "v148", "v149", "v150", "v151", "v152", "v153", "v154", "v155", "v156", "v157", "v158", "v159", "v192", "v193", "v194", "v195", "v196", "v197", "v198", "v199", "v200", "v201", "v202", "v203", "v204", "v205", "v206", "v207", "v208", "v209", "v210", "v211", "v212", "v213", "v214", "v215", "v216", "v217", "v218", "v219", "v220", "v221", "v222", "v223", "v224", "v225", "v226", "v227", "v228", "v229", "v230", "v231", "v232", "v233", "v234", "v235", "v236", "v237", "v238", "v239", "v240", "v241", "v242", "v243", "v244", "v245", "v246", "v247", "v248", "v249", "v250", "v251", "v252", "v253", "v254", "v255", "a0", "a1", "a2", "a3", "a4", "a5", "a6", "a7", "a8", "a9", "a10", "a11", "a12", "a13", "a14", "a15", "a16", "a17", "a18", "a19", "a20", "a21", "a22", "a23", "a24", "a25", "a26", "a27", "a28", "a29", "a30", "a31", "a32", "a33", "a34", "a35", "a36", "a37", "a38", "a39", "a40", "a41", "a42", "a43", "a44", "a45", "a46", "a47", "a48", "a49", "a50", "a51", "a52", "a53", "a54", "a55", "a56", "a57", "a58", "a59", "a60", "a61", "a62", "a63", "a64", "a65", "a66", "a67", "a68", "a69", "a70", "a71", "a72", "a73", "a74", "a75", "a76", "a77", "a78", "a79", "a80", "a81", "a82", "a83", "a84", "a85", "a86", "a87", "a88", "a89", "a90", "a91", "a92", "a93", "a94", "a95", "a96", "a97", "a98", "a99", "a100", "a101", "a102", "a103", "a104", "a105", "a106", "a107", "a108", "a109", "a110", "a111", "a112", "a113", "a114", "a115", "a116", "a117", "a118", "a119", "a120", "a121", "a122", "a123", "a124", "a125", "a126", "a127", "a128", "a129", "a130", "a131", "a132", "a133", "a134", "a135", "a136", "a137", "a138", "a139", "a140", "a141", "a142", "a143", "a144", "a145", "a146", "a147", "a148", "a149", "a150", "a151", "a152", "a153", "a154", "a155", "a156", "a157", "a158", "a159", "a160", "a161", "a162", "a163", "a164", "a165", "a166", "a167", "a168", "a169", "a170", "a171", "a172", "a173", "a174", "a175", "a176", "a177", "a178", "a179", "a180", "a181", "a182", "a183", "a184", "a185", "a186", "a187", "a188", "a189", "a190", "a191", "a192", "a193", "a194", "a195", "a196", "a197", "a198", "a199", "a200", "a201", "a202", "a203", "a204", "a205", "a206", "a207", "a208", "a209", "a210", "a211", "a212", "a213", "a214", "a215", "a216", "a217", "a218", "a219", "a220", "a221", "a222", "a223", "a224", "a225", "a226", "a227", "a228", "a229", "a230", "a231", "a232", "a233", "a234", "a235", "a236", "a237", "a238", "a239", "a240", "a241", "a242", "a243", "a244", "a245", "a246", "a247", "a248", "a249", "a250", "a251", "a252", "a253", "a254", "a255", "vcc", "scc", "memory")
#define SETUP u32x16 sa = {}, sb = {}, va = {}, vb = {}; const kparams_t P = kparams(); setsrd(sa, 0, ws_base(P->ws), WS_SPAN); const int lane = lane_id(), w = c.w; \
    const int g = lane >> 4, n = lane & 15, q = (lane >> 2) & 3, p = lane & 3, r8 = lane >> 3, c8 = lane & 7; \
    (void)g; (void)n; (void)q; (void)p; (void)r8; (void)c8; va[0] = lane * 16; vb[10] = lane ? 0x7ffffff0u : blockIdx.x * 4; sb[14] = w; sb[15] = O_DBG + (blockIdx.x * 4 + w) * 4608;
DI unsigned trg(int tip, int g, int q, int p) { return (8 * g + q) * 128 + (((4 * tip + 2 * (p >> 1)) ^ ((q >> 1) | ((g & 1) << 2))) * 16) + (p & 1) * 8; }

template <int KIND> DI void role_g(Ctx& c, int kind, int cu) {
    SETUP
    const int nd = KIND == 2 ? 13 : 10;
    sa[10] = w * 8192; sa[13] = O_XN + w * nd * 1024; sa[14] = w * nd * 1024;
    va[4] = one(lane, FREG(kind) + fw(4 * cu + w)); va[7] = g * 512 + n * 16; va[14] = F_X + fw(lane);
    if constexpr (KIND != 3) {
        setsrd(sa, 4, kind == 0 ? P->Wk : kind == 1 ? P->Wv : P->Wq, DM * NH * HD * 2);
        sa[8] = (cu * (KIND == 2 ? HD : 256) + w * 64) * 2; sa[9] = 8 * NH * HD * 2;
        va[1] = r8 * (NH * HD * 2) + ((c8 ^ ((r8 >> 1) & 1)) * 16);
        va[2] = w * 8192 + trg(0, g, q, p); va[3] = w * 8192 + trg(1, g, q, p);
        if constexpr (KIND == 0) {
            sa[11] = O_KX + kind * 24 * MB + (8 * cu + 2 * w) * 2048; sa[12] = NH * 12288;
            RUN_RETURN("s_nop 4\n"
"v_xor_b32 v12, 64, v1\n"
"s_mov_b32 s68, s49\n"
"s_mov_b32 s69, 0\n"
"s_mov_b32 s71, s50\n"
"s_mov_b32 s22, 46301184\n"
"s_mov_b32 s23, 46317568\n"
"s_cmp_lg_u32 s66, 0\n"
"s_cbranch_scc1 GP1_%=\n"
"v_mov_b32 v24, 0\n"
"s_mov_b64 exec, 1\n"
"buffer_atomic_add v24, v26, s[36:39], s23 offen sc0 sc1\n"
"s_mov_b64 exec, -1\n"
"GP1_%=:\n"
"s_mul_i32 s77, s45, 0\n"
"s_add_u32 s77, s77, s44\n"
"s_add_u32 m0, s46, 65536\n"
"s_nop 0\n"
"buffer_load_dwordx4 v1, s[40:43], s77 offen lds\n"
"s_add_u32 s77, s77, s45\n"
"s_add_u32 m0, s46, 66560\n"
"s_nop 0\n"
"buffer_load_dwordx4 v12, s[40:43], s77 offen lds\n"
"s_add_u32 s77, s77, s45\n"
"s_add_u32 m0, s46, 67584\n"
"s_nop 0\n"
"buffer_load_dwordx4 v1, s[40:43], s77 offen lds\n"
"s_add_u32 s77, s77, s45\n"
"s_add_u32 m0, s46, 68608\n"
"s_nop 0\n"
"buffer_load_dwordx4 v12, s[40:43], s77 offen lds\n"
"s_add_u32 s77, s77, s45\n"
"s_add_u32 m0, s46, 69632\n"
"s_nop 0\n"
"buffer_load_dwordx4 v1, s[40:43], s77 offen lds\n"
"s_add_u32 s77, s77, s45\n"
"s_add_u32 m0, s46, 70656\n"
"s_nop 0\n"
"buffer_load_dwordx4 v12, s[40:43], s77 offen lds\n"
"s_add_u32 s77, s77, s45\n"
"s_add_u32 m0, s46, 71680\n"
"s_nop 0\n"
"buffer_load_dwordx4 v1, s[40:43], s77 offen lds\n"
"s_add_u32 s77, s77, s45\n"
"s_add_u32 m0, s46, 72704\n"
"s_nop 0\n"
"buffer_load_dwordx4 v12, s[40:43], s77 offen lds\n"
"s_add_u32 s77, s77, s45\n"
"s_mul_i32 s77, s45, 8\n"
"s_add_u32 s77, s77, s44\n"
"s_add_u32 m0, s46, 98304\n"
"s_nop 0\n"
"buffer_load_dwordx4 v1, s[40:43], s77 offen lds\n"
"s_add_u32 s77, s77, s45\n"
"s_add_u32 m0, s46, 99328\n"
"s_nop 0\n"
"buffer_load_dwordx4 v12, s[40:43], s77 offen lds\n"
"s_add_u32 s77, s77, s45\n"
"s_add_u32 m0, s46, 100352\n"
"s_nop 0\n"
"buffer_load_dwordx4 v1, s[40:43], s77 offen lds\n"
"s_add_u32 s77, s77, s45\n"
"s_add_u32 m0, s46, 101376\n"
"s_nop 0\n"
"buffer_load_dwordx4 v12, s[40:43], s77 offen lds\n"
"s_add_u32 s77, s77, s45\n"
"s_add_u32 m0, s46, 102400\n"
"s_nop 0\n"
"buffer_load_dwordx4 v1, s[40:43], s77 offen lds\n"
"s_add_u32 s77, s77, s45\n"
"s_add_u32 m0, s46, 103424\n"
"s_nop 0\n"
"buffer_load_dwordx4 v12, s[40:43], s77 offen lds\n"
"s_add_u32 s77, s77, s45\n"
"s_add_u32 m0, s46, 104448\n"
"s_nop 0\n"
"buffer_load_dwordx4 v1, s[40:43], s77 offen lds\n"
"s_add_u32 s77, s77, s45\n"
"s_add_u32 m0, s46, 105472\n"
"s_nop 0\n"
"buffer_load_dwordx4 v12, s[40:43], s77 offen lds\n"
"s_add_u32 s77, s77, s45\n"
"s_waitcnt vmcnt(8)\n"
"s_waitcnt lgkmcnt(0)\n"
"s_cmp_lg_u32 s66, 0\n"
"s_cbranch_scc1 GP2_%=\n"
"v_or_b32 v24, 1, v24\n"
"v_add_u32 v24, 1, v24\n"
"buffer_store_dword v24, v26, s[36:39], s22 offen sc1\n"
"GP2_%=:\n"
"s_mul_i32 s77, s45, 16\n"
"s_add_u32 s77, s77, s44\n"
"s_add_u32 m0, s46, 131072\n"
"s_nop 0\n"
"buffer_load_dwordx4 v1, s[40:43], s77 offen lds\n"
"s_add_u32 s77, s77, s45\n"
"s_add_u32 m0, s46, 132096\n"
"s_nop 0\n"
"buffer_load_dwordx4 v12, s[40:43], s77 offen lds\n"
"s_add_u32 s77, s77, s45\n"
"s_add_u32 m0, s46, 133120\n"
"s_nop 0\n"
"buffer_load_dwordx4 v1, s[40:43], s77 offen lds\n"
"s_add_u32 s77, s77, s45\n"
"s_add_u32 m0, s46, 134144\n"
"s_nop 0\n"
"buffer_load_dwordx4 v12, s[40:43], s77 offen lds\n"
"s_add_u32 s77, s77, s45\n"
"s_add_u32 m0, s46, 135168\n"
"s_nop 0\n"
"buffer_load_dwordx4 v1, s[40:43], s77 offen lds\n"
"s_add_u32 s77, s77, s45\n"
"s_add_u32 m0, s46, 136192\n"
"s_nop 0\n"
"buffer_load_dwordx4 v12, s[40:43], s77 offen lds\n"
"s_add_u32 s77, s77, s45\n"
"s_add_u32 m0, s46, 137216\n"
"s_nop 0\n"
"buffer_load_dwordx4 v1, s[40:43], s77 offen lds\n"
"s_add_u32 s77, s77, s45\n"
"s_add_u32 m0, s46, 138240\n"
"s_nop 0\n"
"buffer_load_dwordx4 v12, s[40:43], s77 offen lds\n"
"s_add_u32 s77, s77, s45\n"
"v_add_u32 v8, 65536, v2\n"
"v_add_u32 v10, 65536, v3\n"
"v_xor_b32 v9, 16, v8\n"
"v_xor_b32 v11, 16, v10\n"
"ds_read_b64_tr_b16 v[192:193], v8 offset:0\n"
"ds_read_b64_tr_b16 v[194:195], v8 offset:512\n"
"ds_read_b64_tr_b16 v[196:197], v9 offset:0\n"
"ds_read_b64_tr_b16 v[198:199], v9 offset:512\n"
"ds_read_b64_tr_b16 v[200:201], v10 offset:0\n"
"ds_read_b64_tr_b16 v[202:203], v10 offset:512\n"
"ds_read_b64_tr_b16 v[204:205], v11 offset:0\n"
"ds_read_b64_tr_b16 v[206:207], v11 offset:512\n"
"ds_read_b64_tr_b16 v[208:209], v8 offset:4096\n"
"ds_read_b64_tr_b16 v[210:211], v8 offset:4608\n"
"ds_read_b64_tr_b16 v[212:213], v9 offset:4096\n"
"ds_read_b64_tr_b16 v[214:215], v9 offset:4608\n"
"ds_read_b64_tr_b16 v[216:217], v10 offset:4096\n"
"ds_read_b64_tr_b16 v[218:219], v10 offset:4608\n"
"ds_read_b64_tr_b16 v[220:221], v11 offset:4096\n"
"ds_read_b64_tr_b16 v[222:223], v11 offset:4608\n"
"s_waitcnt vmcnt(8)\n"
"s_waitcnt lgkmcnt(0)\n"
"s_mul_i32 s77, s45, 24\n"
"s_add_u32 s77, s77, s44\n"
"s_add_u32 m0, s46, 65536\n"
"s_nop 0\n"
"buffer_load_dwordx4 v1, s[40:43], s77 offen lds\n"
"s_add_u32 s77, s77, s45\n"
"s_add_u32 m0, s46, 66560\n"
"s_nop 0\n"
"buffer_load_dwordx4 v12, s[40:43], s77 offen lds\n"
"s_add_u32 s77, s77, s45\n"
"s_add_u32 m0, s46, 67584\n"
"s_nop 0\n"
"buffer_load_dwordx4 v1, s[40:43], s77 offen lds\n"
"s_add_u32 s77, s77, s45\n"
"s_add_u32 m0, s46, 68608\n"
"s_nop 0\n"
"buffer_load_dwordx4 v12, s[40:43], s77 offen lds\n"
"s_add_u32 s77, s77, s45\n"
"s_add_u32 m0, s46, 69632\n"
"s_nop 0\n"
"buffer_load_dwordx4 v1, s[40:43], s77 offen lds\n"
"s_add_u32 s77, s77, s45\n"
"s_add_u32 m0, s46, 70656\n"
"s_nop 0\n"
"buffer_load_dwordx4 v12, s[40:43], s77 offen lds\n"
"s_add_u32 s77, s77, s45\n"
"s_add_u32 m0, s46, 71680\n"
"s_nop 0\n"
"buffer_load_dwordx4 v1, s[40:43], s77 offen lds\n"
"s_add_u32 s77, s77, s45\n"
"s_add_u32 m0, s46, 72704\n"
"s_nop 0\n"
"buffer_load_dwordx4 v12, s[40:43], s77 offen lds\n"
"s_add_u32 s77, s77, s45\n"
"v_add_u32 v8, 98304, v2\n"
"v_add_u32 v10, 98304, v3\n"
"v_xor_b32 v9, 16, v8\n"
"v_xor_b32 v11, 16, v10\n"
"ds_read_b64_tr_b16 v[224:225], v8 offset:0\n"
"ds_read_b64_tr_b16 v[226:227], v8 offset:512\n"
"ds_read_b64_tr_b16 v[228:229], v9 offset:0\n"
"ds_read_b64_tr_b16 v[230:231], v9 offset:512\n"
"ds_read_b64_tr_b16 v[232:233], v10 offset:0\n"
"ds_read_b64_tr_b16 v[234:235], v10 offset:512\n"
"ds_read_b64_tr_b16 v[236:237], v11 offset:0\n"
"ds_read_b64_tr_b16 v[238:239], v11 offset:512\n"
"ds_read_b64_tr_b16 v[240:241], v8 offset:4096\n"
"ds_read_b64_tr_b16 v[242:243], v8 offset:4608\n"
"ds_read_b64_tr_b16 v[244:245], v9 offset:4096\n"
"ds_read_b64_tr_b16 v[246:247], v9 offset:4608\n"
"ds_read_b64_tr_b16 v[248:249], v10 offset:4096\n"
"ds_read_b64_tr_b16 v[250:251], v10 offset:4608\n"
"ds_read_b64_tr_b16 v[252:253], v11 offset:4096\n"
"ds_read_b64_tr_b16 v[254:255], v11 offset:4608\n"
"s_waitcnt vmcnt(8)\n"
"s_waitcnt lgkmcnt(0)\n"
"s_mul_i32 s77, s45, 32\n"
"s_add_u32 s77, s77, s44\n"
"s_add_u32 m0, s46, 98304\n"
"s_nop 0\n"
"buffer_load_dwordx4 v1, s[40:43], s77 offen lds\n"
"s_add_u32 s77, s77, s45\n"
"s_add_u32 m0, s46, 99328\n"
"s_nop 0\n"
"buffer_load_dwordx4 v12, s[40:43], s77 offen lds\n"
"s_add_u32 s77, s77, s45\n"
"s_add_u32 m0, s46, 100352\n"
"s_nop 0\n"
"buffer_load_dwordx4 v1, s[40:43], s77 offen lds\n"
"s_add_u32 s77, s77, s45\n"
"s_add_u32 m0, s46, 101376\n"
"s_nop 0\n"
"buffer_load_dwordx4 v12, s[40:43], s77 offen lds\n"
"s_add_u32 s77, s77, s45\n"
"s_add_u32 m0, s46, 102400\n"
"s_nop 0\n"
"buffer_load_dwordx4 v1, s[40:43], s77 offen lds\n"
"s_add_u32 s77, s77, s45\n"
"s_add_u32 m0, s46, 103424\n"
"s_nop 0\n"
"buffer_load_dwordx4 v12, s[40:43], s77 offen lds\n"
"s_add_u32 s77, s77, s45\n"
"s_add_u32 m0, s46, 104448\n"
"s_nop 0\n"
"buffer_load_dwordx4 v1, s[40:43], s77 offen lds\n"
"s_add_u32 s77, s77, s45\n"
"s_add_u32 m0, s46, 105472\n"
"s_nop 0\n"
"buffer_load_dwordx4 v12, s[40:43], s77 offen lds\n"
"s_add_u32 s77, s77, s45\n"
"v_add_u32 v8, 131072, v2\n"
"v_add_u32 v10, 131072, v3\n"
"v_xor_b32 v9, 16, v8\n"
"v_xor_b32 v11, 16, v10\n"
"ds_read_b64_tr_b16 a[0:1], v8 offset:0\n"
"ds_read_b64_tr_b16 a[2:3], v8 offset:512\n"
"ds_read_b64_tr_b16 a[4:5], v9 offset:0\n"
"ds_read_b64_tr_b16 a[6:7], v9 offset:512\n"
"ds_read_b64_tr_b16 a[8:9], v10 offset:0\n"
"ds_read_b64_tr_b16 a[10:11], v10 offset:512\n"
"ds_read_b64_tr_b16 a[12:13], v11 offset:0\n"
"ds_read_b64_tr_b16 a[14:15], v11 offset:512\n"
"ds_read_b64_tr_b16 a[16:17], v8 offset:4096\n"
"ds_read_b64_tr_b16 a[18:19], v8 offset:4608\n"
"ds_read_b64_tr_b16 a[20:21], v9 offset:4096\n"
"ds_read_b64_tr_b16 a[22:23], v9 offset:4608\n"
"ds_read_b64_tr_b16 a[24:25], v10 offset:4096\n"
"ds_read_b64_tr_b16 a[26:27], v10 offset:4608\n"
"ds_read_b64_tr_b16 a[28:29], v11 offset:4096\n"
"ds_read_b64_tr_b16 a[30:31], v11 offset:4608\n"
"s_waitcnt vmcnt(8)\n"
"s_waitcnt lgkmcnt(0)\n"
"s_mul_i32 s77, s45, 40\n"
"s_add_u32 s77, s77, s44\n"
"s_add_u32 m0, s46, 131072\n"
"s_nop 0\n"
"buffer_load_dwordx4 v1, s[40:43], s77 offen lds\n"
"s_add_u32 s77, s77, s45\n"
"s_add_u32 m0, s46, 132096\n"
"s_nop 0\n"
"buffer_load_dwordx4 v12, s[40:43], s77 offen lds\n"
"s_add_u32 s77, s77, s45\n"
"s_add_u32 m0, s46, 133120\n"
"s_nop 0\n"
"buffer_load_dwordx4 v1, s[40:43], s77 offen lds\n"
"s_add_u32 s77, s77, s45\n"
"s_add_u32 m0, s46, 134144\n"
"s_nop 0\n"
"buffer_load_dwordx4 v12, s[40:43], s77 offen lds\n"
"s_add_u32 s77, s77, s45\n"
"s_add_u32 m0, s46, 135168\n"
"s_nop 0\n"
"buffer_load_dwordx4 v1, s[40:43], s77 offen lds\n"
"s_add_u32 s77, s77, s45\n"
"s_add_u32 m0, s46, 136192\n"
"s_nop 0\n"
"buffer_load_dwordx4 v12, s[40:43], s77 offen lds\n"
"s_add_u32 s77, s77, s45\n"
"s_add_u32 m0, s46, 137216\n"
"s_nop 0\n"
"buffer_load_dwordx4 v1, s[40:43], s77 offen lds\n"
"s_add_u32 s77, s77, s45\n"
"s_add_u32 m0, s46, 138240\n"
"s_nop 0\n"
"buffer_load_dwordx4 v12, s[40:43], s77 offen lds\n"
"s_add_u32 s77, s77, s45\n"
"v_add_u32 v8, 65536, v2\n"
"v_add_u32 v10, 65536, v3\n"
"v_xor_b32 v9, 16, v8\n"
"v_xor_b32 v11, 16, v10\n"
"ds_read_b64_tr_b16 a[32:33], v8 offset:0\n"
"ds_read_b64_tr_b16 a[34:35], v8 offset:512\n"
"ds_read_b64_tr_b16 a[36:37], v9 offset:0\n"
"ds_read_b64_tr_b16 a[38:39], v9 offset:512\n"
"ds_read_b64_tr_b16 a[40:41], v10 offset:0\n"
"ds_read_b64_tr_b16 a[42:43], v10 offset:512\n"
"ds_read_b64_tr_b16 a[44:45], v11 offset:0\n"
"ds_read_b64_tr_b16 a[46:47], v11 offset:512\n"
"ds_read_b64_tr_b16 a[48:49], v8 offset:4096\n"
"ds_read_b64_tr_b16 a[50:51], v8 offset:4608\n"
"ds_read_b64_tr_b16 a[52:53], v9 offset:4096\n"
"ds_read_b64_tr_b16 a[54:55], v9 offset:4608\n"
"ds_read_b64_tr_b16 a[56:57], v10 offset:4096\n"
"ds_read_b64_tr_b16 a[58:59], v10 offset:4608\n"
"ds_read_b64_tr_b16 a[60:61], v11 offset:4096\n"
"ds_read_b64_tr_b16 a[62:63], v11 offset:4608\n"
"s_waitcnt vmcnt(8)\n"
"s_waitcnt lgkmcnt(0)\n"
"s_mul_i32 s77, s45, 48\n"
"s_add_u32 s77, s77, s44\n"
"s_add_u32 m0, s46, 65536\n"
"s_nop 0\n"
"buffer_load_dwordx4 v1, s[40:43], s77 offen lds\n"
"s_add_u32 s77, s77, s45\n"
"s_add_u32 m0, s46, 66560\n"
"s_nop 0\n"
"buffer_load_dwordx4 v12, s[40:43], s77 offen lds\n"
"s_add_u32 s77, s77, s45\n"
"s_add_u32 m0, s46, 67584\n"
"s_nop 0\n"
"buffer_load_dwordx4 v1, s[40:43], s77 offen lds\n"
"s_add_u32 s77, s77, s45\n"
"s_add_u32 m0, s46, 68608\n"
"s_nop 0\n"
"buffer_load_dwordx4 v12, s[40:43], s77 offen lds\n"
"s_add_u32 s77, s77, s45\n"
"s_add_u32 m0, s46, 69632\n"
"s_nop 0\n"
"buffer_load_dwordx4 v1, s[40:43], s77 offen lds\n"
"s_add_u32 s77, s77, s45\n"
"s_add_u32 m0, s46, 70656\n"
"s_nop 0\n"
"buffer_load_dwordx4 v12, s[40:43], s77 offen lds\n"
"s_add_u32 s77, s77, s45\n"
"s_add_u32 m0, s46, 71680\n"
"s_nop 0\n"
"buffer_load_dwordx4 v1, s[40:43], s77 offen lds\n"
"s_add_u32 s77, s77, s45\n"
"s_add_u32 m0, s46, 72704\n"
"s_nop 0\n"
"buffer_load_dwordx4 v12, s[40:43], s77 offen lds\n"
"s_add_u32 s77, s77, s45\n"
"v_add_u32 v8, 98304, v2\n"
"v_add_u32 v10, 98304, v3\n"
"v_xor_b32 v9, 16, v8\n"
"v_xor_b32 v11, 16, v10\n"
"ds_read_b64_tr_b16 a[64:65], v8 offset:0\n"
"ds_read_b64_tr_b16 a[66:67], v8 offset:512\n"
"ds_read_b64_tr_b16 a[68:69], v9 offset:0\n"
"ds_read_b64_tr_b16 a[70:71], v9 offset:512\n"
"ds_read_b64_tr_b16 a[72:73], v10 offset:0\n"
"ds_read_b64_tr_b16 a[74:75], v10 offset:512\n"
"ds_read_b64_tr_b16 a[76:77], v11 offset:0\n"
"ds_read_b64_tr_b16 a[78:79], v11 offset:512\n"
"ds_read_b64_tr_b16 a[80:81], v8 offset:4096\n"
"ds_read_b64_tr_b16 a[82:83], v8 offset:4608\n"
"ds_read_b64_tr_b16 a[84:85], v9 offset:4096\n"
"ds_read_b64_tr_b16 a[86:87], v9 offset:4608\n"
"ds_read_b64_tr_b16 a[88:89], v10 offset:4096\n"
"ds_read_b64_tr_b16 a[90:91], v10 offset:4608\n"
"ds_read_b64_tr_b16 a[92:93], v11 offset:4096\n"
"ds_read_b64_tr_b16 a[94:95], v11 offset:4608\n"
"s_waitcnt vmcnt(8)\n"
"s_waitcnt lgkmcnt(0)\n"
"s_mul_i32 s77, s45, 56\n"
"s_add_u32 s77, s77, s44\n"
"s_add_u32 m0, s46, 98304\n"
"s_nop 0\n"
"buffer_load_dwordx4 v1, s[40:43], s77 offen lds\n"
"s_add_u32 s77, s77, s45\n"
"s_add_u32 m0, s46, 99328\n"
"s_nop 0\n"
"buffer_load_dwordx4 v12, s[40:43], s77 offen lds\n"
"s_add_u32 s77, s77, s45\n"
"s_add_u32 m0, s46, 100352\n"
"s_nop 0\n"
"buffer_load_dwordx4 v1, s[40:43], s77 offen lds\n"
"s_add_u32 s77, s77, s45\n"
"s_add_u32 m0, s46, 101376\n"
"s_nop 0\n"
"buffer_load_dwordx4 v12, s[40:43], s77 offen lds\n"
"s_add_u32 s77, s77, s45\n"
"s_add_u32 m0, s46, 102400\n"
"s_nop 0\n"
"buffer_load_dwordx4 v1, s[40:43], s77 offen lds\n"
"s_add_u32 s77, s77, s45\n"
"s_add_u32 m0, s46, 103424\n"
"s_nop 0\n"
"buffer_load_dwordx4 v12, s[40:43], s77 offen lds\n"
"s_add_u32 s77, s77, s45\n"
"s_add_u32 m0, s46, 104448\n"
"s_nop 0\n"
"buffer_load_dwordx4 v1, s[40:43], s77 offen lds\n"
"s_add_u32 s77, s77, s45\n"
"s_add_u32 m0, s46, 105472\n"
"s_nop 0\n"
"buffer_load_dwordx4 v12, s[40:43], s77 offen lds\n"
"s_add_u32 s77, s77, s45\n"
"v_add_u32 v8, 131072, v2\n"
"v_add_u32 v10, 131072, v3\n"
"v_xor_b32 v9, 16, v8\n"
"v_xor_b32 v11, 16, v10\n"
"ds_read_b64_tr_b16 a[96:97], v8 offset:0\n"
"ds_read_b64_tr_b16 a[98:99], v8 offset:512\n"
"ds_read_b64_tr_b16 a[100:101], v9 offset:0\n"
"ds_read_b64_tr_b16 a[102:103], v9 offset:512\n"
"ds_read_b64_tr_b16 a[104:105], v10 offset:0\n"
"ds_read_b64_tr_b16 a[106:107], v10 offset:512\n"
"ds_read_b64_tr_b16 a[108:109], v11 offset:0\n"
"ds_read_b64_tr_b16 a[110:111], v11 offset:512\n"
"ds_read_b64_tr_b16 a[112:113], v8 offset:4096\n"
"ds_read_b64_tr_b16 a[114:115], v8 offset:4608\n"
"ds_read_b64_tr_b16 a[116:117], v9 offset:4096\n"
"ds_read_b64_tr_b16 a[118:119], v9 offset:4608\n"
"ds_read_b64_tr_b16 a[120:121], v10 offset:4096\n"
"ds_read_b64_tr_b16 a[122:123], v10 offset:4608\n"
"ds_read_b64_tr_b16 a[124:125], v11 offset:4096\n"
"ds_read_b64_tr_b16 a[126:127], v11 offset:4608\n"
"s_waitcnt vmcnt(8)\n"
"s_waitcnt lgkmcnt(0)\n"
"s_mul_i32 s77, s45, 64\n"
"s_add_u32 s77, s77, s44\n"
"s_add_u32 m0, s46, 131072\n"
"s_nop 0\n"
"buffer_load_dwordx4 v1, s[40:43], s77 offen lds\n"
"s_add_u32 s77, s77, s45\n"
"s_add_u32 m0, s46, 132096\n"
"s_nop 0\n"
"buffer_load_dwordx4 v12, s[40:43], s77 offen lds\n"
"s_add_u32 s77, s77, s45\n"
"s_add_u32 m0, s46, 133120\n"
"s_nop 0\n"
"buffer_load_dwordx4 v1, s[40:43], s77 offen lds\n"
"s_add_u32 s77, s77, s45\n"
"s_add_u32 m0, s46, 134144\n"
"s_nop 0\n"
"buffer_load_dwordx4 v12, s[40:43], s77 offen lds\n"
"s_add_u32 s77, s77, s45\n"
"s_add_u32 m0, s46, 135168\n"
"s_nop 0\n"
"buffer_load_dwordx4 v1, s[40:43], s77 offen lds\n"
"s_add_u32 s77, s77, s45\n"
"s_add_u32 m0, s46, 136192\n"
"s_nop 0\n"
"buffer_load_dwordx4 v12, s[40:43], s77 offen lds\n"
"s_add_u32 s77, s77, s45\n"
"s_add_u32 m0, s46, 137216\n"
"s_nop 0\n"
"buffer_load_dwordx4 v1, s[40:43], s77 offen lds\n"
"s_add_u32 s77, s77, s45\n"
"s_add_u32 m0, s46, 138240\n"
"s_nop 0\n"
"buffer_load_dwordx4 v12, s[40:43], s77 offen lds\n"
"s_add_u32 s77, s77, s45\n"
"v_add_u32 v8, 65536, v2\n"
"v_add_u32 v10, 65536, v3\n"
"v_xor_b32 v9, 16, v8\n"
"v_xor_b32 v11, 16, v10\n"
"ds_read_b64_tr_b16 a[128:129], v8 offset:0\n"
"ds_read_b64_tr_b16 a[130:131], v8 offset:512\n"
"ds_read_b64_tr_b16 a[132:133], v9 offset:0\n"
"ds_read_b64_tr_b16 a[134:135], v9 offset:512\n"
"ds_read_b64_tr_b16 a[136:137], v10 offset:0\n"
"ds_read_b64_tr_b16 a[138:139], v10 offset:512\n"
"ds_read_b64_tr_b16 a[140:141], v11 offset:0\n"
"ds_read_b64_tr_b16 a[142:143], v11 offset:512\n"
"ds_read_b64_tr_b16 a[144:145], v8 offset:4096\n"
"ds_read_b64_tr_b16 a[146:147], v8 offset:4608\n"
"ds_read_b64_tr_b16 a[148:149], v9 offset:4096\n"
"ds_read_b64_tr_b16 a[150:151], v9 offset:4608\n"
"ds_read_b64_tr_b16 a[152:153], v10 offset:4096\n"
"ds_read_b64_tr_b16 a[154:155], v10 offset:4608\n"
"ds_read_b64_tr_b16 a[156:157], v11 offset:4096\n"
"ds_read_b64_tr_b16 a[158:159], v11 offset:4608\n"
"s_waitcnt vmcnt(8)\n"
"s_waitcnt lgkmcnt(0)\n"
"s_mul_i32 s77, s45, 72\n"
"s_add_u32 s77, s77, s44\n"
"s_add_u32 m0, s46, 65536\n"
"s_nop 0\n"
"buffer_load_dwordx4 v1, s[40:43], s77 offen lds\n"
"s_add_u32 s77, s77, s45\n"
"s_add_u32 m0, s46, 66560\n"
"s_nop 0\n"
"buffer_load_dwordx4 v12, s[40:43], s77 offen lds\n"
"s_add_u32 s77, s77, s45\n"
"s_add_u32 m0, s46, 67584\n"
"s_nop 0\n"
"buffer_load_dwordx4 v1, s[40:43], s77 offen lds\n"
"s_add_u32 s77, s77, s45\n"
"s_add_u32 m0, s46, 68608\n"
"s_nop 0\n"
"buffer_load_dwordx4 v12, s[40:43], s77 offen lds\n"
"s_add_u32 s77, s77, s45\n"
"s_add_u32 m0, s46, 69632\n"
"s_nop 0\n"
"buffer_load_dwordx4 v1, s[40:43], s77 offen lds\n"
"s_add_u32 s77, s77, s45\n"
"s_add_u32 m0, s46, 70656\n"
"s_nop 0\n"
"buffer_load_dwordx4 v12, s[40:43], s77 offen lds\n"
"s_add_u32 s77, s77, s45\n"
"s_add_u32 m0, s46, 71680\n"
"s_nop 0\n"
"buffer_load_dwordx4 v1, s[40:43], s77 offen lds\n"
"s_add_u32 s77, s77, s45\n"
"s_add_u32 m0, s46, 72704\n"
"s_nop 0\n"
"buffer_load_dwordx4 v12, s[40:43], s77 offen lds\n"
"s_add_u32 s77, s77, s45\n"
"v_add_u32 v8, 98304, v2\n"
"v_add_u32 v10, 98304, v3\n"
"v_xor_b32 v9, 16, v8\n"
"v_xor_b32 v11, 16, v10\n"
"ds_read_b64_tr_b16 a[160:161], v8 offset:0\n"
"ds_read_b64_tr_b16 a[162:163], v8 offset:512\n"
"ds_read_b64_tr_b16 a[164:165], v9 offset:0\n"
"ds_read_b64_tr_b16 a[166:167], v9 offset:512\n"
"ds_read_b64_tr_b16 a[168:169], v10 offset:0\n"
"ds_read_b64_tr_b16 a[170:171], v10 offset:512\n"
"ds_read_b64_tr_b16 a[172:173], v11 offset:0\n"
"ds_read_b64_tr_b16 a[174:175], v11 offset:512\n"
"ds_read_b64_tr_b16 a[176:177], v8 offset:4096\n"
"ds_read_b64_tr_b16 a[178:179], v8 offset:4608\n"
"ds_read_b64_tr_b16 a[180:181], v9 offset:4096\n"
"ds_read_b64_tr_b16 a[182:183], v9 offset:4608\n"
"ds_read_b64_tr_b16 a[184:185], v10 offset:4096\n"
"ds_read_b64_tr_b16 a[186:187], v10 offset:4608\n"
"ds_read_b64_tr_b16 a[188:189], v11 offset:4096\n"
"ds_read_b64_tr_b16 a[190:191], v11 offset:4608\n"
"s_cmp_lg_u32 s66, 0\n"
"s_cbranch_scc1 GP3_%=\n"
"s_mov_b32 s76, 131072\n"
"s_mov_b64 exec, 1\n"
"s_branch GP5_%=\n"
"GP4_%=:\n"
"s_sleep 4\n"
"GP5_%=:\n"
"v_mov_b32 v29, 0\n"
"buffer_atomic_add v29, v26, s[36:39], s23 offen sc0 sc1\n"
"s_sub_u32 s76, s76, 1\n"
"s_cbranch_scc1 GPABORT_%=\n"
"s_waitcnt vmcnt(0)\n"
"v_xor_b32 v29, v29, v24\n"
"v_cmp_lt_u32 vcc, 1, v29\n"
"s_nop 0\n"
"s_cbranch_vccnz GP4_%=\n"
"v_mov_b32 v28, 61440\n"
"ds_write_b32 v28, v29\n"
"v_mov_b32 v24, 1\n"
"s_add_u32 s22, s23, 16384\n"
"buffer_store_dword v24, v26, s[36:39], s22 offen sc1\n"
"s_mov_b64 exec, -1\n"
"GP3_%=:\n"
"s_waitcnt lgkmcnt(0)\n"
"s_barrier\n"
"s_add_u32 m0, s71, 0\n"
"s_add_u32 s72, s68, 0\n"
"buffer_load_dwordx4 v0, s[36:39], s72 offen offset:0 sc1 lds\n"
"buffer_load_dwordx4 v0, s[36:39], s72 offen offset:1024 sc1 lds\n"
"buffer_load_dwordx4 v0, s[36:39], s72 offen offset:2048 sc1 lds\n"
"buffer_load_dwordx4 v0, s[36:39], s72 offen offset:3072 sc1 lds\n"
"s_add_u32 m0, s71, 4096\n"
"s_add_u32 s72, s68, 4096\n"
"buffer_load_dwordx4 v0, s[36:39], s72 offen offset:0 sc1 lds\n"
"buffer_load_dwordx4 v0, s[36:39], s72 offen offset:1024 sc1 lds\n"
"buffer_load_dwordx4 v0, s[36:39], s72 offen offset:2048 sc1 lds\n"
"buffer_load_dwordx4 v0, s[36:39], s72 offen offset:3072 sc1 lds\n"
"s_add_u32 m0, s71, 8192\n"
"s_add_u32 s72, s68, 8192\n"
"buffer_load_dwordx4 v0, s[36:39], s72 offen offset:0 sc1 lds\n"
"buffer_load_dwordx4 v0, s[36:39], s72 offen offset:1024 sc1 lds\n"
"s_add_u32 s68, s68, 40960\n"
"s_add_u32 s69, s69, 40960\n"
"s_cmp_eq_u32 s69, 122880\n"
"s_cselect_b32 s69, 0, s69\n"
"s_add_u32 s71, s69, s50\n"
"v_mov_b32 v28, 61440\n"
"ds_read_b32 v28, v28\n"
"s_waitcnt lgkmcnt(0)\n"
"v_readfirstlane_b32 s22, v28\n"
"s_nop 0\n"
"s_lshl_b32 s22, s22, 13\n"
"v_xor_b32 v4, s22, v4\n"
"s_waitcnt vmcnt(8)\n"
"s_waitcnt lgkmcnt(0)\n"
"v_add_u32 v8, 131072, v2\n"
"v_add_u32 v10, 131072, v3\n"
"v_xor_b32 v9, 16, v8\n"
"v_xor_b32 v11, 16, v10\n"
"ds_read_b64_tr_b16 a[192:193], v8 offset:0\n"
"ds_read_b64_tr_b16 a[194:195], v8 offset:512\n"
"ds_read_b64_tr_b16 a[196:197], v9 offset:0\n"
"ds_read_b64_tr_b16 a[198:199], v9 offset:512\n"
"ds_read_b64_tr_b16 a[200:201], v10 offset:0\n"
"ds_read_b64_tr_b16 a[202:203], v10 offset:512\n"
"ds_read_b64_tr_b16 a[204:205], v11 offset:0\n"
"ds_read_b64_tr_b16 a[206:207], v11 offset:512\n"
"ds_read_b64_tr_b16 a[208:209], v8 offset:4096\n"
"ds_read_b64_tr_b16 a[210:211], v8 offset:4608\n"
"ds_read_b64_tr_b16 a[212:213], v9 offset:4096\n"
"ds_read_b64_tr_b16 a[214:215], v9 offset:4608\n"
"ds_read_b64_tr_b16 a[216:217], v10 offset:4096\n"
"ds_read_b64_tr_b16 a[218:219], v10 offset:4608\n"
"ds_read_b64_tr_b16 a[220:221], v11 offset:4096\n"
"ds_read_b64_tr_b16 a[222:223], v11 offset:4608\n"
"s_waitcnt vmcnt(0)\n"
"s_waitcnt lgkmcnt(0)\n"
"v_add_u32 v8, 65536, v2\n"
"v_add_u32 v10, 65536, v3\n"
"v_xor_b32 v9, 16, v8\n"
"v_xor_b32 v11, 16, v10\n"
"ds_read_b64_tr_b16 a[224:225], v8 offset:0\n"
"ds_read_b64_tr_b16 a[226:227], v8 offset:512\n"
"ds_read_b64_tr_b16 a[228:229], v9 offset:0\n"
"ds_read_b64_tr_b16 a[230:231], v9 offset:512\n"
"ds_read_b64_tr_b16 a[232:233], v10 offset:0\n"
"ds_read_b64_tr_b16 a[234:235], v10 offset:512\n"
"ds_read_b64_tr_b16 a[236:237], v11 offset:0\n"
"ds_read_b64_tr_b16 a[238:239], v11 offset:512\n"
"ds_read_b64_tr_b16 a[240:241], v8 offset:4096\n"
"ds_read_b64_tr_b16 a[242:243], v8 offset:4608\n"
"ds_read_b64_tr_b16 a[244:245], v9 offset:4096\n"
"ds_read_b64_tr_b16 a[246:247], v9 offset:4608\n"
"ds_read_b64_tr_b16 a[248:249], v10 offset:4096\n"
"ds_read_b64_tr_b16 a[250:251], v10 offset:4608\n"
"ds_read_b64_tr_b16 a[252:253], v11 offset:4096\n"
"ds_read_b64_tr_b16 a[254:255], v11 offset:4608\n"
"s_waitcnt lgkmcnt(0)\n"
"s_barrier\n"
"s_add_u32 m0, s71, 0\n"
"s_add_u32 s72, s68, 0\n"
"buffer_load_dwordx4 v0, s[36:39], s72 offen offset:0 sc1 lds\n"
"buffer_load_dwordx4 v0, s[36:39], s72 offen offset:1024 sc1 lds\n"
"buffer_load_dwordx4 v0, s[36:39], s72 offen offset:2048 sc1 lds\n"
"buffer_load_dwordx4 v0, s[36:39], s72 offen offset:3072 sc1 lds\n"
"s_add_u32 m0, s71, 4096\n"
"s_add_u32 s72, s68, 4096\n"
"buffer_load_dwordx4 v0, s[36:39], s72 offen offset:0 sc1 lds\n"
"buffer_load_dwordx4 v0, s[36:39], s72 offen offset:1024 sc1 lds\n"
"buffer_load_dwordx4 v0, s[36:39], s72 offen offset:2048 sc1 lds\n"
"buffer_load_dwordx4 v0, s[36:39], s72 offen offset:3072 sc1 lds\n"
"s_add_u32 m0, s71, 8192\n"
"s_add_u32 s72, s68, 8192\n"
"buffer_load_dwordx4 v0, s[36:39], s72 offen offset:0 sc1 lds\n"
"buffer_load_dwordx4 v0, s[36:39], s72 offen offset:1024 sc1 lds\n"
"s_add_u32 s68, s68, 40960\n"
"s_add_u32 s69, s69, 40960\n"
"s_cmp_eq_u32 s69, 122880\n"
"s_cselect_b32 s69, 0, s69\n"
"s_add_u32 s71, s69, s50\n"
"s_mov_b32 s70, 0\n"
"s_mov_b32 s73, 0\n"
"s_mov_b32 s74, 234881024\n"
"v_mov_b32 v5, v7\n"
"ds_read_b128 v[96:99], v5 offset:0\n"
"ds_read_b128 v[100:103], v5 offset:256\n"
"ds_read_b128 v[104:107], v5 offset:2048\n"
"ds_read_b128 v[108:111], v5 offset:2304\n"
"ds_read_b128 v[112:115], v5 offset:4096\n"
"ds_read_b128 v[116:119], v5 offset:4352\n"
"s_mov_b32 s75, 0xbfb8aa3b\n"
"s_mov_b32 s79, 12\n"
"s_nop 0\n"
"GP6_%=:\n"
"s_waitcnt lgkmcnt(5)\n"
"v_mfma_f32_16x16x32_bf16 v[32:35], v[192:195], v[96:99], 0\n"
"v_mfma_f32_16x16x32_bf16 v[36:39], v[196:199], v[96:99], 0\n"
"v_mfma_f32_16x16x32_bf16 v[48:51], v[200:203], v[96:99], 0\n"
"ds_read_b128 v[120:123], v5 offset:6144\n"
"v_mfma_f32_16x16x32_bf16 v[52:55], v[204:207], v[96:99], 0\n"
"s_waitcnt lgkmcnt(5)\n"
"v_mfma_f32_16x16x32_bf16 v[40:43], v[192:195], v[100:103], 0\n"
"v_mfma_f32_16x16x32_bf16 v[44:47], v[196:199], v[100:103], 0\n"
"v_mfma_f32_16x16x32_bf16 v[56:59], v[200:203], v[100:103], 0\n"
"ds_read_b128 v[124:127], v5 offset:6400\n"
"v_mfma_f32_16x16x32_bf16 v[60:63], v[204:207], v[100:103], 0\n"
"s_waitcnt lgkmcnt(5)\n"
"v_mfma_f32_16x16x32_bf16 v[32:35], v[208:211], v[104:107], v[32:35]\n"
"v_cvt_pk_bf16_f32 v128, v64, v65\n"
"v_mfma_f32_16x16x32_bf16 v[36:39], v[212:215], v[104:107], v[36:39]\n"
"v_cvt_pk_bf16_f32 v129, v66, v67\n"
"v_mfma_f32_16x16x32_bf16 v[48:51], v[216:219], v[104:107], v[48:51]\n"
"ds_read_b128 v[96:99], v5 offset:8192\n"
"v_mfma_f32_16x16x32_bf16 v[52:55], v[220:223], v[104:107], v[52:55]\n"
"v_cvt_pk_bf16_f32 v130, v68, v69\n"
"s_waitcnt lgkmcnt(5)\n"
"v_mfma_f32_16x16x32_bf16 v[40:43], v[208:211], v[108:111], v[40:43]\n"
"v_cvt_pk_bf16_f32 v131, v70, v71\n"
"v_mfma_f32_16x16x32_bf16 v[44:47], v[212:215], v[108:111], v[44:47]\n"
"buffer_store_dwordx4 v[128:131], v7, s[36:39], s74 offen offset:0 sc1\n"
"v_mfma_f32_16x16x32_bf16 v[56:59], v[216:219], v[108:111], v[56:59]\n"
"ds_read_b128 v[100:103], v5 offset:8448\n"
"v_mfma_f32_16x16x32_bf16 v[60:63], v[220:223], v[108:111], v[60:63]\n"
"v_cvt_pk_bf16_f32 v132, v72, v73\n"
"s_waitcnt lgkmcnt(5)\n"
"v_mfma_f32_16x16x32_bf16 v[32:35], v[224:227], v[112:115], v[32:35]\n"
"v_cvt_pk_bf16_f32 v133, v74, v75\n"
"v_mfma_f32_16x16x32_bf16 v[36:39], v[228:231], v[112:115], v[36:39]\n"
"v_cvt_pk_bf16_f32 v134, v76, v77\n"
"v_mfma_f32_16x16x32_bf16 v[48:51], v[232:235], v[112:115], v[48:51]\n"
"ds_read_b128 v[104:107], v5 offset:10240\n"
"v_mfma_f32_16x16x32_bf16 v[52:55], v[236:239], v[112:115], v[52:55]\n"
"v_cvt_pk_bf16_f32 v135, v78, v79\n"
"s_waitcnt lgkmcnt(5)\n"
"v_mfma_f32_16x16x32_bf16 v[40:43], v[224:227], v[116:119], v[40:43]\n"
"buffer_store_dwordx4 v[132:135], v7, s[36:39], s74 offen offset:256 sc1\n"
"v_mfma_f32_16x16x32_bf16 v[44:47], v[228:231], v[116:119], v[44:47]\n"
"v_cvt_pk_bf16_f32 v136, v80, v81\n"
"v_mfma_f32_16x16x32_bf16 v[56:59], v[232:235], v[116:119], v[56:59]\n"
"ds_read_b128 v[108:111], v5 offset:10496\n"
"v_mfma_f32_16x16x32_bf16 v[60:63], v[236:239], v[116:119], v[60:63]\n"
"v_cvt_pk_bf16_f32 v137, v82, v83\n"
"s_waitcnt lgkmcnt(5)\n"
"v_mfma_f32_16x16x32_bf16 v[32:35], v[240:243], v[120:123], v[32:35]\n"
"v_cvt_pk_bf16_f32 v138, v84, v85\n"
"v_mfma_f32_16x16x32_bf16 v[36:39], v[244:247], v[120:123], v[36:39]\n"
"v_cvt_pk_bf16_f32 v139, v86, v87\n"
"v_mfma_f32_16x16x32_bf16 v[48:51], v[248:251], v[120:123], v[48:51]\n"
"ds_read_b128 v[112:115], v5 offset:12288\n"
"v_mfma_f32_16x16x32_bf16 v[52:55], v[252:255], v[120:123], v[52:55]\n"
"buffer_store_dwordx4 v[136:139], v7, s[36:39], s74 offen offset:2048 sc1\n"
"s_waitcnt lgkmcnt(5)\n"
"v_mfma_f32_16x16x32_bf16 v[40:43], v[240:243], v[124:127], v[40:43]\n"
"v_cvt_pk_bf16_f32 v140, v88, v89\n"
"v_mfma_f32_16x16x32_bf16 v[44:47], v[244:247], v[124:127], v[44:47]\n"
"v_cvt_pk_bf16_f32 v141, v90, v91\n"
"v_mfma_f32_16x16x32_bf16 v[56:59], v[248:251], v[124:127], v[56:59]\n"
"ds_read_b128 v[116:119], v5 offset:12544\n"
"v_mfma_f32_16x16x32_bf16 v[60:63], v[252:255], v[124:127], v[60:63]\n"
"v_cvt_pk_bf16_f32 v142, v92, v93\n"
"s_waitcnt lgkmcnt(5)\n"
"v_mfma_f32_16x16x32_bf16 v[32:35], a[0:3], v[96:99], v[32:35]\n"
"v_cvt_pk_bf16_f32 v143, v94, v95\n"
"v_mfma_f32_16x16x32_bf16 v[36:39], a[4:7], v[96:99], v[36:39]\n"
"buffer_store_dwordx4 v[140:143], v7, s[36:39], s74 offen offset:2304 sc1\n"
"v_mfma_f32_16x16x32_bf16 v[48:51], a[8:11], v[96:99], v[48:51]\n"
"ds_read_b128 v[120:123], v5 offset:14336\n"
"v_mfma_f32_16x16x32_bf16 v[52:55], a[12:15], v[96:99], v[52:55]\n"
"s_waitcnt lgkmcnt(5)\n"
"v_mfma_f32_16x16x32_bf16 v[40:43], a[0:3], v[100:103], v[40:43]\n"
"v_mfma_f32_16x16x32_bf16 v[44:47], a[4:7], v[100:103], v[44:47]\n"
"v_mfma_f32_16x16x32_bf16 v[56:59], a[8:11], v[100:103], v[56:59]\n"
"ds_read_b128 v[124:127], v5 offset:14592\n"
"v_mfma_f32_16x16x32_bf16 v[60:63], a[12:15], v[100:103], v[60:63]\n"
"s_waitcnt lgkmcnt(5)\n"
"v_mfma_f32_16x16x32_bf16 v[32:35], a[16:19], v[104:107], v[32:35]\n"
"v_mfma_f32_16x16x32_bf16 v[36:39], a[20:23], v[104:107], v[36:39]\n"
"v_mfma_f32_16x16x32_bf16 v[48:51], a[24:27], v[104:107], v[48:51]\n"
"ds_read_b128 v[96:99], v5 offset:16384\n"
"v_mfma_f32_16x16x32_bf16 v[52:55], a[28:31], v[104:107], v[52:55]\n"
"s_waitcnt lgkmcnt(5)\n"
"v_mfma_f32_16x16x32_bf16 v[40:43], a[16:19], v[108:111], v[40:43]\n"
"v_mfma_f32_16x16x32_bf16 v[44:47], a[20:23], v[108:111], v[44:47]\n"
"v_mfma_f32_16x16x32_bf16 v[56:59], a[24:27], v[108:111], v[56:59]\n"
"ds_read_b128 v[100:103], v5 offset:16640\n"
"v_mfma_f32_16x16x32_bf16 v[60:63], a[28:31], v[108:111], v[60:63]\n"
"s_waitcnt lgkmcnt(5)\n"
"v_mfma_f32_16x16x32_bf16 v[32:35], a[32:35], v[112:115], v[32:35]\n"
"v_mfma_f32_16x16x32_bf16 v[36:39], a[36:39], v[112:115], v[36:39]\n"
"v_mfma_f32_16x16x32_bf16 v[48:51], a[40:43], v[112:115], v[48:51]\n"
"ds_read_b128 v[104:107], v5 offset:18432\n"
"v_mfma_f32_16x16x32_bf16 v[52:55], a[44:47], v[112:115], v[52:55]\n"
"s_waitcnt lgkmcnt(5)\n"
"v_mfma_f32_16x16x32_bf16 v[40:43], a[32:35], v[116:119], v[40:43]\n"
"v_mfma_f32_16x16x32_bf16 v[44:47], a[36:39], v[116:119], v[44:47]\n"
"v_mfma_f32_16x16x32_bf16 v[56:59], a[40:43], v[116:119], v[56:59]\n"
"ds_read_b128 v[108:111], v5 offset:18688\n"
"v_mfma_f32_16x16x32_bf16 v[60:63], a[44:47], v[116:119], v[60:63]\n"
"s_waitcnt lgkmcnt(5)\n"
"v_mfma_f32_16x16x32_bf16 v[32:35], a[48:51], v[120:123], v[32:35]\n"
"v_mfma_f32_16x16x32_bf16 v[36:39], a[52:55], v[120:123], v[36:39]\n"
"v_mfma_f32_16x16x32_bf16 v[48:51], a[56:59], v[120:123], v[48:51]\n"
"ds_read_b128 v[112:115], v5 offset:20480\n"
"v_mfma_f32_16x16x32_bf16 v[52:55], a[60:63], v[120:123], v[52:55]\n"
"s_waitcnt lgkmcnt(5)\n"
"v_mfma_f32_16x16x32_bf16 v[40:43], a[48:51], v[124:127], v[40:43]\n"
"v_mfma_f32_16x16x32_bf16 v[44:47], a[52:55], v[124:127], v[44:47]\n"
"v_mfma_f32_16x16x32_bf16 v[56:59], a[56:59], v[124:127], v[56:59]\n"
"ds_read_b128 v[116:119], v5 offset:20736\n"
"v_mfma_f32_16x16x32_bf16 v[60:63], a[60:63], v[124:127], v[60:63]\n"
"s_waitcnt lgkmcnt(5)\n"
"v_mfma_f32_16x16x32_bf16 v[32:35], a[64:67], v[96:99], v[32:35]\n"
"v_mfma_f32_16x16x32_bf16 v[36:39], a[68:71], v[96:99], v[36:39]\n"
"v_mfma_f32_16x16x32_bf16 v[48:51], a[72:75], v[96:99], v[48:51]\n"
"ds_read_b128 v[120:123], v5 offset:22528\n"
"v_mfma_f32_16x16x32_bf16 v[52:55], a[76:79], v[96:99], v[52:55]\n"
"s_waitcnt lgkmcnt(5)\n"
"v_mfma_f32_16x16x32_bf16 v[40:43], a[64:67], v[100:103], v[40:43]\n"
"v_mfma_f32_16x16x32_bf16 v[44:47], a[68:71], v[100:103], v[44:47]\n"
"v_mfma_f32_16x16x32_bf16 v[56:59], a[72:75], v[100:103], v[56:59]\n"
"ds_read_b128 v[124:127], v5 offset:22784\n"
"v_mfma_f32_16x16x32_bf16 v[60:63], a[76:79], v[100:103], v[60:63]\n"
"s_waitcnt lgkmcnt(5)\n"
"v_mfma_f32_16x16x32_bf16 v[32:35], a[80:83], v[104:107], v[32:35]\n"
"v_mfma_f32_16x16x32_bf16 v[36:39], a[84:87], v[104:107], v[36:39]\n"
"v_mfma_f32_16x16x32_bf16 v[48:51], a[88:91], v[104:107], v[48:51]\n"
"ds_read_b128 v[96:99], v5 offset:24576\n"
"v_mfma_f32_16x16x32_bf16 v[52:55], a[92:95], v[104:107], v[52:55]\n"
"s_waitcnt lgkmcnt(5)\n"
"v_mfma_f32_16x16x32_bf16 v[40:43], a[80:83], v[108:111], v[40:43]\n"
"v_mfma_f32_16x16x32_bf16 v[44:47], a[84:87], v[108:111], v[44:47]\n"
"v_mfma_f32_16x16x32_bf16 v[56:59], a[88:91], v[108:111], v[56:59]\n"
"ds_read_b128 v[100:103], v5 offset:24832\n"
"v_mfma_f32_16x16x32_bf16 v[60:63], a[92:95], v[108:111], v[60:63]\n"
"s_waitcnt lgkmcnt(5)\n"
"v_mfma_f32_16x16x32_bf16 v[32:35], a[96:99], v[112:115], v[32:35]\n"
"v_mfma_f32_16x16x32_bf16 v[36:39], a[100:103], v[112:115], v[36:39]\n"
"v_mfma_f32_16x16x32_bf16 v[48:51], a[104:107], v[112:115], v[48:51]\n"
"ds_read_b128 v[104:107], v5 offset:26624\n"
"v_mfma_f32_16x16x32_bf16 v[52:55], a[108:111], v[112:115], v[52:55]\n"
"v_mov_b32 v6, s73\n"
"s_waitcnt lgkmcnt(5)\n"
"v_mfma_f32_16x16x32_bf16 v[40:43], a[96:99], v[116:119], v[40:43]\n"
"s_waitcnt vmcnt(0)\n"
"v_mfma_f32_16x16x32_bf16 v[44:47], a[100:103], v[116:119], v[44:47]\n"
"buffer_store_dword v6, v4, s[36:39], 0 offen sc1\n"
"v_mfma_f32_16x16x32_bf16 v[56:59], a[104:107], v[116:119], v[56:59]\n"
"ds_read_b128 v[108:111], v5 offset:26880\n"
"v_mfma_f32_16x16x32_bf16 v[60:63], a[108:111], v[116:119], v[60:63]\n"
"s_mov_b32 s74, s47\n"
"s_add_u32 s47, s47, s48\n"
"s_waitcnt lgkmcnt(5)\n"
"v_mfma_f32_16x16x32_bf16 v[32:35], a[112:115], v[120:123], v[32:35]\n"
"s_add_u32 s73, s73, 1\n"
"v_mfma_f32_16x16x32_bf16 v[36:39], a[116:119], v[120:123], v[36:39]\n"
"s_add_u32 m0, s71, 0\n"
"s_add_u32 s72, s68, 0\n"
"buffer_load_dwordx4 v0, s[36:39], s72 offen offset:0 sc1 lds\n"
"v_mfma_f32_16x16x32_bf16 v[48:51], a[120:123], v[120:123], v[48:51]\n"
"ds_read_b128 v[112:115], v5 offset:28672\n"
"v_mfma_f32_16x16x32_bf16 v[52:55], a[124:127], v[120:123], v[52:55]\n"
"s_waitcnt lgkmcnt(5)\n"
"v_mfma_f32_16x16x32_bf16 v[40:43], a[112:115], v[124:127], v[40:43]\n"
"v_mfma_f32_16x16x32_bf16 v[44:47], a[116:119], v[124:127], v[44:47]\n"
"buffer_load_dwordx4 v0, s[36:39], s72 offen offset:1024 sc1 lds\n"
"v_mfma_f32_16x16x32_bf16 v[56:59], a[120:123], v[124:127], v[56:59]\n"
"ds_read_b128 v[116:119], v5 offset:28928\n"
"v_mfma_f32_16x16x32_bf16 v[60:63], a[124:127], v[124:127], v[60:63]\n"
"s_waitcnt lgkmcnt(5)\n"
"v_mfma_f32_16x16x32_bf16 v[32:35], a[128:131], v[96:99], v[32:35]\n"
"v_mfma_f32_16x16x32_bf16 v[36:39], a[132:135], v[96:99], v[36:39]\n"
"buffer_load_dwordx4 v0, s[36:39], s72 offen offset:2048 sc1 lds\n"
"v_mfma_f32_16x16x32_bf16 v[48:51], a[136:139], v[96:99], v[48:51]\n"
"ds_read_b128 v[120:123], v5 offset:30720\n"
"v_mfma_f32_16x16x32_bf16 v[52:55], a[140:143], v[96:99], v[52:55]\n"
"s_waitcnt lgkmcnt(5)\n"
"v_mfma_f32_16x16x32_bf16 v[40:43], a[128:131], v[100:103], v[40:43]\n"
"v_mfma_f32_16x16x32_bf16 v[44:47], a[132:135], v[100:103], v[44:47]\n"
"buffer_load_dwordx4 v0, s[36:39], s72 offen offset:3072 sc1 lds\n"
"v_mfma_f32_16x16x32_bf16 v[56:59], a[136:139], v[100:103], v[56:59]\n"
"ds_read_b128 v[124:127], v5 offset:30976\n"
"v_mfma_f32_16x16x32_bf16 v[60:63], a[140:143], v[100:103], v[60:63]\n"
"s_waitcnt lgkmcnt(5)\n"
"v_mfma_f32_16x16x32_bf16 v[32:35], a[144:147], v[104:107], v[32:35]\n"
"v_mfma_f32_16x16x32_bf16 v[36:39], a[148:151], v[104:107], v[36:39]\n"
"s_add_u32 m0, s71, 4096\n"
"s_add_u32 s72, s68, 4096\n"
"buffer_load_dwordx4 v0, s[36:39], s72 offen offset:0 sc1 lds\n"
"v_mfma_f32_16x16x32_bf16 v[48:51], a[152:155], v[104:107], v[48:51]\n"
"ds_read_b128 v[96:99], v5 offset:32768\n"
"v_mfma_f32_16x16x32_bf16 v[52:55], a[156:159], v[104:107], v[52:55]\n"
"s_waitcnt lgkmcnt(5)\n"
"v_mfma_f32_16x16x32_bf16 v[40:43], a[144:147], v[108:111], v[40:43]\n"
"v_mfma_f32_16x16x32_bf16 v[44:47], a[148:151], v[108:111], v[44:47]\n"
"buffer_load_dwordx4 v0, s[36:39], s72 offen offset:1024 sc1 lds\n"
"v_mfma_f32_16x16x32_bf16 v[56:59], a[152:155], v[108:111], v[56:59]\n"
"ds_read_b128 v[100:103], v5 offset:33024\n"
"v_mfma_f32_16x16x32_bf16 v[60:63], a[156:159], v[108:111], v[60:63]\n"
"s_waitcnt lgkmcnt(5)\n"
"v_mfma_f32_16x16x32_bf16 v[32:35], a[160:163], v[112:115], v[32:35]\n"
"v_mfma_f32_16x16x32_bf16 v[36:39], a[164:167], v[112:115], v[36:39]\n"
"buffer_load_dwordx4 v0, s[36:39], s72 offen offset:2048 sc1 lds\n"
"v_mfma_f32_16x16x32_bf16 v[48:51], a[168:171], v[112:115], v[48:51]\n"
"ds_read_b128 v[104:107], v5 offset:34816\n"
"v_mfma_f32_16x16x32_bf16 v[52:55], a[172:175], v[112:115], v[52:55]\n"
"s_waitcnt lgkmcnt(5)\n"
"v_mfma_f32_16x16x32_bf16 v[40:43], a[160:163], v[116:119], v[40:43]\n"
"v_mfma_f32_16x16x32_bf16 v[44:47], a[164:167], v[116:119], v[44:47]\n"
"buffer_load_dwordx4 v0, s[36:39], s72 offen offset:3072 sc1 lds\n"
"v_mfma_f32_16x16x32_bf16 v[56:59], a[168:171], v[116:119], v[56:59]\n"
"ds_read_b128 v[108:111], v5 offset:35072\n"
"v_mfma_f32_16x16x32_bf16 v[60:63], a[172:175], v[116:119], v[60:63]\n"
"s_waitcnt lgkmcnt(5)\n"
"v_mfma_f32_16x16x32_bf16 v[32:35], a[176:179], v[120:123], v[32:35]\n"
"v_mfma_f32_16x16x32_bf16 v[36:39], a[180:183], v[120:123], v[36:39]\n"
"s_add_u32 m0, s71, 8192\n"
"s_add_u32 s72, s68, 8192\n"
"buffer_load_dwordx4 v0, s[36:39], s72 offen offset:0 sc1 lds\n"
"v_mfma_f32_16x16x32_bf16 v[48:51], a[184:187], v[120:123], v[48:51]\n"
"ds_read_b128 v[112:115], v5 offset:36864\n"
"v_mfma_f32_16x16x32_bf16 v[52:55], a[188:191], v[120:123], v[52:55]\n"
"s_waitcnt lgkmcnt(5)\n"
"v_mfma_f32_16x16x32_bf16 v[40:43], a[176:179], v[124:127], v[40:43]\n"
"v_mfma_f32_16x16x32_bf16 v[44:47], a[180:183], v[124:127], v[44:47]\n"
"buffer_load_dwordx4 v0, s[36:39], s72 offen offset:1024 sc1 lds\n"
"v_mfma_f32_16x16x32_bf16 v[56:59], a[184:187], v[124:127], v[56:59]\n"
"ds_read_b128 v[116:119], v5 offset:37120\n"
"v_mfma_f32_16x16x32_bf16 v[60:63], a[188:191], v[124:127], v[60:63]\n"
"s_waitcnt lgkmcnt(5)\n"
"v_mfma_f32_16x16x32_bf16 v[32:35], a[192:195], v[96:99], v[32:35]\n"
"v_mfma_f32_16x16x32_bf16 v[36:39], a[196:199], v[96:99], v[36:39]\n"
"v_mfma_f32_16x16x32_bf16 v[48:51], a[200:203], v[96:99], v[48:51]\n"
"ds_read_b128 v[120:123], v5 offset:38912\n"
"v_mfma_f32_16x16x32_bf16 v[52:55], a[204:207], v[96:99], v[52:55]\n"
"s_waitcnt lgkmcnt(5)\n"
"v_mfma_f32_16x16x32_bf16 v[40:43], a[192:195], v[100:103], v[40:43]\n"
"v_mfma_f32_16x16x32_bf16 v[44:47], a[196:199], v[100:103], v[44:47]\n"
"v_mfma_f32_16x16x32_bf16 v[56:59], a[200:203], v[100:103], v[56:59]\n"
"ds_read_b128 v[124:127], v5 offset:39168\n"
"v_mfma_f32_16x16x32_bf16 v[60:63], a[204:207], v[100:103], v[60:63]\n"
"s_waitcnt lgkmcnt(5)\n"
"v_mfma_f32_16x16x32_bf16 v[32:35], a[208:211], v[104:107], v[32:35]\n"
"v_mfma_f32_16x16x32_bf16 v[36:39], a[212:215], v[104:107], v[36:39]\n"
"v_mfma_f32_16x16x32_bf16 v[48:51], a[216:219], v[104:107], v[48:51]\n"
"s_add_u32 s70, s70, 40960\n"
"s_cmp_eq_u32 s70, 122880\n"
"s_cselect_b32 s70, 0, s70\n"
"v_mfma_f32_16x16x32_bf16 v[52:55], a[220:223], v[104:107], v[52:55]\n"
"s_waitcnt lgkmcnt(4)\n"
"v_mfma_f32_16x16x32_bf16 v[40:43], a[208:211], v[108:111], v[40:43]\n"
"v_add_u32 v5, s70, v7\n"
"v_mfma_f32_16x16x32_bf16 v[44:47], a[212:215], v[108:111], v[44:47]\n"
"v_mfma_f32_16x16x32_bf16 v[56:59], a[216:219], v[108:111], v[56:59]\n"
"s_waitcnt lgkmcnt(0)\n"
"s_barrier\n"
"v_mfma_f32_16x16x32_bf16 v[60:63], a[220:223], v[108:111], v[60:63]\n"
"ds_read_b128 v[96:99], v5 offset:0\n"
"v_mfma_f32_16x16x32_bf16 v[32:35], a[224:227], v[112:115], v[32:35]\n"
"v_mfma_f32_16x16x32_bf16 v[36:39], a[228:231], v[112:115], v[36:39]\n"
"ds_read_b128 v[100:103], v5 offset:256\n"
"v_mfma_f32_16x16x32_bf16 v[48:51], a[232:235], v[112:115], v[48:51]\n"
"v_mfma_f32_16x16x32_bf16 v[52:55], a[236:239], v[112:115], v[52:55]\n"
"ds_read_b128 v[104:107], v5 offset:2048\n"
"v_mfma_f32_16x16x32_bf16 v[40:43], a[224:227], v[116:119], v[40:43]\n"
"v_mfma_f32_16x16x32_bf16 v[44:47], a[228:231], v[116:119], v[44:47]\n"
"v_mfma_f32_16x16x32_bf16 v[56:59], a[232:235], v[116:119], v[56:59]\n"
"ds_read_b128 v[108:111], v5 offset:2304\n"
"v_mfma_f32_16x16x32_bf16 v[60:63], a[236:239], v[116:119], v[60:63]\n"
"v_mfma_f32_16x16x32_bf16 v[32:35], a[240:243], v[120:123], v[32:35]\n"
"v_mfma_f32_16x16x32_bf16 v[36:39], a[244:247], v[120:123], v[36:39]\n"
"v_mfma_f32_16x16x32_bf16 v[48:51], a[248:251], v[120:123], v[48:51]\n"
"ds_read_b128 v[112:115], v5 offset:4096\n"
"v_mfma_f32_16x16x32_bf16 v[52:55], a[252:255], v[120:123], v[52:55]\n"
"v_mfma_f32_16x16x32_bf16 v[40:43], a[240:243], v[124:127], v[40:43]\n"
"s_add_u32 s68, s68, 40960\n"
"s_add_u32 s69, s69, 40960\n"
"v_mfma_f32_16x16x32_bf16 v[44:47], a[244:247], v[124:127], v[44:47]\n"
"s_cmp_eq_u32 s69, 122880\n"
"s_cselect_b32 s69, 0, s69\n"
"v_mfma_f32_16x16x32_bf16 v[56:59], a[248:251], v[124:127], v[56:59]\n"
"ds_read_b128 v[116:119], v5 offset:4352\n"
"v_mfma_f32_16x16x32_bf16 v[60:63], a[252:255], v[124:127], v[60:63]\n"
"s_add_u32 s71, s69, s50\n"
"s_waitcnt lgkmcnt(5)\n"
"v_mfma_f32_16x16x32_bf16 v[64:67], v[192:195], v[96:99], 0\n"
"v_mfma_f32_16x16x32_bf16 v[68:71], v[196:199], v[96:99], 0\n"
"v_mfma_f32_16x16x32_bf16 v[80:83], v[200:203], v[96:99], 0\n"
"ds_read_b128 v[120:123], v5 offset:6144\n"
"v_mfma_f32_16x16x32_bf16 v[84:87], v[204:207], v[96:99], 0\n"
"s_waitcnt lgkmcnt(5)\n"
"v_mfma_f32_16x16x32_bf16 v[72:75], v[192:195], v[100:103], 0\n"
"v_mfma_f32_16x16x32_bf16 v[76:79], v[196:199], v[100:103], 0\n"
"v_mfma_f32_16x16x32_bf16 v[88:91], v[200:203], v[100:103], 0\n"
"ds_read_b128 v[124:127], v5 offset:6400\n"
"v_mfma_f32_16x16x32_bf16 v[92:95], v[204:207], v[100:103], 0\n"
"s_waitcnt lgkmcnt(5)\n"
"v_mfma_f32_16x16x32_bf16 v[64:67], v[208:211], v[104:107], v[64:67]\n"
"v_cvt_pk_bf16_f32 v128, v32, v33\n"
"v_mfma_f32_16x16x32_bf16 v[68:71], v[212:215], v[104:107], v[68:71]\n"
"v_cvt_pk_bf16_f32 v129, v34, v35\n"
"v_mfma_f32_16x16x32_bf16 v[80:83], v[216:219], v[104:107], v[80:83]\n"
"ds_read_b128 v[96:99], v5 offset:8192\n"
"v_mfma_f32_16x16x32_bf16 v[84:87], v[220:223], v[104:107], v[84:87]\n"
"v_cvt_pk_bf16_f32 v130, v36, v37\n"
"s_waitcnt lgkmcnt(5)\n"
"v_mfma_f32_16x16x32_bf16 v[72:75], v[208:211], v[108:111], v[72:75]\n"
"v_cvt_pk_bf16_f32 v131, v38, v39\n"
"v_mfma_f32_16x16x32_bf16 v[76:79], v[212:215], v[108:111], v[76:79]\n"
"buffer_store_dwordx4 v[128:131], v7, s[36:39], s74 offen offset:0 sc1\n"
"v_mfma_f32_16x16x32_bf16 v[88:91], v[216:219], v[108:111], v[88:91]\n"
"ds_read_b128 v[100:103], v5 offset:8448\n"
"v_mfma_f32_16x16x32_bf16 v[92:95], v[220:223], v[108:111], v[92:95]\n"
"v_cvt_pk_bf16_f32 v132, v40, v41\n"
"s_waitcnt lgkmcnt(5)\n"
"v_mfma_f32_16x16x32_bf16 v[64:67], v[224:227], v[112:115], v[64:67]\n"
"v_cvt_pk_bf16_f32 v133, v42, v43\n"
"v_mfma_f32_16x16x32_bf16 v[68:71], v[228:231], v[112:115], v[68:71]\n"
"v_cvt_pk_bf16_f32 v134, v44, v45\n"
"v_mfma_f32_16x16x32_bf16 v[80:83], v[232:235], v[112:115], v[80:83]\n"
"ds_read_b128 v[104:107], v5 offset:10240\n"
"v_mfma_f32_16x16x32_bf16 v[84:87], v[236:239], v[112:115], v[84:87]\n"
"v_cvt_pk_bf16_f32 v135, v46, v47\n"
"s_waitcnt lgkmcnt(5)\n"
"v_mfma_f32_16x16x32_bf16 v[72:75], v[224:227], v[116:119], v[72:75]\n"
"buffer_store_dwordx4 v[132:135], v7, s[36:39], s74 offen offset:256 sc1\n"
"v_mfma_f32_16x16x32_bf16 v[76:79], v[228:231], v[116:119], v[76:79]\n"
"v_cvt_pk_bf16_f32 v136, v48, v49\n"
"v_mfma_f32_16x16x32_bf16 v[88:91], v[232:235], v[116:119], v[88:91]\n"
"ds_read_b128 v[108:111], v5 offset:10496\n"
"v_mfma_f32_16x16x32_bf16 v[92:95], v[236:239], v[116:119], v[92:95]\n"
"v_cvt_pk_bf16_f32 v137, v50, v51\n"
"s_waitcnt lgkmcnt(5)\n"
"v_mfma_f32_16x16x32_bf16 v[64:67], v[240:243], v[120:123], v[64:67]\n"
"v_cvt_pk_bf16_f32 v138, v52, v53\n"
"v_mfma_f32_16x16x32_bf16 v[68:71], v[244:247], v[120:123], v[68:71]\n"
"v_cvt_pk_bf16_f32 v139, v54, v55\n"
"v_mfma_f32_16x16x32_bf16 v[80:83], v[248:251], v[120:123], v[80:83]\n"
"ds_read_b128 v[112:115], v5 offset:12288\n"
"v_mfma_f32_16x16x32_bf16 v[84:87], v[252:255], v[120:123], v[84:87]\n"
"buffer_store_dwordx4 v[136:139], v7, s[36:39], s74 offen offset:2048 sc1\n"
"s_waitcnt lgkmcnt(5)\n"
"v_mfma_f32_16x16x32_bf16 v[72:75], v[240:243], v[124:127], v[72:75]\n"
"v_cvt_pk_bf16_f32 v140, v56, v57\n"
"v_mfma_f32_16x16x32_bf16 v[76:79], v[244:247], v[124:127], v[76:79]\n"
"v_cvt_pk_bf16_f32 v141, v58, v59\n"
"v_mfma_f32_16x16x32_bf16 v[88:91], v[248:251], v[124:127], v[88:91]\n"
"ds_read_b128 v[116:119], v5 offset:12544\n"
"v_mfma_f32_16x16x32_bf16 v[92:95], v[252:255], v[124:127], v[92:95]\n"
"v_cvt_pk_bf16_f32 v142, v60, v61\n"
"s_waitcnt lgkmcnt(5)\n"
"v_mfma_f32_16x16x32_bf16 v[64:67], a[0:3], v[96:99], v[64:67]\n"
"v_cvt_pk_bf16_f32 v143, v62, v63\n"
"v_mfma_f32_16x16x32_bf16 v[68:71], a[4:7], v[96:99], v[68:71]\n"
"buffer_store_dwordx4 v[140:143], v7, s[36:39], s74 offen offset:2304 sc1\n"
"v_mfma_f32_16x16x32_bf16 v[80:83], a[8:11], v[96:99], v[80:83]\n"
"ds_read_b128 v[120:123], v5 offset:14336\n"
"v_mfma_f32_16x16x32_bf16 v[84:87], a[12:15], v[96:99], v[84:87]\n"
"s_waitcnt lgkmcnt(5)\n"
"v_mfma_f32_16x16x32_bf16 v[72:75], a[0:3], v[100:103], v[72:75]\n"
"v_mfma_f32_16x16x32_bf16 v[76:79], a[4:7], v[100:103], v[76:79]\n"
"v_mfma_f32_16x16x32_bf16 v[88:91], a[8:11], v[100:103], v[88:91]\n"
"ds_read_b128 v[124:127], v5 offset:14592\n"
"v_mfma_f32_16x16x32_bf16 v[92:95], a[12:15], v[100:103], v[92:95]\n"
"s_waitcnt lgkmcnt(5)\n"
"v_mfma_f32_16x16x32_bf16 v[64:67], a[16:19], v[104:107], v[64:67]\n"
"v_mfma_f32_16x16x32_bf16 v[68:71], a[20:23], v[104:107], v[68:71]\n"
"v_mfma_f32_16x16x32_bf16 v[80:83], a[24:27], v[104:107], v[80:83]\n"
"ds_read_b128 v[96:99], v5 offset:16384\n"
"v_mfma_f32_16x16x32_bf16 v[84:87], a[28:31], v[104:107], v[84:87]\n"
"s_waitcnt lgkmcnt(5)\n"
"v_mfma_f32_16x16x32_bf16 v[72:75], a[16:19], v[108:111], v[72:75]\n"
"v_mfma_f32_16x16x32_bf16 v[76:79], a[20:23], v[108:111], v[76:79]\n"
"v_mfma_f32_16x16x32_bf16 v[88:91], a[24:27], v[108:111], v[88:91]\n"
"ds_read_b128 v[100:103], v5 offset:16640\n"
"v_mfma_f32_16x16x32_bf16 v[92:95], a[28:31], v[108:111], v[92:95]\n"
"s_waitcnt lgkmcnt(5)\n"
"v_mfma_f32_16x16x32_bf16 v[64:67], a[32:35], v[112:115], v[64:67]\n"
"v_mfma_f32_16x16x32_bf16 v[68:71], a[36:39], v[112:115], v[68:71]\n"
"v_mfma_f32_16x16x32_bf16 v[80:83], a[40:43], v[112:115], v[80:83]\n"
"ds_read_b128 v[104:107], v5 offset:18432\n"
"v_mfma_f32_16x16x32_bf16 v[84:87], a[44:47], v[112:115], v[84:87]\n"
"s_waitcnt lgkmcnt(5)\n"
"v_mfma_f32_16x16x32_bf16 v[72:75], a[32:35], v[116:119], v[72:75]\n"
"v_mfma_f32_16x16x32_bf16 v[76:79], a[36:39], v[116:119], v[76:79]\n"
"v_mfma_f32_16x16x32_bf16 v[88:91], a[40:43], v[116:119], v[88:91]\n"
"ds_read_b128 v[108:111], v5 offset:18688\n"
"v_mfma_f32_16x16x32_bf16 v[92:95], a[44:47], v[116:119], v[92:95]\n"
"s_waitcnt lgkmcnt(5)\n"
"v_mfma_f32_16x16x32_bf16 v[64:67], a[48:51], v[120:123], v[64:67]\n"
"v_mfma_f32_16x16x32_bf16 v[68:71], a[52:55], v[120:123], v[68:71]\n"
"v_mfma_f32_16x16x32_bf16 v[80:83], a[56:59], v[120:123], v[80:83]\n"
"ds_read_b128 v[112:115], v5 offset:20480\n"
"v_mfma_f32_16x16x32_bf16 v[84:87], a[60:63], v[120:123], v[84:87]\n"
"s_waitcnt lgkmcnt(5)\n"
"v_mfma_f32_16x16x32_bf16 v[72:75], a[48:51], v[124:127], v[72:75]\n"
"v_mfma_f32_16x16x32_bf16 v[76:79], a[52:55], v[124:127], v[76:79]\n"
"v_mfma_f32_16x16x32_bf16 v[88:91], a[56:59], v[124:127], v[88:91]\n"
"ds_read_b128 v[116:119], v5 offset:20736\n"
"v_mfma_f32_16x16x32_bf16 v[92:95], a[60:63], v[124:127], v[92:95]\n"
"s_waitcnt lgkmcnt(5)\n"
"v_mfma_f32_16x16x32_bf16 v[64:67], a[64:67], v[96:99], v[64:67]\n"
"v_mfma_f32_16x16x32_bf16 v[68:71], a[68:71], v[96:99], v[68:71]\n"
"v_mfma_f32_16x16x32_bf16 v[80:83], a[72:75], v[96:99], v[80:83]\n"
"ds_read_b128 v[120:123], v5 offset:22528\n"
"v_mfma_f32_16x16x32_bf16 v[84:87], a[76:79], v[96:99], v[84:87]\n"
"s_waitcnt lgkmcnt(5)\n"
"v_mfma_f32_16x16x32_bf16 v[72:75], a[64:67], v[100:103], v[72:75]\n"
"v_mfma_f32_16x16x32_bf16 v[76:79], a[68:71], v[100:103], v[76:79]\n"
"v_mfma_f32_16x16x32_bf16 v[88:91], a[72:75], v[100:103], v[88:91]\n"
"ds_read_b128 v[124:127], v5 offset:22784\n"
"v_mfma_f32_16x16x32_bf16 v[92:95], a[76:79], v[100:103], v[92:95]\n"
"s_waitcnt lgkmcnt(5)\n"
"v_mfma_f32_16x16x32_bf16 v[64:67], a[80:83], v[104:107], v[64:67]\n"
"v_mfma_f32_16x16x32_bf16 v[68:71], a[84:87], v[104:107], v[68:71]\n"
"v_mfma_f32_16x16x32_bf16 v[80:83], a[88:91], v[104:107], v[80:83]\n"
"ds_read_b128 v[96:99], v5 offset:24576\n"
"v_mfma_f32_16x16x32_bf16 v[84:87], a[92:95], v[104:107], v[84:87]\n"
"s_waitcnt lgkmcnt(5)\n"
"v_mfma_f32_16x16x32_bf16 v[72:75], a[80:83], v[108:111], v[72:75]\n"
"v_mfma_f32_16x16x32_bf16 v[76:79], a[84:87], v[108:111], v[76:79]\n"
"v_mfma_f32_16x16x32_bf16 v[88:91], a[88:91], v[108:111], v[88:91]\n"
"ds_read_b128 v[100:103], v5 offset:24832\n"
"v_mfma_f32_16x16x32_bf16 v[92:95], a[92:95], v[108:111], v[92:95]\n"
"s_waitcnt lgkmcnt(5)\n"
"v_mfma_f32_16x16x32_bf16 v[64:67], a[96:99], v[112:115], v[64:67]\n"
"v_mfma_f32_16x16x32_bf16 v[68:71], a[100:103], v[112:115], v[68:71]\n"
"v_mfma_f32_16x16x32_bf16 v[80:83], a[104:107], v[112:115], v[80:83]\n"
"ds_read_b128 v[104:107], v5 offset:26624\n"
"v_mfma_f32_16x16x32_bf16 v[84:87], a[108:111], v[112:115], v[84:87]\n"
"v_mov_b32 v6, s73\n"
"s_waitcnt lgkmcnt(5)\n"
"v_mfma_f32_16x16x32_bf16 v[72:75], a[96:99], v[116:119], v[72:75]\n"
"s_waitcnt vmcnt(0)\n"
"v_mfma_f32_16x16x32_bf16 v[76:79], a[100:103], v[116:119], v[76:79]\n"
"buffer_store_dword v6, v4, s[36:39], 0 offen sc1\n"
"v_mfma_f32_16x16x32_bf16 v[88:91], a[104:107], v[116:119], v[88:91]\n"
"ds_read_b128 v[108:111], v5 offset:26880\n"
"v_mfma_f32_16x16x32_bf16 v[92:95], a[108:111], v[116:119], v[92:95]\n"
"s_mov_b32 s74, s47\n"
"s_add_u32 s47, s47, s48\n"
"s_waitcnt lgkmcnt(5)\n"
"v_mfma_f32_16x16x32_bf16 v[64:67], a[112:115], v[120:123], v[64:67]\n"
"s_add_u32 s73, s73, 1\n"
"v_mfma_f32_16x16x32_bf16 v[68:71], a[116:119], v[120:123], v[68:71]\n"
"s_add_u32 m0, s71, 0\n"
"s_add_u32 s72, s68, 0\n"
"buffer_load_dwordx4 v0, s[36:39], s72 offen offset:0 sc1 lds\n"
"v_mfma_f32_16x16x32_bf16 v[80:83], a[120:123], v[120:123], v[80:83]\n"
"ds_read_b128 v[112:115], v5 offset:28672\n"
"v_mfma_f32_16x16x32_bf16 v[84:87], a[124:127], v[120:123], v[84:87]\n"
"s_waitcnt lgkmcnt(5)\n"
"v_mfma_f32_16x16x32_bf16 v[72:75], a[112:115], v[124:127], v[72:75]\n"
"v_mfma_f32_16x16x32_bf16 v[76:79], a[116:119], v[124:127], v[76:79]\n"
"buffer_load_dwordx4 v0, s[36:39], s72 offen offset:1024 sc1 lds\n"
"v_mfma_f32_16x16x32_bf16 v[88:91], a[120:123], v[124:127], v[88:91]\n"
"ds_read_b128 v[116:119], v5 offset:28928\n"
"v_mfma_f32_16x16x32_bf16 v[92:95], a[124:127], v[124:127], v[92:95]\n"
"s_waitcnt lgkmcnt(5)\n"
"v_mfma_f32_16x16x32_bf16 v[64:67], a[128:131], v[96:99], v[64:67]\n"
"v_mfma_f32_16x16x32_bf16 v[68:71], a[132:135], v[96:99], v[68:71]\n"
"buffer_load_dwordx4 v0, s[36:39], s72 offen offset:2048 sc1 lds\n"
"v_mfma_f32_16x16x32_bf16 v[80:83], a[136:139], v[96:99], v[80:83]\n"
"ds_read_b128 v[120:123], v5 offset:30720\n"
"v_mfma_f32_16x16x32_bf16 v[84:87], a[140:143], v[96:99], v[84:87]\n"
"s_waitcnt lgkmcnt(5)\n"
"v_mfma_f32_16x16x32_bf16 v[72:75], a[128:131], v[100:103], v[72:75]\n"
"v_mfma_f32_16x16x32_bf16 v[76:79], a[132:135], v[100:103], v[76:79]\n"
"buffer_load_dwordx4 v0, s[36:39], s72 offen offset:3072 sc1 lds\n"
"v_mfma_f32_16x16x32_bf16 v[88:91], a[136:139], v[100:103], v[88:91]\n"
"ds_read_b128 v[124:127], v5 offset:30976\n"
"v_mfma_f32_16x16x32_bf16 v[92:95], a[140:143], v[100:103], v[92:95]\n"
"s_waitcnt lgkmcnt(5)\n"
"v_mfma_f32_16x16x32_bf16 v[64:67], a[144:147], v[104:107], v[64:67]\n"
"v_mfma_f32_16x16x32_bf16 v[68:71], a[148:151], v[104:107], v[68:71]\n"
"s_add_u32 m0, s71, 4096\n"
"s_add_u32 s72, s68, 4096\n"
"buffer_load_dwordx4 v0, s[36:39], s72 offen offset:0 sc1 lds\n"
"v_mfma_f32_16x16x32_bf16 v[80:83], a[152:155], v[104:107], v[80:83]\n"
"ds_read_b128 v[96:99], v5 offset:32768\n"
"v_mfma_f32_16x16x32_bf16 v[84:87], a[156:159], v[104:107], v[84:87]\n"
"s_waitcnt lgkmcnt(5)\n"
"v_mfma_f32_16x16x32_bf16 v[72:75], a[144:147], v[108:111], v[72:75]\n"
"v_mfma_f32_16x16x32_bf16 v[76:79], a[148:151], v[108:111], v[76:79]\n"
"buffer_load_dwordx4 v0, s[36:39], s72 offen offset:1024 sc1 lds\n"
"v_mfma_f32_16x16x32_bf16 v[88:91], a[152:155], v[108:111], v[88:91]\n"
"ds_read_b128 v[100:103], v5 offset:33024\n"
"v_mfma_f32_16x16x32_bf16 v[92:95], a[156:159], v[108:111], v[92:95]\n"
"s_waitcnt lgkmcnt(5)\n"
"v_mfma_f32_16x16x32_bf16 v[64:67], a[160:163], v[112:115], v[64:67]\n"
"v_mfma_f32_16x16x32_bf16 v[68:71], a[164:167], v[112:115], v[68:71]\n"
"buffer_load_dwordx4 v0, s[36:39], s72 offen offset:2048 sc1 lds\n"
"v_mfma_f32_16x16x32_bf16 v[80:83], a[168:171], v[112:115], v[80:83]\n"
"ds_read_b128 v[104:107], v5 offset:34816\n"
"v_mfma_f32_16x16x32_bf16 v[84:87], a[172:175], v[112:115], v[84:87]\n"
"s_waitcnt lgkmcnt(5)\n"
"v_mfma_f32_16x16x32_bf16 v[72:75], a[160:163], v[116:119], v[72:75]\n"
"v_mfma_f32_16x16x32_bf16 v[76:79], a[164:167], v[116:119], v[76:79]\n"
"buffer_load_dwordx4 v0, s[36:39], s72 offen offset:3072 sc1 lds\n"
"v_mfma_f32_16x16x32_bf16 v[88:91], a[168:171], v[116:119], v[88:91]\n"
"ds_read_b128 v[108:111], v5 offset:35072\n"
"v_mfma_f32_16x16x32_bf16 v[92:95], a[172:175], v[116:119], v[92:95]\n"
"s_waitcnt lgkmcnt(5)\n"
"v_mfma_f32_16x16x32_bf16 v[64:67], a[176:179], v[120:123], v[64:67]\n"
"v_mfma_f32_16x16x32_bf16 v[68:71], a[180:183], v[120:123], v[68:71]\n"
"s_add_u32 m0, s71, 8192\n"
"s_add_u32 s72, s68, 8192\n"
"buffer_load_dwordx4 v0, s[36:39], s72 offen offset:0 sc1 lds\n"
"v_mfma_f32_16x16x32_bf16 v[80:83], a[184:187], v[120:123], v[80:83]\n"
"ds_read_b128 v[112:115], v5 offset:36864\n"
"v_mfma_f32_16x16x32_bf16 v[84:87], a[188:191], v[120:123], v[84:87]\n"
"s_waitcnt lgkmcnt(5)\n"
"v_mfma_f32_16x16x32_bf16 v[72:75], a[176:179], v[124:127], v[72:75]\n"
"v_mfma_f32_16x16x32_bf16 v[76:79], a[180:183], v[124:127], v[76:79]\n"
"buffer_load_dwordx4 v0, s[36:39], s72 offen offset:1024 sc1 lds\n"
"v_mfma_f32_16x16x32_bf16 v[88:91], a[184:187], v[124:127], v[88:91]\n"
"ds_read_b128 v[116:119], v5 offset:37120\n"
"v_mfma_f32_16x16x32_bf16 v[92:95], a[188:191], v[124:127], v[92:95]\n"
"s_waitcnt lgkmcnt(5)\n"
"v_mfma_f32_16x16x32_bf16 v[64:67], a[192:195], v[96:99], v[64:67]\n"
"v_mfma_f32_16x16x32_bf16 v[68:71], a[196:199], v[96:99], v[68:71]\n"
"v_mfma_f32_16x16x32_bf16 v[80:83], a[200:203], v[96:99], v[80:83]\n"
"ds_read_b128 v[120:123], v5 offset:38912\n"
"v_mfma_f32_16x16x32_bf16 v[84:87], a[204:207], v[96:99], v[84:87]\n"
"s_waitcnt lgkmcnt(5)\n"
"v_mfma_f32_16x16x32_bf16 v[72:75], a[192:195], v[100:103], v[72:75]\n"
"v_mfma_f32_16x16x32_bf16 v[76:79], a[196:199], v[100:103], v[76:79]\n"
"v_mfma_f32_16x16x32_bf16 v[88:91], a[200:203], v[100:103], v[88:91]\n"
"ds_read_b128 v[124:127], v5 offset:39168\n"
"v_mfma_f32_16x16x32_bf16 v[92:95], a[204:207], v[100:103], v[92:95]\n"
"s_waitcnt lgkmcnt(5)\n"
"v_mfma_f32_16x16x32_bf16 v[64:67], a[208:211], v[104:107], v[64:67]\n"
"v_mfma_f32_16x16x32_bf16 v[68:71], a[212:215], v[104:107], v[68:71]\n"
"v_mfma_f32_16x16x32_bf16 v[80:83], a[216:219], v[104:107], v[80:83]\n"
"s_add_u32 s70, s70, 40960\n"
"s_cmp_eq_u32 s70, 122880\n"
"s_cselect_b32 s70, 0, s70\n"
"v_mfma_f32_16x16x32_bf16 v[84:87], a[220:223], v[104:107], v[84:87]\n"
"s_waitcnt lgkmcnt(4)\n"
"v_mfma_f32_16x16x32_bf16 v[72:75], a[208:211], v[108:111], v[72:75]\n"
"v_add_u32 v5, s70, v7\n"
"v_mfma_f32_16x16x32_bf16 v[76:79], a[212:215], v[108:111], v[76:79]\n"
"v_mfma_f32_16x16x32_bf16 v[88:91], a[216:219], v[108:111], v[88:91]\n"
"s_waitcnt lgkmcnt(0)\n"
"s_barrier\n"
"v_mfma_f32_16x16x32_bf16 v[92:95], a[220:223], v[108:111], v[92:95]\n"
"ds_read_b128 v[96:99], v5 offset:0\n"
"v_mfma_f32_16x16x32_bf16 v[64:67], a[224:227], v[112:115], v[64:67]\n"
"v_mfma_f32_16x16x32_bf16 v[68:71], a[228:231], v[112:115], v[68:71]\n"
"ds_read_b128 v[100:103], v5 offset:256\n"
"v_mfma_f32_16x16x32_bf16 v[80:83], a[232:235], v[112:115], v[80:83]\n"
"v_mfma_f32_16x16x32_bf16 v[84:87], a[236:239], v[112:115], v[84:87]\n"
"ds_read_b128 v[104:107], v5 offset:2048\n"
"v_mfma_f32_16x16x32_bf16 v[72:75], a[224:227], v[116:119], v[72:75]\n"
"v_mfma_f32_16x16x32_bf16 v[76:79], a[228:231], v[116:119], v[76:79]\n"
"v_mfma_f32_16x16x32_bf16 v[88:91], a[232:235], v[116:119], v[88:91]\n"
"ds_read_b128 v[108:111], v5 offset:2304\n"
"v_mfma_f32_16x16x32_bf16 v[92:95], a[236:239], v[116:119], v[92:95]\n"
"v_mfma_f32_16x16x32_bf16 v[64:67], a[240:243], v[120:123], v[64:67]\n"
"v_mfma_f32_16x16x32_bf16 v[68:71], a[244:247], v[120:123], v[68:71]\n"
"v_mfma_f32_16x16x32_bf16 v[80:83], a[248:251], v[120:123], v[80:83]\n"
"ds_read_b128 v[112:115], v5 offset:4096\n"
"v_mfma_f32_16x16x32_bf16 v[84:87], a[252:255], v[120:123], v[84:87]\n"
"v_mfma_f32_16x16x32_bf16 v[72:75], a[240:243], v[124:127], v[72:75]\n"
"s_add_u32 s68, s68, 40960\n"
"s_add_u32 s69, s69, 40960\n"
"v_mfma_f32_16x16x32_bf16 v[76:79], a[244:247], v[124:127], v[76:79]\n"
"s_cmp_eq_u32 s69, 122880\n"
"s_cselect_b32 s69, 0, s69\n"
"v_mfma_f32_16x16x32_bf16 v[88:91], a[248:251], v[124:127], v[88:91]\n"
"ds_read_b128 v[116:119], v5 offset:4352\n"
"v_mfma_f32_16x16x32_bf16 v[92:95], a[252:255], v[124:127], v[92:95]\n"
"s_add_u32 s71, s69, s50\n"
"s_cmp_eq_u32 s73, s79\n"
"s_cbranch_scc0 GP6_%=\n"
"s_cmp_eq_u32 s73, 64\n"
"s_cbranch_scc1 GP7_%=\n"
"s_mov_b32 s76, 131072\n"
"s_branch GP9_%=\n"
"GP8_%=:\n"
"s_sleep 2\n"
"GP9_%=:\n"
"buffer_load_dword v15, v14, s[36:39], 0 offen sc1\n"
"s_sub_u32 s76, s76, 1\n"
"s_cbranch_scc1 GPABORT_%=\n"
"s_waitcnt vmcnt(0)\n"
"v_cmp_gt_u32 vcc, 1, v15\n"
"s_nop 0\n"
"s_cbranch_vccnz GP8_%=\n"
"s_mov_b32 s79, 64\n"
"s_branch GP6_%=\n"
"GP7_%=:\n"
"v_cvt_pk_bf16_f32 v128, v64, v65\n"
"v_cvt_pk_bf16_f32 v129, v66, v67\n"
"v_cvt_pk_bf16_f32 v130, v68, v69\n"
"v_cvt_pk_bf16_f32 v131, v70, v71\n"
"buffer_store_dwordx4 v[128:131], v7, s[36:39], s74 offen offset:0 sc1\n"
"v_cvt_pk_bf16_f32 v132, v72, v73\n"
"v_cvt_pk_bf16_f32 v133, v74, v75\n"
"v_cvt_pk_bf16_f32 v134, v76, v77\n"
"v_cvt_pk_bf16_f32 v135, v78, v79\n"
"buffer_store_dwordx4 v[132:135], v7, s[36:39], s74 offen offset:256 sc1\n"
"v_cvt_pk_bf16_f32 v136, v80, v81\n"
"v_cvt_pk_bf16_f32 v137, v82, v83\n"
"v_cvt_pk_bf16_f32 v138, v84, v85\n"
"v_cvt_pk_bf16_f32 v139, v86, v87\n"
"buffer_store_dwordx4 v[136:139], v7, s[36:39], s74 offen offset:2048 sc1\n"
"v_cvt_pk_bf16_f32 v140, v88, v89\n"
"v_cvt_pk_bf16_f32 v141, v90, v91\n"
"v_cvt_pk_bf16_f32 v142, v92, v93\n"
"v_cvt_pk_bf16_f32 v143, v94, v95\n"
"buffer_store_dwordx4 v[140:143], v7, s[36:39], s74 offen offset:2304 sc1\n"
"s_waitcnt vmcnt(0)\n"
"v_mov_b32 v6, s73\n"
"buffer_store_dword v6, v4, s[36:39], 0 offen sc1\n"
"s_lshr_b32 s36, s22, 13\n"
"s_waitcnt vmcnt(0) lgkmcnt(0)\n"
"s_barrier\n"
"s_branch GPEND_%=\n"
"GPABORT_%=:\n"
"s_mov_b64 exec, -1\n"
"s_nop 7\n"
"s_nop 7\n"
"v_mov_b32 v0, 46268544\n"
"v_mov_b32 v1, 1\n"
"buffer_store_dword v1, v0, s[36:39], 0 offen sc1\n"
"s_endpgm\n"
"GPEND_%=:\n"
"s_nop 7\n"
"s_nop 7\n");
            c.par = sa[0];
        } else {
            const int hh = lane >> 5;
            sa[11] = 122880 + w * 4096;
            sb[0] = O_KX + cu * 12288; sb[1] = O_KM + cu * 3072; sb[2] = O_PTX + cu * 2048; sb[3] = O_PTM + cu * 512;
            vb[0] = (2 * (r8 & 3) + hh) * 128 + c8 * 16;
            vb[1] = hh ? F_K + fw(3 * cu + lane % 3) : F_KM + cu * 128; vb[2] = hh; vb[3] = one(lane, F_PT + cu * 128);
            vb[4] = (lane & 31) - 4 * hh; vb[5] = (lane & 31) * 16 + hh * 8;
            RUN("s_nop 4\n"
"s_cmp_eq_u32 s66, 3\n"
"s_cbranch_scc1 A1_%=\n"
"v_xor_b32 v12, 64, v1\n"
"s_mov_b32 s68, s49\n"
"s_mov_b32 s69, 0\n"
"s_mov_b32 s71, s50\n"
"s_sleep 37\n"
"s_sleep 37\n"
"s_sleep 37\n"
"s_sleep 37\n"
"s_mov_b32 s22, 46301184\n"
"s_mov_b32 s23, 46317568\n"
"s_cmp_lg_u32 s66, 0\n"
"s_cbranch_scc1 A2_%=\n"
"v_mov_b32 v24, 0\n"
"s_mov_b64 exec, 1\n"
"buffer_atomic_add v24, v26, s[36:39], s23 offen sc0 sc1\n"
"s_mov_b64 exec, -1\n"
"A2_%=:\n"
"s_mul_i32 s77, s45, 0\n"
"s_add_u32 s77, s77, s44\n"
"s_add_u32 m0, s46, 65536\n"
"s_nop 0\n"
"buffer_load_dwordx4 v1, s[40:43], s77 offen lds\n"
"s_add_u32 s77, s77, s45\n"
"s_add_u32 m0, s46, 66560\n"
"s_nop 0\n"
"buffer_load_dwordx4 v12, s[40:43], s77 offen lds\n"
"s_add_u32 s77, s77, s45\n"
"s_add_u32 m0, s46, 67584\n"
"s_nop 0\n"
"buffer_load_dwordx4 v1, s[40:43], s77 offen lds\n"
"s_add_u32 s77, s77, s45\n"
"s_add_u32 m0, s46, 68608\n"
"s_nop 0\n"
"buffer_load_dwordx4 v12, s[40:43], s77 offen lds\n"
"s_add_u32 s77, s77, s45\n"
"s_add_u32 m0, s46, 69632\n"
"s_nop 0\n"
"buffer_load_dwordx4 v1, s[40:43], s77 offen lds\n"
"s_add_u32 s77, s77, s45\n"
"s_add_u32 m0, s46, 70656\n"
"s_nop 0\n"
"buffer_load_dwordx4 v12, s[40:43], s77 offen lds\n"
"s_add_u32 s77, s77, s45\n"
"s_add_u32 m0, s46, 71680\n"
"s_nop 0\n"
"buffer_load_dwordx4 v1, s[40:43], s77 offen lds\n"
"s_add_u32 s77, s77, s45\n"
"s_add_u32 m0, s46, 72704\n"
"s_nop 0\n"
"buffer_load_dwordx4 v12, s[40:43], s77 offen lds\n"
"s_add_u32 s77, s77, s45\n"
"s_mul_i32 s77, s45, 8\n"
"s_add_u32 s77, s77, s44\n"
"s_add_u32 m0, s46, 98304\n"
"s_nop 0\n"
"buffer_load_dwordx4 v1, s[40:43], s77 offen lds\n"
"s_add_u32 s77, s77, s45\n"
"s_add_u32 m0, s46, 99328\n"
"s_nop 0\n"
"buffer_load_dwordx4 v12, s[40:43], s77 offen lds\n"
"s_add_u32 s77, s77, s45\n"
"s_add_u32 m0, s46, 100352\n"
"s_nop 0\n"
"buffer_load_dwordx4 v1, s[40:43], s77 offen lds\n"
"s_add_u32 s77, s77, s45\n"
"s_add_u32 m0, s46, 101376\n"
"s_nop 0\n"
"buffer_load_dwordx4 v12, s[40:43], s77 offen lds\n"
"s_add_u32 s77, s77, s45\n"
"s_add_u32 m0, s46, 102400\n"
"s_nop 0\n"
"buffer_load_dwordx4 v1, s[40:43], s77 offen lds\n"
"s_add_u32 s77, s77, s45\n"
"s_add_u32 m0, s46, 103424\n"
"s_nop 0\n"
"buffer_load_dwordx4 v12, s[40:43], s77 offen lds\n"
"s_add_u32 s77, s77, s45\n"
"s_add_u32 m0, s46, 104448\n"
"s_nop 0\n"
"buffer_load_dwordx4 v1, s[40:43], s77 offen lds\n"
"s_add_u32 s77, s77, s45\n"
"s_add_u32 m0, s46, 105472\n"
"s_nop 0\n"
"buffer_load_dwordx4 v12, s[40:43], s77 offen lds\n"
"s_add_u32 s77, s77, s45\n"
"s_waitcnt vmcnt(8)\n"
"s_waitcnt lgkmcnt(0)\n"
"s_cmp_lg_u32 s66, 0\n"
"s_cbranch_scc1 A3_%=\n"
"v_or_b32 v24, 1, v24\n"
"v_add_u32 v24, 1, v24\n"
"buffer_store_dword v24, v26, s[36:39], s22 offen sc1\n"
"A3_%=:\n"
"s_mul_i32 s77, s45, 16\n"
"s_add_u32 s77, s77, s44\n"
"s_add_u32 m0, s46, 131072\n"
"s_nop 0\n"
"buffer_load_dwordx4 v1, s[40:43], s77 offen lds\n"
"s_add_u32 s77, s77, s45\n"
"s_add_u32 m0, s46, 132096\n"
"s_nop 0\n"
"buffer_load_dwordx4 v12, s[40:43], s77 offen lds\n"
"s_add_u32 s77, s77, s45\n"
"s_add_u32 m0, s46, 133120\n"
"s_nop 0\n"
"buffer_load_dwordx4 v1, s[40:43], s77 offen lds\n"
"s_add_u32 s77, s77, s45\n"
"s_add_u32 m0, s46, 134144\n"
"s_nop 0\n"
"buffer_load_dwordx4 v12, s[40:43], s77 offen lds\n"
"s_add_u32 s77, s77, s45\n"
"s_add_u32 m0, s46, 135168\n"
"s_nop 0\n"
"buffer_load_dwordx4 v1, s[40:43], s77 offen lds\n"
"s_add_u32 s77, s77, s45\n"
"s_add_u32 m0, s46, 136192\n"
"s_nop 0\n"
"buffer_load_dwordx4 v12, s[40:43], s77 offen lds\n"
"s_add_u32 s77, s77, s45\n"
"s_add_u32 m0, s46, 137216\n"
"s_nop 0\n"
"buffer_load_dwordx4 v1, s[40:43], s77 offen lds\n"
"s_add_u32 s77, s77, s45\n"
"s_add_u32 m0, s46, 138240\n"
"s_nop 0\n"
"buffer_load_dwordx4 v12, s[40:43], s77 offen lds\n"
"s_add_u32 s77, s77, s45\n"
"v_add_u32 v8, 65536, v2\n"
"v_add_u32 v10, 65536, v3\n"
"v_xor_b32 v9, 16, v8\n"
"v_xor_b32 v11, 16, v10\n"
"ds_read_b64_tr_b16 v[192:193], v8 offset:0\n"
"ds_read_b64_tr_b16 v[194:195], v8 offset:512\n"
"ds_read_b64_tr_b16 v[196:197], v9 offset:0\n"
"ds_read_b64_tr_b16 v[198:199], v9 offset:512\n"
"ds_read_b64_tr_b16 v[200:201], v10 offset:0\n"
"ds_read_b64_tr_b16 v[202:203], v10 offset:512\n"
"ds_read_b64_tr_b16 v[204:205], v11 offset:0\n"
"ds_read_b64_tr_b16 v[206:207], v11 offset:512\n"
"ds_read_b64_tr_b16 v[208:209], v8 offset:4096\n"
"ds_read_b64_tr_b16 v[210:211], v8 offset:4608\n"
"ds_read_b64_tr_b16 v[212:213], v9 offset:4096\n"
"ds_read_b64_tr_b16 v[214:215], v9 offset:4608\n"
"ds_read_b64_tr_b16 v[216:217], v10 offset:4096\n"
"ds_read_b64_tr_b16 v[218:219], v10 offset:4608\n"
"ds_read_b64_tr_b16 v[220:221], v11 offset:4096\n"
"ds_read_b64_tr_b16 v[222:223], v11 offset:4608\n"
"s_waitcnt vmcnt(8)\n"
"s_waitcnt lgkmcnt(0)\n"
"s_mul_i32 s77, s45, 24\n"
"s_add_u32 s77, s77, s44\n"
"s_add_u32 m0, s46, 65536\n"
"s_nop 0\n"
"buffer_load_dwordx4 v1, s[40:43], s77 offen lds\n"
"s_add_u32 s77, s77, s45\n"
"s_add_u32 m0, s46, 66560\n"
"s_nop 0\n"
"buffer_load_dwordx4 v12, s[40:43], s77 offen lds\n"
"s_add_u32 s77, s77, s45\n"
"s_add_u32 m0, s46, 67584\n"
"s_nop 0\n"
"buffer_load_dwordx4 v1, s[40:43], s77 offen lds\n"
"s_add_u32 s77, s77, s45\n"
"s_add_u32 m0, s46, 68608\n"
"s_nop 0\n"
"buffer_load_dwordx4 v12, s[40:43], s77 offen lds\n"
"s_add_u32 s77, s77, s45\n"
"s_add_u32 m0, s46, 69632\n"
"s_nop 0\n"
"buffer_load_dwordx4 v1, s[40:43], s77 offen lds\n"
"s_add_u32 s77, s77, s45\n"
"s_add_u32 m0, s46, 70656\n"
"s_nop 0\n"
"buffer_load_dwordx4 v12, s[40:43], s77 offen lds\n"
"s_add_u32 s77, s77, s45\n"
"s_add_u32 m0, s46, 71680\n"
"s_nop 0\n"
"buffer_load_dwordx4 v1, s[40:43], s77 offen lds\n"
"s_add_u32 s77, s77, s45\n"
"s_add_u32 m0, s46, 72704\n"
"s_nop 0\n"
"buffer_load_dwordx4 v12, s[40:43], s77 offen lds\n"
"s_add_u32 s77, s77, s45\n"
"v_add_u32 v8, 98304, v2\n"
"v_add_u32 v10, 98304, v3\n"
"v_xor_b32 v9, 16, v8\n"
"v_xor_b32 v11, 16, v10\n"
"ds_read_b64_tr_b16 v[224:225], v8 offset:0\n"
"ds_read_b64_tr_b16 v[226:227], v8 offset:512\n"
"ds_read_b64_tr_b16 v[228:229], v9 offset:0\n"
"ds_read_b64_tr_b16 v[230:231], v9 offset:512\n"
"ds_read_b64_tr_b16 v[232:233], v10 offset:0\n"
"ds_read_b64_tr_b16 v[234:235], v10 offset:512\n"
"ds_read_b64_tr_b16 v[236:237], v11 offset:0\n"
"ds_read_b64_tr_b16 v[238:239], v11 offset:512\n"
"ds_read_b64_tr_b16 v[240:241], v8 offset:4096\n"
"ds_read_b64_tr_b16 v[242:243], v8 offset:4608\n"
"ds_read_b64_tr_b16 v[244:245], v9 offset:4096\n"
"ds_read_b64_tr_b16 v[246:247], v9 offset:4608\n"
"ds_read_b64_tr_b16 v[248:249], v10 offset:4096\n"
"ds_read_b64_tr_b16 v[250:251], v10 offset:4608\n"
"ds_read_b64_tr_b16 v[252:253], v11 offset:4096\n"
"ds_read_b64_tr_b16 v[254:255], v11 offset:4608\n"
"s_waitcnt vmcnt(8)\n"
"s_waitcnt lgkmcnt(0)\n"
"s_mul_i32 s77, s45, 32\n"
"s_add_u32 s77, s77, s44\n"
"s_add_u32 m0, s46, 98304\n"
"s_nop 0\n"
"buffer_load_dwordx4 v1, s[40:43], s77 offen lds\n"
"s_add_u32 s77, s77, s45\n"
"s_add_u32 m0, s46, 99328\n"
"s_nop 0\n"
"buffer_load_dwordx4 v12, s[40:43], s77 offen lds\n"
"s_add_u32 s77, s77, s45\n"
"s_add_u32 m0, s46, 100352\n"
"s_nop 0\n"
"buffer_load_dwordx4 v1, s[40:43], s77 offen lds\n"
"s_add_u32 s77, s77, s45\n"
"s_add_u32 m0, s46, 101376\n"
"s_nop 0\n"
"buffer_load_dwordx4 v12, s[40:43], s77 offen lds\n"
"s_add_u32 s77, s77, s45\n"
"s_add_u32 m0, s46, 102400\n"
"s_nop 0\n"
"buffer_load_dwordx4 v1, s[40:43], s77 offen lds\n"
"s_add_u32 s77, s77, s45\n"
"s_add_u32 m0, s46, 103424\n"
"s_nop 0\n"
"buffer_load_dwordx4 v12, s[40:43], s77 offen lds\n"
"s_add_u32 s77, s77, s45\n"
"s_add_u32 m0, s46, 104448\n"
"s_nop 0\n"
"buffer_load_dwordx4 v1, s[40:43], s77 offen lds\n"
"s_add_u32 s77, s77, s45\n"
"s_add_u32 m0, s46, 105472\n"
"s_nop 0\n"
"buffer_load_dwordx4 v12, s[40:43], s77 offen lds\n"
"s_add_u32 s77, s77, s45\n"
"v_add_u32 v8, 131072, v2\n"
"v_add_u32 v10, 131072, v3\n"
"v_xor_b32 v9, 16, v8\n"
"v_xor_b32 v11, 16, v10\n"
"ds_read_b64_tr_b16 a[0:1], v8 offset:0\n"
"ds_read_b64_tr_b16 a[2:3], v8 offset:512\n"
"ds_read_b64_tr_b16 a[4:5], v9 offset:0\n"
"ds_read_b64_tr_b16 a[6:7], v9 offset:512\n"
"ds_read_b64_tr_b16 a[8:9], v10 offset:0\n"
"ds_read_b64_tr_b16 a[10:11], v10 offset:512\n"
"ds_read_b64_tr_b16 a[12:13], v11 offset:0\n"
"ds_read_b64_tr_b16 a[14:15], v11 offset:512\n"
"ds_read_b64_tr_b16 a[16:17], v8 offset:4096\n"
"ds_read_b64_tr_b16 a[18:19], v8 offset:4608\n"
"ds_read_b64_tr_b16 a[20:21], v9 offset:4096\n"
"ds_read_b64_tr_b16 a[22:23], v9 offset:4608\n"
"ds_read_b64_tr_b16 a[24:25], v10 offset:4096\n"
"ds_read_b64_tr_b16 a[26:27], v10 offset:4608\n"
"ds_read_b64_tr_b16 a[28:29], v11 offset:4096\n"
"ds_read_b64_tr_b16 a[30:31], v11 offset:4608\n"
"s_waitcnt vmcnt(8)\n"
"s_waitcnt lgkmcnt(0)\n"
"s_mul_i32 s77, s45, 40\n"
"s_add_u32 s77, s77, s44\n"
"s_add_u32 m0, s46, 131072\n"
"s_nop 0\n"
"buffer_load_dwordx4 v1, s[40:43], s77 offen lds\n"
"s_add_u32 s77, s77, s45\n"
"s_add_u32 m0, s46, 132096\n"
"s_nop 0\n"
"buffer_load_dwordx4 v12, s[40:43], s77 offen lds\n"
"s_add_u32 s77, s77, s45\n"
"s_add_u32 m0, s46, 133120\n"
"s_nop 0\n"
"buffer_load_dwordx4 v1, s[40:43], s77 offen lds\n"
"s_add_u32 s77, s77, s45\n"
"s_add_u32 m0, s46, 134144\n"
"s_nop 0\n"
"buffer_load_dwordx4 v12, s[40:43], s77 offen lds\n"
"s_add_u32 s77, s77, s45\n"
"s_add_u32 m0, s46, 135168\n"
"s_nop 0\n"
"buffer_load_dwordx4 v1, s[40:43], s77 offen lds\n"
"s_add_u32 s77, s77, s45\n"
"s_add_u32 m0, s46, 136192\n"
"s_nop 0\n"
"buffer_load_dwordx4 v12, s[40:43], s77 offen lds\n"
"s_add_u32 s77, s77, s45\n"
"s_add_u32 m0, s46, 137216\n"
"s_nop 0\n"
"buffer_load_dwordx4 v1, s[40:43], s77 offen lds\n"
"s_add_u32 s77, s77, s45\n"
"s_add_u32 m0, s46, 138240\n"
"s_nop 0\n"
"buffer_load_dwordx4 v12, s[40:43], s77 offen lds\n"
"s_add_u32 s77, s77, s45\n"
"v_add_u32 v8, 65536, v2\n"
"v_add_u32 v10, 65536, v3\n"
"v_xor_b32 v9, 16, v8\n"
"v_xor_b32 v11, 16, v10\n"
"ds_read_b64_tr_b16 a[32:33], v8 offset:0\n"
"ds_read_b64_tr_b16 a[34:35], v8 offset:512\n"
"ds_read_b64_tr_b16 a[36:37], v9 offset:0\n"
"ds_read_b64_tr_b16 a[38:39], v9 offset:512\n"
"ds_read_b64_tr_b16 a[40:41], v10 offset:0\n"
"ds_read_b64_tr_b16 a[42:43], v10 offset:512\n"
"ds_read_b64_tr_b16 a[44:45], v11 offset:0\n"
"ds_read_b64_tr_b16 a[46:47], v11 offset:512\n"
"ds_read_b64_tr_b16 a[48:49], v8 offset:4096\n"
"ds_read_b64_tr_b16 a[50:51], v8 offset:4608\n"
"ds_read_b64_tr_b16 a[52:53], v9 offset:4096\n"
"ds_read_b64_tr_b16 a[54:55], v9 offset:4608\n"
"ds_read_b64_tr_b16 a[56:57], v10 offset:4096\n"
"ds_read_b64_tr_b16 a[58:59], v10 offset:4608\n"
"ds_read_b64_tr_b16 a[60:61], v11 offset:4096\n"
"ds_read_b64_tr_b16 a[62:63], v11 offset:4608\n"
"s_waitcnt vmcnt(8)\n"
"s_waitcnt lgkmcnt(0)\n"
"s_mul_i32 s77, s45, 48\n"
"s_add_u32 s77, s77, s44\n"
"s_add_u32 m0, s46, 65536\n"
"s_nop 0\n"
"buffer_load_dwordx4 v1, s[40:43], s77 offen lds\n"
"s_add_u32 s77, s77, s45\n"
"s_add_u32 m0, s46, 66560\n"
"s_nop 0\n"
"buffer_load_dwordx4 v12, s[40:43], s77 offen lds\n"
"s_add_u32 s77, s77, s45\n"
"s_add_u32 m0, s46, 67584\n"
"s_nop 0\n"
"buffer_load_dwordx4 v1, s[40:43], s77 offen lds\n"
"s_add_u32 s77, s77, s45\n"
"s_add_u32 m0, s46, 68608\n"
"s_nop 0\n"
"buffer_load_dwordx4 v12, s[40:43], s77 offen lds\n"
"s_add_u32 s77, s77, s45\n"
"s_add_u32 m0, s46, 69632\n"
"s_nop 0\n"
"buffer_load_dwordx4 v1, s[40:43], s77 offen lds\n"
"s_add_u32 s77, s77, s45\n"
"s_add_u32 m0, s46, 70656\n"
"s_nop 0\n"
"buffer_load_dwordx4 v12, s[40:43], s77 offen lds\n"
"s_add_u32 s77, s77, s45\n"
"s_add_u32 m0, s46, 71680\n"
"s_nop 0\n"
"buffer_load_dwordx4 v1, s[40:43], s77 offen lds\n"
"s_add_u32 s77, s77, s45\n"
"s_add_u32 m0, s46, 72704\n"
"s_nop 0\n"
"buffer_load_dwordx4 v12, s[40:43], s77 offen lds\n"
"s_add_u32 s77, s77, s45\n"
"v_add_u32 v8, 98304, v2\n"
"v_add_u32 v10, 98304, v3\n"
"v_xor_b32 v9, 16, v8\n"
"v_xor_b32 v11, 16, v10\n"
"ds_read_b64_tr_b16 a[64:65], v8 offset:0\n"
"ds_read_b64_tr_b16 a[66:67], v8 offset:512\n"
"ds_read_b64_tr_b16 a[68:69], v9 offset:0\n"
"ds_read_b64_tr_b16 a[70:71], v9 offset:512\n"
"ds_read_b64_tr_b16 a[72:73], v10 offset:0\n"
"ds_read_b64_tr_b16 a[74:75], v10 offset:512\n"
"ds_read_b64_tr_b16 a[76:77], v11 offset:0\n"
"ds_read_b64_tr_b16 a[78:79], v11 offset:512\n"
"ds_read_b64_tr_b16 a[80:81], v8 offset:4096\n"
"ds_read_b64_tr_b16 a[82:83], v8 offset:4608\n"
"ds_read_b64_tr_b16 a[84:85], v9 offset:4096\n"
"ds_read_b64_tr_b16 a[86:87], v9 offset:4608\n"
"ds_read_b64_tr_b16 a[88:89], v10 offset:4096\n"
"ds_read_b64_tr_b16 a[90:91], v10 offset:4608\n"
"ds_read_b64_tr_b16 a[92:93], v11 offset:4096\n"
"ds_read_b64_tr_b16 a[94:95], v11 offset:4608\n"
"s_waitcnt vmcnt(8)\n"
"s_waitcnt lgkmcnt(0)\n"
"s_mul_i32 s77, s45, 56\n"
"s_add_u32 s77, s77, s44\n"
"s_add_u32 m0, s46, 98304\n"
"s_nop 0\n"
"buffer_load_dwordx4 v1, s[40:43], s77 offen lds\n"
"s_add_u32 s77, s77, s45\n"
"s_add_u32 m0, s46, 99328\n"
"s_nop 0\n"
"buffer_load_dwordx4 v12, s[40:43], s77 offen lds\n"
"s_add_u32 s77, s77, s45\n"
"s_add_u32 m0, s46, 100352\n"
"s_nop 0\n"
"buffer_load_dwordx4 v1, s[40:43], s77 offen lds\n"
"s_add_u32 s77, s77, s45\n"
"s_add_u32 m0, s46, 101376\n"
"s_nop 0\n"
"buffer_load_dwordx4 v12, s[40:43], s77 offen lds\n"
"s_add_u32 s77, s77, s45\n"
"s_add_u32 m0, s46, 102400\n"
"s_nop 0\n"
"buffer_load_dwordx4 v1, s[40:43], s77 offen lds\n"
"s_add_u32 s77, s77, s45\n"
"s_add_u32 m0, s46, 103424\n"
"s_nop 0\n"
"buffer_load_dwordx4 v12, s[40:43], s77 offen lds\n"
"s_add_u32 s77, s77, s45\n"
"s_add_u32 m0, s46, 104448\n"
"s_nop 0\n"
"buffer_load_dwordx4 v1, s[40:43], s77 offen lds\n"
"s_add_u32 s77, s77, s45\n"
"s_add_u32 m0, s46, 105472\n"
"s_nop 0\n"
"buffer_load_dwordx4 v12, s[40:43], s77 offen lds\n"
"s_add_u32 s77, s77, s45\n"
"v_add_u32 v8, 131072, v2\n"
"v_add_u32 v10, 131072, v3\n"
"v_xor_b32 v9, 16, v8\n"
"v_xor_b32 v11, 16, v10\n"
"ds_read_b64_tr_b16 a[96:97], v8 offset:0\n"
"ds_read_b64_tr_b16 a[98:99], v8 offset:512\n"
"ds_read_b64_tr_b16 a[100:101], v9 offset:0\n"
"ds_read_b64_tr_b16 a[102:103], v9 offset:512\n"
"ds_read_b64_tr_b16 a[104:105], v10 offset:0\n"
"ds_read_b64_tr_b16 a[106:107], v10 offset:512\n"
"ds_read_b64_tr_b16 a[108:109], v11 offset:0\n"
"ds_read_b64_tr_b16 a[110:111], v11 offset:512\n"
"ds_read_b64_tr_b16 a[112:113], v8 offset:4096\n"
"ds_read_b64_tr_b16 a[114:115], v8 offset:4608\n"
"ds_read_b64_tr_b16 a[116:117], v9 offset:4096\n"
"ds_read_b64_tr_b16 a[118:119], v9 offset:4608\n"
"ds_read_b64_tr_b16 a[120:121], v10 offset:4096\n"
"ds_read_b64_tr_b16 a[122:123], v10 offset:4608\n"
"ds_read_b64_tr_b16 a[124:125], v11 offset:4096\n"
"ds_read_b64_tr_b16 a[126:127], v11 offset:4608\n"
"s_waitcnt vmcnt(8)\n"
"s_waitcnt lgkmcnt(0)\n"
"s_mul_i32 s77, s45, 64\n"
"s_add_u32 s77, s77, s44\n"
"s_add_u32 m0, s46, 131072\n"
"s_nop 0\n"
"buffer_load_dwordx4 v1, s[40:43], s77 offen lds\n"
"s_add_u32 s77, s77, s45\n"
"s_add_u32 m0, s46, 132096\n"
"s_nop 0\n"
"buffer_load_dwordx4 v12, s[40:43], s77 offen lds\n"
"s_add_u32 s77, s77, s45\n"
"s_add_u32 m0, s46, 133120\n"
"s_nop 0\n"
"buffer_load_dwordx4 v1, s[40:43], s77 offen lds\n"
"s_add_u32 s77, s77, s45\n"
"s_add_u32 m0, s46, 134144\n"
"s_nop 0\n"
"buffer_load_dwordx4 v12, s[40:43], s77 offen lds\n"
"s_add_u32 s77, s77, s45\n"
"s_add_u32 m0, s46, 135168\n"
"s_nop 0\n"
"buffer_load_dwordx4 v1, s[40:43], s77 offen lds\n"
"s_add_u32 s77, s77, s45\n"
"s_add_u32 m0, s46, 136192\n"
"s_nop 0\n"
"buffer_load_dwordx4 v12, s[40:43], s77 offen lds\n"
"s_add_u32 s77, s77, s45\n"
"s_add_u32 m0, s46, 137216\n"
"s_nop 0\n"
"buffer_load_dwordx4 v1, s[40:43], s77 offen lds\n"
"s_add_u32 s77, s77, s45\n"
"s_add_u32 m0, s46, 138240\n"
"s_nop 0\n"
"buffer_load_dwordx4 v12, s[40:43], s77 offen lds\n"
"s_add_u32 s77, s77, s45\n"
"v_add_u32 v8, 65536, v2\n"
"v_add_u32 v10, 65536, v3\n"
"v_xor_b32 v9, 16, v8\n"
"v_xor_b32 v11, 16, v10\n"
"ds_read_b64_tr_b16 a[128:129], v8 offset:0\n"
"ds_read_b64_tr_b16 a[130:131], v8 offset:512\n"
"ds_read_b64_tr_b16 a[132:133], v9 offset:0\n"
"ds_read_b64_tr_b16 a[134:135], v9 offset:512\n"
"ds_read_b64_tr_b16 a[136:137], v10 offset:0\n"
"ds_read_b64_tr_b16 a[138:139], v10 offset:512\n"
"ds_read_b64_tr_b16 a[140:141], v11 offset:0\n"
"ds_read_b64_tr_b16 a[142:143], v11 offset:512\n"
"ds_read_b64_tr_b16 a[144:145], v8 offset:4096\n"
"ds_read_b64_tr_b16 a[146:147], v8 offset:4608\n"
"ds_read_b64_tr_b16 a[148:149], v9 offset:4096\n"
"ds_read_b64_tr_b16 a[150:151], v9 offset:4608\n"
"ds_read_b64_tr_b16 a[152:153], v10 offset:4096\n"
"ds_read_b64_tr_b16 a[154:155], v10 offset:4608\n"
"ds_read_b64_tr_b16 a[156:157], v11 offset:4096\n"
"ds_read_b64_tr_b16 a[158:159], v11 offset:4608\n"
"s_waitcnt vmcnt(8)\n"
"s_waitcnt lgkmcnt(0)\n"
"s_mul_i32 s77, s45, 72\n"
"s_add_u32 s77, s77, s44\n"
"s_add_u32 m0, s46, 65536\n"
"s_nop 0\n"
"buffer_load_dwordx4 v1, s[40:43], s77 offen lds\n"
"s_add_u32 s77, s77, s45\n"
"s_add_u32 m0, s46, 66560\n"
"s_nop 0\n"
"buffer_load_dwordx4 v12, s[40:43], s77 offen lds\n"
"s_add_u32 s77, s77, s45\n"
"s_add_u32 m0, s46, 67584\n"
"s_nop 0\n"
"buffer_load_dwordx4 v1, s[40:43], s77 offen lds\n"
"s_add_u32 s77, s77, s45\n"
"s_add_u32 m0, s46, 68608\n"
"s_nop 0\n"
"buffer_load_dwordx4 v12, s[40:43], s77 offen lds\n"
"s_add_u32 s77, s77, s45\n"
"s_add_u32 m0, s46, 69632\n"
"s_nop 0\n"
"buffer_load_dwordx4 v1, s[40:43], s77 offen lds\n"
"s_add_u32 s77, s77, s45\n"
"s_add_u32 m0, s46, 70656\n"
"s_nop 0\n"
"buffer_load_dwordx4 v12, s[40:43], s77 offen lds\n"
"s_add_u32 s77, s77, s45\n"
"s_add_u32 m0, s46, 71680\n"
"s_nop 0\n"
"buffer_load_dwordx4 v1, s[40:43], s77 offen lds\n"
"s_add_u32 s77, s77, s45\n"
"s_add_u32 m0, s46, 72704\n"
"s_nop 0\n"
"buffer_load_dwordx4 v12, s[40:43], s77 offen lds\n"
"s_add_u32 s77, s77, s45\n"
"v_add_u32 v8, 98304, v2\n"
"v_add_u32 v10, 98304, v3\n"
"v_xor_b32 v9, 16, v8\n"
"v_xor_b32 v11, 16, v10\n"
"ds_read_b64_tr_b16 a[160:161], v8 offset:0\n"
"ds_read_b64_tr_b16 a[162:163], v8 offset:512\n"
"ds_read_b64_tr_b16 a[164:165], v9 offset:0\n"
"ds_read_b64_tr_b16 a[166:167], v9 offset:512\n"
"ds_read_b64_tr_b16 a[168:169], v10 offset:0\n"
"ds_read_b64_tr_b16 a[170:171], v10 offset:512\n"
"ds_read_b64_tr_b16 a[172:173], v11 offset:0\n"
"ds_read_b64_tr_b16 a[174:175], v11 offset:512\n"
"ds_read_b64_tr_b16 a[176:177], v8 offset:4096\n"
"ds_read_b64_tr_b16 a[178:179], v8 offset:4608\n"
"ds_read_b64_tr_b16 a[180:181], v9 offset:4096\n"
"ds_read_b64_tr_b16 a[182:183], v9 offset:4608\n"
"ds_read_b64_tr_b16 a[184:185], v10 offset:4096\n"
"ds_read_b64_tr_b16 a[186:187], v10 offset:4608\n"
"ds_read_b64_tr_b16 a[188:189], v11 offset:4096\n"
"ds_read_b64_tr_b16 a[190:191], v11 offset:4608\n"
"s_cmp_lg_u32 s66, 0\n"
"s_cbranch_scc1 A4_%=\n"
"s_mov_b32 s76, 131072\n"
"s_mov_b64 exec, 1\n"
"s_branch A6_%=\n"
"A5_%=:\n"
"s_sleep 4\n"
"A6_%=:\n"
"v_mov_b32 v29, 0\n"
"buffer_atomic_add v29, v26, s[36:39], s23 offen sc0 sc1\n"
"s_sub_u32 s76, s76, 1\n"
"s_cbranch_scc1 AABORT_%=\n"
"s_waitcnt vmcnt(0)\n"
"v_xor_b32 v29, v29, v24\n"
"v_cmp_lt_u32 vcc, 1, v29\n"
"s_nop 0\n"
"s_cbranch_vccnz A5_%=\n"
"v_mov_b32 v28, 61440\n"
"ds_write_b32 v28, v29\n"
"v_mov_b32 v24, 1\n"
"s_add_u32 s22, s23, 16384\n"
"buffer_store_dword v24, v26, s[36:39], s22 offen sc1\n"
"s_mov_b64 exec, -1\n"
"A4_%=:\n"
"s_waitcnt lgkmcnt(0)\n"
"s_barrier\n"
"s_add_u32 m0, s71, 0\n"
"s_add_u32 s72, s68, 0\n"
"buffer_load_dwordx4 v0, s[36:39], s72 offen offset:0 sc1 lds\n"
"buffer_load_dwordx4 v0, s[36:39], s72 offen offset:1024 sc1 lds\n"
"buffer_load_dwordx4 v0, s[36:39], s72 offen offset:2048 sc1 lds\n"
"buffer_load_dwordx4 v0, s[36:39], s72 offen offset:3072 sc1 lds\n"
"s_add_u32 m0, s71, 4096\n"
"s_add_u32 s72, s68, 4096\n"
"buffer_load_dwordx4 v0, s[36:39], s72 offen offset:0 sc1 lds\n"
"buffer_load_dwordx4 v0, s[36:39], s72 offen offset:1024 sc1 lds\n"
"buffer_load_dwordx4 v0, s[36:39], s72 offen offset:2048 sc1 lds\n"
"buffer_load_dwordx4 v0, s[36:39], s72 offen offset:3072 sc1 lds\n"
"s_add_u32 m0, s71, 8192\n"
"s_add_u32 s72, s68, 8192\n"
"buffer_load_dwordx4 v0, s[36:39], s72 offen offset:0 sc1 lds\n"
"buffer_load_dwordx4 v0, s[36:39], s72 offen offset:1024 sc1 lds\n"
"buffer_load_dwordx4 v0, s[36:39], s72 offen offset:2048 sc1 lds\n"
"buffer_load_dwordx4 v0, s[36:39], s72 offen offset:3072 sc1 lds\n"
"s_add_u32 m0, s71, 12288\n"
"s_add_u32 s72, s68, 12288\n"
"buffer_load_dwordx4 v0, s[36:39], s72 offen offset:0 sc1 lds\n"
"buffer_load_dwordx4 v0, s[36:39], s72 offen offset:1024 sc1 lds\n"
"s_add_u32 s68, s68, 40960\n"
"s_add_u32 s69, s69, 40960\n"
"s_cmp_eq_u32 s69, 122880\n"
"s_cselect_b32 s69, 0, s69\n"
"s_add_u32 s71, s69, s50\n"
"v_mov_b32 v28, 61440\n"
"ds_read_b32 v28, v28\n"
"s_waitcnt lgkmcnt(0)\n"
"v_readfirstlane_b32 s22, v28\n"
"s_nop 0\n"
"s_lshl_b32 s22, s22, 13\n"
"v_xor_b32 v4, s22, v4\n"
"s_waitcnt vmcnt(8)\n"
"s_waitcnt lgkmcnt(0)\n"
"v_add_u32 v8, 131072, v2\n"
"v_add_u32 v10, 131072, v3\n"
"v_xor_b32 v9, 16, v8\n"
"v_xor_b32 v11, 16, v10\n"
"ds_read_b64_tr_b16 a[192:193], v8 offset:0\n"
"ds_read_b64_tr_b16 a[194:195], v8 offset:512\n"
"ds_read_b64_tr_b16 a[196:197], v9 offset:0\n"
"ds_read_b64_tr_b16 a[198:199], v9 offset:512\n"
"ds_read_b64_tr_b16 a[200:201], v10 offset:0\n"
"ds_read_b64_tr_b16 a[202:203], v10 offset:512\n"
"ds_read_b64_tr_b16 a[204:205], v11 offset:0\n"
"ds_read_b64_tr_b16 a[206:207], v11 offset:512\n"
"ds_read_b64_tr_b16 a[208:209], v8 offset:4096\n"
"ds_read_b64_tr_b16 a[210:211], v8 offset:4608\n"
"ds_read_b64_tr_b16 a[212:213], v9 offset:4096\n"
"ds_read_b64_tr_b16 a[214:215], v9 offset:4608\n"
"ds_read_b64_tr_b16 a[216:217], v10 offset:4096\n"
"ds_read_b64_tr_b16 a[218:219], v10 offset:4608\n"
"ds_read_b64_tr_b16 a[220:221], v11 offset:4096\n"
"ds_read_b64_tr_b16 a[222:223], v11 offset:4608\n"
"s_waitcnt vmcnt(0)\n"
"s_waitcnt lgkmcnt(0)\n"
"v_add_u32 v8, 65536, v2\n"
"v_add_u32 v10, 65536, v3\n"
"v_xor_b32 v9, 16, v8\n"
"v_xor_b32 v11, 16, v10\n"
"ds_read_b64_tr_b16 a[224:225], v8 offset:0\n"
"ds_read_b64_tr_b16 a[226:227], v8 offset:512\n"
"ds_read_b64_tr_b16 a[228:229], v9 offset:0\n"
"ds_read_b64_tr_b16 a[230:231], v9 offset:512\n"
"ds_read_b64_tr_b16 a[232:233], v10 offset:0\n"
"ds_read_b64_tr_b16 a[234:235], v10 offset:512\n"
"ds_read_b64_tr_b16 a[236:237], v11 offset:0\n"
"ds_read_b64_tr_b16 a[238:239], v11 offset:512\n"
"ds_read_b64_tr_b16 a[240:241], v8 offset:4096\n"
"ds_read_b64_tr_b16 a[242:243], v8 offset:4608\n"
"ds_read_b64_tr_b16 a[244:245], v9 offset:4096\n"
"ds_read_b64_tr_b16 a[246:247], v9 offset:4608\n"
"ds_read_b64_tr_b16 a[248:249], v10 offset:4096\n"
"ds_read_b64_tr_b16 a[250:251], v10 offset:4608\n"
"ds_read_b64_tr_b16 a[252:253], v11 offset:4096\n"
"ds_read_b64_tr_b16 a[254:255], v11 offset:4608\n"
"s_waitcnt lgkmcnt(0)\n"
"s_barrier\n"
"s_add_u32 m0, s71, 0\n"
"s_add_u32 s72, s68, 0\n"
"buffer_load_dwordx4 v0, s[36:39], s72 offen offset:0 sc1 lds\n"
"buffer_load_dwordx4 v0, s[36:39], s72 offen offset:1024 sc1 lds\n"
"buffer_load_dwordx4 v0, s[36:39], s72 offen offset:2048 sc1 lds\n"
"buffer_load_dwordx4 v0, s[36:39], s72 offen offset:3072 sc1 lds\n"
"s_add_u32 m0, s71, 4096\n"
"s_add_u32 s72, s68, 4096\n"
"buffer_load_dwordx4 v0, s[36:39], s72 offen offset:0 sc1 lds\n"
"buffer_load_dwordx4 v0, s[36:39], s72 offen offset:1024 sc1 lds\n"
"buffer_load_dwordx4 v0, s[36:39], s72 offen offset:2048 sc1 lds\n"
"buffer_load_dwordx4 v0, s[36:39], s72 offen offset:3072 sc1 lds\n"
"s_add_u32 m0, s71, 8192\n"
"s_add_u32 s72, s68, 8192\n"
"buffer_load_dwordx4 v0, s[36:39], s72 offen offset:0 sc1 lds\n"
"buffer_load_dwordx4 v0, s[36:39], s72 offen offset:1024 sc1 lds\n"
"buffer_load_dwordx4 v0, s[36:39], s72 offen offset:2048 sc1 lds\n"
"buffer_load_dwordx4 v0, s[36:39], s72 offen offset:3072 sc1 lds\n"
"s_add_u32 m0, s71, 12288\n"
"s_add_u32 s72, s68, 12288\n"
"buffer_load_dwordx4 v0, s[36:39], s72 offen offset:0 sc1 lds\n"
"buffer_load_dwordx4 v0, s[36:39], s72 offen offset:1024 sc1 lds\n"
"s_add_u32 s68, s68, 40960\n"
"s_add_u32 s69, s69, 40960\n"
"s_cmp_eq_u32 s69, 122880\n"
"s_cselect_b32 s69, 0, s69\n"
"s_add_u32 s71, s69, s50\n"
"s_mov_b32 s70, 0\n"
"s_mov_b32 s73, 0\n"
"s_mov_b32 s74, 234881024\n"
"v_mov_b32 v5, v7\n"
"v_add_u32 v13, s47, v7\n"
"ds_read_b128 v[96:99], v5 offset:0\n"
"ds_read_b128 v[100:103], v5 offset:256\n"
"ds_read_b128 v[104:107], v5 offset:2048\n"
"ds_read_b128 v[108:111], v5 offset:2304\n"
"ds_read_b128 v[112:115], v5 offset:4096\n"
"ds_read_b128 v[116:119], v5 offset:4352\n"
"s_mov_b32 s75, 0xbfb8aa3b\n"
"s_mov_b32 s79, 12\n"
"s_nop 0\n"
"A7_%=:\n"
"s_waitcnt lgkmcnt(5)\n"
"v_mfma_f32_16x16x32_bf16 v[32:35], v[192:195], v[96:99], 0\n"
"v_mfma_f32_16x16x32_bf16 v[36:39], v[196:199], v[96:99], 0\n"
"v_mfma_f32_16x16x32_bf16 v[48:51], v[200:203], v[96:99], 0\n"
"ds_read_b128 v[120:123], v5 offset:6144\n"
"v_mfma_f32_16x16x32_bf16 v[52:55], v[204:207], v[96:99], 0\n"
"s_waitcnt lgkmcnt(5)\n"
"v_mfma_f32_16x16x32_bf16 v[40:43], v[192:195], v[100:103], 0\n"
"v_mfma_f32_16x16x32_bf16 v[44:47], v[196:199], v[100:103], 0\n"
"v_mfma_f32_16x16x32_bf16 v[56:59], v[200:203], v[100:103], 0\n"
"ds_read_b128 v[124:127], v5 offset:6400\n"
"v_mfma_f32_16x16x32_bf16 v[60:63], v[204:207], v[100:103], 0\n"
"s_waitcnt lgkmcnt(5)\n"
"v_mfma_f32_16x16x32_bf16 v[32:35], v[208:211], v[104:107], v[32:35]\n"
"v_cvt_pk_bf16_f32 v128, v64, v65\n"
"v_mfma_f32_16x16x32_bf16 v[36:39], v[212:215], v[104:107], v[36:39]\n"
"v_cvt_pk_bf16_f32 v129, v66, v67\n"
"v_mfma_f32_16x16x32_bf16 v[48:51], v[216:219], v[104:107], v[48:51]\n"
"ds_read_b128 v[96:99], v5 offset:8192\n"
"v_mfma_f32_16x16x32_bf16 v[52:55], v[220:223], v[104:107], v[52:55]\n"
"v_cvt_pk_bf16_f32 v130, v68, v69\n"
"s_waitcnt lgkmcnt(5)\n"
"v_mfma_f32_16x16x32_bf16 v[40:43], v[208:211], v[108:111], v[40:43]\n"
"v_cvt_pk_bf16_f32 v131, v70, v71\n"
"v_mfma_f32_16x16x32_bf16 v[44:47], v[212:215], v[108:111], v[44:47]\n"
"ds_write_b128 v13, v[128:131] offset:12288\n"
"v_mfma_f32_16x16x32_bf16 v[56:59], v[216:219], v[108:111], v[56:59]\n"
"ds_read_b128 v[100:103], v5 offset:8448\n"
"v_mfma_f32_16x16x32_bf16 v[60:63], v[220:223], v[108:111], v[60:63]\n"
"v_cvt_pk_bf16_f32 v132, v72, v73\n"
"s_waitcnt lgkmcnt(6)\n"
"v_mfma_f32_16x16x32_bf16 v[32:35], v[224:227], v[112:115], v[32:35]\n"
"v_cvt_pk_bf16_f32 v133, v74, v75\n"
"v_mfma_f32_16x16x32_bf16 v[36:39], v[228:231], v[112:115], v[36:39]\n"
"v_cvt_pk_bf16_f32 v134, v76, v77\n"
"v_mfma_f32_16x16x32_bf16 v[48:51], v[232:235], v[112:115], v[48:51]\n"
"ds_read_b128 v[104:107], v5 offset:10240\n"
"v_mfma_f32_16x16x32_bf16 v[52:55], v[236:239], v[112:115], v[52:55]\n"
"v_cvt_pk_bf16_f32 v135, v78, v79\n"
"s_waitcnt lgkmcnt(6)\n"
"v_mfma_f32_16x16x32_bf16 v[40:43], v[224:227], v[116:119], v[40:43]\n"
"ds_write_b128 v13, v[132:135] offset:12544\n"
"v_mfma_f32_16x16x32_bf16 v[44:47], v[228:231], v[116:119], v[44:47]\n"
"v_cvt_pk_bf16_f32 v136, v80, v81\n"
"v_mfma_f32_16x16x32_bf16 v[56:59], v[232:235], v[116:119], v[56:59]\n"
"ds_read_b128 v[108:111], v5 offset:10496\n"
"v_mfma_f32_16x16x32_bf16 v[60:63], v[236:239], v[116:119], v[60:63]\n"
"v_cvt_pk_bf16_f32 v137, v82, v83\n"
"s_waitcnt lgkmcnt(7)\n"
"v_mfma_f32_16x16x32_bf16 v[32:35], v[240:243], v[120:123], v[32:35]\n"
"v_cvt_pk_bf16_f32 v138, v84, v85\n"
"v_mfma_f32_16x16x32_bf16 v[36:39], v[244:247], v[120:123], v[36:39]\n"
"v_cvt_pk_bf16_f32 v139, v86, v87\n"
"v_mfma_f32_16x16x32_bf16 v[48:51], v[248:251], v[120:123], v[48:51]\n"
"ds_read_b128 v[112:115], v5 offset:12288\n"
"v_mfma_f32_16x16x32_bf16 v[52:55], v[252:255], v[120:123], v[52:55]\n"
"ds_write_b128 v13, v[136:139] offset:14336\n"
"s_waitcnt lgkmcnt(8)\n"
"v_mfma_f32_16x16x32_bf16 v[40:43], v[240:243], v[124:127], v[40:43]\n"
"v_cvt_pk_bf16_f32 v140, v88, v89\n"
"v_mfma_f32_16x16x32_bf16 v[44:47], v[244:247], v[124:127], v[44:47]\n"
"v_cvt_pk_bf16_f32 v141, v90, v91\n"
"v_mfma_f32_16x16x32_bf16 v[56:59], v[248:251], v[124:127], v[56:59]\n"
"ds_read_b128 v[116:119], v5 offset:12544\n"
"v_mfma_f32_16x16x32_bf16 v[60:63], v[252:255], v[124:127], v[60:63]\n"
"v_cvt_pk_bf16_f32 v142, v92, v93\n"
"s_waitcnt lgkmcnt(8)\n"
"v_mfma_f32_16x16x32_bf16 v[32:35], a[0:3], v[96:99], v[32:35]\n"
"v_cvt_pk_bf16_f32 v143, v94, v95\n"
"v_mfma_f32_16x16x32_bf16 v[36:39], a[4:7], v[96:99], v[36:39]\n"
"ds_write_b128 v13, v[140:143] offset:14592\n"
"v_mfma_f32_16x16x32_bf16 v[48:51], a[8:11], v[96:99], v[48:51]\n"
"ds_read_b128 v[120:123], v5 offset:14336\n"
"v_mfma_f32_16x16x32_bf16 v[52:55], a[12:15], v[96:99], v[52:55]\n"
"s_waitcnt lgkmcnt(8)\n"
"v_mfma_f32_16x16x32_bf16 v[40:43], a[0:3], v[100:103], v[40:43]\n"
"v_mfma_f32_16x16x32_bf16 v[44:47], a[4:7], v[100:103], v[44:47]\n"
"v_mfma_f32_16x16x32_bf16 v[56:59], a[8:11], v[100:103], v[56:59]\n"
"ds_read_b128 v[124:127], v5 offset:14592\n"
"v_mfma_f32_16x16x32_bf16 v[60:63], a[12:15], v[100:103], v[60:63]\n"
"s_waitcnt lgkmcnt(8)\n"
"v_mfma_f32_16x16x32_bf16 v[32:35], a[16:19], v[104:107], v[32:35]\n"
"v_mfma_f32_16x16x32_bf16 v[36:39], a[20:23], v[104:107], v[36:39]\n"
"v_mfma_f32_16x16x32_bf16 v[48:51], a[24:27], v[104:107], v[48:51]\n"
"ds_read_b128 v[96:99], v5 offset:16384\n"
"v_mfma_f32_16x16x32_bf16 v[52:55], a[28:31], v[104:107], v[52:55]\n"
"s_waitcnt lgkmcnt(7)\n"
"v_mfma_f32_16x16x32_bf16 v[40:43], a[16:19], v[108:111], v[40:43]\n"
"v_mfma_f32_16x16x32_bf16 v[44:47], a[20:23], v[108:111], v[44:47]\n"
"v_mfma_f32_16x16x32_bf16 v[56:59], a[24:27], v[108:111], v[56:59]\n"
"ds_read_b128 v[100:103], v5 offset:16640\n"
"v_mfma_f32_16x16x32_bf16 v[60:63], a[28:31], v[108:111], v[60:63]\n"
"s_waitcnt lgkmcnt(7)\n"
"v_mfma_f32_16x16x32_bf16 v[32:35], a[32:35], v[112:115], v[32:35]\n"
"v_mfma_f32_16x16x32_bf16 v[36:39], a[36:39], v[112:115], v[36:39]\n"
"v_mfma_f32_16x16x32_bf16 v[48:51], a[40:43], v[112:115], v[48:51]\n"
"ds_read_b128 v[104:107], v5 offset:18432\n"
"v_mfma_f32_16x16x32_bf16 v[52:55], a[44:47], v[112:115], v[52:55]\n"
"s_waitcnt lgkmcnt(6)\n"
"v_mfma_f32_16x16x32_bf16 v[40:43], a[32:35], v[116:119], v[40:43]\n"
"v_mfma_f32_16x16x32_bf16 v[44:47], a[36:39], v[116:119], v[44:47]\n"
"v_mfma_f32_16x16x32_bf16 v[56:59], a[40:43], v[116:119], v[56:59]\n"
"ds_read_b128 v[108:111], v5 offset:18688\n"
"v_mfma_f32_16x16x32_bf16 v[60:63], a[44:47], v[116:119], v[60:63]\n"
"s_waitcnt lgkmcnt(5)\n"
"v_mfma_f32_16x16x32_bf16 v[32:35], a[48:51], v[120:123], v[32:35]\n"
"v_mfma_f32_16x16x32_bf16 v[36:39], a[52:55], v[120:123], v[36:39]\n"
"v_mfma_f32_16x16x32_bf16 v[48:51], a[56:59], v[120:123], v[48:51]\n"
"ds_read_b128 v[112:115], v5 offset:20480\n"
"v_mfma_f32_16x16x32_bf16 v[52:55], a[60:63], v[120:123], v[52:55]\n"
"s_waitcnt lgkmcnt(5)\n"
"v_mfma_f32_16x16x32_bf16 v[40:43], a[48:51], v[124:127], v[40:43]\n"
"v_mfma_f32_16x16x32_bf16 v[44:47], a[52:55], v[124:127], v[44:47]\n"
"v_mfma_f32_16x16x32_bf16 v[56:59], a[56:59], v[124:127], v[56:59]\n"
"ds_read_b128 v[116:119], v5 offset:20736\n"
"v_mfma_f32_16x16x32_bf16 v[60:63], a[60:63], v[124:127], v[60:63]\n"
"s_waitcnt lgkmcnt(5)\n"
"v_mfma_f32_16x16x32_bf16 v[32:35], a[64:67], v[96:99], v[32:35]\n"
"v_mfma_f32_16x16x32_bf16 v[36:39], a[68:71], v[96:99], v[36:39]\n"
"v_mfma_f32_16x16x32_bf16 v[48:51], a[72:75], v[96:99], v[48:51]\n"
"ds_read_b128 v[120:123], v5 offset:22528\n"
"v_mfma_f32_16x16x32_bf16 v[52:55], a[76:79], v[96:99], v[52:55]\n"
"s_waitcnt lgkmcnt(5)\n"
"v_mfma_f32_16x16x32_bf16 v[40:43], a[64:67], v[100:103], v[40:43]\n"
"v_mfma_f32_16x16x32_bf16 v[44:47], a[68:71], v[100:103], v[44:47]\n"
"v_mfma_f32_16x16x32_bf16 v[56:59], a[72:75], v[100:103], v[56:59]\n"
"ds_read_b128 v[124:127], v5 offset:22784\n"
"v_mfma_f32_16x16x32_bf16 v[60:63], a[76:79], v[100:103], v[60:63]\n"
"s_waitcnt lgkmcnt(5)\n"
"v_mfma_f32_16x16x32_bf16 v[32:35], a[80:83], v[104:107], v[32:35]\n"
"v_mfma_f32_16x16x32_bf16 v[36:39], a[84:87], v[104:107], v[36:39]\n"
"v_mfma_f32_16x16x32_bf16 v[48:51], a[88:91], v[104:107], v[48:51]\n"
"ds_read_b128 v[96:99], v5 offset:24576\n"
"v_mfma_f32_16x16x32_bf16 v[52:55], a[92:95], v[104:107], v[52:55]\n"
"s_waitcnt lgkmcnt(5)\n"
"v_mfma_f32_16x16x32_bf16 v[40:43], a[80:83], v[108:111], v[40:43]\n"
"v_mfma_f32_16x16x32_bf16 v[44:47], a[84:87], v[108:111], v[44:47]\n"
"v_mfma_f32_16x16x32_bf16 v[56:59], a[88:91], v[108:111], v[56:59]\n"
"ds_read_b128 v[100:103], v5 offset:24832\n"
"v_mfma_f32_16x16x32_bf16 v[60:63], a[92:95], v[108:111], v[60:63]\n"
"s_waitcnt lgkmcnt(5)\n"
"v_mfma_f32_16x16x32_bf16 v[32:35], a[96:99], v[112:115], v[32:35]\n"
"v_mfma_f32_16x16x32_bf16 v[36:39], a[100:103], v[112:115], v[36:39]\n"
"v_mfma_f32_16x16x32_bf16 v[48:51], a[104:107], v[112:115], v[48:51]\n"
"ds_read_b128 v[104:107], v5 offset:26624\n"
"v_mfma_f32_16x16x32_bf16 v[52:55], a[108:111], v[112:115], v[52:55]\n"
"s_waitcnt lgkmcnt(5)\n"
"v_mfma_f32_16x16x32_bf16 v[40:43], a[96:99], v[116:119], v[40:43]\n"
"s_waitcnt vmcnt(0)\n"
"v_mfma_f32_16x16x32_bf16 v[44:47], a[100:103], v[116:119], v[44:47]\n"
"v_mfma_f32_16x16x32_bf16 v[56:59], a[104:107], v[116:119], v[56:59]\n"
"ds_read_b128 v[108:111], v5 offset:26880\n"
"v_mfma_f32_16x16x32_bf16 v[60:63], a[108:111], v[116:119], v[60:63]\n"
"s_mov_b32 s74, s47\n"
"s_add_u32 s47, s47, s48\n"
"s_waitcnt lgkmcnt(5)\n"
"v_mfma_f32_16x16x32_bf16 v[32:35], a[112:115], v[120:123], v[32:35]\n"
"s_add_u32 s73, s73, 1\n"
"v_mfma_f32_16x16x32_bf16 v[36:39], a[116:119], v[120:123], v[36:39]\n"
"s_add_u32 m0, s71, 0\n"
"s_add_u32 s72, s68, 0\n"
"buffer_load_dwordx4 v0, s[36:39], s72 offen offset:0 sc1 lds\n"
"v_mfma_f32_16x16x32_bf16 v[48:51], a[120:123], v[120:123], v[48:51]\n"
"ds_read_b128 v[112:115], v5 offset:28672\n"
"v_mfma_f32_16x16x32_bf16 v[52:55], a[124:127], v[120:123], v[52:55]\n"
"buffer_load_dwordx4 v0, s[36:39], s72 offen offset:1024 sc1 lds\n"
"s_waitcnt lgkmcnt(5)\n"
"v_mfma_f32_16x16x32_bf16 v[40:43], a[112:115], v[124:127], v[40:43]\n"
"v_mfma_f32_16x16x32_bf16 v[44:47], a[116:119], v[124:127], v[44:47]\n"
"v_mfma_f32_16x16x32_bf16 v[56:59], a[120:123], v[124:127], v[56:59]\n"
"ds_read_b128 v[116:119], v5 offset:28928\n"
"v_mfma_f32_16x16x32_bf16 v[60:63], a[124:127], v[124:127], v[60:63]\n"
"buffer_load_dwordx4 v0, s[36:39], s72 offen offset:2048 sc1 lds\n"
"s_waitcnt lgkmcnt(5)\n"
"v_mfma_f32_16x16x32_bf16 v[32:35], a[128:131], v[96:99], v[32:35]\n"
"v_mfma_f32_16x16x32_bf16 v[36:39], a[132:135], v[96:99], v[36:39]\n"
"buffer_load_dwordx4 v0, s[36:39], s72 offen offset:3072 sc1 lds\n"
"v_mfma_f32_16x16x32_bf16 v[48:51], a[136:139], v[96:99], v[48:51]\n"
"ds_read_b128 v[120:123], v5 offset:30720\n"
"v_mfma_f32_16x16x32_bf16 v[52:55], a[140:143], v[96:99], v[52:55]\n"
"s_waitcnt lgkmcnt(5)\n"
"v_mfma_f32_16x16x32_bf16 v[40:43], a[128:131], v[100:103], v[40:43]\n"
"v_mfma_f32_16x16x32_bf16 v[44:47], a[132:135], v[100:103], v[44:47]\n"
"s_add_u32 m0, s71, 4096\n"
"s_add_u32 s72, s68, 4096\n"
"buffer_load_dwordx4 v0, s[36:39], s72 offen offset:0 sc1 lds\n"
"v_mfma_f32_16x16x32_bf16 v[56:59], a[136:139], v[100:103], v[56:59]\n"
"ds_read_b128 v[124:127], v5 offset:30976\n"
"v_mfma_f32_16x16x32_bf16 v[60:63], a[140:143], v[100:103], v[60:63]\n"
"buffer_load_dwordx4 v0, s[36:39], s72 offen offset:1024 sc1 lds\n"
"s_waitcnt lgkmcnt(5)\n"
"v_mfma_f32_16x16x32_bf16 v[32:35], a[144:147], v[104:107], v[32:35]\n"
"v_mfma_f32_16x16x32_bf16 v[36:39], a[148:151], v[104:107], v[36:39]\n"
"v_mfma_f32_16x16x32_bf16 v[48:51], a[152:155], v[104:107], v[48:51]\n"
"ds_read_b128 v[96:99], v5 offset:32768\n"
"v_mfma_f32_16x16x32_bf16 v[52:55], a[156:159], v[104:107], v[52:55]\n"
"buffer_load_dwordx4 v0, s[36:39], s72 offen offset:2048 sc1 lds\n"
"s_waitcnt lgkmcnt(5)\n"
"v_mfma_f32_16x16x32_bf16 v[40:43], a[144:147], v[108:111], v[40:43]\n"
"v_mfma_f32_16x16x32_bf16 v[44:47], a[148:151], v[108:111], v[44:47]\n"
"buffer_load_dwordx4 v0, s[36:39], s72 offen offset:3072 sc1 lds\n"
"v_mfma_f32_16x16x32_bf16 v[56:59], a[152:155], v[108:111], v[56:59]\n"
"ds_read_b128 v[100:103], v5 offset:33024\n"
"v_mfma_f32_16x16x32_bf16 v[60:63], a[156:159], v[108:111], v[60:63]\n"
"s_waitcnt lgkmcnt(5)\n"
"v_mfma_f32_16x16x32_bf16 v[32:35], a[160:163], v[112:115], v[32:35]\n"
"v_mfma_f32_16x16x32_bf16 v[36:39], a[164:167], v[112:115], v[36:39]\n"
"s_add_u32 m0, s71, 8192\n"
"s_add_u32 s72, s68, 8192\n"
"buffer_load_dwordx4 v0, s[36:39], s72 offen offset:0 sc1 lds\n"
"v_mfma_f32_16x16x32_bf16 v[48:51], a[168:171], v[112:115], v[48:51]\n"
"ds_read_b128 v[104:107], v5 offset:34816\n"
"v_mfma_f32_16x16x32_bf16 v[52:55], a[172:175], v[112:115], v[52:55]\n"
"buffer_load_dwordx4 v0, s[36:39], s72 offen offset:1024 sc1 lds\n"
"s_waitcnt lgkmcnt(5)\n"
"v_mfma_f32_16x16x32_bf16 v[40:43], a[160:163], v[116:119], v[40:43]\n"
"v_mfma_f32_16x16x32_bf16 v[44:47], a[164:167], v[116:119], v[44:47]\n"
"v_mfma_f32_16x16x32_bf16 v[56:59], a[168:171], v[116:119], v[56:59]\n"
"ds_read_b128 v[108:111], v5 offset:35072\n"
"v_mfma_f32_16x16x32_bf16 v[60:63], a[172:175], v[116:119], v[60:63]\n"
"buffer_load_dwordx4 v0, s[36:39], s72 offen offset:2048 sc1 lds\n"
"s_waitcnt lgkmcnt(5)\n"
"v_mfma_f32_16x16x32_bf16 v[32:35], a[176:179], v[120:123], v[32:35]\n"
"v_mfma_f32_16x16x32_bf16 v[36:39], a[180:183], v[120:123], v[36:39]\n"
"buffer_load_dwordx4 v0, s[36:39], s72 offen offset:3072 sc1 lds\n"
"v_mfma_f32_16x16x32_bf16 v[48:51], a[184:187], v[120:123], v[48:51]\n"
"ds_read_b128 v[112:115], v5 offset:36864\n"
"v_mfma_f32_16x16x32_bf16 v[52:55], a[188:191], v[120:123], v[52:55]\n"
"s_waitcnt lgkmcnt(5)\n"
"v_mfma_f32_16x16x32_bf16 v[40:43], a[176:179], v[124:127], v[40:43]\n"
"v_mfma_f32_16x16x32_bf16 v[44:47], a[180:183], v[124:127], v[44:47]\n"
"s_add_u32 m0, s71, 12288\n"
"s_add_u32 s72, s68, 12288\n"
"buffer_load_dwordx4 v0, s[36:39], s72 offen offset:0 sc1 lds\n"
"v_mfma_f32_16x16x32_bf16 v[56:59], a[184:187], v[124:127], v[56:59]\n"
"ds_read_b128 v[116:119], v5 offset:37120\n"
"v_mfma_f32_16x16x32_bf16 v[60:63], a[188:191], v[124:127], v[60:63]\n"
"buffer_load_dwordx4 v0, s[36:39], s72 offen offset:1024 sc1 lds\n"
"s_waitcnt lgkmcnt(5)\n"
"v_mfma_f32_16x16x32_bf16 v[32:35], a[192:195], v[96:99], v[32:35]\n"
"v_mfma_f32_16x16x32_bf16 v[36:39], a[196:199], v[96:99], v[36:39]\n"
"v_mfma_f32_16x16x32_bf16 v[48:51], a[200:203], v[96:99], v[48:51]\n"
"ds_read_b128 v[120:123], v5 offset:38912\n"
"v_mfma_f32_16x16x32_bf16 v[52:55], a[204:207], v[96:99], v[52:55]\n"
"s_waitcnt lgkmcnt(5)\n"
"v_mfma_f32_16x16x32_bf16 v[40:43], a[192:195], v[100:103], v[40:43]\n"
"v_mfma_f32_16x16x32_bf16 v[44:47], a[196:199], v[100:103], v[44:47]\n"
"v_mfma_f32_16x16x32_bf16 v[56:59], a[200:203], v[100:103], v[56:59]\n"
"ds_read_b128 v[124:127], v5 offset:39168\n"
"v_mfma_f32_16x16x32_bf16 v[60:63], a[204:207], v[100:103], v[60:63]\n"
"s_waitcnt lgkmcnt(5)\n"
"v_mfma_f32_16x16x32_bf16 v[32:35], a[208:211], v[104:107], v[32:35]\n"
"v_mfma_f32_16x16x32_bf16 v[36:39], a[212:215], v[104:107], v[36:39]\n"
"v_mfma_f32_16x16x32_bf16 v[48:51], a[216:219], v[104:107], v[48:51]\n"
"s_add_u32 s70, s70, 40960\n"
"s_cmp_eq_u32 s70, 122880\n"
"s_cselect_b32 s70, 0, s70\n"
"v_mfma_f32_16x16x32_bf16 v[52:55], a[220:223], v[104:107], v[52:55]\n"
"s_waitcnt lgkmcnt(4)\n"
"v_mfma_f32_16x16x32_bf16 v[40:43], a[208:211], v[108:111], v[40:43]\n"
"v_add_u32 v5, s70, v7\n"
"v_mfma_f32_16x16x32_bf16 v[44:47], a[212:215], v[108:111], v[44:47]\n"
"v_mfma_f32_16x16x32_bf16 v[56:59], a[216:219], v[108:111], v[56:59]\n"
"s_waitcnt lgkmcnt(0)\n"
"s_barrier\n"
"v_mfma_f32_16x16x32_bf16 v[60:63], a[220:223], v[108:111], v[60:63]\n"
"ds_read_b128 v[96:99], v5 offset:0\n"
"v_mfma_f32_16x16x32_bf16 v[32:35], a[224:227], v[112:115], v[32:35]\n"
"v_mfma_f32_16x16x32_bf16 v[36:39], a[228:231], v[112:115], v[36:39]\n"
"ds_read_b128 v[100:103], v5 offset:256\n"
"v_mfma_f32_16x16x32_bf16 v[48:51], a[232:235], v[112:115], v[48:51]\n"
"v_mfma_f32_16x16x32_bf16 v[52:55], a[236:239], v[112:115], v[52:55]\n"
"ds_read_b128 v[104:107], v5 offset:2048\n"
"v_mfma_f32_16x16x32_bf16 v[40:43], a[224:227], v[116:119], v[40:43]\n"
"v_mfma_f32_16x16x32_bf16 v[44:47], a[228:231], v[116:119], v[44:47]\n"
"v_mfma_f32_16x16x32_bf16 v[56:59], a[232:235], v[116:119], v[56:59]\n"
"ds_read_b128 v[108:111], v5 offset:2304\n"
"v_mfma_f32_16x16x32_bf16 v[60:63], a[236:239], v[116:119], v[60:63]\n"
"v_mfma_f32_16x16x32_bf16 v[32:35], a[240:243], v[120:123], v[32:35]\n"
"v_mfma_f32_16x16x32_bf16 v[36:39], a[244:247], v[120:123], v[36:39]\n"
"v_mfma_f32_16x16x32_bf16 v[48:51], a[248:251], v[120:123], v[48:51]\n"
"ds_read_b128 v[112:115], v5 offset:4096\n"
"v_mfma_f32_16x16x32_bf16 v[52:55], a[252:255], v[120:123], v[52:55]\n"
"v_mfma_f32_16x16x32_bf16 v[40:43], a[240:243], v[124:127], v[40:43]\n"
"s_add_u32 s68, s68, 40960\n"
"s_add_u32 s69, s69, 40960\n"
"v_mfma_f32_16x16x32_bf16 v[44:47], a[244:247], v[124:127], v[44:47]\n"
"s_cmp_eq_u32 s69, 122880\n"
"s_cselect_b32 s69, 0, s69\n"
"v_mfma_f32_16x16x32_bf16 v[56:59], a[248:251], v[124:127], v[56:59]\n"
"ds_read_b128 v[116:119], v5 offset:4352\n"
"v_mfma_f32_16x16x32_bf16 v[60:63], a[252:255], v[124:127], v[60:63]\n"
"s_add_u32 s71, s69, s50\n"
"s_waitcnt lgkmcnt(5)\n"
"v_mfma_f32_16x16x32_bf16 v[64:67], v[192:195], v[96:99], 0\n"
"v_mfma_f32_16x16x32_bf16 v[68:71], v[196:199], v[96:99], 0\n"
"v_mfma_f32_16x16x32_bf16 v[80:83], v[200:203], v[96:99], 0\n"
"ds_read_b128 v[120:123], v5 offset:6144\n"
"v_mfma_f32_16x16x32_bf16 v[84:87], v[204:207], v[96:99], 0\n"
"s_waitcnt lgkmcnt(5)\n"
"v_mfma_f32_16x16x32_bf16 v[72:75], v[192:195], v[100:103], 0\n"
"v_mfma_f32_16x16x32_bf16 v[76:79], v[196:199], v[100:103], 0\n"
"v_mfma_f32_16x16x32_bf16 v[88:91], v[200:203], v[100:103], 0\n"
"ds_read_b128 v[124:127], v5 offset:6400\n"
"v_mfma_f32_16x16x32_bf16 v[92:95], v[204:207], v[100:103], 0\n"
"s_waitcnt lgkmcnt(5)\n"
"v_mfma_f32_16x16x32_bf16 v[64:67], v[208:211], v[104:107], v[64:67]\n"
"v_cvt_pk_bf16_f32 v128, v32, v33\n"
"v_mfma_f32_16x16x32_bf16 v[68:71], v[212:215], v[104:107], v[68:71]\n"
"v_cvt_pk_bf16_f32 v129, v34, v35\n"
"v_mfma_f32_16x16x32_bf16 v[80:83], v[216:219], v[104:107], v[80:83]\n"
"ds_read_b128 v[96:99], v5 offset:8192\n"
"v_mfma_f32_16x16x32_bf16 v[84:87], v[220:223], v[104:107], v[84:87]\n"
"v_cvt_pk_bf16_f32 v130, v36, v37\n"
"s_waitcnt lgkmcnt(5)\n"
"v_mfma_f32_16x16x32_bf16 v[72:75], v[208:211], v[108:111], v[72:75]\n"
"v_cvt_pk_bf16_f32 v131, v38, v39\n"
"v_mfma_f32_16x16x32_bf16 v[76:79], v[212:215], v[108:111], v[76:79]\n"
"ds_write_b128 v13, v[128:131] offset:0\n"
"v_mfma_f32_16x16x32_bf16 v[88:91], v[216:219], v[108:111], v[88:91]\n"
"ds_read_b128 v[100:103], v5 offset:8448\n"
"v_mfma_f32_16x16x32_bf16 v[92:95], v[220:223], v[108:111], v[92:95]\n"
"v_cvt_pk_bf16_f32 v132, v40, v41\n"
"s_waitcnt lgkmcnt(6)\n"
"v_mfma_f32_16x16x32_bf16 v[64:67], v[224:227], v[112:115], v[64:67]\n"
"v_cvt_pk_bf16_f32 v133, v42, v43\n"
"v_mfma_f32_16x16x32_bf16 v[68:71], v[228:231], v[112:115], v[68:71]\n"
"v_cvt_pk_bf16_f32 v134, v44, v45\n"
"v_mfma_f32_16x16x32_bf16 v[80:83], v[232:235], v[112:115], v[80:83]\n"
"ds_read_b128 v[104:107], v5 offset:10240\n"
"v_mfma_f32_16x16x32_bf16 v[84:87], v[236:239], v[112:115], v[84:87]\n"
"v_cvt_pk_bf16_f32 v135, v46, v47\n"
"s_waitcnt lgkmcnt(6)\n"
"v_mfma_f32_16x16x32_bf16 v[72:75], v[224:227], v[116:119], v[72:75]\n"
"ds_write_b128 v13, v[132:135] offset:256\n"
"v_mfma_f32_16x16x32_bf16 v[76:79], v[228:231], v[116:119], v[76:79]\n"
"v_cvt_pk_bf16_f32 v136, v48, v49\n"
"v_mfma_f32_16x16x32_bf16 v[88:91], v[232:235], v[116:119], v[88:91]\n"
"ds_read_b128 v[108:111], v5 offset:10496\n"
"v_mfma_f32_16x16x32_bf16 v[92:95], v[236:239], v[116:119], v[92:95]\n"
"v_cvt_pk_bf16_f32 v137, v50, v51\n"
"s_waitcnt lgkmcnt(7)\n"
"v_mfma_f32_16x16x32_bf16 v[64:67], v[240:243], v[120:123], v[64:67]\n"
"v_cvt_pk_bf16_f32 v138, v52, v53\n"
"v_mfma_f32_16x16x32_bf16 v[68:71], v[244:247], v[120:123], v[68:71]\n"
"v_cvt_pk_bf16_f32 v139, v54, v55\n"
"v_mfma_f32_16x16x32_bf16 v[80:83], v[248:251], v[120:123], v[80:83]\n"
"ds_read_b128 v[112:115], v5 offset:12288\n"
"v_mfma_f32_16x16x32_bf16 v[84:87], v[252:255], v[120:123], v[84:87]\n"
"ds_write_b128 v13, v[136:139] offset:2048\n"
"s_waitcnt lgkmcnt(8)\n"
"v_mfma_f32_16x16x32_bf16 v[72:75], v[240:243], v[124:127], v[72:75]\n"
"v_cvt_pk_bf16_f32 v140, v56, v57\n"
"v_mfma_f32_16x16x32_bf16 v[76:79], v[244:247], v[124:127], v[76:79]\n"
"v_cvt_pk_bf16_f32 v141, v58, v59\n"
"v_mfma_f32_16x16x32_bf16 v[88:91], v[248:251], v[124:127], v[88:91]\n"
"ds_read_b128 v[116:119], v5 offset:12544\n"
"v_mfma_f32_16x16x32_bf16 v[92:95], v[252:255], v[124:127], v[92:95]\n"
"v_cvt_pk_bf16_f32 v142, v60, v61\n"
"s_waitcnt lgkmcnt(8)\n"
"v_mfma_f32_16x16x32_bf16 v[64:67], a[0:3], v[96:99], v[64:67]\n"
"v_cvt_pk_bf16_f32 v143, v62, v63\n"
"v_mfma_f32_16x16x32_bf16 v[68:71], a[4:7], v[96:99], v[68:71]\n"
"ds_write_b128 v13, v[140:143] offset:2304\n"
"v_mfma_f32_16x16x32_bf16 v[80:83], a[8:11], v[96:99], v[80:83]\n"
"ds_read_b128 v[120:123], v5 offset:14336\n"
"v_mfma_f32_16x16x32_bf16 v[84:87], a[12:15], v[96:99], v[84:87]\n"
"s_waitcnt lgkmcnt(8)\n"
"v_mfma_f32_16x16x32_bf16 v[72:75], a[0:3], v[100:103], v[72:75]\n"
"v_mfma_f32_16x16x32_bf16 v[76:79], a[4:7], v[100:103], v[76:79]\n"
"v_mfma_f32_16x16x32_bf16 v[88:91], a[8:11], v[100:103], v[88:91]\n"
"ds_read_b128 v[124:127], v5 offset:14592\n"
"v_mfma_f32_16x16x32_bf16 v[92:95], a[12:15], v[100:103], v[92:95]\n"
"s_waitcnt lgkmcnt(8)\n"
"v_mfma_f32_16x16x32_bf16 v[64:67], a[16:19], v[104:107], v[64:67]\n"
"v_mfma_f32_16x16x32_bf16 v[68:71], a[20:23], v[104:107], v[68:71]\n"
"v_mfma_f32_16x16x32_bf16 v[80:83], a[24:27], v[104:107], v[80:83]\n"
"ds_read_b128 v[96:99], v5 offset:16384\n"
"v_mfma_f32_16x16x32_bf16 v[84:87], a[28:31], v[104:107], v[84:87]\n"
"s_waitcnt lgkmcnt(7)\n"
"v_mfma_f32_16x16x32_bf16 v[72:75], a[16:19], v[108:111], v[72:75]\n"
"v_mfma_f32_16x16x32_bf16 v[76:79], a[20:23], v[108:111], v[76:79]\n"
"v_mfma_f32_16x16x32_bf16 v[88:91], a[24:27], v[108:111], v[88:91]\n"
"ds_read_b128 v[100:103], v5 offset:16640\n"
"v_mfma_f32_16x16x32_bf16 v[92:95], a[28:31], v[108:111], v[92:95]\n"
"s_waitcnt lgkmcnt(7)\n"
"v_mfma_f32_16x16x32_bf16 v[64:67], a[32:35], v[112:115], v[64:67]\n"
"v_mfma_f32_16x16x32_bf16 v[68:71], a[36:39], v[112:115], v[68:71]\n"
"v_mfma_f32_16x16x32_bf16 v[80:83], a[40:43], v[112:115], v[80:83]\n"
"ds_read_b128 v[104:107], v5 offset:18432\n"
"v_mfma_f32_16x16x32_bf16 v[84:87], a[44:47], v[112:115], v[84:87]\n"
"s_waitcnt lgkmcnt(6)\n"
"v_mfma_f32_16x16x32_bf16 v[72:75], a[32:35], v[116:119], v[72:75]\n"
"v_mfma_f32_16x16x32_bf16 v[76:79], a[36:39], v[116:119], v[76:79]\n"
"v_mfma_f32_16x16x32_bf16 v[88:91], a[40:43], v[116:119], v[88:91]\n"
"ds_read_b128 v[108:111], v5 offset:18688\n"
"v_mfma_f32_16x16x32_bf16 v[92:95], a[44:47], v[116:119], v[92:95]\n"
"s_waitcnt lgkmcnt(5)\n"
"v_mfma_f32_16x16x32_bf16 v[64:67], a[48:51], v[120:123], v[64:67]\n"
"v_mfma_f32_16x16x32_bf16 v[68:71], a[52:55], v[120:123], v[68:71]\n"
"v_mfma_f32_16x16x32_bf16 v[80:83], a[56:59], v[120:123], v[80:83]\n"
"ds_read_b128 v[112:115], v5 offset:20480\n"
"v_mfma_f32_16x16x32_bf16 v[84:87], a[60:63], v[120:123], v[84:87]\n"
"s_waitcnt lgkmcnt(5)\n"
"v_mfma_f32_16x16x32_bf16 v[72:75], a[48:51], v[124:127], v[72:75]\n"
"v_mfma_f32_16x16x32_bf16 v[76:79], a[52:55], v[124:127], v[76:79]\n"
"v_mfma_f32_16x16x32_bf16 v[88:91], a[56:59], v[124:127], v[88:91]\n"
"ds_read_b128 v[116:119], v5 offset:20736\n"
"v_mfma_f32_16x16x32_bf16 v[92:95], a[60:63], v[124:127], v[92:95]\n"
"s_waitcnt lgkmcnt(5)\n"
"v_mfma_f32_16x16x32_bf16 v[64:67], a[64:67], v[96:99], v[64:67]\n"
"v_mfma_f32_16x16x32_bf16 v[68:71], a[68:71], v[96:99], v[68:71]\n"
"v_mfma_f32_16x16x32_bf16 v[80:83], a[72:75], v[96:99], v[80:83]\n"
"ds_read_b128 v[120:123], v5 offset:22528\n"
"v_mfma_f32_16x16x32_bf16 v[84:87], a[76:79], v[96:99], v[84:87]\n"
"s_waitcnt lgkmcnt(5)\n"
"v_mfma_f32_16x16x32_bf16 v[72:75], a[64:67], v[100:103], v[72:75]\n"
"v_mfma_f32_16x16x32_bf16 v[76:79], a[68:71], v[100:103], v[76:79]\n"
"v_mfma_f32_16x16x32_bf16 v[88:91], a[72:75], v[100:103], v[88:91]\n"
"ds_read_b128 v[124:127], v5 offset:22784\n"
"v_mfma_f32_16x16x32_bf16 v[92:95], a[76:79], v[100:103], v[92:95]\n"
"s_waitcnt lgkmcnt(5)\n"
"v_mfma_f32_16x16x32_bf16 v[64:67], a[80:83], v[104:107], v[64:67]\n"
"v_mfma_f32_16x16x32_bf16 v[68:71], a[84:87], v[104:107], v[68:71]\n"
"v_mfma_f32_16x16x32_bf16 v[80:83], a[88:91], v[104:107], v[80:83]\n"
"ds_read_b128 v[96:99], v5 offset:24576\n"
"v_mfma_f32_16x16x32_bf16 v[84:87], a[92:95], v[104:107], v[84:87]\n"
"s_waitcnt lgkmcnt(5)\n"
"v_mfma_f32_16x16x32_bf16 v[72:75], a[80:83], v[108:111], v[72:75]\n"
"v_mfma_f32_16x16x32_bf16 v[76:79], a[84:87], v[108:111], v[76:79]\n"
"v_mfma_f32_16x16x32_bf16 v[88:91], a[88:91], v[108:111], v[88:91]\n"
"ds_read_b128 v[100:103], v5 offset:24832\n"
"v_mfma_f32_16x16x32_bf16 v[92:95], a[92:95], v[108:111], v[92:95]\n"
"s_waitcnt lgkmcnt(5)\n"
"v_mfma_f32_16x16x32_bf16 v[64:67], a[96:99], v[112:115], v[64:67]\n"
"v_mfma_f32_16x16x32_bf16 v[68:71], a[100:103], v[112:115], v[68:71]\n"
"v_mfma_f32_16x16x32_bf16 v[80:83], a[104:107], v[112:115], v[80:83]\n"
"ds_read_b128 v[104:107], v5 offset:26624\n"
"v_mfma_f32_16x16x32_bf16 v[84:87], a[108:111], v[112:115], v[84:87]\n"
"s_waitcnt lgkmcnt(5)\n"
"v_mfma_f32_16x16x32_bf16 v[72:75], a[96:99], v[116:119], v[72:75]\n"
"s_waitcnt vmcnt(0)\n"
"v_mfma_f32_16x16x32_bf16 v[76:79], a[100:103], v[116:119], v[76:79]\n"
"v_mfma_f32_16x16x32_bf16 v[88:91], a[104:107], v[116:119], v[88:91]\n"
"ds_read_b128 v[108:111], v5 offset:26880\n"
"v_mfma_f32_16x16x32_bf16 v[92:95], a[108:111], v[116:119], v[92:95]\n"
"s_mov_b32 s74, s47\n"
"s_add_u32 s47, s47, s48\n"
"s_waitcnt lgkmcnt(5)\n"
"v_mfma_f32_16x16x32_bf16 v[64:67], a[112:115], v[120:123], v[64:67]\n"
"s_add_u32 s73, s73, 1\n"
"v_mfma_f32_16x16x32_bf16 v[68:71], a[116:119], v[120:123], v[68:71]\n"
"s_add_u32 m0, s71, 0\n"
"s_add_u32 s72, s68, 0\n"
"buffer_load_dwordx4 v0, s[36:39], s72 offen offset:0 sc1 lds\n"
"v_mfma_f32_16x16x32_bf16 v[80:83], a[120:123], v[120:123], v[80:83]\n"
"ds_read_b128 v[112:115], v5 offset:28672\n"
"v_mfma_f32_16x16x32_bf16 v[84:87], a[124:127], v[120:123], v[84:87]\n"
"buffer_load_dwordx4 v0, s[36:39], s72 offen offset:1024 sc1 lds\n"
"s_waitcnt lgkmcnt(5)\n"
"v_mfma_f32_16x16x32_bf16 v[72:75], a[112:115], v[124:127], v[72:75]\n"
"v_mfma_f32_16x16x32_bf16 v[76:79], a[116:119], v[124:127], v[76:79]\n"
"v_mfma_f32_16x16x32_bf16 v[88:91], a[120:123], v[124:127], v[88:91]\n"
"ds_read_b128 v[116:119], v5 offset:28928\n"
"v_mfma_f32_16x16x32_bf16 v[92:95], a[124:127], v[124:127], v[92:95]\n"
"buffer_load_dwordx4 v0, s[36:39], s72 offen offset:2048 sc1 lds\n"
"s_waitcnt lgkmcnt(5)\n"
"v_mfma_f32_16x16x32_bf16 v[64:67], a[128:131], v[96:99], v[64:67]\n"
"v_mfma_f32_16x16x32_bf16 v[68:71], a[132:135], v[96:99], v[68:71]\n"
"buffer_load_dwordx4 v0, s[36:39], s72 offen offset:3072 sc1 lds\n"
"v_mfma_f32_16x16x32_bf16 v[80:83], a[136:139], v[96:99], v[80:83]\n"
"ds_read_b128 v[120:123], v5 offset:30720\n"
"v_mfma_f32_16x16x32_bf16 v[84:87], a[140:143], v[96:99], v[84:87]\n"
"s_waitcnt lgkmcnt(5)\n"
"v_mfma_f32_16x16x32_bf16 v[72:75], a[128:131], v[100:103], v[72:75]\n"
"v_mfma_f32_16x16x32_bf16 v[76:79], a[132:135], v[100:103], v[76:79]\n"
"s_add_u32 m0, s71, 4096\n"
"s_add_u32 s72, s68, 4096\n"
"buffer_load_dwordx4 v0, s[36:39], s72 offen offset:0 sc1 lds\n"
"v_mfma_f32_16x16x32_bf16 v[88:91], a[136:139], v[100:103], v[88:91]\n"
"ds_read_b128 v[124:127], v5 offset:30976\n"
"v_mfma_f32_16x16x32_bf16 v[92:95], a[140:143], v[100:103], v[92:95]\n"
"buffer_load_dwordx4 v0, s[36:39], s72 offen offset:1024 sc1 lds\n"
"s_waitcnt lgkmcnt(5)\n"
"v_mfma_f32_16x16x32_bf16 v[64:67], a[144:147], v[104:107], v[64:67]\n"
"v_mfma_f32_16x16x32_bf16 v[68:71], a[148:151], v[104:107], v[68:71]\n"
"v_mfma_f32_16x16x32_bf16 v[80:83], a[152:155], v[104:107], v[80:83]\n"
"ds_read_b128 v[96:99], v5 offset:32768\n"
"v_mfma_f32_16x16x32_bf16 v[84:87], a[156:159], v[104:107], v[84:87]\n"
"buffer_load_dwordx4 v0, s[36:39], s72 offen offset:2048 sc1 lds\n"
"s_waitcnt lgkmcnt(5)\n"
"v_mfma_f32_16x16x32_bf16 v[72:75], a[144:147], v[108:111], v[72:75]\n"
"v_mfma_f32_16x16x32_bf16 v[76:79], a[148:151], v[108:111], v[76:79]\n"
"buffer_load_dwordx4 v0, s[36:39], s72 offen offset:3072 sc1 lds\n"
"v_mfma_f32_16x16x32_bf16 v[88:91], a[152:155], v[108:111], v[88:91]\n"
"ds_read_b128 v[100:103], v5 offset:33024\n"
"v_mfma_f32_16x16x32_bf16 v[92:95], a[156:159], v[108:111], v[92:95]\n"
"s_waitcnt lgkmcnt(5)\n"
"v_mfma_f32_16x16x32_bf16 v[64:67], a[160:163], v[112:115], v[64:67]\n"
"v_mfma_f32_16x16x32_bf16 v[68:71], a[164:167], v[112:115], v[68:71]\n"
"s_add_u32 m0, s71, 8192\n"
"s_add_u32 s72, s68, 8192\n"
"buffer_load_dwordx4 v0, s[36:39], s72 offen offset:0 sc1 lds\n"
"v_mfma_f32_16x16x32_bf16 v[80:83], a[168:171], v[112:115], v[80:83]\n"
"ds_read_b128 v[104:107], v5 offset:34816\n"
"v_mfma_f32_16x16x32_bf16 v[84:87], a[172:175], v[112:115], v[84:87]\n"
"buffer_load_dwordx4 v0, s[36:39], s72 offen offset:1024 sc1 lds\n"
"s_waitcnt lgkmcnt(5)\n"
"v_mfma_f32_16x16x32_bf16 v[72:75], a[160:163], v[116:119], v[72:75]\n"
"v_mfma_f32_16x16x32_bf16 v[76:79], a[164:167], v[116:119], v[76:79]\n"
"v_mfma_f32_16x16x32_bf16 v[88:91], a[168:171], v[116:119], v[88:91]\n"
"ds_read_b128 v[108:111], v5 offset:35072\n"
"v_mfma_f32_16x16x32_bf16 v[92:95], a[172:175], v[116:119], v[92:95]\n"
"buffer_load_dwordx4 v0, s[36:39], s72 offen offset:2048 sc1 lds\n"
"s_waitcnt lgkmcnt(5)\n"
"v_mfma_f32_16x16x32_bf16 v[64:67], a[176:179], v[120:123], v[64:67]\n"
"v_mfma_f32_16x16x32_bf16 v[68:71], a[180:183], v[120:123], v[68:71]\n"
"buffer_load_dwordx4 v0, s[36:39], s72 offen offset:3072 sc1 lds\n"
"v_mfma_f32_16x16x32_bf16 v[80:83], a[184:187], v[120:123], v[80:83]\n"
"ds_read_b128 v[112:115], v5 offset:36864\n"
"v_mfma_f32_16x16x32_bf16 v[84:87], a[188:191], v[120:123], v[84:87]\n"
"s_waitcnt lgkmcnt(5)\n"
"v_mfma_f32_16x16x32_bf16 v[72:75], a[176:179], v[124:127], v[72:75]\n"
"v_mfma_f32_16x16x32_bf16 v[76:79], a[180:183], v[124:127], v[76:79]\n"
"s_add_u32 m0, s71, 12288\n"
"s_add_u32 s72, s68, 12288\n"
"buffer_load_dwordx4 v0, s[36:39], s72 offen offset:0 sc1 lds\n"
"v_mfma_f32_16x16x32_bf16 v[88:91], a[184:187], v[124:127], v[88:91]\n"
"ds_read_b128 v[116:119], v5 offset:37120\n"
"v_mfma_f32_16x16x32_bf16 v[92:95], a[188:191], v[124:127], v[92:95]\n"
"buffer_load_dwordx4 v0, s[36:39], s72 offen offset:1024 sc1 lds\n"
"s_waitcnt lgkmcnt(5)\n"
"v_mfma_f32_16x16x32_bf16 v[64:67], a[192:195], v[96:99], v[64:67]\n"
"v_mfma_f32_16x16x32_bf16 v[68:71], a[196:199], v[96:99], v[68:71]\n"
"v_mfma_f32_16x16x32_bf16 v[80:83], a[200:203], v[96:99], v[80:83]\n"
"ds_read_b128 v[120:123], v5 offset:38912\n"
"v_mfma_f32_16x16x32_bf16 v[84:87], a[204:207], v[96:99], v[84:87]\n"
"s_waitcnt lgkmcnt(5)\n"
"v_mfma_f32_16x16x32_bf16 v[72:75], a[192:195], v[100:103], v[72:75]\n"
"v_mfma_f32_16x16x32_bf16 v[76:79], a[196:199], v[100:103], v[76:79]\n"
"v_mfma_f32_16x16x32_bf16 v[88:91], a[200:203], v[100:103], v[88:91]\n"
"ds_read_b128 v[124:127], v5 offset:39168\n"
"v_mfma_f32_16x16x32_bf16 v[92:95], a[204:207], v[100:103], v[92:95]\n"
"s_waitcnt lgkmcnt(5)\n"
"v_mfma_f32_16x16x32_bf16 v[64:67], a[208:211], v[104:107], v[64:67]\n"
"v_mfma_f32_16x16x32_bf16 v[68:71], a[212:215], v[104:107], v[68:71]\n"
"v_mfma_f32_16x16x32_bf16 v[80:83], a[216:219], v[104:107], v[80:83]\n"
"s_add_u32 s70, s70, 40960\n"
"s_cmp_eq_u32 s70, 122880\n"
"s_cselect_b32 s70, 0, s70\n"
"v_mfma_f32_16x16x32_bf16 v[84:87], a[220:223], v[104:107], v[84:87]\n"
"s_waitcnt lgkmcnt(4)\n"
"v_mfma_f32_16x16x32_bf16 v[72:75], a[208:211], v[108:111], v[72:75]\n"
"v_add_u32 v5, s70, v7\n"
"v_mfma_f32_16x16x32_bf16 v[76:79], a[212:215], v[108:111], v[76:79]\n"
"v_mfma_f32_16x16x32_bf16 v[88:91], a[216:219], v[108:111], v[88:91]\n"
"s_waitcnt lgkmcnt(0)\n"
"s_barrier\n"
"v_mfma_f32_16x16x32_bf16 v[92:95], a[220:223], v[108:111], v[92:95]\n"
"ds_read_b128 v[96:99], v5 offset:0\n"
"v_mfma_f32_16x16x32_bf16 v[64:67], a[224:227], v[112:115], v[64:67]\n"
"v_mfma_f32_16x16x32_bf16 v[68:71], a[228:231], v[112:115], v[68:71]\n"
"ds_read_b128 v[100:103], v5 offset:256\n"
"v_mfma_f32_16x16x32_bf16 v[80:83], a[232:235], v[112:115], v[80:83]\n"
"v_mfma_f32_16x16x32_bf16 v[84:87], a[236:239], v[112:115], v[84:87]\n"
"ds_read_b128 v[104:107], v5 offset:2048\n"
"v_mfma_f32_16x16x32_bf16 v[72:75], a[224:227], v[116:119], v[72:75]\n"
"v_mfma_f32_16x16x32_bf16 v[76:79], a[228:231], v[116:119], v[76:79]\n"
"v_mfma_f32_16x16x32_bf16 v[88:91], a[232:235], v[116:119], v[88:91]\n"
"ds_read_b128 v[108:111], v5 offset:2304\n"
"v_mfma_f32_16x16x32_bf16 v[92:95], a[236:239], v[116:119], v[92:95]\n"
"v_mfma_f32_16x16x32_bf16 v[64:67], a[240:243], v[120:123], v[64:67]\n"
"v_mfma_f32_16x16x32_bf16 v[68:71], a[244:247], v[120:123], v[68:71]\n"
"v_mfma_f32_16x16x32_bf16 v[80:83], a[248:251], v[120:123], v[80:83]\n"
"ds_read_b128 v[112:115], v5 offset:4096\n"
"v_mfma_f32_16x16x32_bf16 v[84:87], a[252:255], v[120:123], v[84:87]\n"
"v_mfma_f32_16x16x32_bf16 v[72:75], a[240:243], v[124:127], v[72:75]\n"
"s_add_u32 s68, s68, 40960\n"
"s_add_u32 s69, s69, 40960\n"
"v_mfma_f32_16x16x32_bf16 v[76:79], a[244:247], v[124:127], v[76:79]\n"
"s_cmp_eq_u32 s69, 122880\n"
"s_cselect_b32 s69, 0, s69\n"
"v_mfma_f32_16x16x32_bf16 v[88:91], a[248:251], v[124:127], v[88:91]\n"
"ds_read_b128 v[116:119], v5 offset:4352\n"
"v_mfma_f32_16x16x32_bf16 v[92:95], a[252:255], v[124:127], v[92:95]\n"
"s_add_u32 s71, s69, s50\n"
"s_cmp_eq_u32 s73, s79\n"
"s_cbranch_scc0 A7_%=\n"
"s_cmp_eq_u32 s73, 64\n"
"s_cbranch_scc1 A8_%=\n"
"s_mov_b32 s76, 131072\n"
"s_branch A10_%=\n"
"A9_%=:\n"
"s_sleep 2\n"
"A10_%=:\n"
"buffer_load_dword v15, v14, s[36:39], 0 offen sc1\n"
"s_sub_u32 s76, s76, 1\n"
"s_cbranch_scc1 AABORT_%=\n"
"s_waitcnt vmcnt(0)\n"
"v_cmp_gt_u32 vcc, 1, v15\n"
"s_nop 0\n"
"s_cbranch_vccnz A9_%=\n"
"s_mov_b32 s79, 64\n"
"s_branch A7_%=\n"
"A8_%=:\n"
"v_cvt_pk_bf16_f32 v128, v64, v65\n"
"v_cvt_pk_bf16_f32 v129, v66, v67\n"
"v_cvt_pk_bf16_f32 v130, v68, v69\n"
"v_cvt_pk_bf16_f32 v131, v70, v71\n"
"ds_write_b128 v13, v[128:131] offset:12288\n"
"v_cvt_pk_bf16_f32 v132, v72, v73\n"
"v_cvt_pk_bf16_f32 v133, v74, v75\n"
"v_cvt_pk_bf16_f32 v134, v76, v77\n"
"v_cvt_pk_bf16_f32 v135, v78, v79\n"
"ds_write_b128 v13, v[132:135] offset:12544\n"
"v_cvt_pk_bf16_f32 v136, v80, v81\n"
"v_cvt_pk_bf16_f32 v137, v82, v83\n"
"v_cvt_pk_bf16_f32 v138, v84, v85\n"
"v_cvt_pk_bf16_f32 v139, v86, v87\n"
"ds_write_b128 v13, v[136:139] offset:14336\n"
"v_cvt_pk_bf16_f32 v140, v88, v89\n"
"v_cvt_pk_bf16_f32 v141, v90, v91\n"
"v_cvt_pk_bf16_f32 v142, v92, v93\n"
"v_cvt_pk_bf16_f32 v143, v94, v95\n"
"ds_write_b128 v13, v[140:143] offset:14592\n"
"s_waitcnt lgkmcnt(0)\n"
"s_barrier\n"
"s_endpgm\n"
"A1_%=:\n"
"s_mov_b32 s74, 0x3dd53b94\n"
"v_mov_b32 v6, 0xff800000\n"
"v_add_u32 v5, 122880, v0\n"
"s_barrier\n"
"v_mov_b32 v28, 61440\n"
"ds_read_b32 v28, v28\n"
"s_waitcnt lgkmcnt(0)\n"
"v_readfirstlane_b32 s22, v28\n"
"s_nop 0\n"
"s_lshl_b32 s22, s22, 13\n"
"v_xor_b32 v17, s22, v17\n"
"v_xor_b32 v19, s22, v19\n"
"s_barrier\n"
"s_barrier\n"
"s_mov_b32 s73, 0\n"
"v_cmp_gt_i32 s[4:5], 0, v20\n"
"v_cmp_gt_i32 s[6:7], 1, v20\n"
"v_cmp_gt_i32 s[8:9], 2, v20\n"
"v_cmp_gt_i32 s[10:11], 3, v20\n"
"v_cmp_gt_i32 s[12:13], 8, v20\n"
"v_cmp_gt_i32 s[14:15], 9, v20\n"
"v_cmp_gt_i32 s[16:17], 10, v20\n"
"v_cmp_gt_i32 s[18:19], 11, v20\n"
"v_cmp_gt_i32 s[20:21], 16, v20\n"
"v_cmp_gt_i32 s[22:23], 17, v20\n"
"v_cmp_gt_i32 s[24:25], 18, v20\n"
"v_cmp_gt_i32 s[26:27], 19, v20\n"
"v_cmp_gt_i32 s[28:29], 24, v20\n"
"v_cmp_gt_i32 s[30:31], 25, v20\n"
"v_cmp_gt_i32 s[32:33], 26, v20\n"
"v_cmp_gt_i32 s[34:35], 27, v20\n"
"s_mov_b32 s76, 131072\n"
"s_branch A12_%=\n"
"A11_%=:\n"
"s_sleep 2\n"
"A12_%=:\n"
"buffer_load_dword v7, v17, s[36:39], 0 offen sc1\n"
"s_sub_u32 s76, s76, 1\n"
"s_cbranch_scc1 AABORT_%=\n"
"s_waitcnt vmcnt(0)\n"
"v_cmp_gt_u32 vcc, v18, v7\n"
"s_nop 0\n"
"s_cbranch_vccnz A11_%=\n"
"s_add_u32 s70, s52, 0\n"
"buffer_load_dwordx4 v[128:131], v0, s[36:39], s70 offen offset:0 sc1\n"
"buffer_load_dwordx4 v[132:135], v0, s[36:39], s70 offen offset:1024 sc1\n"
"buffer_load_dwordx4 v[136:139], v0, s[36:39], s70 offen offset:2048 sc1\n"
"buffer_load_dwordx4 v[140:143], v0, s[36:39], s70 offen offset:3072 sc1\n"
"s_add_u32 s70, s52, 4096\n"
"buffer_load_dwordx4 v[144:147], v0, s[36:39], s70 offen offset:0 sc1\n"
"buffer_load_dwordx4 v[148:151], v0, s[36:39], s70 offen offset:1024 sc1\n"
"buffer_load_dwordx4 v[152:155], v0, s[36:39], s70 offen offset:2048 sc1\n"
"buffer_load_dwordx4 v[156:159], v0, s[36:39], s70 offen offset:3072 sc1\n"
"s_add_u32 s70, s52, 8192\n"
"buffer_load_dwordx4 v[160:163], v0, s[36:39], s70 offen offset:0 sc1\n"
"buffer_load_dwordx4 v[164:167], v0, s[36:39], s70 offen offset:1024 sc1\n"
"buffer_load_dwordx4 v[168:171], v0, s[36:39], s70 offen offset:2048 sc1\n"
"buffer_load_dwordx4 v[172:175], v0, s[36:39], s70 offen offset:3072 sc1\n"
"s_add_u32 s52, s52, 393216\n"
"s_waitcnt vmcnt(0)\n"
"s_nop 0\n"
"A13_%=:\n"
"s_barrier\n"
"ds_read_b128 v[32:35], v5 offset:0\n"
"ds_read_b128 v[36:39], v5 offset:1024\n"
"ds_read_b128 v[40:43], v5 offset:2048\n"
"ds_read_b128 v[44:47], v5 offset:3072\n"
"ds_read_b128 v[48:51], v5 offset:4096\n"
"ds_read_b128 v[52:55], v5 offset:5120\n"
"ds_read_b128 v[56:59], v5 offset:6144\n"
"ds_read_b128 v[60:63], v5 offset:7168\n"
"ds_read_b128 v[64:67], v5 offset:8192\n"
"ds_read_b128 v[68:71], v5 offset:9216\n"
"ds_read_b128 v[72:75], v5 offset:10240\n"
"ds_read_b128 v[76:79], v5 offset:11264\n"
"v_mov_b32 v7, -1\n"
"s_mov_b32 exec_lo, 1\n"
"s_mov_b32 exec_hi, 7\n"
"v_mov_b32 v7, 0\n"
"buffer_atomic_add v7, v17, s[36:39], 0 offen sc0 sc1\n"
"s_mov_b64 exec, -1\n"
"s_waitcnt vmcnt(4)\n"
"s_waitcnt lgkmcnt(11)\n"
"v_mfma_f32_32x32x16_bf16 v[188:203], v[128:131], v[32:35], 0\n"
"s_waitcnt lgkmcnt(10)\n"
"v_mfma_f32_32x32x16_bf16 v[188:203], v[132:135], v[36:39], v[188:203]\n"
"s_waitcnt lgkmcnt(9)\n"
"v_mfma_f32_32x32x16_bf16 v[188:203], v[136:139], v[40:43], v[188:203]\n"
"s_waitcnt lgkmcnt(8)\n"
"v_mfma_f32_32x32x16_bf16 v[188:203], v[140:143], v[44:47], v[188:203]\n"
"s_waitcnt lgkmcnt(7)\n"
"v_mfma_f32_32x32x16_bf16 v[188:203], v[144:147], v[48:51], v[188:203]\n"
"s_waitcnt lgkmcnt(6)\n"
"v_mfma_f32_32x32x16_bf16 v[188:203], v[148:151], v[52:55], v[188:203]\n"
"s_waitcnt lgkmcnt(5)\n"
"v_mfma_f32_32x32x16_bf16 v[188:203], v[152:155], v[56:59], v[188:203]\n"
"s_waitcnt lgkmcnt(4)\n"
"v_mfma_f32_32x32x16_bf16 v[188:203], v[156:159], v[60:63], v[188:203]\n"
"s_waitcnt lgkmcnt(3)\n"
"v_mfma_f32_32x32x16_bf16 v[188:203], v[160:163], v[64:67], v[188:203]\n"
"s_waitcnt lgkmcnt(2)\n"
"v_mfma_f32_32x32x16_bf16 v[188:203], v[164:167], v[68:71], v[188:203]\n"
"s_waitcnt lgkmcnt(1)\n"
"v_mfma_f32_32x32x16_bf16 v[188:203], v[168:171], v[72:75], v[188:203]\n"
"s_waitcnt lgkmcnt(0)\n"
"v_mfma_f32_32x32x16_bf16 v[188:203], v[172:175], v[76:79], v[188:203]\n"
"s_waitcnt vmcnt(0)\n"
"v_mov_b32 v14, s73\n"
"buffer_store_dword v14, v19, s[36:39], 0 offen sc1\n"
"s_add_u32 s73, s73, 1\n"
"v_add_u32 v13, s73, v18\n"
"v_min_u32 v13, 64, v13\n"
"v_cmp_gt_u32 vcc, v13, v7\n"
"s_nop 0\n"
"s_cbranch_vccz A14_%=\n"
"s_mov_b32 s76, 131072\n"
"s_mov_b32 exec_lo, 1\n"
"s_mov_b32 exec_hi, 7\n"
"s_mov_b32 s79, 0\n"
"A15_%=:\n"
"s_sleep 1\n"
"v_mov_b32 v7, 0\n"
"buffer_atomic_add v7, v17, s[36:39], s79 offen sc0 sc1\n"
"s_sub_u32 s76, s76, 1\n"
"s_cbranch_scc1 AABORT_%=\n"
"s_waitcnt vmcnt(0)\n"
"v_cmp_gt_u32 vcc, v13, v7\n"
"s_nop 0\n"
"s_cbranch_vccnz A15_%=\n"
"s_mov_b64 exec, -1\n"
"A14_%=:\n"
"buffer_load_dwordx4 v[176:179], v16, s[36:39], s53 offen offset:0 sc1\n"
"buffer_load_dwordx4 v[180:183], v16, s[36:39], s53 offen offset:1024 sc1\n"
"buffer_load_dwordx4 v[184:187], v16, s[36:39], s53 offen offset:2048 sc1\n"
"s_add_u32 s70, s52, 0\n"
"buffer_load_dwordx4 v[128:131], v0, s[36:39], s70 offen offset:0 sc1\n"
"buffer_load_dwordx4 v[132:135], v0, s[36:39], s70 offen offset:1024 sc1\n"
"buffer_load_dwordx4 v[136:139], v0, s[36:39], s70 offen offset:2048 sc1\n"
"buffer_load_dwordx4 v[140:143], v0, s[36:39], s70 offen offset:3072 sc1\n"
"s_add_u32 s70, s52, 4096\n"
"buffer_load_dwordx4 v[144:147], v0, s[36:39], s70 offen offset:0 sc1\n"
"buffer_load_dwordx4 v[148:151], v0, s[36:39], s70 offen offset:1024 sc1\n"
"buffer_load_dwordx4 v[152:155], v0, s[36:39], s70 offen offset:2048 sc1\n"
"buffer_load_dwordx4 v[156:159], v0, s[36:39], s70 offen offset:3072 sc1\n"
"s_add_u32 s70, s52, 8192\n"
"buffer_load_dwordx4 v[160:163], v0, s[36:39], s70 offen offset:0 sc1\n"
"buffer_load_dwordx4 v[164:167], v0, s[36:39], s70 offen offset:1024 sc1\n"
"buffer_load_dwordx4 v[168:171], v0, s[36:39], s70 offen offset:2048 sc1\n"
"buffer_load_dwordx4 v[172:175], v0, s[36:39], s70 offen offset:3072 sc1\n"
"s_add_u32 s52, s52, 393216\n"
"v_cndmask_b32 v188, v188, v6, s[4:5]\n"
"v_cndmask_b32 v189, v189, v6, s[6:7]\n"
"v_cndmask_b32 v190, v190, v6, s[8:9]\n"
"v_cndmask_b32 v191, v191, v6, s[10:11]\n"
"v_cndmask_b32 v192, v192, v6, s[12:13]\n"
"v_cndmask_b32 v193, v193, v6, s[14:15]\n"
"v_cndmask_b32 v194, v194, v6, s[16:17]\n"
"v_cndmask_b32 v195, v195, v6, s[18:19]\n"
"v_cndmask_b32 v196, v196, v6, s[20:21]\n"
"v_cndmask_b32 v197, v197, v6, s[22:23]\n"
"v_cndmask_b32 v198, v198, v6, s[24:25]\n"
"v_cndmask_b32 v199, v199, v6, s[26:27]\n"
"v_cndmask_b32 v200, v200, v6, s[28:29]\n"
"v_cndmask_b32 v201, v201, v6, s[30:31]\n"
"v_cndmask_b32 v202, v202, v6, s[32:33]\n"
"v_cndmask_b32 v203, v203, v6, s[34:35]\n"
"v_max_f32 v8, v188, v189\n"
"v_max3_f32 v8, v8, v190, v191\n"
"v_max3_f32 v8, v8, v192, v193\n"
"v_max3_f32 v8, v8, v194, v195\n"
"v_max3_f32 v8, v8, v196, v197\n"
"v_max3_f32 v8, v8, v198, v199\n"
"v_max3_f32 v8, v8, v200, v201\n"
"v_max3_f32 v8, v8, v202, v203\n"
"v_mov_b32 v9, v8\n"
"s_nop 1\n"
"v_permlane32_swap_b32 v8, v9\n"
"v_max_f32 v8, v8, v9\n"
"v_mul_f32 v9, s74, v8\n"
"v_fma_f32 v188, v188, s74, -v9\n"
"v_fma_f32 v189, v189, s74, -v9\n"
"v_fma_f32 v190, v190, s74, -v9\n"
"v_fma_f32 v191, v191, s74, -v9\n"
"v_fma_f32 v192, v192, s74, -v9\n"
"v_fma_f32 v193, v193, s74, -v9\n"
"v_fma_f32 v194, v194, s74, -v9\n"
"v_fma_f32 v195, v195, s74, -v9\n"
"v_fma_f32 v196, v196, s74, -v9\n"
"v_fma_f32 v197, v197, s74, -v9\n"
"v_fma_f32 v198, v198, s74, -v9\n"
"v_fma_f32 v199, v199, s74, -v9\n"
"v_fma_f32 v200, v200, s74, -v9\n"
"v_fma_f32 v201, v201, s74, -v9\n"
"v_fma_f32 v202, v202, s74, -v9\n"
"v_fma_f32 v203, v203, s74, -v9\n"
"v_exp_f32 v188, v188\n"
"v_exp_f32 v189, v189\n"
"v_exp_f32 v190, v190\n"
"v_exp_f32 v191, v191\n"
"v_exp_f32 v192, v192\n"
"v_exp_f32 v193, v193\n"
"v_exp_f32 v194, v194\n"
"v_exp_f32 v195, v195\n"
"v_exp_f32 v196, v196\n"
"v_exp_f32 v197, v197\n"
"v_exp_f32 v198, v198\n"
"v_exp_f32 v199, v199\n"
"v_exp_f32 v200, v200\n"
"v_exp_f32 v201, v201\n"
"v_exp_f32 v202, v202\n"
"v_exp_f32 v203, v203\n"
"v_add_f32 v204, v188, v192\n"
"v_add_f32 v205, v189, v193\n"
"v_add_f32 v206, v190, v194\n"
"v_add_f32 v207, v191, v195\n"
"v_add_f32 v204, v204, v196\n"
"v_add_f32 v205, v205, v197\n"
"v_add_f32 v206, v206, v198\n"
"v_add_f32 v207, v207, v199\n"
"v_add_f32 v204, v204, v200\n"
"v_add_f32 v205, v205, v201\n"
"v_add_f32 v206, v206, v202\n"
"v_add_f32 v207, v207, v203\n"
"v_add_f32 v204, v204, v205\n"
"v_add_f32 v206, v206, v207\n"
"v_add_f32 v15, v204, v206\n"
"s_mov_b32 s76, 131072\n"
"v_mov_b32 v14, s73\n"
"s_cmp_gt_u32 s73, 62\n"
"s_cselect_b32 s72, 4096, s38\n"
"s_waitcnt vmcnt(12)\n"
"A16_%=:\n"
"v_max3_u32 v9, v176, v177, 0\n"
"v_max3_u32 v9, v178, v179, v9\n"
"v_max3_u32 v9, v180, v181, v9\n"
"v_max3_u32 v9, v182, v183, v9\n"
"v_max3_u32 v9, v184, v185, v9\n"
"v_max3_u32 v9, v186, v187, v9\n"
"v_cmp_eq_u32 vcc, -1, v9\n"
"s_nop 0\n"
"s_cbranch_vccz A17_%=\n"
"buffer_load_dwordx4 v[176:179], v16, s[36:39], s53 offen offset:0 sc1\n"
"buffer_load_dwordx4 v[180:183], v16, s[36:39], s53 offen offset:1024 sc1\n"
"buffer_load_dwordx4 v[184:187], v16, s[36:39], s53 offen offset:2048 sc1\n"
"s_sub_u32 s76, s76, 1\n"
"s_cbranch_scc1 AABORT_%=\n"
"s_waitcnt vmcnt(0)\n"
"s_branch A16_%=\n"
"A17_%=:\n"
"buffer_store_dword v14, v19, s[36:39], s72 offen sc1\n"
"s_add_u32 s53, s53, 98304\n"
"v_mfma_f32_32x32x16_bf16 a[0:15], v[176:179], v[32:35], 0\n"
"v_mfma_f32_32x32x16_bf16 a[16:31], v[176:179], v[36:39], 0\n"
"v_mfma_f32_32x32x16_bf16 a[32:47], v[176:179], v[40:43], 0\n"
"v_mfma_f32_32x32x16_bf16 a[48:63], v[176:179], v[44:47], 0\n"
"v_mfma_f32_32x32x16_bf16 a[0:15], v[180:183], v[48:51], a[0:15]\n"
"v_mfma_f32_32x32x16_bf16 a[16:31], v[180:183], v[52:55], a[16:31]\n"
"v_mfma_f32_32x32x16_bf16 a[32:47], v[180:183], v[56:59], a[32:47]\n"
"v_mfma_f32_32x32x16_bf16 a[48:63], v[180:183], v[60:63], a[48:63]\n"
"v_mfma_f32_32x32x16_bf16 a[0:15], v[184:187], v[64:67], a[0:15]\n"
"v_mfma_f32_32x32x16_bf16 a[16:31], v[184:187], v[68:71], a[16:31]\n"
"v_mfma_f32_32x32x16_bf16 a[32:47], v[184:187], v[72:75], a[32:47]\n"
"v_mfma_f32_32x32x16_bf16 a[48:63], v[184:187], v[76:79], a[48:63]\n"
"s_nop 8\n"
"v_accvgpr_read_b32 v204, a0\n"
"v_accvgpr_read_b32 v205, a1\n"
"v_accvgpr_read_b32 v206, a2\n"
"v_accvgpr_read_b32 v207, a3\n"
"v_accvgpr_read_b32 v208, a20\n"
"v_accvgpr_read_b32 v209, a21\n"
"v_accvgpr_read_b32 v210, a22\n"
"v_accvgpr_read_b32 v211, a23\n"
"v_accvgpr_read_b32 v212, a40\n"
"v_accvgpr_read_b32 v213, a41\n"
"v_accvgpr_read_b32 v214, a42\n"
"v_accvgpr_read_b32 v215, a43\n"
"v_accvgpr_read_b32 v216, a60\n"
"v_accvgpr_read_b32 v217, a61\n"
"v_accvgpr_read_b32 v218, a62\n"
"v_accvgpr_read_b32 v219, a63\n"
"v_add_f32 v204, v204, v208\n"
"v_add_f32 v212, v212, v216\n"
"v_add_f32 v205, v205, v209\n"
"v_add_f32 v213, v213, v217\n"
"v_add_f32 v206, v206, v210\n"
"v_add_f32 v214, v214, v218\n"
"v_add_f32 v207, v207, v211\n"
"v_add_f32 v215, v215, v219\n"
"v_add_f32 v204, v204, v212\n"
"v_add_f32 v205, v205, v213\n"
"v_add_f32 v206, v206, v214\n"
"v_add_f32 v207, v207, v215\n"
"v_max3_f32 v9, v8, v204, v205\n"
"v_max3_f32 v10, v9, v206, v207\n"
"v_max3_f32 v9, v9, v206, v207\n"
"s_nop 1\n"
"v_permlane32_swap_b32 v9, v10\n"
"v_max_f32 v9, v9, v10\n"
"v_mul_f32 v9, s74, v9\n"
"v_fma_f32 v204, v204, s74, -v9\n"
"v_fma_f32 v205, v205, s74, -v9\n"
"v_fma_f32 v206, v206, s74, -v9\n"
"v_fma_f32 v207, v207, s74, -v9\n"
"v_fma_f32 v8, v8, s74, -v9\n"
"v_exp_f32 v204, v204\n"
"v_exp_f32 v205, v205\n"
"v_exp_f32 v206, v206\n"
"v_exp_f32 v207, v207\n"
"v_exp_f32 v8, v8\n"
"v_add_f32 v9, v204, v205\n"
"v_add_f32 v10, v206, v207\n"
"v_fma_f32 v9, v8, v15, v9\n"
"v_add_f32 v11, v9, v10\n"
"v_add_f32 v9, v9, v10\n"
"s_nop 1\n"
"v_permlane32_swap_b32 v9, v11\n"
"v_add_f32 v9, v9, v11\n"
"v_rcp_f32 v9, v9\n"
"s_nop 0\n"
"v_mul_f32 v8, v8, v9\n"
"v_mul_f32 v188, v188, v8\n"
"v_mul_f32 v189, v189, v8\n"
"v_mul_f32 v190, v190, v8\n"
"v_mul_f32 v191, v191, v8\n"
"v_mul_f32 v192, v192, v8\n"
"v_mul_f32 v193, v193, v8\n"
"v_mul_f32 v194, v194, v8\n"
"v_mul_f32 v195, v195, v8\n"
"v_mul_f32 v196, v196, v8\n"
"v_mul_f32 v197, v197, v8\n"
"v_mul_f32 v198, v198, v8\n"
"v_mul_f32 v199, v199, v8\n"
"v_mul_f32 v200, v200, v8\n"
"v_mul_f32 v201, v201, v8\n"
"v_mul_f32 v202, v202, v8\n"
"v_mul_f32 v203, v203, v8\n"
"v_mul_f32 v204, v204, v9\n"
"v_mul_f32 v205, v205, v9\n"
"v_mul_f32 v206, v206, v9\n"
"v_mul_f32 v207, v207, v9\n"
"v_cvt_pk_bf16_f32 v208, v188, v189\n"
"v_cvt_pk_bf16_f32 v209, v190, v191\n"
"v_cvt_pk_bf16_f32 v210, v192, v193\n"
"v_cvt_pk_bf16_f32 v211, v194, v195\n"
"v_cvt_pk_bf16_f32 v212, v196, v197\n"
"v_cvt_pk_bf16_f32 v213, v198, v199\n"
"v_cvt_pk_bf16_f32 v214, v200, v201\n"
"v_cvt_pk_bf16_f32 v215, v202, v203\n"
"v_cvt_pk_bf16_f32 v216, v204, v205\n"
"v_cvt_pk_bf16_f32 v217, v206, v207\n"
"v_min_u32 v208, -2, v208\n"
"v_min_u32 v209, -2, v209\n"
"v_min_u32 v210, -2, v210\n"
"v_min_u32 v211, -2, v211\n"
"v_min_u32 v212, -2, v212\n"
"v_min_u32 v213, -2, v213\n"
"v_min_u32 v214, -2, v214\n"
"v_min_u32 v215, -2, v215\n"
"v_min_u32 v216, -2, v216\n"
"v_min_u32 v217, -2, v217\n"
"buffer_store_dwordx4 v[208:211], v0, s[36:39], s54 offen offset:0 sc1\n"
"buffer_store_dwordx4 v[212:215], v0, s[36:39], s54 offen offset:1024 sc1\n"
"buffer_store_dwordx2 v[216:217], v21, s[36:39], s55 offen sc1\n"
"s_add_u32 s54, s54, 65536\n"
"s_add_u32 s55, s55, 16384\n"
"s_barrier\n"
"ds_read_b128 v[32:35], v5 offset:12288\n"
"ds_read_b128 v[36:39], v5 offset:13312\n"
"ds_read_b128 v[40:43], v5 offset:14336\n"
"ds_read_b128 v[44:47], v5 offset:15360\n"
"ds_read_b128 v[48:51], v5 offset:16384\n"
"ds_read_b128 v[52:55], v5 offset:17408\n"
"ds_read_b128 v[56:59], v5 offset:18432\n"
"ds_read_b128 v[60:63], v5 offset:19456\n"
"ds_read_b128 v[64:67], v5 offset:20480\n"
"ds_read_b128 v[68:71], v5 offset:21504\n"
"ds_read_b128 v[72:75], v5 offset:22528\n"
"ds_read_b128 v[76:79], v5 offset:23552\n"
"v_mov_b32 v7, -1\n"
"s_mov_b32 exec_lo, 1\n"
"s_mov_b32 exec_hi, 7\n"
"v_mov_b32 v7, 0\n"
"buffer_atomic_add v7, v17, s[36:39], 0 offen sc0 sc1\n"
"s_mov_b64 exec, -1\n"
"s_waitcnt vmcnt(4)\n"
"s_waitcnt lgkmcnt(11)\n"
"v_mfma_f32_32x32x16_bf16 v[188:203], v[128:131], v[32:35], 0\n"
"s_waitcnt lgkmcnt(10)\n"
"v_mfma_f32_32x32x16_bf16 v[188:203], v[132:135], v[36:39], v[188:203]\n"
"s_waitcnt lgkmcnt(9)\n"
"v_mfma_f32_32x32x16_bf16 v[188:203], v[136:139], v[40:43], v[188:203]\n"
"s_waitcnt lgkmcnt(8)\n"
"v_mfma_f32_32x32x16_bf16 v[188:203], v[140:143], v[44:47], v[188:203]\n"
"s_waitcnt lgkmcnt(7)\n"
"v_mfma_f32_32x32x16_bf16 v[188:203], v[144:147], v[48:51], v[188:203]\n"
"s_waitcnt lgkmcnt(6)\n"
"v_mfma_f32_32x32x16_bf16 v[188:203], v[148:151], v[52:55], v[188:203]\n"
"s_waitcnt lgkmcnt(5)\n"
"v_mfma_f32_32x32x16_bf16 v[188:203], v[152:155], v[56:59], v[188:203]\n"
"s_waitcnt lgkmcnt(4)\n"
"v_mfma_f32_32x32x16_bf16 v[188:203], v[156:159], v[60:63], v[188:203]\n"
"s_waitcnt lgkmcnt(3)\n"
"v_mfma_f32_32x32x16_bf16 v[188:203], v[160:163], v[64:67], v[188:203]\n"
"s_waitcnt lgkmcnt(2)\n"
"v_mfma_f32_32x32x16_bf16 v[188:203], v[164:167], v[68:71], v[188:203]\n"
"s_waitcnt lgkmcnt(1)\n"
"v_mfma_f32_32x32x16_bf16 v[188:203], v[168:171], v[72:75], v[188:203]\n"
"s_waitcnt lgkmcnt(0)\n"
"v_mfma_f32_32x32x16_bf16 v[188:203], v[172:175], v[76:79], v[188:203]\n"
"s_waitcnt vmcnt(0)\n"
"v_mov_b32 v14, s73\n"
"buffer_store_dword v14, v19, s[36:39], 0 offen sc1\n"
"s_add_u32 s73, s73, 1\n"
"v_add_u32 v13, s73, v18\n"
"v_min_u32 v13, 64, v13\n"
"v_cmp_gt_u32 vcc, v13, v7\n"
"s_nop 0\n"
"s_cbranch_vccz A18_%=\n"
"s_mov_b32 s76, 131072\n"
"s_mov_b32 exec_lo, 1\n"
"s_mov_b32 exec_hi, 7\n"
"s_mov_b32 s79, 0\n"
"A19_%=:\n"
"s_sleep 1\n"
"v_mov_b32 v7, 0\n"
"buffer_atomic_add v7, v17, s[36:39], s79 offen sc0 sc1\n"
"s_sub_u32 s76, s76, 1\n"
"s_cbranch_scc1 AABORT_%=\n"
"s_waitcnt vmcnt(0)\n"
"v_cmp_gt_u32 vcc, v13, v7\n"
"s_nop 0\n"
"s_cbranch_vccnz A19_%=\n"
"s_mov_b64 exec, -1\n"
"A18_%=:\n"
"buffer_load_dwordx4 v[176:179], v16, s[36:39], s53 offen offset:0 sc1\n"
"buffer_load_dwordx4 v[180:183], v16, s[36:39], s53 offen offset:1024 sc1\n"
"buffer_load_dwordx4 v[184:187], v16, s[36:39], s53 offen offset:2048 sc1\n"
"s_add_u32 s70, s52, 0\n"
"buffer_load_dwordx4 v[128:131], v0, s[36:39], s70 offen offset:0 sc1\n"
"buffer_load_dwordx4 v[132:135], v0, s[36:39], s70 offen offset:1024 sc1\n"
"buffer_load_dwordx4 v[136:139], v0, s[36:39], s70 offen offset:2048 sc1\n"
"buffer_load_dwordx4 v[140:143], v0, s[36:39], s70 offen offset:3072 sc1\n"
"s_add_u32 s70, s52, 4096\n"
"buffer_load_dwordx4 v[144:147], v0, s[36:39], s70 offen offset:0 sc1\n"
"buffer_load_dwordx4 v[148:151], v0, s[36:39], s70 offen offset:1024 sc1\n"
"buffer_load_dwordx4 v[152:155], v0, s[36:39], s70 offen offset:2048 sc1\n"
"buffer_load_dwordx4 v[156:159], v0, s[36:39], s70 offen offset:3072 sc1\n"
"s_add_u32 s70, s52, 8192\n"
"buffer_load_dwordx4 v[160:163], v0, s[36:39], s70 offen offset:0 sc1\n"
"buffer_load_dwordx4 v[164:167], v0, s[36:39], s70 offen offset:1024 sc1\n"
"buffer_load_dwordx4 v[168:171], v0, s[36:39], s70 offen offset:2048 sc1\n"
"buffer_load_dwordx4 v[172:175], v0, s[36:39], s70 offen offset:3072 sc1\n"
"s_add_u32 s52, s52, 393216\n"
"v_cndmask_b32 v188, v188, v6, s[4:5]\n"
"v_cndmask_b32 v189, v189, v6, s[6:7]\n"
"v_cndmask_b32 v190, v190, v6, s[8:9]\n"
"v_cndmask_b32 v191, v191, v6, s[10:11]\n"
"v_cndmask_b32 v192, v192, v6, s[12:13]\n"
"v_cndmask_b32 v193, v193, v6, s[14:15]\n"
"v_cndmask_b32 v194, v194, v6, s[16:17]\n"
"v_cndmask_b32 v195, v195, v6, s[18:19]\n"
"v_cndmask_b32 v196, v196, v6, s[20:21]\n"
"v_cndmask_b32 v197, v197, v6, s[22:23]\n"
"v_cndmask_b32 v198, v198, v6, s[24:25]\n"
"v_cndmask_b32 v199, v199, v6, s[26:27]\n"
"v_cndmask_b32 v200, v200, v6, s[28:29]\n"
"v_cndmask_b32 v201, v201, v6, s[30:31]\n"
"v_cndmask_b32 v202, v202, v6, s[32:33]\n"
"v_cndmask_b32 v203, v203, v6, s[34:35]\n"
"v_max_f32 v8, v188, v189\n"
"v_max3_f32 v8, v8, v190, v191\n"
"v_max3_f32 v8, v8, v192, v193\n"
"v_max3_f32 v8, v8, v194, v195\n"
"v_max3_f32 v8, v8, v196, v197\n"
"v_max3_f32 v8, v8, v198, v199\n"
"v_max3_f32 v8, v8, v200, v201\n"
"v_max3_f32 v8, v8, v202, v203\n"
"v_mov_b32 v9, v8\n"
"s_nop 1\n"
"v_permlane32_swap_b32 v8, v9\n"
"v_max_f32 v8, v8, v9\n"
"v_mul_f32 v9, s74, v8\n"
"v_fma_f32 v188, v188, s74, -v9\n"
"v_fma_f32 v189, v189, s74, -v9\n"
"v_fma_f32 v190, v190, s74, -v9\n"
"v_fma_f32 v191, v191, s74, -v9\n"
"v_fma_f32 v192, v192, s74, -v9\n"
"v_fma_f32 v193, v193, s74, -v9\n"
"v_fma_f32 v194, v194, s74, -v9\n"
"v_fma_f32 v195, v195, s74, -v9\n"
"v_fma_f32 v196, v196, s74, -v9\n"
"v_fma_f32 v197, v197, s74, -v9\n"
"v_fma_f32 v198, v198, s74, -v9\n"
"v_fma_f32 v199, v199, s74, -v9\n"
"v_fma_f32 v200, v200, s74, -v9\n"
"v_fma_f32 v201, v201, s74, -v9\n"
"v_fma_f32 v202, v202, s74, -v9\n"
"v_fma_f32 v203, v203, s74, -v9\n"
"v_exp_f32 v188, v188\n"
"v_exp_f32 v189, v189\n"
"v_exp_f32 v190, v190\n"
"v_exp_f32 v191, v191\n"
"v_exp_f32 v192, v192\n"
"v_exp_f32 v193, v193\n"
"v_exp_f32 v194, v194\n"
"v_exp_f32 v195, v195\n"
"v_exp_f32 v196, v196\n"
"v_exp_f32 v197, v197\n"
"v_exp_f32 v198, v198\n"
"v_exp_f32 v199, v199\n"
"v_exp_f32 v200, v200\n"
"v_exp_f32 v201, v201\n"
"v_exp_f32 v202, v202\n"
"v_exp_f32 v203, v203\n"
"v_add_f32 v204, v188, v192\n"
"v_add_f32 v205, v189, v193\n"
"v_add_f32 v206, v190, v194\n"
"v_add_f32 v207, v191, v195\n"
"v_add_f32 v204, v204, v196\n"
"v_add_f32 v205, v205, v197\n"
"v_add_f32 v206, v206, v198\n"
"v_add_f32 v207, v207, v199\n"
"v_add_f32 v204, v204, v200\n"
"v_add_f32 v205, v205, v201\n"
"v_add_f32 v206, v206, v202\n"
"v_add_f32 v207, v207, v203\n"
"v_add_f32 v204, v204, v205\n"
"v_add_f32 v206, v206, v207\n"
"v_add_f32 v15, v204, v206\n"
"s_mov_b32 s76, 131072\n"
"v_mov_b32 v14, s73\n"
"s_cmp_gt_u32 s73, 62\n"
"s_cselect_b32 s72, 4096, s38\n"
"s_waitcnt vmcnt(12)\n"
"A20_%=:\n"
"v_max3_u32 v9, v176, v177, 0\n"
"v_max3_u32 v9, v178, v179, v9\n"
"v_max3_u32 v9, v180, v181, v9\n"
"v_max3_u32 v9, v182, v183, v9\n"
"v_max3_u32 v9, v184, v185, v9\n"
"v_max3_u32 v9, v186, v187, v9\n"
"v_cmp_eq_u32 vcc, -1, v9\n"
"s_nop 0\n"
"s_cbranch_vccz A21_%=\n"
"buffer_load_dwordx4 v[176:179], v16, s[36:39], s53 offen offset:0 sc1\n"
"buffer_load_dwordx4 v[180:183], v16, s[36:39], s53 offen offset:1024 sc1\n"
"buffer_load_dwordx4 v[184:187], v16, s[36:39], s53 offen offset:2048 sc1\n"
"s_sub_u32 s76, s76, 1\n"
"s_cbranch_scc1 AABORT_%=\n"
"s_waitcnt vmcnt(0)\n"
"s_branch A20_%=\n"
"A21_%=:\n"
"buffer_store_dword v14, v19, s[36:39], s72 offen sc1\n"
"s_add_u32 s53, s53, 98304\n"
"v_mfma_f32_32x32x16_bf16 a[0:15], v[176:179], v[32:35], 0\n"
"v_mfma_f32_32x32x16_bf16 a[16:31], v[176:179], v[36:39], 0\n"
"v_mfma_f32_32x32x16_bf16 a[32:47], v[176:179], v[40:43], 0\n"
"v_mfma_f32_32x32x16_bf16 a[48:63], v[176:179], v[44:47], 0\n"
"v_mfma_f32_32x32x16_bf16 a[0:15], v[180:183], v[48:51], a[0:15]\n"
"v_mfma_f32_32x32x16_bf16 a[16:31], v[180:183], v[52:55], a[16:31]\n"
"v_mfma_f32_32x32x16_bf16 a[32:47], v[180:183], v[56:59], a[32:47]\n"
"v_mfma_f32_32x32x16_bf16 a[48:63], v[180:183], v[60:63], a[48:63]\n"
"v_mfma_f32_32x32x16_bf16 a[0:15], v[184:187], v[64:67], a[0:15]\n"
"v_mfma_f32_32x32x16_bf16 a[16:31], v[184:187], v[68:71], a[16:31]\n"
"v_mfma_f32_32x32x16_bf16 a[32:47], v[184:187], v[72:75], a[32:47]\n"
"v_mfma_f32_32x32x16_bf16 a[48:63], v[184:187], v[76:79], a[48:63]\n"
"s_nop 8\n"
"v_accvgpr_read_b32 v204, a0\n"
"v_accvgpr_read_b32 v205, a1\n"
"v_accvgpr_read_b32 v206, a2\n"
"v_accvgpr_read_b32 v207, a3\n"
"v_accvgpr_read_b32 v208, a20\n"
"v_accvgpr_read_b32 v209, a21\n"
"v_accvgpr_read_b32 v210, a22\n"
"v_accvgpr_read_b32 v211, a23\n"
"v_accvgpr_read_b32 v212, a40\n"
"v_accvgpr_read_b32 v213, a41\n"
"v_accvgpr_read_b32 v214, a42\n"
"v_accvgpr_read_b32 v215, a43\n"
"v_accvgpr_read_b32 v216, a60\n"
"v_accvgpr_read_b32 v217, a61\n"
"v_accvgpr_read_b32 v218, a62\n"
"v_accvgpr_read_b32 v219, a63\n"
"v_add_f32 v204, v204, v208\n"
"v_add_f32 v212, v212, v216\n"
"v_add_f32 v205, v205, v209\n"
"v_add_f32 v213, v213, v217\n"
"v_add_f32 v206, v206, v210\n"
"v_add_f32 v214, v214, v218\n"
"v_add_f32 v207, v207, v211\n"
"v_add_f32 v215, v215, v219\n"
"v_add_f32 v204, v204, v212\n"
"v_add_f32 v205, v205, v213\n"
"v_add_f32 v206, v206, v214\n"
"v_add_f32 v207, v207, v215\n"
"v_max3_f32 v9, v8, v204, v205\n"
"v_max3_f32 v10, v9, v206, v207\n"
"v_max3_f32 v9, v9, v206, v207\n"
"s_nop 1\n"
"v_permlane32_swap_b32 v9, v10\n"
"v_max_f32 v9, v9, v10\n"
"v_mul_f32 v9, s74, v9\n"
"v_fma_f32 v204, v204, s74, -v9\n"
"v_fma_f32 v205, v205, s74, -v9\n"
"v_fma_f32 v206, v206, s74, -v9\n"
"v_fma_f32 v207, v207, s74, -v9\n"
"v_fma_f32 v8, v8, s74, -v9\n"
"v_exp_f32 v204, v204\n"
"v_exp_f32 v205, v205\n"
"v_exp_f32 v206, v206\n"
"v_exp_f32 v207, v207\n"
"v_exp_f32 v8, v8\n"
"v_add_f32 v9, v204, v205\n"
"v_add_f32 v10, v206, v207\n"
"v_fma_f32 v9, v8, v15, v9\n"
"v_add_f32 v11, v9, v10\n"
"v_add_f32 v9, v9, v10\n"
"s_nop 1\n"
"v_permlane32_swap_b32 v9, v11\n"
"v_add_f32 v9, v9, v11\n"
"v_rcp_f32 v9, v9\n"
"s_nop 0\n"
"v_mul_f32 v8, v8, v9\n"
"v_mul_f32 v188, v188, v8\n"
"v_mul_f32 v189, v189, v8\n"
"v_mul_f32 v190, v190, v8\n"
"v_mul_f32 v191, v191, v8\n"
"v_mul_f32 v192, v192, v8\n"
"v_mul_f32 v193, v193, v8\n"
"v_mul_f32 v194, v194, v8\n"
"v_mul_f32 v195, v195, v8\n"
"v_mul_f32 v196, v196, v8\n"
"v_mul_f32 v197, v197, v8\n"
"v_mul_f32 v198, v198, v8\n"
"v_mul_f32 v199, v199, v8\n"
"v_mul_f32 v200, v200, v8\n"
"v_mul_f32 v201, v201, v8\n"
"v_mul_f32 v202, v202, v8\n"
"v_mul_f32 v203, v203, v8\n"
"v_mul_f32 v204, v204, v9\n"
"v_mul_f32 v205, v205, v9\n"
"v_mul_f32 v206, v206, v9\n"
"v_mul_f32 v207, v207, v9\n"
"v_cvt_pk_bf16_f32 v208, v188, v189\n"
"v_cvt_pk_bf16_f32 v209, v190, v191\n"
"v_cvt_pk_bf16_f32 v210, v192, v193\n"
"v_cvt_pk_bf16_f32 v211, v194, v195\n"
"v_cvt_pk_bf16_f32 v212, v196, v197\n"
"v_cvt_pk_bf16_f32 v213, v198, v199\n"
"v_cvt_pk_bf16_f32 v214, v200, v201\n"
"v_cvt_pk_bf16_f32 v215, v202, v203\n"
"v_cvt_pk_bf16_f32 v216, v204, v205\n"
"v_cvt_pk_bf16_f32 v217, v206, v207\n"
"v_min_u32 v208, -2, v208\n"
"v_min_u32 v209, -2, v209\n"
"v_min_u32 v210, -2, v210\n"
"v_min_u32 v211, -2, v211\n"
"v_min_u32 v212, -2, v212\n"
"v_min_u32 v213, -2, v213\n"
"v_min_u32 v214, -2, v214\n"
"v_min_u32 v215, -2, v215\n"
"v_min_u32 v216, -2, v216\n"
"v_min_u32 v217, -2, v217\n"
"buffer_store_dwordx4 v[208:211], v0, s[36:39], s54 offen offset:0 sc1\n"
"buffer_store_dwordx4 v[212:215], v0, s[36:39], s54 offen offset:1024 sc1\n"
"buffer_store_dwordx2 v[216:217], v21, s[36:39], s55 offen sc1\n"
"s_add_u32 s54, s54, 65536\n"
"s_add_u32 s55, s55, 16384\n"
"s_cmp_lt_u32 s73, 64\n"
"s_cbranch_scc1 A13_%=\n"
"s_waitcnt vmcnt(0)\n"
"v_mov_b32 v14, s73\n"
"buffer_store_dword v14, v19, s[36:39], 0 offen sc1\n"
"s_endpgm\n"
"AABORT_%=:\n"
"s_mov_b64 exec, -1\n"
"s_nop 7\n"
"s_nop 7\n"
"v_mov_b32 v0, 46268544\n"
"v_mov_b32 v1, 1\n"
"buffer_store_dword v1, v0, s[36:39], 0 offen sc1\n"
"s_endpgm\n");
        }
    } else {
        setsrd(sa, 4, w < 2 ? P->wg : P->wu, DM * FF * 2);
        sa[8] = (cu * 128 + (w & 1) * 64) * 2; sa[9] = 8 * FF * 2;
        sa[11] = O_HF + (4 * cu + w) * 2048; sa[12] = 48 * 2048;
        va[1] = r8 * (FF * 2) + ((c8 ^ ((r8 >> 1) & 1)) * 16);
        va[2] = (w >> 1) * 8192 + trg(w & 1, g, q, p); va[3] = va[2] + 16384;
        RUN("s_nop 4\n"
"v_xor_b32 v12, 64, v1\n"
"s_mov_b32 s68, s49\n"
"s_mov_b32 s69, 0\n"
"s_mov_b32 s71, s50\n"
"s_sleep 37\n"
"s_sleep 37\n"
"s_sleep 37\n"
"s_sleep 37\n"
"s_mov_b32 s22, 46301184\n"
"s_mov_b32 s23, 46317568\n"
"s_cmp_lg_u32 s66, 0\n"
"s_cbranch_scc1 GF1_%=\n"
"v_mov_b32 v24, 0\n"
"s_mov_b64 exec, 1\n"
"buffer_atomic_add v24, v26, s[36:39], s23 offen sc0 sc1\n"
"s_mov_b64 exec, -1\n"
"GF1_%=:\n"
"s_mul_i32 s77, s45, 0\n"
"s_add_u32 s77, s77, s44\n"
"s_add_u32 m0, s46, 65536\n"
"s_nop 0\n"
"buffer_load_dwordx4 v1, s[40:43], s77 offen lds\n"
"s_add_u32 s77, s77, s45\n"
"s_add_u32 m0, s46, 66560\n"
"s_nop 0\n"
"buffer_load_dwordx4 v12, s[40:43], s77 offen lds\n"
"s_add_u32 s77, s77, s45\n"
"s_add_u32 m0, s46, 67584\n"
"s_nop 0\n"
"buffer_load_dwordx4 v1, s[40:43], s77 offen lds\n"
"s_add_u32 s77, s77, s45\n"
"s_add_u32 m0, s46, 68608\n"
"s_nop 0\n"
"buffer_load_dwordx4 v12, s[40:43], s77 offen lds\n"
"s_add_u32 s77, s77, s45\n"
"s_add_u32 m0, s46, 69632\n"
"s_nop 0\n"
"buffer_load_dwordx4 v1, s[40:43], s77 offen lds\n"
"s_add_u32 s77, s77, s45\n"
"s_add_u32 m0, s46, 70656\n"
"s_nop 0\n"
"buffer_load_dwordx4 v12, s[40:43], s77 offen lds\n"
"s_add_u32 s77, s77, s45\n"
"s_add_u32 m0, s46, 71680\n"
"s_nop 0\n"
"buffer_load_dwordx4 v1, s[40:43], s77 offen lds\n"
"s_add_u32 s77, s77, s45\n"
"s_add_u32 m0, s46, 72704\n"
"s_nop 0\n"
"buffer_load_dwordx4 v12, s[40:43], s77 offen lds\n"
"s_add_u32 s77, s77, s45\n"
"s_mul_i32 s77, s45, 8\n"
"s_add_u32 s77, s77, s44\n"
"s_add_u32 m0, s46, 98304\n"
"s_nop 0\n"
"buffer_load_dwordx4 v1, s[40:43], s77 offen lds\n"
"s_add_u32 s77, s77, s45\n"
"s_add_u32 m0, s46, 99328\n"
"s_nop 0\n"
"buffer_load_dwordx4 v12, s[40:43], s77 offen lds\n"
"s_add_u32 s77, s77, s45\n"
"s_add_u32 m0, s46, 100352\n"
"s_nop 0\n"
"buffer_load_dwordx4 v1, s[40:43], s77 offen lds\n"
"s_add_u32 s77, s77, s45\n"
"s_add_u32 m0, s46, 101376\n"
"s_nop 0\n"
"buffer_load_dwordx4 v12, s[40:43], s77 offen lds\n"
"s_add_u32 s77, s77, s45\n"
"s_add_u32 m0, s46, 102400\n"
"s_nop 0\n"
"buffer_load_dwordx4 v1, s[40:43], s77 offen lds\n"
"s_add_u32 s77, s77, s45\n"
"s_add_u32 m0, s46, 103424\n"
"s_nop 0\n"
"buffer_load_dwordx4 v12, s[40:43], s77 offen lds\n"
"s_add_u32 s77, s77, s45\n"
"s_add_u32 m0, s46, 104448\n"
"s_nop 0\n"
"buffer_load_dwordx4 v1, s[40:43], s77 offen lds\n"
"s_add_u32 s77, s77, s45\n"
"s_add_u32 m0, s46, 105472\n"
"s_nop 0\n"
"buffer_load_dwordx4 v12, s[40:43], s77 offen lds\n"
"s_add_u32 s77, s77, s45\n"
"s_waitcnt vmcnt(8)\n"
"s_waitcnt lgkmcnt(0)\n"
"s_barrier\n"
"s_cmp_lg_u32 s66, 0\n"
"s_cbranch_scc1 GF2_%=\n"
"v_or_b32 v24, 1, v24\n"
"v_add_u32 v24, 1, v24\n"
"buffer_store_dword v24, v26, s[36:39], s22 offen sc1\n"
"GF2_%=:\n"
"s_mul_i32 s77, s45, 16\n"
"s_add_u32 s77, s77, s44\n"
"s_add_u32 m0, s46, 131072\n"
"s_nop 0\n"
"buffer_load_dwordx4 v1, s[40:43], s77 offen lds\n"
"s_add_u32 s77, s77, s45\n"
"s_add_u32 m0, s46, 132096\n"
"s_nop 0\n"
"buffer_load_dwordx4 v12, s[40:43], s77 offen lds\n"
"s_add_u32 s77, s77, s45\n"
"s_add_u32 m0, s46, 133120\n"
"s_nop 0\n"
"buffer_load_dwordx4 v1, s[40:43], s77 offen lds\n"
"s_add_u32 s77, s77, s45\n"
"s_add_u32 m0, s46, 134144\n"
"s_nop 0\n"
"buffer_load_dwordx4 v12, s[40:43], s77 offen lds\n"
"s_add_u32 s77, s77, s45\n"
"s_add_u32 m0, s46, 135168\n"
"s_nop 0\n"
"buffer_load_dwordx4 v1, s[40:43], s77 offen lds\n"
"s_add_u32 s77, s77, s45\n"
"s_add_u32 m0, s46, 136192\n"
"s_nop 0\n"
"buffer_load_dwordx4 v12, s[40:43], s77 offen lds\n"
"s_add_u32 s77, s77, s45\n"
"s_add_u32 m0, s46, 137216\n"
"s_nop 0\n"
"buffer_load_dwordx4 v1, s[40:43], s77 offen lds\n"
"s_add_u32 s77, s77, s45\n"
"s_add_u32 m0, s46, 138240\n"
"s_nop 0\n"
"buffer_load_dwordx4 v12, s[40:43], s77 offen lds\n"
"s_add_u32 s77, s77, s45\n"
"v_add_u32 v8, 65536, v2\n"
"v_add_u32 v10, 65536, v3\n"
"v_xor_b32 v9, 16, v8\n"
"v_xor_b32 v11, 16, v10\n"
"ds_read_b64_tr_b16 v[192:193], v8 offset:0\n"
"ds_read_b64_tr_b16 v[194:195], v8 offset:512\n"
"ds_read_b64_tr_b16 v[196:197], v9 offset:0\n"
"ds_read_b64_tr_b16 v[198:199], v9 offset:512\n"
"ds_read_b64_tr_b16 v[200:201], v10 offset:0\n"
"ds_read_b64_tr_b16 v[202:203], v10 offset:512\n"
"ds_read_b64_tr_b16 v[204:205], v11 offset:0\n"
"ds_read_b64_tr_b16 v[206:207], v11 offset:512\n"
"ds_read_b64_tr_b16 v[208:209], v8 offset:4096\n"
"ds_read_b64_tr_b16 v[210:211], v8 offset:4608\n"
"ds_read_b64_tr_b16 v[212:213], v9 offset:4096\n"
"ds_read_b64_tr_b16 v[214:215], v9 offset:4608\n"
"ds_read_b64_tr_b16 v[216:217], v10 offset:4096\n"
"ds_read_b64_tr_b16 v[218:219], v10 offset:4608\n"
"ds_read_b64_tr_b16 v[220:221], v11 offset:4096\n"
"ds_read_b64_tr_b16 v[222:223], v11 offset:4608\n"
"s_waitcnt vmcnt(8)\n"
"s_waitcnt lgkmcnt(0)\n"
"s_barrier\n"
"s_mul_i32 s77, s45, 24\n"
"s_add_u32 s77, s77, s44\n"
"s_add_u32 m0, s46, 65536\n"
"s_nop 0\n"
"buffer_load_dwordx4 v1, s[40:43], s77 offen lds\n"
"s_add_u32 s77, s77, s45\n"
"s_add_u32 m0, s46, 66560\n"
"s_nop 0\n"
"buffer_load_dwordx4 v12, s[40:43], s77 offen lds\n"
"s_add_u32 s77, s77, s45\n"
"s_add_u32 m0, s46, 67584\n"
"s_nop 0\n"
"buffer_load_dwordx4 v1, s[40:43], s77 offen lds\n"
"s_add_u32 s77, s77, s45\n"
"s_add_u32 m0, s46, 68608\n"
"s_nop 0\n"
"buffer_load_dwordx4 v12, s[40:43], s77 offen lds\n"
"s_add_u32 s77, s77, s45\n"
"s_add_u32 m0, s46, 69632\n"
"s_nop 0\n"
"buffer_load_dwordx4 v1, s[40:43], s77 offen lds\n"
"s_add_u32 s77, s77, s45\n"
"s_add_u32 m0, s46, 70656\n"
"s_nop 0\n"
"buffer_load_dwordx4 v12, s[40:43], s77 offen lds\n"
"s_add_u32 s77, s77, s45\n"
"s_add_u32 m0, s46, 71680\n"
"s_nop 0\n"
"buffer_load_dwordx4 v1, s[40:43], s77 offen lds\n"
"s_add_u32 s77, s77, s45\n"
"s_add_u32 m0, s46, 72704\n"
"s_nop 0\n"
"buffer_load_dwordx4 v12, s[40:43], s77 offen lds\n"
"s_add_u32 s77, s77, s45\n"
"v_add_u32 v8, 98304, v2\n"
"v_add_u32 v10, 98304, v3\n"
"v_xor_b32 v9, 16, v8\n"
"v_xor_b32 v11, 16, v10\n"
"ds_read_b64_tr_b16 v[224:225], v8 offset:0\n"
"ds_read_b64_tr_b16 v[226:227], v8 offset:512\n"
"ds_read_b64_tr_b16 v[228:229], v9 offset:0\n"
"ds_read_b64_tr_b16 v[230:231], v9 offset:512\n"
"ds_read_b64_tr_b16 v[232:233], v10 offset:0\n"
"ds_read_b64_tr_b16 v[234:235], v10 offset:512\n"
"ds_read_b64_tr_b16 v[236:237], v11 offset:0\n"
"ds_read_b64_tr_b16 v[238:239], v11 offset:512\n"
"ds_read_b64_tr_b16 v[240:241], v8 offset:4096\n"
"ds_read_b64_tr_b16 v[242:243], v8 offset:4608\n"
"ds_read_b64_tr_b16 v[244:245], v9 offset:4096\n"
"ds_read_b64_tr_b16 v[246:247], v9 offset:4608\n"
"ds_read_b64_tr_b16 v[248:249], v10 offset:4096\n"
"ds_read_b64_tr_b16 v[250:251], v10 offset:4608\n"
"ds_read_b64_tr_b16 v[252:253], v11 offset:4096\n"
"ds_read_b64_tr_b16 v[254:255], v11 offset:4608\n"
"s_waitcnt vmcnt(8)\n"
"s_waitcnt lgkmcnt(0)\n"
"s_barrier\n"
"s_mul_i32 s77, s45, 32\n"
"s_add_u32 s77, s77, s44\n"
"s_add_u32 m0, s46, 98304\n"
"s_nop 0\n"
"buffer_load_dwordx4 v1, s[40:43], s77 offen lds\n"
"s_add_u32 s77, s77, s45\n"
"s_add_u32 m0, s46, 99328\n"
"s_nop 0\n"
"buffer_load_dwordx4 v12, s[40:43], s77 offen lds\n"
"s_add_u32 s77, s77, s45\n"
"s_add_u32 m0, s46, 100352\n"
"s_nop 0\n"
"buffer_load_dwordx4 v1, s[40:43], s77 offen lds\n"
"s_add_u32 s77, s77, s45\n"
"s_add_u32 m0, s46, 101376\n"
"s_nop 0\n"
"buffer_load_dwordx4 v12, s[40:43], s77 offen lds\n"
"s_add_u32 s77, s77, s45\n"
"s_add_u32 m0, s46, 102400\n"
"s_nop 0\n"
"buffer_load_dwordx4 v1, s[40:43], s77 offen lds\n"
"s_add_u32 s77, s77, s45\n"
"s_add_u32 m0, s46, 103424\n"
"s_nop 0\n"
"buffer_load_dwordx4 v12, s[40:43], s77 offen lds\n"
"s_add_u32 s77, s77, s45\n"
"s_add_u32 m0, s46, 104448\n"
"s_nop 0\n"
"buffer_load_dwordx4 v1, s[40:43], s77 offen lds\n"
"s_add_u32 s77, s77, s45\n"
"s_add_u32 m0, s46, 105472\n"
"s_nop 0\n"
"buffer_load_dwordx4 v12, s[40:43], s77 offen lds\n"
"s_add_u32 s77, s77, s45\n"
"v_add_u32 v8, 131072, v2\n"
"v_add_u32 v10, 131072, v3\n"
"v_xor_b32 v9, 16, v8\n"
"v_xor_b32 v11, 16, v10\n"
"ds_read_b64_tr_b16 a[0:1], v8 offset:0\n"
"ds_read_b64_tr_b16 a[2:3], v8 offset:512\n"
"ds_read_b64_tr_b16 a[4:5], v9 offset:0\n"
"ds_read_b64_tr_b16 a[6:7], v9 offset:512\n"
"ds_read_b64_tr_b16 a[8:9], v10 offset:0\n"
"ds_read_b64_tr_b16 a[10:11], v10 offset:512\n"
"ds_read_b64_tr_b16 a[12:13], v11 offset:0\n"
"ds_read_b64_tr_b16 a[14:15], v11 offset:512\n"
"ds_read_b64_tr_b16 a[16:17], v8 offset:4096\n"
"ds_read_b64_tr_b16 a[18:19], v8 offset:4608\n"
"ds_read_b64_tr_b16 a[20:21], v9 offset:4096\n"
"ds_read_b64_tr_b16 a[22:23], v9 offset:4608\n"
"ds_read_b64_tr_b16 a[24:25], v10 offset:4096\n"
"ds_read_b64_tr_b16 a[26:27], v10 offset:4608\n"
"ds_read_b64_tr_b16 a[28:29], v11 offset:4096\n"
"ds_read_b64_tr_b16 a[30:31], v11 offset:4608\n"
"s_waitcnt vmcnt(8)\n"
"s_waitcnt lgkmcnt(0)\n"
"s_barrier\n"
"s_mul_i32 s77, s45, 40\n"
"s_add_u32 s77, s77, s44\n"
"s_add_u32 m0, s46, 131072\n"
"s_nop 0\n"
"buffer_load_dwordx4 v1, s[40:43], s77 offen lds\n"
"s_add_u32 s77, s77, s45\n"
"s_add_u32 m0, s46, 132096\n"
"s_nop 0\n"
"buffer_load_dwordx4 v12, s[40:43], s77 offen lds\n"
"s_add_u32 s77, s77, s45\n"
"s_add_u32 m0, s46, 133120\n"
"s_nop 0\n"
"buffer_load_dwordx4 v1, s[40:43], s77 offen lds\n"
"s_add_u32 s77, s77, s45\n"
"s_add_u32 m0, s46, 134144\n"
"s_nop 0\n"
"buffer_load_dwordx4 v12, s[40:43], s77 offen lds\n"
"s_add_u32 s77, s77, s45\n"
"s_add_u32 m0, s46, 135168\n"
"s_nop 0\n"
"buffer_load_dwordx4 v1, s[40:43], s77 offen lds\n"
"s_add_u32 s77, s77, s45\n"
"s_add_u32 m0, s46, 136192\n"
"s_nop 0\n"
"buffer_load_dwordx4 v12, s[40:43], s77 offen lds\n"
"s_add_u32 s77, s77, s45\n"
"s_add_u32 m0, s46, 137216\n"
"s_nop 0\n"
"buffer_load_dwordx4 v1, s[40:43], s77 offen lds\n"
"s_add_u32 s77, s77, s45\n"
"s_add_u32 m0, s46, 138240\n"
"s_nop 0\n"
"buffer_load_dwordx4 v12, s[40:43], s77 offen lds\n"
"s_add_u32 s77, s77, s45\n"
"v_add_u32 v8, 65536, v2\n"
"v_add_u32 v10, 65536, v3\n"
"v_xor_b32 v9, 16, v8\n"
"v_xor_b32 v11, 16, v10\n"
"ds_read_b64_tr_b16 a[32:33], v8 offset:0\n"
"ds_read_b64_tr_b16 a[34:35], v8 offset:512\n"
"ds_read_b64_tr_b16 a[36:37], v9 offset:0\n"
"ds_read_b64_tr_b16 a[38:39], v9 offset:512\n"
"ds_read_b64_tr_b16 a[40:41], v10 offset:0\n"
"ds_read_b64_tr_b16 a[42:43], v10 offset:512\n"
"ds_read_b64_tr_b16 a[44:45], v11 offset:0\n"
"ds_read_b64_tr_b16 a[46:47], v11 offset:512\n"
"ds_read_b64_tr_b16 a[48:49], v8 offset:4096\n"
"ds_read_b64_tr_b16 a[50:51], v8 offset:4608\n"
"ds_read_b64_tr_b16 a[52:53], v9 offset:4096\n"
"ds_read_b64_tr_b16 a[54:55], v9 offset:4608\n"
"ds_read_b64_tr_b16 a[56:57], v10 offset:4096\n"
"ds_read_b64_tr_b16 a[58:59], v10 offset:4608\n"
"ds_read_b64_tr_b16 a[60:61], v11 offset:4096\n"
"ds_read_b64_tr_b16 a[62:63], v11 offset:4608\n"
"s_waitcnt vmcnt(8)\n"
"s_waitcnt lgkmcnt(0)\n"
"s_barrier\n"
"s_mul_i32 s77, s45, 48\n"
"s_add_u32 s77, s77, s44\n"
"s_add_u32 m0, s46, 65536\n"
"s_nop 0\n"
"buffer_load_dwordx4 v1, s[40:43], s77 offen lds\n"
"s_add_u32 s77, s77, s45\n"
"s_add_u32 m0, s46, 66560\n"
"s_nop 0\n"
"buffer_load_dwordx4 v12, s[40:43], s77 offen lds\n"
"s_add_u32 s77, s77, s45\n"
"s_add_u32 m0, s46, 67584\n"
"s_nop 0\n"
"buffer_load_dwordx4 v1, s[40:43], s77 offen lds\n"
"s_add_u32 s77, s77, s45\n"
"s_add_u32 m0, s46, 68608\n"
"s_nop 0\n"
"buffer_load_dwordx4 v12, s[40:43], s77 offen lds\n"
"s_add_u32 s77, s77, s45\n"
"s_add_u32 m0, s46, 69632\n"
"s_nop 0\n"
"buffer_load_dwordx4 v1, s[40:43], s77 offen lds\n"
"s_add_u32 s77, s77, s45\n"
"s_add_u32 m0, s46, 70656\n"
"s_nop 0\n"
"buffer_load_dwordx4 v12, s[40:43], s77 offen lds\n"
"s_add_u32 s77, s77, s45\n"
"s_add_u32 m0, s46, 71680\n"
"s_nop 0\n"
"buffer_load_dwordx4 v1, s[40:43], s77 offen lds\n"
"s_add_u32 s77, s77, s45\n"
"s_add_u32 m0, s46, 72704\n"
"s_nop 0\n"
"buffer_load_dwordx4 v12, s[40:43], s77 offen lds\n"
"s_add_u32 s77, s77, s45\n"
"v_add_u32 v8, 98304, v2\n"
"v_add_u32 v10, 98304, v3\n"
"v_xor_b32 v9, 16, v8\n"
"v_xor_b32 v11, 16, v10\n"
"ds_read_b64_tr_b16 a[64:65], v8 offset:0\n"
"ds_read_b64_tr_b16 a[66:67], v8 offset:512\n"
"ds_read_b64_tr_b16 a[68:69], v9 offset:0\n"
"ds_read_b64_tr_b16 a[70:71], v9 offset:512\n"
"ds_read_b64_tr_b16 a[72:73], v10 offset:0\n"
"ds_read_b64_tr_b16 a[74:75], v10 offset:512\n"
"ds_read_b64_tr_b16 a[76:77], v11 offset:0\n"
"ds_read_b64_tr_b16 a[78:79], v11 offset:512\n"
"ds_read_b64_tr_b16 a[80:81], v8 offset:4096\n"
"ds_read_b64_tr_b16 a[82:83], v8 offset:4608\n"
"ds_read_b64_tr_b16 a[84:85], v9 offset:4096\n"
"ds_read_b64_tr_b16 a[86:87], v9 offset:4608\n"
"ds_read_b64_tr_b16 a[88:89], v10 offset:4096\n"
"ds_read_b64_tr_b16 a[90:91], v10 offset:4608\n"
"ds_read_b64_tr_b16 a[92:93], v11 offset:4096\n"
"ds_read_b64_tr_b16 a[94:95], v11 offset:4608\n"
"s_waitcnt vmcnt(8)\n"
"s_waitcnt lgkmcnt(0)\n"
"s_barrier\n"
"s_mul_i32 s77, s45, 56\n"
"s_add_u32 s77, s77, s44\n"
"s_add_u32 m0, s46, 98304\n"
"s_nop 0\n"
"buffer_load_dwordx4 v1, s[40:43], s77 offen lds\n"
"s_add_u32 s77, s77, s45\n"
"s_add_u32 m0, s46, 99328\n"
"s_nop 0\n"
"buffer_load_dwordx4 v12, s[40:43], s77 offen lds\n"
"s_add_u32 s77, s77, s45\n"
"s_add_u32 m0, s46, 100352\n"
"s_nop 0\n"
"buffer_load_dwordx4 v1, s[40:43], s77 offen lds\n"
"s_add_u32 s77, s77, s45\n"
"s_add_u32 m0, s46, 101376\n"
"s_nop 0\n"
"buffer_load_dwordx4 v12, s[40:43], s77 offen lds\n"
"s_add_u32 s77, s77, s45\n"
"s_add_u32 m0, s46, 102400\n"
"s_nop 0\n"
"buffer_load_dwordx4 v1, s[40:43], s77 offen lds\n"
"s_add_u32 s77, s77, s45\n"
"s_add_u32 m0, s46, 103424\n"
"s_nop 0\n"
"buffer_load_dwordx4 v12, s[40:43], s77 offen lds\n"
"s_add_u32 s77, s77, s45\n"
"s_add_u32 m0, s46, 104448\n"
"s_nop 0\n"
"buffer_load_dwordx4 v1, s[40:43], s77 offen lds\n"
"s_add_u32 s77, s77, s45\n"
"s_add_u32 m0, s46, 105472\n"
"s_nop 0\n"
"buffer_load_dwordx4 v12, s[40:43], s77 offen lds\n"
"s_add_u32 s77, s77, s45\n"
"v_add_u32 v8, 131072, v2\n"
"v_add_u32 v10, 131072, v3\n"
"v_xor_b32 v9, 16, v8\n"
"v_xor_b32 v11, 16, v10\n"
"ds_read_b64_tr_b16 a[96:97], v8 offset:0\n"
"ds_read_b64_tr_b16 a[98:99], v8 offset:512\n"
"ds_read_b64_tr_b16 a[100:101], v9 offset:0\n"
"ds_read_b64_tr_b16 a[102:103], v9 offset:512\n"
"ds_read_b64_tr_b16 a[104:105], v10 offset:0\n"
"ds_read_b64_tr_b16 a[106:107], v10 offset:512\n"
"ds_read_b64_tr_b16 a[108:109], v11 offset:0\n"
"ds_read_b64_tr_b16 a[110:111], v11 offset:512\n"
"ds_read_b64_tr_b16 a[112:113], v8 offset:4096\n"
"ds_read_b64_tr_b16 a[114:115], v8 offset:4608\n"
"ds_read_b64_tr_b16 a[116:117], v9 offset:4096\n"
"ds_read_b64_tr_b16 a[118:119], v9 offset:4608\n"
"ds_read_b64_tr_b16 a[120:121], v10 offset:4096\n"
"ds_read_b64_tr_b16 a[122:123], v10 offset:4608\n"
"ds_read_b64_tr_b16 a[124:125], v11 offset:4096\n"
"ds_read_b64_tr_b16 a[126:127], v11 offset:4608\n"
"s_waitcnt vmcnt(8)\n"
"s_waitcnt lgkmcnt(0)\n"
"s_barrier\n"
"s_mul_i32 s77, s45, 64\n"
"s_add_u32 s77, s77, s44\n"
"s_add_u32 m0, s46, 131072\n"
"s_nop 0\n"
"buffer_load_dwordx4 v1, s[40:43], s77 offen lds\n"
"s_add_u32 s77, s77, s45\n"
"s_add_u32 m0, s46, 132096\n"
"s_nop 0\n"
"buffer_load_dwordx4 v12, s[40:43], s77 offen lds\n"
"s_add_u32 s77, s77, s45\n"
"s_add_u32 m0, s46, 133120\n"
"s_nop 0\n"
"buffer_load_dwordx4 v1, s[40:43], s77 offen lds\n"
"s_add_u32 s77, s77, s45\n"
"s_add_u32 m0, s46, 134144\n"
"s_nop 0\n"
"buffer_load_dwordx4 v12, s[40:43], s77 offen lds\n"
"s_add_u32 s77, s77, s45\n"
"s_add_u32 m0, s46, 135168\n"
"s_nop 0\n"
"buffer_load_dwordx4 v1, s[40:43], s77 offen lds\n"
"s_add_u32 s77, s77, s45\n"
"s_add_u32 m0, s46, 136192\n"
"s_nop 0\n"
"buffer_load_dwordx4 v12, s[40:43], s77 offen lds\n"
"s_add_u32 s77, s77, s45\n"
"s_add_u32 m0, s46, 137216\n"
"s_nop 0\n"
"buffer_load_dwordx4 v1, s[40:43], s77 offen lds\n"
"s_add_u32 s77, s77, s45\n"
"s_add_u32 m0, s46, 138240\n"
"s_nop 0\n"
"buffer_load_dwordx4 v12, s[40:43], s77 offen lds\n"
"s_add_u32 s77, s77, s45\n"
"v_add_u32 v8, 65536, v2\n"
"v_add_u32 v10, 65536, v3\n"
"v_xor_b32 v9, 16, v8\n"
"v_xor_b32 v11, 16, v10\n"
"ds_read_b64_tr_b16 a[128:129], v8 offset:0\n"
"ds_read_b64_tr_b16 a[130:131], v8 offset:512\n"
"ds_read_b64_tr_b16 a[132:133], v9 offset:0\n"
"ds_read_b64_tr_b16 a[134:135], v9 offset:512\n"
"ds_read_b64_tr_b16 a[136:137], v10 offset:0\n"
"ds_read_b64_tr_b16 a[138:139], v10 offset:512\n"
"ds_read_b64_tr_b16 a[140:141], v11 offset:0\n"
"ds_read_b64_tr_b16 a[142:143], v11 offset:512\n"
"ds_read_b64_tr_b16 a[144:145], v8 offset:4096\n"
"ds_read_b64_tr_b16 a[146:147], v8 offset:4608\n"
"ds_read_b64_tr_b16 a[148:149], v9 offset:4096\n"
"ds_read_b64_tr_b16 a[150:151], v9 offset:4608\n"
"ds_read_b64_tr_b16 a[152:153], v10 offset:4096\n"
"ds_read_b64_tr_b16 a[154:155], v10 offset:4608\n"
"ds_read_b64_tr_b16 a[156:157], v11 offset:4096\n"
"ds_read_b64_tr_b16 a[158:159], v11 offset:4608\n"
"s_waitcnt vmcnt(8)\n"
"s_waitcnt lgkmcnt(0)\n"
"s_barrier\n"
"s_mul_i32 s77, s45, 72\n"
"s_add_u32 s77, s77, s44\n"
"s_add_u32 m0, s46, 65536\n"
"s_nop 0\n"
"buffer_load_dwordx4 v1, s[40:43], s77 offen lds\n"
"s_add_u32 s77, s77, s45\n"
"s_add_u32 m0, s46, 66560\n"
"s_nop 0\n"
"buffer_load_dwordx4 v12, s[40:43], s77 offen lds\n"
"s_add_u32 s77, s77, s45\n"
"s_add_u32 m0, s46, 67584\n"
"s_nop 0\n"
"buffer_load_dwordx4 v1, s[40:43], s77 offen lds\n"
"s_add_u32 s77, s77, s45\n"
"s_add_u32 m0, s46, 68608\n"
"s_nop 0\n"
"buffer_load_dwordx4 v12, s[40:43], s77 offen lds\n"
"s_add_u32 s77, s77, s45\n"
"s_add_u32 m0, s46, 69632\n"
"s_nop 0\n"
"buffer_load_dwordx4 v1, s[40:43], s77 offen lds\n"
"s_add_u32 s77, s77, s45\n"
"s_add_u32 m0, s46, 70656\n"
"s_nop 0\n"
"buffer_load_dwordx4 v12, s[40:43], s77 offen lds\n"
"s_add_u32 s77, s77, s45\n"
"s_add_u32 m0, s46, 71680\n"
"s_nop 0\n"
"buffer_load_dwordx4 v1, s[40:43], s77 offen lds\n"
"s_add_u32 s77, s77, s45\n"
"s_add_u32 m0, s46, 72704\n"
"s_nop 0\n"
"buffer_load_dwordx4 v12, s[40:43], s77 offen lds\n"
"s_add_u32 s77, s77, s45\n"
"v_add_u32 v8, 98304, v2\n"
"v_add_u32 v10, 98304, v3\n"
"v_xor_b32 v9, 16, v8\n"
"v_xor_b32 v11, 16, v10\n"
"ds_read_b64_tr_b16 a[160:161], v8 offset:0\n"
"ds_read_b64_tr_b16 a[162:163], v8 offset:512\n"
"ds_read_b64_tr_b16 a[164:165], v9 offset:0\n"
"ds_read_b64_tr_b16 a[166:167], v9 offset:512\n"
"ds_read_b64_tr_b16 a[168:169], v10 offset:0\n"
"ds_read_b64_tr_b16 a[170:171], v10 offset:512\n"
"ds_read_b64_tr_b16 a[172:173], v11 offset:0\n"
"ds_read_b64_tr_b16 a[174:175], v11 offset:512\n"
"ds_read_b64_tr_b16 a[176:177], v8 offset:4096\n"
"ds_read_b64_tr_b16 a[178:179], v8 offset:4608\n"
"ds_read_b64_tr_b16 a[180:181], v9 offset:4096\n"
"ds_read_b64_tr_b16 a[182:183], v9 offset:4608\n"
"ds_read_b64_tr_b16 a[184:185], v10 offset:4096\n"
"ds_read_b64_tr_b16 a[186:187], v10 offset:4608\n"
"ds_read_b64_tr_b16 a[188:189], v11 offset:4096\n"
"ds_read_b64_tr_b16 a[190:191], v11 offset:4608\n"
"s_cmp_lg_u32 s66, 0\n"
"s_cbranch_scc1 GF3_%=\n"
"s_mov_b32 s76, 131072\n"
"s_mov_b64 exec, 1\n"
"s_branch GF5_%=\n"
"GF4_%=:\n"
"s_sleep 4\n"
"GF5_%=:\n"
"v_mov_b32 v29, 0\n"
"buffer_atomic_add v29, v26, s[36:39], s23 offen sc0 sc1\n"
"s_sub_u32 s76, s76, 1\n"
"s_cbranch_scc1 GFABORT_%=\n"
"s_waitcnt vmcnt(0)\n"
"v_xor_b32 v29, v29, v24\n"
"v_cmp_lt_u32 vcc, 1, v29\n"
"s_nop 0\n"
"s_cbranch_vccnz GF4_%=\n"
"v_mov_b32 v28, 61440\n"
"ds_write_b32 v28, v29\n"
"v_mov_b32 v24, 1\n"
"s_add_u32 s22, s23, 16384\n"
"buffer_store_dword v24, v26, s[36:39], s22 offen sc1\n"
"s_mov_b64 exec, -1\n"
"GF3_%=:\n"
"s_waitcnt lgkmcnt(0)\n"
"s_barrier\n"
"s_add_u32 m0, s71, 0\n"
"s_add_u32 s72, s68, 0\n"
"buffer_load_dwordx4 v0, s[36:39], s72 offen offset:0 sc1 lds\n"
"buffer_load_dwordx4 v0, s[36:39], s72 offen offset:1024 sc1 lds\n"
"buffer_load_dwordx4 v0, s[36:39], s72 offen offset:2048 sc1 lds\n"
"buffer_load_dwordx4 v0, s[36:39], s72 offen offset:3072 sc1 lds\n"
"s_add_u32 m0, s71, 4096\n"
"s_add_u32 s72, s68, 4096\n"
"buffer_load_dwordx4 v0, s[36:39], s72 offen offset:0 sc1 lds\n"
"buffer_load_dwordx4 v0, s[36:39], s72 offen offset:1024 sc1 lds\n"
"buffer_load_dwordx4 v0, s[36:39], s72 offen offset:2048 sc1 lds\n"
"buffer_load_dwordx4 v0, s[36:39], s72 offen offset:3072 sc1 lds\n"
"s_add_u32 m0, s71, 8192\n"
"s_add_u32 s72, s68, 8192\n"
"buffer_load_dwordx4 v0, s[36:39], s72 offen offset:0 sc1 lds\n"
"buffer_load_dwordx4 v0, s[36:39], s72 offen offset:1024 sc1 lds\n"
"s_add_u32 s68, s68, 40960\n"
"s_add_u32 s69, s69, 40960\n"
"s_cmp_eq_u32 s69, 122880\n"
"s_cselect_b32 s69, 0, s69\n"
"s_add_u32 s71, s69, s50\n"
"v_mov_b32 v28, 61440\n"
"ds_read_b32 v28, v28\n"
"s_waitcnt lgkmcnt(0)\n"
"v_readfirstlane_b32 s22, v28\n"
"s_nop 0\n"
"s_lshl_b32 s22, s22, 13\n"
"v_xor_b32 v4, s22, v4\n"
"s_waitcnt vmcnt(8)\n"
"s_waitcnt lgkmcnt(0)\n"
"s_barrier\n"
"v_add_u32 v8, 131072, v2\n"
"v_add_u32 v10, 131072, v3\n"
"v_xor_b32 v9, 16, v8\n"
"v_xor_b32 v11, 16, v10\n"
"ds_read_b64_tr_b16 a[192:193], v8 offset:0\n"
"ds_read_b64_tr_b16 a[194:195], v8 offset:512\n"
"ds_read_b64_tr_b16 a[196:197], v9 offset:0\n"
"ds_read_b64_tr_b16 a[198:199], v9 offset:512\n"
"ds_read_b64_tr_b16 a[200:201], v10 offset:0\n"
"ds_read_b64_tr_b16 a[202:203], v10 offset:512\n"
"ds_read_b64_tr_b16 a[204:205], v11 offset:0\n"
"ds_read_b64_tr_b16 a[206:207], v11 offset:512\n"
"ds_read_b64_tr_b16 a[208:209], v8 offset:4096\n"
"ds_read_b64_tr_b16 a[210:211], v8 offset:4608\n"
"ds_read_b64_tr_b16 a[212:213], v9 offset:4096\n"
"ds_read_b64_tr_b16 a[214:215], v9 offset:4608\n"
"ds_read_b64_tr_b16 a[216:217], v10 offset:4096\n"
"ds_read_b64_tr_b16 a[218:219], v10 offset:4608\n"
"ds_read_b64_tr_b16 a[220:221], v11 offset:4096\n"
"ds_read_b64_tr_b16 a[222:223], v11 offset:4608\n"
"s_waitcnt vmcnt(0)\n"
"s_waitcnt lgkmcnt(0)\n"
"s_barrier\n"
"v_add_u32 v8, 65536, v2\n"
"v_add_u32 v10, 65536, v3\n"
"v_xor_b32 v9, 16, v8\n"
"v_xor_b32 v11, 16, v10\n"
"ds_read_b64_tr_b16 a[224:225], v8 offset:0\n"
"ds_read_b64_tr_b16 a[226:227], v8 offset:512\n"
"ds_read_b64_tr_b16 a[228:229], v9 offset:0\n"
"ds_read_b64_tr_b16 a[230:231], v9 offset:512\n"
"ds_read_b64_tr_b16 a[232:233], v10 offset:0\n"
"ds_read_b64_tr_b16 a[234:235], v10 offset:512\n"
"ds_read_b64_tr_b16 a[236:237], v11 offset:0\n"
"ds_read_b64_tr_b16 a[238:239], v11 offset:512\n"
"ds_read_b64_tr_b16 a[240:241], v8 offset:4096\n"
"ds_read_b64_tr_b16 a[242:243], v8 offset:4608\n"
"ds_read_b64_tr_b16 a[244:245], v9 offset:4096\n"
"ds_read_b64_tr_b16 a[246:247], v9 offset:4608\n"
"ds_read_b64_tr_b16 a[248:249], v10 offset:4096\n"
"ds_read_b64_tr_b16 a[250:251], v10 offset:4608\n"
"ds_read_b64_tr_b16 a[252:253], v11 offset:4096\n"
"ds_read_b64_tr_b16 a[254:255], v11 offset:4608\n"
"s_waitcnt lgkmcnt(0)\n"
"s_barrier\n"
"s_add_u32 m0, s71, 0\n"
"s_add_u32 s72, s68, 0\n"
"buffer_load_dwordx4 v0, s[36:39], s72 offen offset:0 sc1 lds\n"
"buffer_load_dwordx4 v0, s[36:39], s72 offen offset:1024 sc1 lds\n"
"buffer_load_dwordx4 v0, s[36:39], s72 offen offset:2048 sc1 lds\n"
"buffer_load_dwordx4 v0, s[36:39], s72 offen offset:3072 sc1 lds\n"
"s_add_u32 m0, s71, 4096\n"
"s_add_u32 s72, s68, 4096\n"
"buffer_load_dwordx4 v0, s[36:39], s72 offen offset:0 sc1 lds\n"
"buffer_load_dwordx4 v0, s[36:39], s72 offen offset:1024 sc1 lds\n"
"buffer_load_dwordx4 v0, s[36:39], s72 offen offset:2048 sc1 lds\n"
"buffer_load_dwordx4 v0, s[36:39], s72 offen offset:3072 sc1 lds\n"
"s_add_u32 m0, s71, 8192\n"
"s_add_u32 s72, s68, 8192\n"
"buffer_load_dwordx4 v0, s[36:39], s72 offen offset:0 sc1 lds\n"
"buffer_load_dwordx4 v0, s[36:39], s72 offen offset:1024 sc1 lds\n"
"s_add_u32 s68, s68, 40960\n"
"s_add_u32 s69, s69, 40960\n"
"s_cmp_eq_u32 s69, 122880\n"
"s_cselect_b32 s69, 0, s69\n"
"s_add_u32 s71, s69, s50\n"
"s_mov_b32 s70, 0\n"
"s_mov_b32 s73, 0\n"
"s_mov_b32 s74, 234881024\n"
"v_mov_b32 v5, v7\n"
"ds_read_b128 v[96:99], v5 offset:0\n"
"ds_read_b128 v[100:103], v5 offset:256\n"
"ds_read_b128 v[104:107], v5 offset:2048\n"
"ds_read_b128 v[108:111], v5 offset:2304\n"
"ds_read_b128 v[112:115], v5 offset:4096\n"
"ds_read_b128 v[116:119], v5 offset:4352\n"
"s_mov_b32 s75, 0xbfb8aa3b\n"
"s_mov_b32 s79, 12\n"
"s_nop 0\n"
"GF6_%=:\n"
"s_waitcnt lgkmcnt(5)\n"
"v_mfma_f32_16x16x32_bf16 v[32:35], v[192:195], v[96:99], 0\n"
"v_mfma_f32_16x16x32_bf16 v[36:39], v[196:199], v[96:99], 0\n"
"v_mfma_f32_16x16x32_bf16 v[48:51], v[200:203], v[96:99], 0\n"
"ds_read_b128 v[120:123], v5 offset:6144\n"
"v_mfma_f32_16x16x32_bf16 v[52:55], v[204:207], v[96:99], 0\n"
"s_waitcnt lgkmcnt(5)\n"
"v_mfma_f32_16x16x32_bf16 v[40:43], v[192:195], v[100:103], 0\n"
"v_mfma_f32_16x16x32_bf16 v[44:47], v[196:199], v[100:103], 0\n"
"v_mfma_f32_16x16x32_bf16 v[56:59], v[200:203], v[100:103], 0\n"
"ds_read_b128 v[124:127], v5 offset:6400\n"
"v_mfma_f32_16x16x32_bf16 v[60:63], v[204:207], v[100:103], 0\n"
"s_waitcnt lgkmcnt(5)\n"
"v_mfma_f32_16x16x32_bf16 v[32:35], v[208:211], v[104:107], v[32:35]\n"
"v_mul_f32 v144, s75, v64\n"
"v_mul_f32 v145, s75, v65\n"
"v_mfma_f32_16x16x32_bf16 v[36:39], v[212:215], v[104:107], v[36:39]\n"
"v_mul_f32 v146, s75, v66\n"
"v_mul_f32 v147, s75, v67\n"
"v_mfma_f32_16x16x32_bf16 v[48:51], v[216:219], v[104:107], v[48:51]\n"
"ds_read_b128 v[96:99], v5 offset:8192\n"
"v_mfma_f32_16x16x32_bf16 v[52:55], v[220:223], v[104:107], v[52:55]\n"
"v_mul_f32 v148, s75, v68\n"
"v_mul_f32 v149, s75, v69\n"
"s_waitcnt lgkmcnt(5)\n"
"v_mfma_f32_16x16x32_bf16 v[40:43], v[208:211], v[108:111], v[40:43]\n"
"v_mul_f32 v150, s75, v70\n"
"v_mul_f32 v151, s75, v71\n"
"v_mfma_f32_16x16x32_bf16 v[44:47], v[212:215], v[108:111], v[44:47]\n"
"v_exp_f32 v144, v144\n"
"v_mfma_f32_16x16x32_bf16 v[56:59], v[216:219], v[108:111], v[56:59]\n"
"ds_read_b128 v[100:103], v5 offset:8448\n"
"v_mfma_f32_16x16x32_bf16 v[60:63], v[220:223], v[108:111], v[60:63]\n"
"v_exp_f32 v145, v145\n"
"s_waitcnt lgkmcnt(5)\n"
"v_mfma_f32_16x16x32_bf16 v[32:35], v[224:227], v[112:115], v[32:35]\n"
"v_exp_f32 v146, v146\n"
"v_mfma_f32_16x16x32_bf16 v[36:39], v[228:231], v[112:115], v[36:39]\n"
"v_exp_f32 v147, v147\n"
"v_mfma_f32_16x16x32_bf16 v[48:51], v[232:235], v[112:115], v[48:51]\n"
"ds_read_b128 v[104:107], v5 offset:10240\n"
"v_mfma_f32_16x16x32_bf16 v[52:55], v[236:239], v[112:115], v[52:55]\n"
"v_exp_f32 v148, v148\n"
"s_waitcnt lgkmcnt(5)\n"
"v_mfma_f32_16x16x32_bf16 v[40:43], v[224:227], v[116:119], v[40:43]\n"
"v_exp_f32 v149, v149\n"
"v_mfma_f32_16x16x32_bf16 v[44:47], v[228:231], v[116:119], v[44:47]\n"
"v_exp_f32 v150, v150\n"
"v_mfma_f32_16x16x32_bf16 v[56:59], v[232:235], v[116:119], v[56:59]\n"
"ds_read_b128 v[108:111], v5 offset:10496\n"
"v_mfma_f32_16x16x32_bf16 v[60:63], v[236:239], v[116:119], v[60:63]\n"
"v_exp_f32 v151, v151\n"
"s_waitcnt lgkmcnt(5)\n"
"v_mfma_f32_16x16x32_bf16 v[32:35], v[240:243], v[120:123], v[32:35]\n"
"v_add_f32 v144, 1.0, v144\n"
"v_add_f32 v145, 1.0, v145\n"
"v_mfma_f32_16x16x32_bf16 v[36:39], v[244:247], v[120:123], v[36:39]\n"
"v_add_f32 v146, 1.0, v146\n"
"v_add_f32 v147, 1.0, v147\n"
"v_mfma_f32_16x16x32_bf16 v[48:51], v[248:251], v[120:123], v[48:51]\n"
"ds_read_b128 v[112:115], v5 offset:12288\n"
"v_mfma_f32_16x16x32_bf16 v[52:55], v[252:255], v[120:123], v[52:55]\n"
"v_add_f32 v148, 1.0, v148\n"
"v_add_f32 v149, 1.0, v149\n"
"s_waitcnt lgkmcnt(5)\n"
"v_mfma_f32_16x16x32_bf16 v[40:43], v[240:243], v[124:127], v[40:43]\n"
"v_add_f32 v150, 1.0, v150\n"
"v_add_f32 v151, 1.0, v151\n"
"v_mfma_f32_16x16x32_bf16 v[44:47], v[244:247], v[124:127], v[44:47]\n"
"v_rcp_f32 v144, v144\n"
"v_mfma_f32_16x16x32_bf16 v[56:59], v[248:251], v[124:127], v[56:59]\n"
"ds_read_b128 v[116:119], v5 offset:12544\n"
"v_mfma_f32_16x16x32_bf16 v[60:63], v[252:255], v[124:127], v[60:63]\n"
"v_rcp_f32 v145, v145\n"
"s_waitcnt lgkmcnt(5)\n"
"v_mfma_f32_16x16x32_bf16 v[32:35], a[0:3], v[96:99], v[32:35]\n"
"v_rcp_f32 v146, v146\n"
"v_mfma_f32_16x16x32_bf16 v[36:39], a[4:7], v[96:99], v[36:39]\n"
"v_rcp_f32 v147, v147\n"
"v_mfma_f32_16x16x32_bf16 v[48:51], a[8:11], v[96:99], v[48:51]\n"
"ds_read_b128 v[120:123], v5 offset:14336\n"
"v_mfma_f32_16x16x32_bf16 v[52:55], a[12:15], v[96:99], v[52:55]\n"
"v_rcp_f32 v148, v148\n"
"s_waitcnt lgkmcnt(5)\n"
"v_mfma_f32_16x16x32_bf16 v[40:43], a[0:3], v[100:103], v[40:43]\n"
"v_rcp_f32 v149, v149\n"
"v_mfma_f32_16x16x32_bf16 v[44:47], a[4:7], v[100:103], v[44:47]\n"
"v_rcp_f32 v150, v150\n"
"v_mfma_f32_16x16x32_bf16 v[56:59], a[8:11], v[100:103], v[56:59]\n"
"ds_read_b128 v[124:127], v5 offset:14592\n"
"v_mfma_f32_16x16x32_bf16 v[60:63], a[12:15], v[100:103], v[60:63]\n"
"v_rcp_f32 v151, v151\n"
"s_waitcnt lgkmcnt(5)\n"
"v_mfma_f32_16x16x32_bf16 v[32:35], a[16:19], v[104:107], v[32:35]\n"
"v_mul_f32 v144, v64, v144\n"
"v_mul_f32 v145, v65, v145\n"
"v_mfma_f32_16x16x32_bf16 v[36:39], a[20:23], v[104:107], v[36:39]\n"
"v_mul_f32 v146, v66, v146\n"
"v_mul_f32 v147, v67, v147\n"
"v_mfma_f32_16x16x32_bf16 v[48:51], a[24:27], v[104:107], v[48:51]\n"
"ds_read_b128 v[96:99], v5 offset:16384\n"
"v_mfma_f32_16x16x32_bf16 v[52:55], a[28:31], v[104:107], v[52:55]\n"
"v_mul_f32 v148, v68, v148\n"
"v_mul_f32 v149, v69, v149\n"
"s_waitcnt lgkmcnt(5)\n"
"v_mfma_f32_16x16x32_bf16 v[40:43], a[16:19], v[108:111], v[40:43]\n"
"v_mul_f32 v150, v70, v150\n"
"v_mul_f32 v151, v71, v151\n"
"v_mfma_f32_16x16x32_bf16 v[44:47], a[20:23], v[108:111], v[44:47]\n"
"v_mul_f32 v144, v80, v144\n"
"v_mul_f32 v145, v81, v145\n"
"v_mfma_f32_16x16x32_bf16 v[56:59], a[24:27], v[108:111], v[56:59]\n"
"ds_read_b128 v[100:103], v5 offset:16640\n"
"v_mfma_f32_16x16x32_bf16 v[60:63], a[28:31], v[108:111], v[60:63]\n"
"v_mul_f32 v146, v82, v146\n"
"v_mul_f32 v147, v83, v147\n"
"s_waitcnt lgkmcnt(5)\n"
"v_mfma_f32_16x16x32_bf16 v[32:35], a[32:35], v[112:115], v[32:35]\n"
"v_mul_f32 v148, v84, v148\n"
"v_mul_f32 v149, v85, v149\n"
"v_mfma_f32_16x16x32_bf16 v[36:39], a[36:39], v[112:115], v[36:39]\n"
"v_mul_f32 v150, v86, v150\n"
"v_mul_f32 v151, v87, v151\n"
"v_mfma_f32_16x16x32_bf16 v[48:51], a[40:43], v[112:115], v[48:51]\n"
"ds_read_b128 v[104:107], v5 offset:18432\n"
"v_mfma_f32_16x16x32_bf16 v[52:55], a[44:47], v[112:115], v[52:55]\n"
"v_cvt_pk_bf16_f32 v128, v144, v145\n"
"v_cvt_pk_bf16_f32 v129, v146, v147\n"
"s_waitcnt lgkmcnt(5)\n"
"v_mfma_f32_16x16x32_bf16 v[40:43], a[32:35], v[116:119], v[40:43]\n"
"v_cvt_pk_bf16_f32 v130, v148, v149\n"
"v_cvt_pk_bf16_f32 v131, v150, v151\n"
"v_mfma_f32_16x16x32_bf16 v[44:47], a[36:39], v[116:119], v[44:47]\n"
"buffer_store_dwordx4 v[128:131], v7, s[36:39], s74 offen offset:0 sc1\n"
"v_mfma_f32_16x16x32_bf16 v[56:59], a[40:43], v[116:119], v[56:59]\n"
"ds_read_b128 v[108:111], v5 offset:18688\n"
"v_mfma_f32_16x16x32_bf16 v[60:63], a[44:47], v[116:119], v[60:63]\n"
"v_mul_f32 v152, s75, v72\n"
"s_waitcnt lgkmcnt(5)\n"
"v_mfma_f32_16x16x32_bf16 v[32:35], a[48:51], v[120:123], v[32:35]\n"
"v_mul_f32 v153, s75, v73\n"
"v_mul_f32 v154, s75, v74\n"
"v_mfma_f32_16x16x32_bf16 v[36:39], a[52:55], v[120:123], v[36:39]\n"
"v_mul_f32 v155, s75, v75\n"
"v_mul_f32 v156, s75, v76\n"
"v_mfma_f32_16x16x32_bf16 v[48:51], a[56:59], v[120:123], v[48:51]\n"
"ds_read_b128 v[112:115], v5 offset:20480\n"
"v_mfma_f32_16x16x32_bf16 v[52:55], a[60:63], v[120:123], v[52:55]\n"
"v_mul_f32 v157, s75, v77\n"
"v_mul_f32 v158, s75, v78\n"
"s_waitcnt lgkmcnt(5)\n"
"v_mfma_f32_16x16x32_bf16 v[40:43], a[48:51], v[124:127], v[40:43]\n"
"v_mul_f32 v159, s75, v79\n"
"v_exp_f32 v152, v152\n"
"v_mfma_f32_16x16x32_bf16 v[44:47], a[52:55], v[124:127], v[44:47]\n"
"v_exp_f32 v153, v153\n"
"v_mfma_f32_16x16x32_bf16 v[56:59], a[56:59], v[124:127], v[56:59]\n"
"ds_read_b128 v[116:119], v5 offset:20736\n"
"v_mfma_f32_16x16x32_bf16 v[60:63], a[60:63], v[124:127], v[60:63]\n"
"v_exp_f32 v154, v154\n"
"s_waitcnt lgkmcnt(5)\n"
"v_mfma_f32_16x16x32_bf16 v[32:35], a[64:67], v[96:99], v[32:35]\n"
"v_exp_f32 v155, v155\n"
"v_mfma_f32_16x16x32_bf16 v[36:39], a[68:71], v[96:99], v[36:39]\n"
"v_exp_f32 v156, v156\n"
"v_mfma_f32_16x16x32_bf16 v[48:51], a[72:75], v[96:99], v[48:51]\n"
"ds_read_b128 v[120:123], v5 offset:22528\n"
"v_mfma_f32_16x16x32_bf16 v[52:55], a[76:79], v[96:99], v[52:55]\n"
"v_exp_f32 v157, v157\n"
"s_waitcnt lgkmcnt(5)\n"
"v_mfma_f32_16x16x32_bf16 v[40:43], a[64:67], v[100:103], v[40:43]\n"
"v_exp_f32 v158, v158\n"
"v_mfma_f32_16x16x32_bf16 v[44:47], a[68:71], v[100:103], v[44:47]\n"
"v_exp_f32 v159, v159\n"
"v_mfma_f32_16x16x32_bf16 v[56:59], a[72:75], v[100:103], v[56:59]\n"
"ds_read_b128 v[124:127], v5 offset:22784\n"
"v_mfma_f32_16x16x32_bf16 v[60:63], a[76:79], v[100:103], v[60:63]\n"
"v_add_f32 v152, 1.0, v152\n"
"s_waitcnt lgkmcnt(5)\n"
"v_mfma_f32_16x16x32_bf16 v[32:35], a[80:83], v[104:107], v[32:35]\n"
"v_add_f32 v153, 1.0, v153\n"
"v_add_f32 v154, 1.0, v154\n"
"v_mfma_f32_16x16x32_bf16 v[36:39], a[84:87], v[104:107], v[36:39]\n"
"v_add_f32 v155, 1.0, v155\n"
"v_add_f32 v156, 1.0, v156\n"
"v_mfma_f32_16x16x32_bf16 v[48:51], a[88:91], v[104:107], v[48:51]\n"
"ds_read_b128 v[96:99], v5 offset:24576\n"
"v_mfma_f32_16x16x32_bf16 v[52:55], a[92:95], v[104:107], v[52:55]\n"
"v_add_f32 v157, 1.0, v157\n"
"v_add_f32 v158, 1.0, v158\n"
"s_waitcnt lgkmcnt(5)\n"
"v_mfma_f32_16x16x32_bf16 v[40:43], a[80:83], v[108:111], v[40:43]\n"
"v_add_f32 v159, 1.0, v159\n"
"v_rcp_f32 v152, v152\n"
"v_mfma_f32_16x16x32_bf16 v[44:47], a[84:87], v[108:111], v[44:47]\n"
"v_rcp_f32 v153, v153\n"
"v_mfma_f32_16x16x32_bf16 v[56:59], a[88:91], v[108:111], v[56:59]\n"
"ds_read_b128 v[100:103], v5 offset:24832\n"
"v_mfma_f32_16x16x32_bf16 v[60:63], a[92:95], v[108:111], v[60:63]\n"
"v_rcp_f32 v154, v154\n"
"s_waitcnt lgkmcnt(5)\n"
"v_mfma_f32_16x16x32_bf16 v[32:35], a[96:99], v[112:115], v[32:35]\n"
"v_rcp_f32 v155, v155\n"
"v_mfma_f32_16x16x32_bf16 v[36:39], a[100:103], v[112:115], v[36:39]\n"
"v_rcp_f32 v156, v156\n"
"v_mfma_f32_16x16x32_bf16 v[48:51], a[104:107], v[112:115], v[48:51]\n"
"ds_read_b128 v[104:107], v5 offset:26624\n"
"v_mfma_f32_16x16x32_bf16 v[52:55], a[108:111], v[112:115], v[52:55]\n"
"v_rcp_f32 v157, v157\n"
"s_waitcnt lgkmcnt(5)\n"
"v_mfma_f32_16x16x32_bf16 v[40:43], a[96:99], v[116:119], v[40:43]\n"
"v_rcp_f32 v158, v158\n"
"v_mfma_f32_16x16x32_bf16 v[44:47], a[100:103], v[116:119], v[44:47]\n"
"v_rcp_f32 v159, v159\n"
"v_mfma_f32_16x16x32_bf16 v[56:59], a[104:107], v[116:119], v[56:59]\n"
"ds_read_b128 v[108:111], v5 offset:26880\n"
"v_mfma_f32_16x16x32_bf16 v[60:63], a[108:111], v[116:119], v[60:63]\n"
"v_mul_f32 v152, v72, v152\n"
"s_waitcnt lgkmcnt(5)\n"
"v_mfma_f32_16x16x32_bf16 v[32:35], a[112:115], v[120:123], v[32:35]\n"
"v_mul_f32 v153, v73, v153\n"
"v_mul_f32 v154, v74, v154\n"
"v_mfma_f32_16x16x32_bf16 v[36:39], a[116:119], v[120:123], v[36:39]\n"
"v_mul_f32 v155, v75, v155\n"
"v_mul_f32 v156, v76, v156\n"
"v_mfma_f32_16x16x32_bf16 v[48:51], a[120:123], v[120:123], v[48:51]\n"
"ds_read_b128 v[112:115], v5 offset:28672\n"
"v_mfma_f32_16x16x32_bf16 v[52:55], a[124:127], v[120:123], v[52:55]\n"
"v_mul_f32 v157, v77, v157\n"
"v_mul_f32 v158, v78, v158\n"
"s_waitcnt lgkmcnt(5)\n"
"v_mfma_f32_16x16x32_bf16 v[40:43], a[112:115], v[124:127], v[40:43]\n"
"v_mul_f32 v159, v79, v159\n"
"v_mul_f32 v152, v88, v152\n"
"v_mfma_f32_16x16x32_bf16 v[44:47], a[116:119], v[124:127], v[44:47]\n"
"v_mul_f32 v153, v89, v153\n"
"v_mul_f32 v154, v90, v154\n"
"v_mfma_f32_16x16x32_bf16 v[56:59], a[120:123], v[124:127], v[56:59]\n"
"ds_read_b128 v[116:119], v5 offset:28928\n"
"v_mfma_f32_16x16x32_bf16 v[60:63], a[124:127], v[124:127], v[60:63]\n"
"v_mul_f32 v155, v91, v155\n"
"v_mul_f32 v156, v92, v156\n"
"s_waitcnt lgkmcnt(5)\n"
"v_mfma_f32_16x16x32_bf16 v[32:35], a[128:131], v[96:99], v[32:35]\n"
"v_mul_f32 v157, v93, v157\n"
"v_mul_f32 v158, v94, v158\n"
"v_mfma_f32_16x16x32_bf16 v[36:39], a[132:135], v[96:99], v[36:39]\n"
"v_mul_f32 v159, v95, v159\n"
"v_cvt_pk_bf16_f32 v132, v152, v153\n"
"v_mfma_f32_16x16x32_bf16 v[48:51], a[136:139], v[96:99], v[48:51]\n"
"ds_read_b128 v[120:123], v5 offset:30720\n"
"v_mfma_f32_16x16x32_bf16 v[52:55], a[140:143], v[96:99], v[52:55]\n"
"v_cvt_pk_bf16_f32 v133, v154, v155\n"
"v_cvt_pk_bf16_f32 v134, v156, v157\n"
"s_waitcnt lgkmcnt(5)\n"
"v_mfma_f32_16x16x32_bf16 v[40:43], a[128:131], v[100:103], v[40:43]\n"
"v_cvt_pk_bf16_f32 v135, v158, v159\n"
"v_mfma_f32_16x16x32_bf16 v[44:47], a[132:135], v[100:103], v[44:47]\n"
"buffer_store_dwordx4 v[132:135], v7, s[36:39], s74 offen offset:256 sc1\n"
"v_mfma_f32_16x16x32_bf16 v[56:59], a[136:139], v[100:103], v[56:59]\n"
"ds_read_b128 v[124:127], v5 offset:30976\n"
"v_mfma_f32_16x16x32_bf16 v[60:63], a[140:143], v[100:103], v[60:63]\n"
"s_waitcnt lgkmcnt(5)\n"
"v_mfma_f32_16x16x32_bf16 v[32:35], a[144:147], v[104:107], v[32:35]\n"
"v_mfma_f32_16x16x32_bf16 v[36:39], a[148:151], v[104:107], v[36:39]\n"
"v_mfma_f32_16x16x32_bf16 v[48:51], a[152:155], v[104:107], v[48:51]\n"
"ds_read_b128 v[96:99], v5 offset:32768\n"
"v_mfma_f32_16x16x32_bf16 v[52:55], a[156:159], v[104:107], v[52:55]\n"
"s_waitcnt lgkmcnt(5)\n"
"v_mfma_f32_16x16x32_bf16 v[40:43], a[144:147], v[108:111], v[40:43]\n"
"v_mfma_f32_16x16x32_bf16 v[44:47], a[148:151], v[108:111], v[44:47]\n"
"v_mfma_f32_16x16x32_bf16 v[56:59], a[152:155], v[108:111], v[56:59]\n"
"ds_read_b128 v[100:103], v5 offset:33024\n"
"v_mfma_f32_16x16x32_bf16 v[60:63], a[156:159], v[108:111], v[60:63]\n"
"s_waitcnt lgkmcnt(5)\n"
"v_mfma_f32_16x16x32_bf16 v[32:35], a[160:163], v[112:115], v[32:35]\n"
"v_mfma_f32_16x16x32_bf16 v[36:39], a[164:167], v[112:115], v[36:39]\n"
"v_mfma_f32_16x16x32_bf16 v[48:51], a[168:171], v[112:115], v[48:51]\n"
"ds_read_b128 v[104:107], v5 offset:34816\n"
"v_mfma_f32_16x16x32_bf16 v[52:55], a[172:175], v[112:115], v[52:55]\n"
"s_waitcnt lgkmcnt(5)\n"
"v_mfma_f32_16x16x32_bf16 v[40:43], a[160:163], v[116:119], v[40:43]\n"
"v_mfma_f32_16x16x32_bf16 v[44:47], a[164:167], v[116:119], v[44:47]\n"
"v_mfma_f32_16x16x32_bf16 v[56:59], a[168:171], v[116:119], v[56:59]\n"
"ds_read_b128 v[108:111], v5 offset:35072\n"
"v_mfma_f32_16x16x32_bf16 v[60:63], a[172:175], v[116:119], v[60:63]\n"
"s_waitcnt lgkmcnt(5)\n"
"v_mfma_f32_16x16x32_bf16 v[32:35], a[176:179], v[120:123], v[32:35]\n"
"v_mfma_f32_16x16x32_bf16 v[36:39], a[180:183], v[120:123], v[36:39]\n"
"v_mfma_f32_16x16x32_bf16 v[48:51], a[184:187], v[120:123], v[48:51]\n"
"ds_read_b128 v[112:115], v5 offset:36864\n"
"v_mfma_f32_16x16x32_bf16 v[52:55], a[188:191], v[120:123], v[52:55]\n"
"v_sub_u32_e64 v6, s73, 1 clamp\n"
"s_waitcnt lgkmcnt(5)\n"
"v_mfma_f32_16x16x32_bf16 v[40:43], a[176:179], v[124:127], v[40:43]\n"
"s_waitcnt vmcnt(2)\n"
"v_mfma_f32_16x16x32_bf16 v[44:47], a[180:183], v[124:127], v[44:47]\n"
"buffer_store_dword v6, v4, s[36:39], 0 offen sc1\n"
"v_mfma_f32_16x16x32_bf16 v[56:59], a[184:187], v[124:127], v[56:59]\n"
"ds_read_b128 v[116:119], v5 offset:37120\n"
"v_mfma_f32_16x16x32_bf16 v[60:63], a[188:191], v[124:127], v[60:63]\n"
"s_mov_b32 s74, s47\n"
"s_add_u32 s47, s47, s48\n"
"s_waitcnt lgkmcnt(5)\n"
"v_mfma_f32_16x16x32_bf16 v[32:35], a[192:195], v[96:99], v[32:35]\n"
"s_add_u32 s73, s73, 1\n"
"v_mfma_f32_16x16x32_bf16 v[36:39], a[196:199], v[96:99], v[36:39]\n"
"s_add_u32 m0, s71, 0\n"
"s_add_u32 s72, s68, 0\n"
"buffer_load_dwordx4 v0, s[36:39], s72 offen offset:0 sc1 lds\n"
"v_mfma_f32_16x16x32_bf16 v[48:51], a[200:203], v[96:99], v[48:51]\n"
"ds_read_b128 v[120:123], v5 offset:38912\n"
"v_mfma_f32_16x16x32_bf16 v[52:55], a[204:207], v[96:99], v[52:55]\n"
"buffer_load_dwordx4 v0, s[36:39], s72 offen offset:1024 sc1 lds\n"
"s_waitcnt lgkmcnt(5)\n"
"v_mfma_f32_16x16x32_bf16 v[40:43], a[192:195], v[100:103], v[40:43]\n"
"v_mfma_f32_16x16x32_bf16 v[44:47], a[196:199], v[100:103], v[44:47]\n"
"buffer_load_dwordx4 v0, s[36:39], s72 offen offset:2048 sc1 lds\n"
"v_mfma_f32_16x16x32_bf16 v[56:59], a[200:203], v[100:103], v[56:59]\n"
"ds_read_b128 v[124:127], v5 offset:39168\n"
"v_mfma_f32_16x16x32_bf16 v[60:63], a[204:207], v[100:103], v[60:63]\n"
"buffer_load_dwordx4 v0, s[36:39], s72 offen offset:3072 sc1 lds\n"
"s_waitcnt lgkmcnt(5)\n"
"v_mfma_f32_16x16x32_bf16 v[32:35], a[208:211], v[104:107], v[32:35]\n"
"v_mfma_f32_16x16x32_bf16 v[36:39], a[212:215], v[104:107], v[36:39]\n"
"s_add_u32 m0, s71, 4096\n"
"s_add_u32 s72, s68, 4096\n"
"buffer_load_dwordx4 v0, s[36:39], s72 offen offset:0 sc1 lds\n"
"v_mfma_f32_16x16x32_bf16 v[48:51], a[216:219], v[104:107], v[48:51]\n"
"s_add_u32 s70, s70, 40960\n"
"s_cmp_eq_u32 s70, 122880\n"
"s_cselect_b32 s70, 0, s70\n"
"v_mfma_f32_16x16x32_bf16 v[52:55], a[220:223], v[104:107], v[52:55]\n"
"buffer_load_dwordx4 v0, s[36:39], s72 offen offset:1024 sc1 lds\n"
"s_waitcnt lgkmcnt(4)\n"
"v_mfma_f32_16x16x32_bf16 v[40:43], a[208:211], v[108:111], v[40:43]\n"
"v_add_u32 v5, s70, v7\n"
"v_mfma_f32_16x16x32_bf16 v[44:47], a[212:215], v[108:111], v[44:47]\n"
"buffer_load_dwordx4 v0, s[36:39], s72 offen offset:2048 sc1 lds\n"
"v_mfma_f32_16x16x32_bf16 v[56:59], a[216:219], v[108:111], v[56:59]\n"
"s_waitcnt lgkmcnt(0)\n"
"s_barrier\n"
"v_mfma_f32_16x16x32_bf16 v[60:63], a[220:223], v[108:111], v[60:63]\n"
"ds_read_b128 v[96:99], v5 offset:0\n"
"v_mfma_f32_16x16x32_bf16 v[32:35], a[224:227], v[112:115], v[32:35]\n"
"v_mfma_f32_16x16x32_bf16 v[36:39], a[228:231], v[112:115], v[36:39]\n"
"ds_read_b128 v[100:103], v5 offset:256\n"
"v_mfma_f32_16x16x32_bf16 v[48:51], a[232:235], v[112:115], v[48:51]\n"
"v_mfma_f32_16x16x32_bf16 v[52:55], a[236:239], v[112:115], v[52:55]\n"
"ds_read_b128 v[104:107], v5 offset:2048\n"
"v_mfma_f32_16x16x32_bf16 v[40:43], a[224:227], v[116:119], v[40:43]\n"
"v_mfma_f32_16x16x32_bf16 v[44:47], a[228:231], v[116:119], v[44:47]\n"
"buffer_load_dwordx4 v0, s[36:39], s72 offen offset:3072 sc1 lds\n"
"v_mfma_f32_16x16x32_bf16 v[56:59], a[232:235], v[116:119], v[56:59]\n"
"ds_read_b128 v[108:111], v5 offset:2304\n"
"v_mfma_f32_16x16x32_bf16 v[60:63], a[236:239], v[116:119], v[60:63]\n"
"s_add_u32 m0, s71, 8192\n"
"s_add_u32 s72, s68, 8192\n"
"buffer_load_dwordx4 v0, s[36:39], s72 offen offset:0 sc1 lds\n"
"v_mfma_f32_16x16x32_bf16 v[32:35], a[240:243], v[120:123], v[32:35]\n"
"v_mfma_f32_16x16x32_bf16 v[36:39], a[244:247], v[120:123], v[36:39]\n"
"buffer_load_dwordx4 v0, s[36:39], s72 offen offset:1024 sc1 lds\n"
"v_mfma_f32_16x16x32_bf16 v[48:51], a[248:251], v[120:123], v[48:51]\n"
"ds_read_b128 v[112:115], v5 offset:4096\n"
"v_mfma_f32_16x16x32_bf16 v[52:55], a[252:255], v[120:123], v[52:55]\n"
"v_mfma_f32_16x16x32_bf16 v[40:43], a[240:243], v[124:127], v[40:43]\n"
"s_add_u32 s68, s68, 40960\n"
"s_add_u32 s69, s69, 40960\n"
"v_mfma_f32_16x16x32_bf16 v[44:47], a[244:247], v[124:127], v[44:47]\n"
"s_cmp_eq_u32 s69, 122880\n"
"s_cselect_b32 s69, 0, s69\n"
"v_mfma_f32_16x16x32_bf16 v[56:59], a[248:251], v[124:127], v[56:59]\n"
"ds_read_b128 v[116:119], v5 offset:4352\n"
"v_mfma_f32_16x16x32_bf16 v[60:63], a[252:255], v[124:127], v[60:63]\n"
"s_add_u32 s71, s69, s50\n"
"s_waitcnt lgkmcnt(5)\n"
"v_mfma_f32_16x16x32_bf16 v[64:67], v[192:195], v[96:99], 0\n"
"v_mfma_f32_16x16x32_bf16 v[68:71], v[196:199], v[96:99], 0\n"
"v_mfma_f32_16x16x32_bf16 v[80:83], v[200:203], v[96:99], 0\n"
"ds_read_b128 v[120:123], v5 offset:6144\n"
"v_mfma_f32_16x16x32_bf16 v[84:87], v[204:207], v[96:99], 0\n"
"s_waitcnt lgkmcnt(5)\n"
"v_mfma_f32_16x16x32_bf16 v[72:75], v[192:195], v[100:103], 0\n"
"v_mfma_f32_16x16x32_bf16 v[76:79], v[196:199], v[100:103], 0\n"
"v_mfma_f32_16x16x32_bf16 v[88:91], v[200:203], v[100:103], 0\n"
"ds_read_b128 v[124:127], v5 offset:6400\n"
"v_mfma_f32_16x16x32_bf16 v[92:95], v[204:207], v[100:103], 0\n"
"s_waitcnt lgkmcnt(5)\n"
"v_mfma_f32_16x16x32_bf16 v[64:67], v[208:211], v[104:107], v[64:67]\n"
"v_mul_f32 v144, s75, v32\n"
"v_mul_f32 v145, s75, v33\n"
"v_mfma_f32_16x16x32_bf16 v[68:71], v[212:215], v[104:107], v[68:71]\n"
"v_mul_f32 v146, s75, v34\n"
"v_mul_f32 v147, s75, v35\n"
"v_mfma_f32_16x16x32_bf16 v[80:83], v[216:219], v[104:107], v[80:83]\n"
"ds_read_b128 v[96:99], v5 offset:8192\n"
"v_mfma_f32_16x16x32_bf16 v[84:87], v[220:223], v[104:107], v[84:87]\n"
"v_mul_f32 v148, s75, v36\n"
"v_mul_f32 v149, s75, v37\n"
"s_waitcnt lgkmcnt(5)\n"
"v_mfma_f32_16x16x32_bf16 v[72:75], v[208:211], v[108:111], v[72:75]\n"
"v_mul_f32 v150, s75, v38\n"
"v_mul_f32 v151, s75, v39\n"
"v_mfma_f32_16x16x32_bf16 v[76:79], v[212:215], v[108:111], v[76:79]\n"
"v_exp_f32 v144, v144\n"
"v_mfma_f32_16x16x32_bf16 v[88:91], v[216:219], v[108:111], v[88:91]\n"
"ds_read_b128 v[100:103], v5 offset:8448\n"
"v_mfma_f32_16x16x32_bf16 v[92:95], v[220:223], v[108:111], v[92:95]\n"
"v_exp_f32 v145, v145\n"
"s_waitcnt lgkmcnt(5)\n"
"v_mfma_f32_16x16x32_bf16 v[64:67], v[224:227], v[112:115], v[64:67]\n"
"v_exp_f32 v146, v146\n"
"v_mfma_f32_16x16x32_bf16 v[68:71], v[228:231], v[112:115], v[68:71]\n"
"v_exp_f32 v147, v147\n"
"v_mfma_f32_16x16x32_bf16 v[80:83], v[232:235], v[112:115], v[80:83]\n"
"ds_read_b128 v[104:107], v5 offset:10240\n"
"v_mfma_f32_16x16x32_bf16 v[84:87], v[236:239], v[112:115], v[84:87]\n"
"v_exp_f32 v148, v148\n"
"s_waitcnt lgkmcnt(5)\n"
"v_mfma_f32_16x16x32_bf16 v[72:75], v[224:227], v[116:119], v[72:75]\n"
"v_exp_f32 v149, v149\n"
"v_mfma_f32_16x16x32_bf16 v[76:79], v[228:231], v[116:119], v[76:79]\n"
"v_exp_f32 v150, v150\n"
"v_mfma_f32_16x16x32_bf16 v[88:91], v[232:235], v[116:119], v[88:91]\n"
"ds_read_b128 v[108:111], v5 offset:10496\n"
"v_mfma_f32_16x16x32_bf16 v[92:95], v[236:239], v[116:119], v[92:95]\n"
"v_exp_f32 v151, v151\n"
"s_waitcnt lgkmcnt(5)\n"
"v_mfma_f32_16x16x32_bf16 v[64:67], v[240:243], v[120:123], v[64:67]\n"
"v_add_f32 v144, 1.0, v144\n"
"v_add_f32 v145, 1.0, v145\n"
"v_mfma_f32_16x16x32_bf16 v[68:71], v[244:247], v[120:123], v[68:71]\n"
"v_add_f32 v146, 1.0, v146\n"
"v_add_f32 v147, 1.0, v147\n"
"v_mfma_f32_16x16x32_bf16 v[80:83], v[248:251], v[120:123], v[80:83]\n"
"ds_read_b128 v[112:115], v5 offset:12288\n"
"v_mfma_f32_16x16x32_bf16 v[84:87], v[252:255], v[120:123], v[84:87]\n"
"v_add_f32 v148, 1.0, v148\n"
"v_add_f32 v149, 1.0, v149\n"
"s_waitcnt lgkmcnt(5)\n"
"v_mfma_f32_16x16x32_bf16 v[72:75], v[240:243], v[124:127], v[72:75]\n"
"v_add_f32 v150, 1.0, v150\n"
"v_add_f32 v151, 1.0, v151\n"
"v_mfma_f32_16x16x32_bf16 v[76:79], v[244:247], v[124:127], v[76:79]\n"
"v_rcp_f32 v144, v144\n"
"v_mfma_f32_16x16x32_bf16 v[88:91], v[248:251], v[124:127], v[88:91]\n"
"ds_read_b128 v[116:119], v5 offset:12544\n"
"v_mfma_f32_16x16x32_bf16 v[92:95], v[252:255], v[124:127], v[92:95]\n"
"v_rcp_f32 v145, v145\n"
"s_waitcnt lgkmcnt(5)\n"
"v_mfma_f32_16x16x32_bf16 v[64:67], a[0:3], v[96:99], v[64:67]\n"
"v_rcp_f32 v146, v146\n"
"v_mfma_f32_16x16x32_bf16 v[68:71], a[4:7], v[96:99], v[68:71]\n"
"v_rcp_f32 v147, v147\n"
"v_mfma_f32_16x16x32_bf16 v[80:83], a[8:11], v[96:99], v[80:83]\n"
"ds_read_b128 v[120:123], v5 offset:14336\n"
"v_mfma_f32_16x16x32_bf16 v[84:87], a[12:15], v[96:99], v[84:87]\n"
"v_rcp_f32 v148, v148\n"
"s_waitcnt lgkmcnt(5)\n"
"v_mfma_f32_16x16x32_bf16 v[72:75], a[0:3], v[100:103], v[72:75]\n"
"v_rcp_f32 v149, v149\n"
"v_mfma_f32_16x16x32_bf16 v[76:79], a[4:7], v[100:103], v[76:79]\n"
"v_rcp_f32 v150, v150\n"
"v_mfma_f32_16x16x32_bf16 v[88:91], a[8:11], v[100:103], v[88:91]\n"
"ds_read_b128 v[124:127], v5 offset:14592\n"
"v_mfma_f32_16x16x32_bf16 v[92:95], a[12:15], v[100:103], v[92:95]\n"
"v_rcp_f32 v151, v151\n"
"s_waitcnt lgkmcnt(5)\n"
"v_mfma_f32_16x16x32_bf16 v[64:67], a[16:19], v[104:107], v[64:67]\n"
"v_mul_f32 v144, v32, v144\n"
"v_mul_f32 v145, v33, v145\n"
"v_mfma_f32_16x16x32_bf16 v[68:71], a[20:23], v[104:107], v[68:71]\n"
"v_mul_f32 v146, v34, v146\n"
"v_mul_f32 v147, v35, v147\n"
"v_mfma_f32_16x16x32_bf16 v[80:83], a[24:27], v[104:107], v[80:83]\n"
"ds_read_b128 v[96:99], v5 offset:16384\n"
"v_mfma_f32_16x16x32_bf16 v[84:87], a[28:31], v[104:107], v[84:87]\n"
"v_mul_f32 v148, v36, v148\n"
"v_mul_f32 v149, v37, v149\n"
"s_waitcnt lgkmcnt(5)\n"
"v_mfma_f32_16x16x32_bf16 v[72:75], a[16:19], v[108:111], v[72:75]\n"
"v_mul_f32 v150, v38, v150\n"
"v_mul_f32 v151, v39, v151\n"
"v_mfma_f32_16x16x32_bf16 v[76:79], a[20:23], v[108:111], v[76:79]\n"
"v_mul_f32 v144, v48, v144\n"
"v_mul_f32 v145, v49, v145\n"
"v_mfma_f32_16x16x32_bf16 v[88:91], a[24:27], v[108:111], v[88:91]\n"
"ds_read_b128 v[100:103], v5 offset:16640\n"
"v_mfma_f32_16x16x32_bf16 v[92:95], a[28:31], v[108:111], v[92:95]\n"
"v_mul_f32 v146, v50, v146\n"
"v_mul_f32 v147, v51, v147\n"
"s_waitcnt lgkmcnt(5)\n"
"v_mfma_f32_16x16x32_bf16 v[64:67], a[32:35], v[112:115], v[64:67]\n"
"v_mul_f32 v148, v52, v148\n"
"v_mul_f32 v149, v53, v149\n"
"v_mfma_f32_16x16x32_bf16 v[68:71], a[36:39], v[112:115], v[68:71]\n"
"v_mul_f32 v150, v54, v150\n"
"v_mul_f32 v151, v55, v151\n"
"v_mfma_f32_16x16x32_bf16 v[80:83], a[40:43], v[112:115], v[80:83]\n"
"ds_read_b128 v[104:107], v5 offset:18432\n"
"v_mfma_f32_16x16x32_bf16 v[84:87], a[44:47], v[112:115], v[84:87]\n"
"v_cvt_pk_bf16_f32 v128, v144, v145\n"
"v_cvt_pk_bf16_f32 v129, v146, v147\n"
"s_waitcnt lgkmcnt(5)\n"
"v_mfma_f32_16x16x32_bf16 v[72:75], a[32:35], v[116:119], v[72:75]\n"
"v_cvt_pk_bf16_f32 v130, v148, v149\n"
"v_cvt_pk_bf16_f32 v131, v150, v151\n"
"v_mfma_f32_16x16x32_bf16 v[76:79], a[36:39], v[116:119], v[76:79]\n"
"buffer_store_dwordx4 v[128:131], v7, s[36:39], s74 offen offset:0 sc1\n"
"v_mfma_f32_16x16x32_bf16 v[88:91], a[40:43], v[116:119], v[88:91]\n"
"ds_read_b128 v[108:111], v5 offset:18688\n"
"v_mfma_f32_16x16x32_bf16 v[92:95], a[44:47], v[116:119], v[92:95]\n"
"v_mul_f32 v152, s75, v40\n"
"s_waitcnt lgkmcnt(5)\n"
"v_mfma_f32_16x16x32_bf16 v[64:67], a[48:51], v[120:123], v[64:67]\n"
"v_mul_f32 v153, s75, v41\n"
"v_mul_f32 v154, s75, v42\n"
"v_mfma_f32_16x16x32_bf16 v[68:71], a[52:55], v[120:123], v[68:71]\n"
"v_mul_f32 v155, s75, v43\n"
"v_mul_f32 v156, s75, v44\n"
"v_mfma_f32_16x16x32_bf16 v[80:83], a[56:59], v[120:123], v[80:83]\n"
"ds_read_b128 v[112:115], v5 offset:20480\n"
"v_mfma_f32_16x16x32_bf16 v[84:87], a[60:63], v[120:123], v[84:87]\n"
"v_mul_f32 v157, s75, v45\n"
"v_mul_f32 v158, s75, v46\n"
"s_waitcnt lgkmcnt(5)\n"
"v_mfma_f32_16x16x32_bf16 v[72:75], a[48:51], v[124:127], v[72:75]\n"
"v_mul_f32 v159, s75, v47\n"
"v_exp_f32 v152, v152\n"
"v_mfma_f32_16x16x32_bf16 v[76:79], a[52:55], v[124:127], v[76:79]\n"
"v_exp_f32 v153, v153\n"
"v_mfma_f32_16x16x32_bf16 v[88:91], a[56:59], v[124:127], v[88:91]\n"
"ds_read_b128 v[116:119], v5 offset:20736\n"
"v_mfma_f32_16x16x32_bf16 v[92:95], a[60:63], v[124:127], v[92:95]\n"
"v_exp_f32 v154, v154\n"
"s_waitcnt lgkmcnt(5)\n"
"v_mfma_f32_16x16x32_bf16 v[64:67], a[64:67], v[96:99], v[64:67]\n"
"v_exp_f32 v155, v155\n"
"v_mfma_f32_16x16x32_bf16 v[68:71], a[68:71], v[96:99], v[68:71]\n"
"v_exp_f32 v156, v156\n"
"v_mfma_f32_16x16x32_bf16 v[80:83], a[72:75], v[96:99], v[80:83]\n"
"ds_read_b128 v[120:123], v5 offset:22528\n"
"v_mfma_f32_16x16x32_bf16 v[84:87], a[76:79], v[96:99], v[84:87]\n"
"v_exp_f32 v157, v157\n"
"s_waitcnt lgkmcnt(5)\n"
"v_mfma_f32_16x16x32_bf16 v[72:75], a[64:67], v[100:103], v[72:75]\n"
"v_exp_f32 v158, v158\n"
"v_mfma_f32_16x16x32_bf16 v[76:79], a[68:71], v[100:103], v[76:79]\n"
"v_exp_f32 v159, v159\n"
"v_mfma_f32_16x16x32_bf16 v[88:91], a[72:75], v[100:103], v[88:91]\n"
"ds_read_b128 v[124:127], v5 offset:22784\n"
"v_mfma_f32_16x16x32_bf16 v[92:95], a[76:79], v[100:103], v[92:95]\n"
"v_add_f32 v152, 1.0, v152\n"
"s_waitcnt lgkmcnt(5)\n"
"v_mfma_f32_16x16x32_bf16 v[64:67], a[80:83], v[104:107], v[64:67]\n"
"v_add_f32 v153, 1.0, v153\n"
"v_add_f32 v154, 1.0, v154\n"
"v_mfma_f32_16x16x32_bf16 v[68:71], a[84:87], v[104:107], v[68:71]\n"
"v_add_f32 v155, 1.0, v155\n"
"v_add_f32 v156, 1.0, v156\n"
"v_mfma_f32_16x16x32_bf16 v[80:83], a[88:91], v[104:107], v[80:83]\n"
"ds_read_b128 v[96:99], v5 offset:24576\n"
"v_mfma_f32_16x16x32_bf16 v[84:87], a[92:95], v[104:107], v[84:87]\n"
"v_add_f32 v157, 1.0, v157\n"
"v_add_f32 v158, 1.0, v158\n"
"s_waitcnt lgkmcnt(5)\n"
"v_mfma_f32_16x16x32_bf16 v[72:75], a[80:83], v[108:111], v[72:75]\n"
"v_add_f32 v159, 1.0, v159\n"
"v_rcp_f32 v152, v152\n"
"v_mfma_f32_16x16x32_bf16 v[76:79], a[84:87], v[108:111], v[76:79]\n"
"v_rcp_f32 v153, v153\n"
"v_mfma_f32_16x16x32_bf16 v[88:91], a[88:91], v[108:111], v[88:91]\n"
"ds_read_b128 v[100:103], v5 offset:24832\n"
"v_mfma_f32_16x16x32_bf16 v[92:95], a[92:95], v[108:111], v[92:95]\n"
"v_rcp_f32 v154, v154\n"
"s_waitcnt lgkmcnt(5)\n"
"v_mfma_f32_16x16x32_bf16 v[64:67], a[96:99], v[112:115], v[64:67]\n"
"v_rcp_f32 v155, v155\n"
"v_mfma_f32_16x16x32_bf16 v[68:71], a[100:103], v[112:115], v[68:71]\n"
"v_rcp_f32 v156, v156\n"
"v_mfma_f32_16x16x32_bf16 v[80:83], a[104:107], v[112:115], v[80:83]\n"
"ds_read_b128 v[104:107], v5 offset:26624\n"
"v_mfma_f32_16x16x32_bf16 v[84:87], a[108:111], v[112:115], v[84:87]\n"
"v_rcp_f32 v157, v157\n"
"s_waitcnt lgkmcnt(5)\n"
"v_mfma_f32_16x16x32_bf16 v[72:75], a[96:99], v[116:119], v[72:75]\n"
"v_rcp_f32 v158, v158\n"
"v_mfma_f32_16x16x32_bf16 v[76:79], a[100:103], v[116:119], v[76:79]\n"
"v_rcp_f32 v159, v159\n"
"v_mfma_f32_16x16x32_bf16 v[88:91], a[104:107], v[116:119], v[88:91]\n"
"ds_read_b128 v[108:111], v5 offset:26880\n"
"v_mfma_f32_16x16x32_bf16 v[92:95], a[108:111], v[116:119], v[92:95]\n"
"v_mul_f32 v152, v40, v152\n"
"s_waitcnt lgkmcnt(5)\n"
"v_mfma_f32_16x16x32_bf16 v[64:67], a[112:115], v[120:123], v[64:67]\n"
"v_mul_f32 v153, v41, v153\n"
"v_mul_f32 v154, v42, v154\n"
"v_mfma_f32_16x16x32_bf16 v[68:71], a[116:119], v[120:123], v[68:71]\n"
"v_mul_f32 v155, v43, v155\n"
"v_mul_f32 v156, v44, v156\n"
"v_mfma_f32_16x16x32_bf16 v[80:83], a[120:123], v[120:123], v[80:83]\n"
"ds_read_b128 v[112:115], v5 offset:28672\n"
"v_mfma_f32_16x16x32_bf16 v[84:87], a[124:127], v[120:123], v[84:87]\n"
"v_mul_f32 v157, v45, v157\n"
"v_mul_f32 v158, v46, v158\n"
"s_waitcnt lgkmcnt(5)\n"
"v_mfma_f32_16x16x32_bf16 v[72:75], a[112:115], v[124:127], v[72:75]\n"
"v_mul_f32 v159, v47, v159\n"
"v_mul_f32 v152, v56, v152\n"
"v_mfma_f32_16x16x32_bf16 v[76:79], a[116:119], v[124:127], v[76:79]\n"
"v_mul_f32 v153, v57, v153\n"
"v_mul_f32 v154, v58, v154\n"
"v_mfma_f32_16x16x32_bf16 v[88:91], a[120:123], v[124:127], v[88:91]\n"
"ds_read_b128 v[116:119], v5 offset:28928\n"
"v_mfma_f32_16x16x32_bf16 v[92:95], a[124:127], v[124:127], v[92:95]\n"
"v_mul_f32 v155, v59, v155\n"
"v_mul_f32 v156, v60, v156\n"
"s_waitcnt lgkmcnt(5)\n"
"v_mfma_f32_16x16x32_bf16 v[64:67], a[128:131], v[96:99], v[64:67]\n"
"v_mul_f32 v157, v61, v157\n"
"v_mul_f32 v158, v62, v158\n"
"v_mfma_f32_16x16x32_bf16 v[68:71], a[132:135], v[96:99], v[68:71]\n"
"v_mul_f32 v159, v63, v159\n"
"v_cvt_pk_bf16_f32 v132, v152, v153\n"
"v_mfma_f32_16x16x32_bf16 v[80:83], a[136:139], v[96:99], v[80:83]\n"
"ds_read_b128 v[120:123], v5 offset:30720\n"
"v_mfma_f32_16x16x32_bf16 v[84:87], a[140:143], v[96:99], v[84:87]\n"
"v_cvt_pk_bf16_f32 v133, v154, v155\n"
"v_cvt_pk_bf16_f32 v134, v156, v157\n"
"s_waitcnt lgkmcnt(5)\n"
"v_mfma_f32_16x16x32_bf16 v[72:75], a[128:131], v[100:103], v[72:75]\n"
"v_cvt_pk_bf16_f32 v135, v158, v159\n"
"v_mfma_f32_16x16x32_bf16 v[76:79], a[132:135], v[100:103], v[76:79]\n"
"buffer_store_dwordx4 v[132:135], v7, s[36:39], s74 offen offset:256 sc1\n"
"v_mfma_f32_16x16x32_bf16 v[88:91], a[136:139], v[100:103], v[88:91]\n"
"ds_read_b128 v[124:127], v5 offset:30976\n"
"v_mfma_f32_16x16x32_bf16 v[92:95], a[140:143], v[100:103], v[92:95]\n"
"s_waitcnt lgkmcnt(5)\n"
"v_mfma_f32_16x16x32_bf16 v[64:67], a[144:147], v[104:107], v[64:67]\n"
"v_mfma_f32_16x16x32_bf16 v[68:71], a[148:151], v[104:107], v[68:71]\n"
"v_mfma_f32_16x16x32_bf16 v[80:83], a[152:155], v[104:107], v[80:83]\n"
"ds_read_b128 v[96:99], v5 offset:32768\n"
"v_mfma_f32_16x16x32_bf16 v[84:87], a[156:159], v[104:107], v[84:87]\n"
"s_waitcnt lgkmcnt(5)\n"
"v_mfma_f32_16x16x32_bf16 v[72:75], a[144:147], v[108:111], v[72:75]\n"
"v_mfma_f32_16x16x32_bf16 v[76:79], a[148:151], v[108:111], v[76:79]\n"
"v_mfma_f32_16x16x32_bf16 v[88:91], a[152:155], v[108:111], v[88:91]\n"
"ds_read_b128 v[100:103], v5 offset:33024\n"
"v_mfma_f32_16x16x32_bf16 v[92:95], a[156:159], v[108:111], v[92:95]\n"
"s_waitcnt lgkmcnt(5)\n"
"v_mfma_f32_16x16x32_bf16 v[64:67], a[160:163], v[112:115], v[64:67]\n"
"v_mfma_f32_16x16x32_bf16 v[68:71], a[164:167], v[112:115], v[68:71]\n"
"v_mfma_f32_16x16x32_bf16 v[80:83], a[168:171], v[112:115], v[80:83]\n"
"ds_read_b128 v[104:107], v5 offset:34816\n"
"v_mfma_f32_16x16x32_bf16 v[84:87], a[172:175], v[112:115], v[84:87]\n"
"s_waitcnt lgkmcnt(5)\n"
"v_mfma_f32_16x16x32_bf16 v[72:75], a[160:163], v[116:119], v[72:75]\n"
"v_mfma_f32_16x16x32_bf16 v[76:79], a[164:167], v[116:119], v[76:79]\n"
"v_mfma_f32_16x16x32_bf16 v[88:91], a[168:171], v[116:119], v[88:91]\n"
"ds_read_b128 v[108:111], v5 offset:35072\n"
"v_mfma_f32_16x16x32_bf16 v[92:95], a[172:175], v[116:119], v[92:95]\n"
"s_waitcnt lgkmcnt(5)\n"
"v_mfma_f32_16x16x32_bf16 v[64:67], a[176:179], v[120:123], v[64:67]\n"
"v_mfma_f32_16x16x32_bf16 v[68:71], a[180:183], v[120:123], v[68:71]\n"
"v_mfma_f32_16x16x32_bf16 v[80:83], a[184:187], v[120:123], v[80:83]\n"
"ds_read_b128 v[112:115], v5 offset:36864\n"
"v_mfma_f32_16x16x32_bf16 v[84:87], a[188:191], v[120:123], v[84:87]\n"
"v_sub_u32_e64 v6, s73, 1 clamp\n"
"s_waitcnt lgkmcnt(5)\n"
"v_mfma_f32_16x16x32_bf16 v[72:75], a[176:179], v[124:127], v[72:75]\n"
"s_waitcnt vmcnt(2)\n"
"v_mfma_f32_16x16x32_bf16 v[76:79], a[180:183], v[124:127], v[76:79]\n"
"buffer_store_dword v6, v4, s[36:39], 0 offen sc1\n"
"v_mfma_f32_16x16x32_bf16 v[88:91], a[184:187], v[124:127], v[88:91]\n"
"ds_read_b128 v[116:119], v5 offset:37120\n"
"v_mfma_f32_16x16x32_bf16 v[92:95], a[188:191], v[124:127], v[92:95]\n"
"s_mov_b32 s74, s47\n"
"s_add_u32 s47, s47, s48\n"
"s_waitcnt lgkmcnt(5)\n"
"v_mfma_f32_16x16x32_bf16 v[64:67], a[192:195], v[96:99], v[64:67]\n"
"s_add_u32 s73, s73, 1\n"
"v_mfma_f32_16x16x32_bf16 v[68:71], a[196:199], v[96:99], v[68:71]\n"
"s_add_u32 m0, s71, 0\n"
"s_add_u32 s72, s68, 0\n"
"buffer_load_dwordx4 v0, s[36:39], s72 offen offset:0 sc1 lds\n"
"v_mfma_f32_16x16x32_bf16 v[80:83], a[200:203], v[96:99], v[80:83]\n"
"ds_read_b128 v[120:123], v5 offset:38912\n"
"v_mfma_f32_16x16x32_bf16 v[84:87], a[204:207], v[96:99], v[84:87]\n"
"buffer_load_dwordx4 v0, s[36:39], s72 offen offset:1024 sc1 lds\n"
"s_waitcnt lgkmcnt(5)\n"
"v_mfma_f32_16x16x32_bf16 v[72:75], a[192:195], v[100:103], v[72:75]\n"
"v_mfma_f32_16x16x32_bf16 v[76:79], a[196:199], v[100:103], v[76:79]\n"
"buffer_load_dwordx4 v0, s[36:39], s72 offen offset:2048 sc1 lds\n"
"v_mfma_f32_16x16x32_bf16 v[88:91], a[200:203], v[100:103], v[88:91]\n"
"ds_read_b128 v[124:127], v5 offset:39168\n"
"v_mfma_f32_16x16x32_bf16 v[92:95], a[204:207], v[100:103], v[92:95]\n"
"buffer_load_dwordx4 v0, s[36:39], s72 offen offset:3072 sc1 lds\n"
"s_waitcnt lgkmcnt(5)\n"
"v_mfma_f32_16x16x32_bf16 v[64:67], a[208:211], v[104:107], v[64:67]\n"
"v_mfma_f32_16x16x32_bf16 v[68:71], a[212:215], v[104:107], v[68:71]\n"
"s_add_u32 m0, s71, 4096\n"
"s_add_u32 s72, s68, 4096\n"
"buffer_load_dwordx4 v0, s[36:39], s72 offen offset:0 sc1 lds\n"
"v_mfma_f32_16x16x32_bf16 v[80:83], a[216:219], v[104:107], v[80:83]\n"
"s_add_u32 s70, s70, 40960\n"
"s_cmp_eq_u32 s70, 122880\n"
"s_cselect_b32 s70, 0, s70\n"
"v_mfma_f32_16x16x32_bf16 v[84:87], a[220:223], v[104:107], v[84:87]\n"
"buffer_load_dwordx4 v0, s[36:39], s72 offen offset:1024 sc1 lds\n"
"s_waitcnt lgkmcnt(4)\n"
"v_mfma_f32_16x16x32_bf16 v[72:75], a[208:211], v[108:111], v[72:75]\n"
"v_add_u32 v5, s70, v7\n"
"v_mfma_f32_16x16x32_bf16 v[76:79], a[212:215], v[108:111], v[76:79]\n"
"buffer_load_dwordx4 v0, s[36:39], s72 offen offset:2048 sc1 lds\n"
"v_mfma_f32_16x16x32_bf16 v[88:91], a[216:219], v[108:111], v[88:91]\n"
"s_waitcnt lgkmcnt(0)\n"
"s_barrier\n"
"v_mfma_f32_16x16x32_bf16 v[92:95], a[220:223], v[108:111], v[92:95]\n"
"ds_read_b128 v[96:99], v5 offset:0\n"
"v_mfma_f32_16x16x32_bf16 v[64:67], a[224:227], v[112:115], v[64:67]\n"
"v_mfma_f32_16x16x32_bf16 v[68:71], a[228:231], v[112:115], v[68:71]\n"
"ds_read_b128 v[100:103], v5 offset:256\n"
"v_mfma_f32_16x16x32_bf16 v[80:83], a[232:235], v[112:115], v[80:83]\n"
"v_mfma_f32_16x16x32_bf16 v[84:87], a[236:239], v[112:115], v[84:87]\n"
"ds_read_b128 v[104:107], v5 offset:2048\n"
"v_mfma_f32_16x16x32_bf16 v[72:75], a[224:227], v[116:119], v[72:75]\n"
"v_mfma_f32_16x16x32_bf16 v[76:79], a[228:231], v[116:119], v[76:79]\n"
"buffer_load_dwordx4 v0, s[36:39], s72 offen offset:3072 sc1 lds\n"
"v_mfma_f32_16x16x32_bf16 v[88:91], a[232:235], v[116:119], v[88:91]\n"
"ds_read_b128 v[108:111], v5 offset:2304\n"
"v_mfma_f32_16x16x32_bf16 v[92:95], a[236:239], v[116:119], v[92:95]\n"
"s_add_u32 m0, s71, 8192\n"
"s_add_u32 s72, s68, 8192\n"
"buffer_load_dwordx4 v0, s[36:39], s72 offen offset:0 sc1 lds\n"
"v_mfma_f32_16x16x32_bf16 v[64:67], a[240:243], v[120:123], v[64:67]\n"
"v_mfma_f32_16x16x32_bf16 v[68:71], a[244:247], v[120:123], v[68:71]\n"
"buffer_load_dwordx4 v0, s[36:39], s72 offen offset:1024 sc1 lds\n"
"v_mfma_f32_16x16x32_bf16 v[80:83], a[248:251], v[120:123], v[80:83]\n"
"ds_read_b128 v[112:115], v5 offset:4096\n"
"v_mfma_f32_16x16x32_bf16 v[84:87], a[252:255], v[120:123], v[84:87]\n"
"v_mfma_f32_16x16x32_bf16 v[72:75], a[240:243], v[124:127], v[72:75]\n"
"s_add_u32 s68, s68, 40960\n"
"s_add_u32 s69, s69, 40960\n"
"v_mfma_f32_16x16x32_bf16 v[76:79], a[244:247], v[124:127], v[76:79]\n"
"s_cmp_eq_u32 s69, 122880\n"
"s_cselect_b32 s69, 0, s69\n"
"v_mfma_f32_16x16x32_bf16 v[88:91], a[248:251], v[124:127], v[88:91]\n"
"ds_read_b128 v[116:119], v5 offset:4352\n"
"v_mfma_f32_16x16x32_bf16 v[92:95], a[252:255], v[124:127], v[92:95]\n"
"s_add_u32 s71, s69, s50\n"
"s_cmp_eq_u32 s73, s79\n"
"s_cbranch_scc0 GF6_%=\n"
"s_cmp_eq_u32 s73, 64\n"
"s_cbranch_scc1 GF7_%=\n"
"s_mov_b32 s76, 131072\n"
"s_branch GF9_%=\n"
"GF8_%=:\n"
"s_sleep 2\n"
"GF9_%=:\n"
"buffer_load_dword v15, v14, s[36:39], 0 offen sc1\n"
"s_sub_u32 s76, s76, 1\n"
"s_cbranch_scc1 GFABORT_%=\n"
"s_waitcnt vmcnt(0)\n"
"v_cmp_gt_u32 vcc, 1, v15\n"
"s_nop 0\n"
"s_cbranch_vccnz GF8_%=\n"
"s_mov_b32 s79, 64\n"
"s_branch GF6_%=\n"
"GF7_%=:\n"
"v_mul_f32 v144, s75, v64\n"
"v_mul_f32 v145, s75, v65\n"
"v_mul_f32 v146, s75, v66\n"
"v_mul_f32 v147, s75, v67\n"
"v_mul_f32 v148, s75, v68\n"
"v_mul_f32 v149, s75, v69\n"
"v_mul_f32 v150, s75, v70\n"
"v_mul_f32 v151, s75, v71\n"
"v_exp_f32 v144, v144\n"
"v_exp_f32 v145, v145\n"
"v_exp_f32 v146, v146\n"
"v_exp_f32 v147, v147\n"
"v_exp_f32 v148, v148\n"
"v_exp_f32 v149, v149\n"
"v_exp_f32 v150, v150\n"
"v_exp_f32 v151, v151\n"
"v_add_f32 v144, 1.0, v144\n"
"v_add_f32 v145, 1.0, v145\n"
"v_add_f32 v146, 1.0, v146\n"
"v_add_f32 v147, 1.0, v147\n"
"v_add_f32 v148, 1.0, v148\n"
"v_add_f32 v149, 1.0, v149\n"
"v_add_f32 v150, 1.0, v150\n"
"v_add_f32 v151, 1.0, v151\n"
"v_rcp_f32 v144, v144\n"
"v_rcp_f32 v145, v145\n"
"v_rcp_f32 v146, v146\n"
"v_rcp_f32 v147, v147\n"
"v_rcp_f32 v148, v148\n"
"v_rcp_f32 v149, v149\n"
"v_rcp_f32 v150, v150\n"
"v_rcp_f32 v151, v151\n"
"v_mul_f32 v144, v64, v144\n"
"v_mul_f32 v145, v65, v145\n"
"v_mul_f32 v146, v66, v146\n"
"v_mul_f32 v147, v67, v147\n"
"v_mul_f32 v148, v68, v148\n"
"v_mul_f32 v149, v69, v149\n"
"v_mul_f32 v150, v70, v150\n"
"v_mul_f32 v151, v71, v151\n"
"v_mul_f32 v144, v80, v144\n"
"v_mul_f32 v145, v81, v145\n"
"v_mul_f32 v146, v82, v146\n"
"v_mul_f32 v147, v83, v147\n"
"v_mul_f32 v148, v84, v148\n"
"v_mul_f32 v149, v85, v149\n"
"v_mul_f32 v150, v86, v150\n"
"v_mul_f32 v151, v87, v151\n"
"v_cvt_pk_bf16_f32 v128, v144, v145\n"
"v_cvt_pk_bf16_f32 v129, v146, v147\n"
"v_cvt_pk_bf16_f32 v130, v148, v149\n"
"v_cvt_pk_bf16_f32 v131, v150, v151\n"
"buffer_store_dwordx4 v[128:131], v7, s[36:39], s74 offen offset:0 sc1\n"
"v_mul_f32 v152, s75, v72\n"
"v_mul_f32 v153, s75, v73\n"
"v_mul_f32 v154, s75, v74\n"
"v_mul_f32 v155, s75, v75\n"
"v_mul_f32 v156, s75, v76\n"
"v_mul_f32 v157, s75, v77\n"
"v_mul_f32 v158, s75, v78\n"
"v_mul_f32 v159, s75, v79\n"
"v_exp_f32 v152, v152\n"
"v_exp_f32 v153, v153\n"
"v_exp_f32 v154, v154\n"
"v_exp_f32 v155, v155\n"
"v_exp_f32 v156, v156\n"
"v_exp_f32 v157, v157\n"
"v_exp_f32 v158, v158\n"
"v_exp_f32 v159, v159\n"
"v_add_f32 v152, 1.0, v152\n"
"v_add_f32 v153, 1.0, v153\n"
"v_add_f32 v154, 1.0, v154\n"
"v_add_f32 v155, 1.0, v155\n"
"v_add_f32 v156, 1.0, v156\n"
"v_add_f32 v157, 1.0, v157\n"
"v_add_f32 v158, 1.0, v158\n"
"v_add_f32 v159, 1.0, v159\n"
"v_rcp_f32 v152, v152\n"
"v_rcp_f32 v153, v153\n"
"v_rcp_f32 v154, v154\n"
"v_rcp_f32 v155, v155\n"
"v_rcp_f32 v156, v156\n"
"v_rcp_f32 v157, v157\n"
"v_rcp_f32 v158, v158\n"
"v_rcp_f32 v159, v159\n"
"v_mul_f32 v152, v72, v152\n"
"v_mul_f32 v153, v73, v153\n"
"v_mul_f32 v154, v74, v154\n"
"v_mul_f32 v155, v75, v155\n"
"v_mul_f32 v156, v76, v156\n"
"v_mul_f32 v157, v77, v157\n"
"v_mul_f32 v158, v78, v158\n"
"v_mul_f32 v159, v79, v159\n"
"v_mul_f32 v152, v88, v152\n"
"v_mul_f32 v153, v89, v153\n"
"v_mul_f32 v154, v90, v154\n"
"v_mul_f32 v155, v91, v155\n"
"v_mul_f32 v156, v92, v156\n"
"v_mul_f32 v157, v93, v157\n"
"v_mul_f32 v158, v94, v158\n"
"v_mul_f32 v159, v95, v159\n"
"v_cvt_pk_bf16_f32 v132, v152, v153\n"
"v_cvt_pk_bf16_f32 v133, v154, v155\n"
"v_cvt_pk_bf16_f32 v134, v156, v157\n"
"v_cvt_pk_bf16_f32 v135, v158, v159\n"
"buffer_store_dwordx4 v[132:135], v7, s[36:39], s74 offen offset:256 sc1\n"
"s_waitcnt vmcnt(0)\n"
"v_mov_b32 v6, s73\n"
"buffer_store_dword v6, v4, s[36:39], 0 offen sc1\n"
"s_endpgm\n"
"GFABORT_%=:\n"
"s_mov_b64 exec, -1\n"
"s_nop 7\n"
"s_nop 7\n"
"v_mov_b32 v0, 46268544\n"
"v_mov_b32 v1, 1\n"
"buffer_store_dword v1, v0, s[36:39], 0 offen sc1\n"
"s_endpgm\n");
    }
}

DI void role_w(const Ctx& c, int h) {
    SETUP
    setsrd(sa, 4, P->Wo, NH * HD * DM * 2);
    sa[8] = (h * HD + 96 * (w & 1)) * (DM * 2) + (w >> 1) * 128; sa[9] = w * 12288;
    sa[10] = O_VX + h * 12288 + w * 3072; sa[11] = w * 3072; sa[12] = O_VW + w * 65536 + h * 2048;
    va[1] = r8 * (DM * 2) + ((c8 ^ (((r8 >> 1) & 3) << 1)) * 16);
    const int row = 16 * (g >> 1) + 4 * (g & 1) + q;
    va[2] = (w >> 1) * 24576 + row * 128 + (((4 * (w & 1) + (p >> 1)) ^ (((row >> 1) & 3) << 1)) * 16) + (p & 1) * 8;
    va[3] = g * 512 + (16 * (n >> 3) + c8) * 16; va[10] = g * 512 + n * 16;
    va[4] = one(lane, F_W + fw(4 * h + w)); va[7] = F_V + fw(3 * h + lane % 3);
    RUN("s_nop 4\n"
"s_mov_b32 s22, 46301184\n"
"s_mov_b32 s23, 46317568\n"
"s_cmp_lg_u32 s66, 0\n"
"s_cbranch_scc1 W1_%=\n"
"v_mov_b32 v24, 0\n"
"s_mov_b64 exec, 1\n"
"buffer_atomic_add v24, v26, s[36:39], s23 offen sc0 sc1\n"
"s_mov_b64 exec, -1\n"
"W1_%=:\n"
"s_add_u32 m0, s45, 0\n"
"s_add_u32 s72, s44, 0\n"
"buffer_load_dwordx4 v1, s[40:43], s72 offen lds\n"
"s_add_u32 m0, s45, 1024\n"
"s_add_u32 s72, s44, 10240\n"
"buffer_load_dwordx4 v1, s[40:43], s72 offen lds\n"
"s_add_u32 m0, s45, 2048\n"
"s_add_u32 s72, s44, 20480\n"
"buffer_load_dwordx4 v1, s[40:43], s72 offen lds\n"
"s_add_u32 m0, s45, 3072\n"
"s_add_u32 s72, s44, 30720\n"
"buffer_load_dwordx4 v1, s[40:43], s72 offen lds\n"
"s_add_u32 m0, s45, 4096\n"
"s_add_u32 s72, s44, 40960\n"
"buffer_load_dwordx4 v1, s[40:43], s72 offen lds\n"
"s_add_u32 m0, s45, 5120\n"
"s_add_u32 s72, s44, 51200\n"
"buffer_load_dwordx4 v1, s[40:43], s72 offen lds\n"
"s_add_u32 m0, s45, 6144\n"
"s_add_u32 s72, s44, 61440\n"
"buffer_load_dwordx4 v1, s[40:43], s72 offen lds\n"
"s_add_u32 m0, s45, 7168\n"
"s_add_u32 s72, s44, 71680\n"
"buffer_load_dwordx4 v1, s[40:43], s72 offen lds\n"
"s_add_u32 m0, s45, 8192\n"
"s_add_u32 s72, s44, 81920\n"
"buffer_load_dwordx4 v1, s[40:43], s72 offen lds\n"
"s_add_u32 m0, s45, 9216\n"
"s_add_u32 s72, s44, 92160\n"
"buffer_load_dwordx4 v1, s[40:43], s72 offen lds\n"
"s_add_u32 m0, s45, 10240\n"
"s_add_u32 s72, s44, 102400\n"
"buffer_load_dwordx4 v1, s[40:43], s72 offen lds\n"
"s_add_u32 m0, s45, 11264\n"
"s_add_u32 s72, s44, 112640\n"
"buffer_load_dwordx4 v1, s[40:43], s72 offen lds\n"
"s_add_u32 m0, s45, 49152\n"
"s_add_u32 s72, s44, 256\n"
"buffer_load_dwordx4 v1, s[40:43], s72 offen lds\n"
"s_add_u32 m0, s45, 50176\n"
"s_add_u32 s72, s44, 10496\n"
"buffer_load_dwordx4 v1, s[40:43], s72 offen lds\n"
"s_add_u32 m0, s45, 51200\n"
"s_add_u32 s72, s44, 20736\n"
"buffer_load_dwordx4 v1, s[40:43], s72 offen lds\n"
"s_add_u32 m0, s45, 52224\n"
"s_add_u32 s72, s44, 30976\n"
"buffer_load_dwordx4 v1, s[40:43], s72 offen lds\n"
"s_add_u32 m0, s45, 53248\n"
"s_add_u32 s72, s44, 41216\n"
"buffer_load_dwordx4 v1, s[40:43], s72 offen lds\n"
"s_add_u32 m0, s45, 54272\n"
"s_add_u32 s72, s44, 51456\n"
"buffer_load_dwordx4 v1, s[40:43], s72 offen lds\n"
"s_add_u32 m0, s45, 55296\n"
"s_add_u32 s72, s44, 61696\n"
"buffer_load_dwordx4 v1, s[40:43], s72 offen lds\n"
"s_add_u32 m0, s45, 56320\n"
"s_add_u32 s72, s44, 71936\n"
"buffer_load_dwordx4 v1, s[40:43], s72 offen lds\n"
"s_add_u32 m0, s45, 57344\n"
"s_add_u32 s72, s44, 82176\n"
"buffer_load_dwordx4 v1, s[40:43], s72 offen lds\n"
"s_add_u32 m0, s45, 58368\n"
"s_add_u32 s72, s44, 92416\n"
"buffer_load_dwordx4 v1, s[40:43], s72 offen lds\n"
"s_add_u32 m0, s45, 59392\n"
"s_add_u32 s72, s44, 102656\n"
"buffer_load_dwordx4 v1, s[40:43], s72 offen lds\n"
"s_add_u32 m0, s45, 60416\n"
"s_add_u32 s72, s44, 112896\n"
"buffer_load_dwordx4 v1, s[40:43], s72 offen lds\n"
"s_waitcnt vmcnt(12)\n"
"s_waitcnt lgkmcnt(0)\n"
"s_barrier\n"
"s_cmp_lg_u32 s66, 0\n"
"s_cbranch_scc1 W2_%=\n"
"v_or_b32 v24, 1, v24\n"
"v_add_u32 v24, 1, v24\n"
"buffer_store_dword v24, v26, s[36:39], s22 offen sc1\n"
"W2_%=:\n"
"s_add_u32 m0, s45, 98304\n"
"s_add_u32 s72, s44, 512\n"
"buffer_load_dwordx4 v1, s[40:43], s72 offen lds\n"
"s_add_u32 m0, s45, 99328\n"
"s_add_u32 s72, s44, 10752\n"
"buffer_load_dwordx4 v1, s[40:43], s72 offen lds\n"
"s_add_u32 m0, s45, 100352\n"
"s_add_u32 s72, s44, 20992\n"
"buffer_load_dwordx4 v1, s[40:43], s72 offen lds\n"
"s_add_u32 m0, s45, 101376\n"
"s_add_u32 s72, s44, 31232\n"
"buffer_load_dwordx4 v1, s[40:43], s72 offen lds\n"
"s_add_u32 m0, s45, 102400\n"
"s_add_u32 s72, s44, 41472\n"
"buffer_load_dwordx4 v1, s[40:43], s72 offen lds\n"
"s_add_u32 m0, s45, 103424\n"
"s_add_u32 s72, s44, 51712\n"
"buffer_load_dwordx4 v1, s[40:43], s72 offen lds\n"
"s_add_u32 m0, s45, 104448\n"
"s_add_u32 s72, s44, 61952\n"
"buffer_load_dwordx4 v1, s[40:43], s72 offen lds\n"
"s_add_u32 m0, s45, 105472\n"
"s_add_u32 s72, s44, 72192\n"
"buffer_load_dwordx4 v1, s[40:43], s72 offen lds\n"
"s_add_u32 m0, s45, 106496\n"
"s_add_u32 s72, s44, 82432\n"
"buffer_load_dwordx4 v1, s[40:43], s72 offen lds\n"
"s_add_u32 m0, s45, 107520\n"
"s_add_u32 s72, s44, 92672\n"
"buffer_load_dwordx4 v1, s[40:43], s72 offen lds\n"
"s_add_u32 m0, s45, 108544\n"
"s_add_u32 s72, s44, 102912\n"
"buffer_load_dwordx4 v1, s[40:43], s72 offen lds\n"
"s_add_u32 m0, s45, 109568\n"
"s_add_u32 s72, s44, 113152\n"
"buffer_load_dwordx4 v1, s[40:43], s72 offen lds\n"
"v_add_u32 v8, 0, v2\n"
"v_xor_b32 v9, 32, v8\n"
"ds_read_b64_tr_b16 a[0:1], v8 offset:0\n"
"ds_read_b64_tr_b16 a[2:3], v8 offset:1024\n"
"ds_read_b64_tr_b16 a[4:5], v8 offset:4096\n"
"ds_read_b64_tr_b16 a[6:7], v8 offset:5120\n"
"ds_read_b64_tr_b16 a[8:9], v8 offset:8192\n"
"ds_read_b64_tr_b16 a[10:11], v8 offset:9216\n"
"ds_read_b64_tr_b16 a[12:13], v8 offset:12288\n"
"ds_read_b64_tr_b16 a[14:15], v8 offset:13312\n"
"ds_read_b64_tr_b16 a[16:17], v8 offset:16384\n"
"ds_read_b64_tr_b16 a[18:19], v8 offset:17408\n"
"ds_read_b64_tr_b16 a[20:21], v8 offset:20480\n"
"ds_read_b64_tr_b16 a[22:23], v8 offset:21504\n"
"ds_read_b64_tr_b16 a[24:25], v9 offset:0\n"
"ds_read_b64_tr_b16 a[26:27], v9 offset:1024\n"
"ds_read_b64_tr_b16 a[28:29], v9 offset:4096\n"
"ds_read_b64_tr_b16 a[30:31], v9 offset:5120\n"
"ds_read_b64_tr_b16 a[32:33], v9 offset:8192\n"
"ds_read_b64_tr_b16 a[34:35], v9 offset:9216\n"
"ds_read_b64_tr_b16 a[36:37], v9 offset:12288\n"
"ds_read_b64_tr_b16 a[38:39], v9 offset:13312\n"
"ds_read_b64_tr_b16 a[40:41], v9 offset:16384\n"
"ds_read_b64_tr_b16 a[42:43], v9 offset:17408\n"
"ds_read_b64_tr_b16 a[44:45], v9 offset:20480\n"
"ds_read_b64_tr_b16 a[46:47], v9 offset:21504\n"
"s_waitcnt vmcnt(12)\n"
"s_waitcnt lgkmcnt(0)\n"
"s_barrier\n"
"s_add_u32 m0, s45, 0\n"
"s_add_u32 s72, s44, 768\n"
"buffer_load_dwordx4 v1, s[40:43], s72 offen lds\n"
"s_add_u32 m0, s45, 1024\n"
"s_add_u32 s72, s44, 11008\n"
"buffer_load_dwordx4 v1, s[40:43], s72 offen lds\n"
"s_add_u32 m0, s45, 2048\n"
"s_add_u32 s72, s44, 21248\n"
"buffer_load_dwordx4 v1, s[40:43], s72 offen lds\n"
"s_add_u32 m0, s45, 3072\n"
"s_add_u32 s72, s44, 31488\n"
"buffer_load_dwordx4 v1, s[40:43], s72 offen lds\n"
"s_add_u32 m0, s45, 4096\n"
"s_add_u32 s72, s44, 41728\n"
"buffer_load_dwordx4 v1, s[40:43], s72 offen lds\n"
"s_add_u32 m0, s45, 5120\n"
"s_add_u32 s72, s44, 51968\n"
"buffer_load_dwordx4 v1, s[40:43], s72 offen lds\n"
"s_add_u32 m0, s45, 6144\n"
"s_add_u32 s72, s44, 62208\n"
"buffer_load_dwordx4 v1, s[40:43], s72 offen lds\n"
"s_add_u32 m0, s45, 7168\n"
"s_add_u32 s72, s44, 72448\n"
"buffer_load_dwordx4 v1, s[40:43], s72 offen lds\n"
"s_add_u32 m0, s45, 8192\n"
"s_add_u32 s72, s44, 82688\n"
"buffer_load_dwordx4 v1, s[40:43], s72 offen lds\n"
"s_add_u32 m0, s45, 9216\n"
"s_add_u32 s72, s44, 92928\n"
"buffer_load_dwordx4 v1, s[40:43], s72 offen lds\n"
"s_add_u32 m0, s45, 10240\n"
"s_add_u32 s72, s44, 103168\n"
"buffer_load_dwordx4 v1, s[40:43], s72 offen lds\n"
"s_add_u32 m0, s45, 11264\n"
"s_add_u32 s72, s44, 113408\n"
"buffer_load_dwordx4 v1, s[40:43], s72 offen lds\n"
"v_add_u32 v8, 49152, v2\n"
"v_xor_b32 v9, 32, v8\n"
"ds_read_b64_tr_b16 a[48:49], v8 offset:0\n"
"ds_read_b64_tr_b16 a[50:51], v8 offset:1024\n"
"ds_read_b64_tr_b16 a[52:53], v8 offset:4096\n"
"ds_read_b64_tr_b16 a[54:55], v8 offset:5120\n"
"ds_read_b64_tr_b16 a[56:57], v8 offset:8192\n"
"ds_read_b64_tr_b16 a[58:59], v8 offset:9216\n"
"ds_read_b64_tr_b16 a[60:61], v8 offset:12288\n"
"ds_read_b64_tr_b16 a[62:63], v8 offset:13312\n"
"ds_read_b64_tr_b16 a[64:65], v8 offset:16384\n"
"ds_read_b64_tr_b16 a[66:67], v8 offset:17408\n"
"ds_read_b64_tr_b16 a[68:69], v8 offset:20480\n"
"ds_read_b64_tr_b16 a[70:71], v8 offset:21504\n"
"ds_read_b64_tr_b16 a[72:73], v9 offset:0\n"
"ds_read_b64_tr_b16 a[74:75], v9 offset:1024\n"
"ds_read_b64_tr_b16 a[76:77], v9 offset:4096\n"
"ds_read_b64_tr_b16 a[78:79], v9 offset:5120\n"
"ds_read_b64_tr_b16 a[80:81], v9 offset:8192\n"
"ds_read_b64_tr_b16 a[82:83], v9 offset:9216\n"
"ds_read_b64_tr_b16 a[84:85], v9 offset:12288\n"
"ds_read_b64_tr_b16 a[86:87], v9 offset:13312\n"
"ds_read_b64_tr_b16 a[88:89], v9 offset:16384\n"
"ds_read_b64_tr_b16 a[90:91], v9 offset:17408\n"
"ds_read_b64_tr_b16 a[92:93], v9 offset:20480\n"
"ds_read_b64_tr_b16 a[94:95], v9 offset:21504\n"
"s_waitcnt vmcnt(12)\n"
"s_waitcnt lgkmcnt(0)\n"
"s_barrier\n"
"s_add_u32 m0, s45, 49152\n"
"s_add_u32 s72, s44, 1024\n"
"buffer_load_dwordx4 v1, s[40:43], s72 offen lds\n"
"s_add_u32 m0, s45, 50176\n"
"s_add_u32 s72, s44, 11264\n"
"buffer_load_dwordx4 v1, s[40:43], s72 offen lds\n"
"s_add_u32 m0, s45, 51200\n"
"s_add_u32 s72, s44, 21504\n"
"buffer_load_dwordx4 v1, s[40:43], s72 offen lds\n"
"s_add_u32 m0, s45, 52224\n"
"s_add_u32 s72, s44, 31744\n"
"buffer_load_dwordx4 v1, s[40:43], s72 offen lds\n"
"s_add_u32 m0, s45, 53248\n"
"s_add_u32 s72, s44, 41984\n"
"buffer_load_dwordx4 v1, s[40:43], s72 offen lds\n"
"s_add_u32 m0, s45, 54272\n"
"s_add_u32 s72, s44, 52224\n"
"buffer_load_dwordx4 v1, s[40:43], s72 offen lds\n"
"s_add_u32 m0, s45, 55296\n"
"s_add_u32 s72, s44, 62464\n"
"buffer_load_dwordx4 v1, s[40:43], s72 offen lds\n"
"s_add_u32 m0, s45, 56320\n"
"s_add_u32 s72, s44, 72704\n"
"buffer_load_dwordx4 v1, s[40:43], s72 offen lds\n"
"s_add_u32 m0, s45, 57344\n"
"s_add_u32 s72, s44, 82944\n"
"buffer_load_dwordx4 v1, s[40:43], s72 offen lds\n"
"s_add_u32 m0, s45, 58368\n"
"s_add_u32 s72, s44, 93184\n"
"buffer_load_dwordx4 v1, s[40:43], s72 offen lds\n"
"s_add_u32 m0, s45, 59392\n"
"s_add_u32 s72, s44, 103424\n"
"buffer_load_dwordx4 v1, s[40:43], s72 offen lds\n"
"s_add_u32 m0, s45, 60416\n"
"s_add_u32 s72, s44, 113664\n"
"buffer_load_dwordx4 v1, s[40:43], s72 offen lds\n"
"v_add_u32 v8, 98304, v2\n"
"v_xor_b32 v9, 32, v8\n"
"ds_read_b64_tr_b16 a[96:97], v8 offset:0\n"
"ds_read_b64_tr_b16 a[98:99], v8 offset:1024\n"
"ds_read_b64_tr_b16 a[100:101], v8 offset:4096\n"
"ds_read_b64_tr_b16 a[102:103], v8 offset:5120\n"
"ds_read_b64_tr_b16 a[104:105], v8 offset:8192\n"
"ds_read_b64_tr_b16 a[106:107], v8 offset:9216\n"
"ds_read_b64_tr_b16 a[108:109], v8 offset:12288\n"
"ds_read_b64_tr_b16 a[110:111], v8 offset:13312\n"
"ds_read_b64_tr_b16 a[112:113], v8 offset:16384\n"
"ds_read_b64_tr_b16 a[114:115], v8 offset:17408\n"
"ds_read_b64_tr_b16 a[116:117], v8 offset:20480\n"
"ds_read_b64_tr_b16 a[118:119], v8 offset:21504\n"
"ds_read_b64_tr_b16 a[120:121], v9 offset:0\n"
"ds_read_b64_tr_b16 a[122:123], v9 offset:1024\n"
"ds_read_b64_tr_b16 a[124:125], v9 offset:4096\n"
"ds_read_b64_tr_b16 a[126:127], v9 offset:5120\n"
"ds_read_b64_tr_b16 a[128:129], v9 offset:8192\n"
"ds_read_b64_tr_b16 a[130:131], v9 offset:9216\n"
"ds_read_b64_tr_b16 a[132:133], v9 offset:12288\n"
"ds_read_b64_tr_b16 a[134:135], v9 offset:13312\n"
"ds_read_b64_tr_b16 a[136:137], v9 offset:16384\n"
"ds_read_b64_tr_b16 a[138:139], v9 offset:17408\n"
"ds_read_b64_tr_b16 a[140:141], v9 offset:20480\n"
"ds_read_b64_tr_b16 a[142:143], v9 offset:21504\n"
"s_waitcnt vmcnt(12)\n"
"s_waitcnt lgkmcnt(0)\n"
"s_barrier\n"
"v_add_u32 v8, 0, v2\n"
"v_xor_b32 v9, 32, v8\n"
"ds_read_b64_tr_b16 a[144:145], v8 offset:0\n"
"ds_read_b64_tr_b16 a[146:147], v8 offset:1024\n"
"ds_read_b64_tr_b16 a[148:149], v8 offset:4096\n"
"ds_read_b64_tr_b16 a[150:151], v8 offset:5120\n"
"ds_read_b64_tr_b16 a[152:153], v8 offset:8192\n"
"ds_read_b64_tr_b16 a[154:155], v8 offset:9216\n"
"ds_read_b64_tr_b16 a[156:157], v8 offset:12288\n"
"ds_read_b64_tr_b16 a[158:159], v8 offset:13312\n"
"ds_read_b64_tr_b16 a[160:161], v8 offset:16384\n"
"ds_read_b64_tr_b16 a[162:163], v8 offset:17408\n"
"ds_read_b64_tr_b16 a[164:165], v8 offset:20480\n"
"ds_read_b64_tr_b16 a[166:167], v8 offset:21504\n"
"ds_read_b64_tr_b16 a[168:169], v9 offset:0\n"
"ds_read_b64_tr_b16 a[170:171], v9 offset:1024\n"
"ds_read_b64_tr_b16 a[172:173], v9 offset:4096\n"
"ds_read_b64_tr_b16 a[174:175], v9 offset:5120\n"
"ds_read_b64_tr_b16 a[176:177], v9 offset:8192\n"
"ds_read_b64_tr_b16 a[178:179], v9 offset:9216\n"
"ds_read_b64_tr_b16 a[180:181], v9 offset:12288\n"
"ds_read_b64_tr_b16 a[182:183], v9 offset:13312\n"
"ds_read_b64_tr_b16 a[184:185], v9 offset:16384\n"
"ds_read_b64_tr_b16 a[186:187], v9 offset:17408\n"
"ds_read_b64_tr_b16 a[188:189], v9 offset:20480\n"
"ds_read_b64_tr_b16 a[190:191], v9 offset:21504\n"
"s_waitcnt vmcnt(0)\n"
"s_waitcnt lgkmcnt(0)\n"
"s_barrier\n"
"v_add_u32 v8, 49152, v2\n"
"v_xor_b32 v9, 32, v8\n"
"ds_read_b64_tr_b16 a[192:193], v8 offset:0\n"
"ds_read_b64_tr_b16 a[194:195], v8 offset:1024\n"
"ds_read_b64_tr_b16 a[196:197], v8 offset:4096\n"
"ds_read_b64_tr_b16 a[198:199], v8 offset:5120\n"
"ds_read_b64_tr_b16 a[200:201], v8 offset:8192\n"
"ds_read_b64_tr_b16 a[202:203], v8 offset:9216\n"
"ds_read_b64_tr_b16 a[204:205], v8 offset:12288\n"
"ds_read_b64_tr_b16 a[206:207], v8 offset:13312\n"
"ds_read_b64_tr_b16 a[208:209], v8 offset:16384\n"
"ds_read_b64_tr_b16 a[210:211], v8 offset:17408\n"
"ds_read_b64_tr_b16 a[212:213], v8 offset:20480\n"
"ds_read_b64_tr_b16 a[214:215], v8 offset:21504\n"
"ds_read_b64_tr_b16 a[216:217], v9 offset:0\n"
"ds_read_b64_tr_b16 a[218:219], v9 offset:1024\n"
"ds_read_b64_tr_b16 a[220:221], v9 offset:4096\n"
"ds_read_b64_tr_b16 a[222:223], v9 offset:5120\n"
"ds_read_b64_tr_b16 a[224:225], v9 offset:8192\n"
"ds_read_b64_tr_b16 a[226:227], v9 offset:9216\n"
"ds_read_b64_tr_b16 a[228:229], v9 offset:12288\n"
"ds_read_b64_tr_b16 a[230:231], v9 offset:13312\n"
"ds_read_b64_tr_b16 a[232:233], v9 offset:16384\n"
"ds_read_b64_tr_b16 a[234:235], v9 offset:17408\n"
"ds_read_b64_tr_b16 a[236:237], v9 offset:20480\n"
"ds_read_b64_tr_b16 a[238:239], v9 offset:21504\n"
"s_cmp_lg_u32 s66, 0\n"
"s_cbranch_scc1 W3_%=\n"
"s_mov_b32 s76, 131072\n"
"s_mov_b64 exec, 1\n"
"s_branch W5_%=\n"
"W4_%=:\n"
"s_sleep 4\n"
"W5_%=:\n"
"v_mov_b32 v29, 0\n"
"buffer_atomic_add v29, v26, s[36:39], s23 offen sc0 sc1\n"
"s_sub_u32 s76, s76, 1\n"
"s_cbranch_scc1 WABORT_%=\n"
"s_waitcnt vmcnt(0)\n"
"v_xor_b32 v29, v29, v24\n"
"v_cmp_lt_u32 vcc, 1, v29\n"
"s_nop 0\n"
"s_cbranch_vccnz W4_%=\n"
"v_mov_b32 v28, 153600\n"
"ds_write_b32 v28, v29\n"
"v_mov_b32 v24, 1\n"
"s_add_u32 s22, s23, 16384\n"
"buffer_store_dword v24, v26, s[36:39], s22 offen sc1\n"
"s_mov_b64 exec, -1\n"
"W3_%=:\n"
"s_waitcnt lgkmcnt(0)\n"
"s_barrier\n"
"v_mov_b32 v28, 153600\n"
"ds_read_b32 v28, v28\n"
"s_waitcnt lgkmcnt(0)\n"
"v_readfirstlane_b32 s22, v28\n"
"s_nop 0\n"
"s_lshl_b32 s22, s22, 13\n"
"v_xor_b32 v4, s22, v4\n"
"v_xor_b32 v7, s22, v7\n"
"s_mov_b32 s69, 0\n"
"s_mov_b32 s70, 0\n"
"s_mov_b32 s73, 0\n"
"s_mov_b32 s75, 1\n"
"v_mov_b32 v11, 0\n"
"ds_write_b32 v11, v11 offset:40960\n"
"ds_write_b32 v11, v11 offset:40964\n"
"v_mov_b32 v14, -1\n"
"s_mov_b64 exec, 7\n"
"v_mov_b32 v14, 0\n"
"s_mov_b64 exec, -1\n"
"s_mov_b32 s76, 131072\n"
"s_mov_b32 exec_lo, 7\n"
"s_mov_b32 exec_hi, 0\n"
"s_mov_b32 s79, 0\n"
"W6_%=:\n"
"s_sleep 1\n"
"v_mov_b32 v9, 0\n"
"buffer_atomic_add v9, v7, s[36:39], s79 offen sc0 sc1\n"
"s_sub_u32 s76, s76, 1\n"
"s_cbranch_scc1 WABORT_%=\n"
"s_waitcnt vmcnt(0)\n"
"v_cmp_gt_u32 vcc, s75, v9\n"
"s_nop 0\n"
"s_cbranch_vccnz W6_%=\n"
"s_mov_b64 exec, -1\n"
"s_add_u32 m0, s69, s47\n"
"s_nop 0\n"
"buffer_load_dwordx4 v0, s[36:39], s46 offen offset:0 sc1 lds\n"
"buffer_load_dwordx4 v0, s[36:39], s46 offen offset:1024 sc1 lds\n"
"buffer_load_dwordx4 v0, s[36:39], s46 offen offset:2048 sc1 lds\n"
"s_add_u32 s46, s46, 393216\n"
"s_add_u32 s69, s69, 12288\n"
"s_cmp_eq_u32 s69, 36864\n"
"s_cselect_b32 s69, 0, s69\n"
"s_waitcnt vmcnt(0)\n"
"s_waitcnt lgkmcnt(0)\n"
"s_barrier\n"
"ds_read_b128 v[32:35], v3 offset:0\n"
"ds_read_b128 v[36:39], v3 offset:128\n"
"ds_read_b128 v[40:43], v3 offset:2048\n"
"ds_read_b128 v[44:47], v3 offset:2176\n"
"ds_read_b128 v[48:51], v3 offset:4096\n"
"ds_read_b128 v[52:55], v3 offset:4224\n"
"ds_read_b128 v[56:59], v3 offset:6144\n"
"ds_read_b128 v[60:63], v3 offset:6272\n"
"ds_read_b128 v[64:67], v3 offset:8192\n"
"ds_read_b128 v[68:71], v3 offset:8320\n"
"ds_read_b128 v[72:75], v3 offset:10240\n"
"ds_read_b128 v[76:79], v3 offset:10368\n"
"s_mov_b32 s74, 234881024\n"
"s_mov_b32 s75, 2\n"
"s_waitcnt lgkmcnt(0)\n"
"s_nop 0\n"
"W7_%=:\n"
"v_mfma_f32_16x16x32_bf16 v[128:131], v[32:35], a[0:3], 0\n"
"v_mov_b32 v9, v14\n"
"s_mov_b64 exec, 7\n"
"buffer_atomic_add v9, v7, s[36:39], 0 offen sc0 sc1\n"
"s_mov_b64 exec, -1\n"
"v_mfma_f32_16x16x32_bf16 v[132:135], v[36:39], a[0:3], 0\n"
"v_mfma_f32_16x16x32_bf16 v[128:131], v[40:43], a[4:7], v[128:131]\n"
"v_mfma_f32_16x16x32_bf16 v[132:135], v[44:47], a[4:7], v[132:135]\n"
"v_mfma_f32_16x16x32_bf16 v[128:131], v[48:51], a[8:11], v[128:131]\n"
"v_mfma_f32_16x16x32_bf16 v[132:135], v[52:55], a[8:11], v[132:135]\n"
"v_mfma_f32_16x16x32_bf16 v[128:131], v[56:59], a[12:15], v[128:131]\n"
"v_cvt_pk_bf16_f32 v164, v136, v137\n"
"v_mfma_f32_16x16x32_bf16 v[132:135], v[60:63], a[12:15], v[132:135]\n"
"v_cvt_pk_bf16_f32 v165, v138, v139\n"
"v_mfma_f32_16x16x32_bf16 v[128:131], v[64:67], a[16:19], v[128:131]\n"
"v_cvt_pk_bf16_f32 v166, v140, v141\n"
"v_mfma_f32_16x16x32_bf16 v[132:135], v[68:71], a[16:19], v[132:135]\n"
"v_cvt_pk_bf16_f32 v167, v142, v143\n"
"v_mfma_f32_16x16x32_bf16 v[128:131], v[72:75], a[20:23], v[128:131]\n"
"buffer_store_dwordx4 v[164:167], v10, s[36:39], s74 offen offset:256 sc1\n"
"v_mfma_f32_16x16x32_bf16 v[132:135], v[76:79], a[20:23], v[132:135]\n"
"s_add_u32 s74, s48, 0\n"
"v_mfma_f32_16x16x32_bf16 v[136:139], v[32:35], a[24:27], 0\n"
"v_mfma_f32_16x16x32_bf16 v[140:143], v[36:39], a[24:27], 0\n"
"v_mfma_f32_16x16x32_bf16 v[136:139], v[40:43], a[28:31], v[136:139]\n"
"v_mfma_f32_16x16x32_bf16 v[140:143], v[44:47], a[28:31], v[140:143]\n"
"v_mfma_f32_16x16x32_bf16 v[136:139], v[48:51], a[32:35], v[136:139]\n"
"v_mfma_f32_16x16x32_bf16 v[140:143], v[52:55], a[32:35], v[140:143]\n"
"v_mfma_f32_16x16x32_bf16 v[136:139], v[56:59], a[36:39], v[136:139]\n"
"v_cvt_pk_bf16_f32 v160, v128, v129\n"
"v_mfma_f32_16x16x32_bf16 v[140:143], v[60:63], a[36:39], v[140:143]\n"
"v_cvt_pk_bf16_f32 v161, v130, v131\n"
"v_mfma_f32_16x16x32_bf16 v[136:139], v[64:67], a[40:43], v[136:139]\n"
"v_cvt_pk_bf16_f32 v162, v132, v133\n"
"v_mfma_f32_16x16x32_bf16 v[140:143], v[68:71], a[40:43], v[140:143]\n"
"v_cvt_pk_bf16_f32 v163, v134, v135\n"
"v_mfma_f32_16x16x32_bf16 v[136:139], v[72:75], a[44:47], v[136:139]\n"
"buffer_store_dwordx4 v[160:163], v10, s[36:39], s74 offen offset:0 sc1\n"
"v_mfma_f32_16x16x32_bf16 v[140:143], v[76:79], a[44:47], v[140:143]\n"
"v_mfma_f32_16x16x32_bf16 v[128:131], v[32:35], a[48:51], 0\n"
"v_mfma_f32_16x16x32_bf16 v[132:135], v[36:39], a[48:51], 0\n"
"v_mov_b32 v12, s75\n"
"v_mfma_f32_16x16x32_bf16 v[128:131], v[40:43], a[52:55], v[128:131]\n"
"s_waitcnt vmcnt(2)\n"
"v_cmp_gt_u32 vcc, s75, v9\n"
"v_mfma_f32_16x16x32_bf16 v[132:135], v[44:47], a[52:55], v[132:135]\n"
"v_mfma_f32_16x16x32_bf16 v[128:131], v[48:51], a[56:59], v[128:131]\n"
"s_cmp_eq_u64 vcc, 0\n"
"s_cselect_b32 s77, s46, 234881024\n"
"s_add_u32 m0, s69, s47\n"
"v_mfma_f32_16x16x32_bf16 v[132:135], v[52:55], a[56:59], v[132:135]\n"
"s_mov_b64 exec, vcc\n"
"ds_write_b32 v11, v12 offset:40960\n"
"s_mov_b64 exec, -1\n"
"v_mfma_f32_16x16x32_bf16 v[128:131], v[56:59], a[60:63], v[128:131]\n"
"v_cvt_pk_bf16_f32 v164, v136, v137\n"
"v_mfma_f32_16x16x32_bf16 v[132:135], v[60:63], a[60:63], v[132:135]\n"
"v_cvt_pk_bf16_f32 v165, v138, v139\n"
"v_mfma_f32_16x16x32_bf16 v[128:131], v[64:67], a[64:67], v[128:131]\n"
"v_cvt_pk_bf16_f32 v166, v140, v141\n"
"v_mfma_f32_16x16x32_bf16 v[132:135], v[68:71], a[64:67], v[132:135]\n"
"v_cvt_pk_bf16_f32 v167, v142, v143\n"
"v_mfma_f32_16x16x32_bf16 v[128:131], v[72:75], a[68:71], v[128:131]\n"
"buffer_store_dwordx4 v[164:167], v10, s[36:39], s74 offen offset:256 sc1\n"
"v_mfma_f32_16x16x32_bf16 v[132:135], v[76:79], a[68:71], v[132:135]\n"
"s_add_u32 s74, s48, 262144\n"
"v_mfma_f32_16x16x32_bf16 v[136:139], v[32:35], a[72:75], 0\n"
"buffer_load_dwordx4 v0, s[36:39], s77 offen offset:0 sc1 lds\n"
"v_mfma_f32_16x16x32_bf16 v[140:143], v[36:39], a[72:75], 0\n"
"v_mfma_f32_16x16x32_bf16 v[136:139], v[40:43], a[76:79], v[136:139]\n"
"buffer_load_dwordx4 v0, s[36:39], s77 offen offset:1024 sc1 lds\n"
"v_mfma_f32_16x16x32_bf16 v[140:143], v[44:47], a[76:79], v[140:143]\n"
"v_mfma_f32_16x16x32_bf16 v[136:139], v[48:51], a[80:83], v[136:139]\n"
"buffer_load_dwordx4 v0, s[36:39], s77 offen offset:2048 sc1 lds\n"
"v_mfma_f32_16x16x32_bf16 v[140:143], v[52:55], a[80:83], v[140:143]\n"
"v_mfma_f32_16x16x32_bf16 v[136:139], v[56:59], a[84:87], v[136:139]\n"
"v_cvt_pk_bf16_f32 v160, v128, v129\n"
"v_mfma_f32_16x16x32_bf16 v[140:143], v[60:63], a[84:87], v[140:143]\n"
"v_cvt_pk_bf16_f32 v161, v130, v131\n"
"v_mfma_f32_16x16x32_bf16 v[136:139], v[64:67], a[88:91], v[136:139]\n"
"v_cvt_pk_bf16_f32 v162, v132, v133\n"
"v_mfma_f32_16x16x32_bf16 v[140:143], v[68:71], a[88:91], v[140:143]\n"
"v_cvt_pk_bf16_f32 v163, v134, v135\n"
"v_mfma_f32_16x16x32_bf16 v[136:139], v[72:75], a[92:95], v[136:139]\n"
"buffer_store_dwordx4 v[160:163], v10, s[36:39], s74 offen offset:0 sc1\n"
"v_mfma_f32_16x16x32_bf16 v[140:143], v[76:79], a[92:95], v[140:143]\n"
"v_mfma_f32_16x16x32_bf16 v[128:131], v[32:35], a[96:99], 0\n"
"v_mov_b32 v6, s73\n"
"v_mfma_f32_16x16x32_bf16 v[132:135], v[36:39], a[96:99], 0\n"
"s_waitcnt vmcnt(6)\n"
"buffer_store_dword v6, v4, s[36:39], 0 offen sc1\n"
"v_mfma_f32_16x16x32_bf16 v[128:131], v[40:43], a[100:103], v[128:131]\n"
"v_mfma_f32_16x16x32_bf16 v[132:135], v[44:47], a[100:103], v[132:135]\n"
"v_mfma_f32_16x16x32_bf16 v[128:131], v[48:51], a[104:107], v[128:131]\n"
"v_mfma_f32_16x16x32_bf16 v[132:135], v[52:55], a[104:107], v[132:135]\n"
"v_mfma_f32_16x16x32_bf16 v[128:131], v[56:59], a[108:111], v[128:131]\n"
"v_cvt_pk_bf16_f32 v164, v136, v137\n"
"v_mfma_f32_16x16x32_bf16 v[132:135], v[60:63], a[108:111], v[132:135]\n"
"v_cvt_pk_bf16_f32 v165, v138, v139\n"
"v_mfma_f32_16x16x32_bf16 v[128:131], v[64:67], a[112:115], v[128:131]\n"
"v_cvt_pk_bf16_f32 v166, v140, v141\n"
"v_mfma_f32_16x16x32_bf16 v[132:135], v[68:71], a[112:115], v[132:135]\n"
"v_cvt_pk_bf16_f32 v167, v142, v143\n"
"v_mfma_f32_16x16x32_bf16 v[128:131], v[72:75], a[116:119], v[128:131]\n"
"buffer_store_dwordx4 v[164:167], v10, s[36:39], s74 offen offset:256 sc1\n"
"v_mfma_f32_16x16x32_bf16 v[132:135], v[76:79], a[116:119], v[132:135]\n"
"s_add_u32 s74, s48, 524288\n"
"v_mfma_f32_16x16x32_bf16 v[136:139], v[32:35], a[120:123], 0\n"
"v_mfma_f32_16x16x32_bf16 v[140:143], v[36:39], a[120:123], 0\n"
"v_mfma_f32_16x16x32_bf16 v[136:139], v[40:43], a[124:127], v[136:139]\n"
"v_mfma_f32_16x16x32_bf16 v[140:143], v[44:47], a[124:127], v[140:143]\n"
"v_mfma_f32_16x16x32_bf16 v[136:139], v[48:51], a[128:131], v[136:139]\n"
"v_mfma_f32_16x16x32_bf16 v[140:143], v[52:55], a[128:131], v[140:143]\n"
"v_mfma_f32_16x16x32_bf16 v[136:139], v[56:59], a[132:135], v[136:139]\n"
"v_cvt_pk_bf16_f32 v160, v128, v129\n"
"v_mfma_f32_16x16x32_bf16 v[140:143], v[60:63], a[132:135], v[140:143]\n"
"v_cvt_pk_bf16_f32 v161, v130, v131\n"
"v_mfma_f32_16x16x32_bf16 v[136:139], v[64:67], a[136:139], v[136:139]\n"
"v_cvt_pk_bf16_f32 v162, v132, v133\n"
"v_mfma_f32_16x16x32_bf16 v[140:143], v[68:71], a[136:139], v[140:143]\n"
"v_cvt_pk_bf16_f32 v163, v134, v135\n"
"v_mfma_f32_16x16x32_bf16 v[136:139], v[72:75], a[140:143], v[136:139]\n"
"buffer_store_dwordx4 v[160:163], v10, s[36:39], s74 offen offset:0 sc1\n"
"v_mfma_f32_16x16x32_bf16 v[140:143], v[76:79], a[140:143], v[140:143]\n"
"v_mfma_f32_16x16x32_bf16 v[128:131], v[32:35], a[144:147], 0\n"
"v_mfma_f32_16x16x32_bf16 v[132:135], v[36:39], a[144:147], 0\n"
"v_mfma_f32_16x16x32_bf16 v[128:131], v[40:43], a[148:151], v[128:131]\n"
"s_add_u32 s70, s70, 12288\n"
"s_cmp_eq_u32 s70, 36864\n"
"s_cselect_b32 s70, 0, s70\n"
"v_mfma_f32_16x16x32_bf16 v[132:135], v[44:47], a[148:151], v[132:135]\n"
"v_add_u32 v5, s70, v3\n"
"v_mfma_f32_16x16x32_bf16 v[128:131], v[48:51], a[152:155], v[128:131]\n"
"v_mfma_f32_16x16x32_bf16 v[132:135], v[52:55], a[152:155], v[132:135]\n"
"v_mfma_f32_16x16x32_bf16 v[128:131], v[56:59], a[156:159], v[128:131]\n"
"v_cvt_pk_bf16_f32 v164, v136, v137\n"
"v_mfma_f32_16x16x32_bf16 v[132:135], v[60:63], a[156:159], v[132:135]\n"
"v_cvt_pk_bf16_f32 v165, v138, v139\n"
"v_mfma_f32_16x16x32_bf16 v[128:131], v[64:67], a[160:163], v[128:131]\n"
"v_cvt_pk_bf16_f32 v166, v140, v141\n"
"v_mfma_f32_16x16x32_bf16 v[132:135], v[68:71], a[160:163], v[132:135]\n"
"v_cvt_pk_bf16_f32 v167, v142, v143\n"
"v_mfma_f32_16x16x32_bf16 v[128:131], v[72:75], a[164:167], v[128:131]\n"
"buffer_store_dwordx4 v[164:167], v10, s[36:39], s74 offen offset:256 sc1\n"
"v_mfma_f32_16x16x32_bf16 v[132:135], v[76:79], a[164:167], v[132:135]\n"
"s_add_u32 s74, s48, 786432\n"
"s_waitcnt vmcnt(5)\n"
"s_waitcnt lgkmcnt(0)\n"
"s_barrier\n"
"v_mfma_f32_16x16x32_bf16 v[136:139], v[32:35], a[168:171], 0\n"
"ds_read_b128 v[80:83], v5 offset:0\n"
"v_mfma_f32_16x16x32_bf16 v[140:143], v[36:39], a[168:171], 0\n"
"v_mfma_f32_16x16x32_bf16 v[136:139], v[40:43], a[172:175], v[136:139]\n"
"ds_read_b128 v[84:87], v5 offset:128\n"
"v_mfma_f32_16x16x32_bf16 v[140:143], v[44:47], a[172:175], v[140:143]\n"
"s_add_u32 s73, s73, 1\n"
"v_mfma_f32_16x16x32_bf16 v[136:139], v[48:51], a[176:179], v[136:139]\n"
"ds_read_b128 v[88:91], v5 offset:2048\n"
"v_mfma_f32_16x16x32_bf16 v[140:143], v[52:55], a[176:179], v[140:143]\n"
"s_mov_b64 exec, vcc\n"
"v_mov_b32 v9, 0\n"
"buffer_atomic_add v9, v7, s[36:39], 0 offen sc0 sc1\n"
"s_mov_b64 exec, -1\n"
"v_mfma_f32_16x16x32_bf16 v[136:139], v[56:59], a[180:183], v[136:139]\n"
"ds_read_b128 v[92:95], v5 offset:2176\n"
"v_cvt_pk_bf16_f32 v160, v128, v129\n"
"v_mfma_f32_16x16x32_bf16 v[140:143], v[60:63], a[180:183], v[140:143]\n"
"v_cvt_pk_bf16_f32 v161, v130, v131\n"
"v_mfma_f32_16x16x32_bf16 v[136:139], v[64:67], a[184:187], v[136:139]\n"
"ds_read_b128 v[96:99], v5 offset:4096\n"
"v_cvt_pk_bf16_f32 v162, v132, v133\n"
"v_mfma_f32_16x16x32_bf16 v[140:143], v[68:71], a[184:187], v[140:143]\n"
"v_cvt_pk_bf16_f32 v163, v134, v135\n"
"v_mfma_f32_16x16x32_bf16 v[136:139], v[72:75], a[188:191], v[136:139]\n"
"buffer_store_dwordx4 v[160:163], v10, s[36:39], s74 offen offset:0 sc1\n"
"v_mfma_f32_16x16x32_bf16 v[140:143], v[76:79], a[188:191], v[140:143]\n"
"ds_read_b128 v[100:103], v5 offset:4224\n"
"v_mfma_f32_16x16x32_bf16 v[128:131], v[32:35], a[192:195], 0\n"
"ds_read_b128 v[104:107], v5 offset:6144\n"
"v_mfma_f32_16x16x32_bf16 v[132:135], v[36:39], a[192:195], 0\n"
"v_mfma_f32_16x16x32_bf16 v[128:131], v[40:43], a[196:199], v[128:131]\n"
"ds_read_b128 v[108:111], v5 offset:6272\n"
"v_mfma_f32_16x16x32_bf16 v[132:135], v[44:47], a[196:199], v[132:135]\n"
"v_mfma_f32_16x16x32_bf16 v[128:131], v[48:51], a[200:203], v[128:131]\n"
"ds_read_b128 v[112:115], v5 offset:8192\n"
"v_mfma_f32_16x16x32_bf16 v[132:135], v[52:55], a[200:203], v[132:135]\n"
"v_mfma_f32_16x16x32_bf16 v[128:131], v[56:59], a[204:207], v[128:131]\n"
"ds_read_b128 v[116:119], v5 offset:8320\n"
"v_cvt_pk_bf16_f32 v164, v136, v137\n"
"v_mfma_f32_16x16x32_bf16 v[132:135], v[60:63], a[204:207], v[132:135]\n"
"v_cvt_pk_bf16_f32 v165, v138, v139\n"
"v_mfma_f32_16x16x32_bf16 v[128:131], v[64:67], a[208:211], v[128:131]\n"
"ds_read_b128 v[120:123], v5 offset:10240\n"
"v_cvt_pk_bf16_f32 v166, v140, v141\n"
"v_mfma_f32_16x16x32_bf16 v[132:135], v[68:71], a[208:211], v[132:135]\n"
"v_cvt_pk_bf16_f32 v167, v142, v143\n"
"v_mfma_f32_16x16x32_bf16 v[128:131], v[72:75], a[212:215], v[128:131]\n"
"buffer_store_dwordx4 v[164:167], v10, s[36:39], s74 offen offset:256 sc1\n"
"v_mfma_f32_16x16x32_bf16 v[132:135], v[76:79], a[212:215], v[132:135]\n"
"ds_read_b128 v[124:127], v5 offset:10368\n"
"s_add_u32 s74, s48, 1048576\n"
"v_mfma_f32_16x16x32_bf16 v[136:139], v[32:35], a[216:219], 0\n"
"v_mfma_f32_16x16x32_bf16 v[140:143], v[36:39], a[216:219], 0\n"
"ds_read_b32 v13, v11 offset:40960\n"
"v_mfma_f32_16x16x32_bf16 v[136:139], v[40:43], a[220:223], v[136:139]\n"
"v_mfma_f32_16x16x32_bf16 v[140:143], v[44:47], a[220:223], v[140:143]\n"
"v_mfma_f32_16x16x32_bf16 v[136:139], v[48:51], a[224:227], v[136:139]\n"
"s_add_u32 s48, s48, 1310720\n"
"v_mfma_f32_16x16x32_bf16 v[140:143], v[52:55], a[224:227], v[140:143]\n"
"v_mfma_f32_16x16x32_bf16 v[136:139], v[56:59], a[228:231], v[136:139]\n"
"v_cvt_pk_bf16_f32 v160, v128, v129\n"
"v_mfma_f32_16x16x32_bf16 v[140:143], v[60:63], a[228:231], v[140:143]\n"
"v_cvt_pk_bf16_f32 v161, v130, v131\n"
"v_mfma_f32_16x16x32_bf16 v[136:139], v[64:67], a[232:235], v[136:139]\n"
"v_cvt_pk_bf16_f32 v162, v132, v133\n"
"s_waitcnt lgkmcnt(0)\n"
"v_cmp_eq_u32 vcc, s75, v13\n"
"v_mfma_f32_16x16x32_bf16 v[140:143], v[68:71], a[232:235], v[140:143]\n"
"v_cvt_pk_bf16_f32 v163, v134, v135\n"
"v_mfma_f32_16x16x32_bf16 v[136:139], v[72:75], a[236:239], v[136:139]\n"
"buffer_store_dwordx4 v[160:163], v10, s[36:39], s74 offen offset:0 sc1\n"
"v_mfma_f32_16x16x32_bf16 v[140:143], v[76:79], a[236:239], v[140:143]\n"
"s_cbranch_vccz W8_%=\n"
"s_waitcnt vmcnt(3)\n"
"v_cmp_gt_u32 vcc, s75, v9\n"
"s_nop 2\n"
"v_cvt_pk_bf16_f32 v164, v136, v137\n"
"v_cvt_pk_bf16_f32 v165, v138, v139\n"
"v_cvt_pk_bf16_f32 v166, v140, v141\n"
"v_cvt_pk_bf16_f32 v167, v142, v143\n"
"buffer_store_dwordx4 v[164:167], v10, s[36:39], s74 offen offset:256 sc1\n"
"s_cbranch_vccz W9_%=\n"
"s_waitcnt vmcnt(0)\n"
"v_mov_b32 v6, s73\n"
"buffer_store_dword v6, v4, s[36:39], 0 offen sc1\n"
"s_mov_b32 s76, 131072\n"
"s_mov_b32 exec_lo, 7\n"
"s_mov_b32 exec_hi, 0\n"
"s_mov_b32 s79, 0\n"
"W10_%=:\n"
"s_sleep 1\n"
"v_mov_b32 v9, 0\n"
"buffer_atomic_add v9, v7, s[36:39], s79 offen sc0 sc1\n"
"s_sub_u32 s76, s76, 1\n"
"s_cbranch_scc1 WABORT_%=\n"
"s_waitcnt vmcnt(0)\n"
"v_cmp_gt_u32 vcc, s75, v9\n"
"s_nop 0\n"
"s_cbranch_vccnz W10_%=\n"
"s_mov_b64 exec, -1\n"
"W9_%=:\n"
"buffer_load_dwordx4 v0, s[36:39], s46 offen offset:0 sc1 lds\n"
"buffer_load_dwordx4 v0, s[36:39], s46 offen offset:1024 sc1 lds\n"
"buffer_load_dwordx4 v0, s[36:39], s46 offen offset:2048 sc1 lds\n"
"s_waitcnt vmcnt(3)\n"
"v_mov_b32 v6, s73\n"
"buffer_store_dword v6, v4, s[36:39], 0 offen sc1\n"
"s_waitcnt vmcnt(1)\n"
"s_barrier\n"
"ds_read_b128 v[80:83], v5 offset:0\n"
"ds_read_b128 v[84:87], v5 offset:128\n"
"ds_read_b128 v[88:91], v5 offset:2048\n"
"ds_read_b128 v[92:95], v5 offset:2176\n"
"ds_read_b128 v[96:99], v5 offset:4096\n"
"ds_read_b128 v[100:103], v5 offset:4224\n"
"ds_read_b128 v[104:107], v5 offset:6144\n"
"ds_read_b128 v[108:111], v5 offset:6272\n"
"ds_read_b128 v[112:115], v5 offset:8192\n"
"ds_read_b128 v[116:119], v5 offset:8320\n"
"ds_read_b128 v[120:123], v5 offset:10240\n"
"ds_read_b128 v[124:127], v5 offset:10368\n"
"s_waitcnt lgkmcnt(0)\n"
"W8_%=:\n"
"s_add_u32 s46, s46, 393216\n"
"s_add_u32 s69, s69, 12288\n"
"s_cmp_eq_u32 s69, 36864\n"
"s_cselect_b32 s69, 0, s69\n"
"s_add_u32 s75, s75, 1\n"
"s_min_u32 s75, s75, 64\n"
"v_mfma_f32_16x16x32_bf16 v[128:131], v[80:83], a[0:3], 0\n"
"v_mov_b32 v9, v14\n"
"s_mov_b64 exec, 7\n"
"buffer_atomic_add v9, v7, s[36:39], 0 offen sc0 sc1\n"
"s_mov_b64 exec, -1\n"
"v_mfma_f32_16x16x32_bf16 v[132:135], v[84:87], a[0:3], 0\n"
"v_mfma_f32_16x16x32_bf16 v[128:131], v[88:91], a[4:7], v[128:131]\n"
"v_mfma_f32_16x16x32_bf16 v[132:135], v[92:95], a[4:7], v[132:135]\n"
"v_mfma_f32_16x16x32_bf16 v[128:131], v[96:99], a[8:11], v[128:131]\n"
"v_mfma_f32_16x16x32_bf16 v[132:135], v[100:103], a[8:11], v[132:135]\n"
"v_mfma_f32_16x16x32_bf16 v[128:131], v[104:107], a[12:15], v[128:131]\n"
"v_cvt_pk_bf16_f32 v164, v136, v137\n"
"v_mfma_f32_16x16x32_bf16 v[132:135], v[108:111], a[12:15], v[132:135]\n"
"v_cvt_pk_bf16_f32 v165, v138, v139\n"
"v_mfma_f32_16x16x32_bf16 v[128:131], v[112:115], a[16:19], v[128:131]\n"
"v_cvt_pk_bf16_f32 v166, v140, v141\n"
"v_mfma_f32_16x16x32_bf16 v[132:135], v[116:119], a[16:19], v[132:135]\n"
"v_cvt_pk_bf16_f32 v167, v142, v143\n"
"v_mfma_f32_16x16x32_bf16 v[128:131], v[120:123], a[20:23], v[128:131]\n"
"buffer_store_dwordx4 v[164:167], v10, s[36:39], s74 offen offset:256 sc1\n"
"v_mfma_f32_16x16x32_bf16 v[132:135], v[124:127], a[20:23], v[132:135]\n"
"s_add_u32 s74, s48, 0\n"
"v_mfma_f32_16x16x32_bf16 v[136:139], v[80:83], a[24:27], 0\n"
"v_mfma_f32_16x16x32_bf16 v[140:143], v[84:87], a[24:27], 0\n"
"v_mfma_f32_16x16x32_bf16 v[136:139], v[88:91], a[28:31], v[136:139]\n"
"v_mfma_f32_16x16x32_bf16 v[140:143], v[92:95], a[28:31], v[140:143]\n"
"v_mfma_f32_16x16x32_bf16 v[136:139], v[96:99], a[32:35], v[136:139]\n"
"v_mfma_f32_16x16x32_bf16 v[140:143], v[100:103], a[32:35], v[140:143]\n"
"v_mfma_f32_16x16x32_bf16 v[136:139], v[104:107], a[36:39], v[136:139]\n"
"v_cvt_pk_bf16_f32 v160, v128, v129\n"
"v_mfma_f32_16x16x32_bf16 v[140:143], v[108:111], a[36:39], v[140:143]\n"
"v_cvt_pk_bf16_f32 v161, v130, v131\n"
"v_mfma_f32_16x16x32_bf16 v[136:139], v[112:115], a[40:43], v[136:139]\n"
"v_cvt_pk_bf16_f32 v162, v132, v133\n"
"v_mfma_f32_16x16x32_bf16 v[140:143], v[116:119], a[40:43], v[140:143]\n"
"v_cvt_pk_bf16_f32 v163, v134, v135\n"
"v_mfma_f32_16x16x32_bf16 v[136:139], v[120:123], a[44:47], v[136:139]\n"
"buffer_store_dwordx4 v[160:163], v10, s[36:39], s74 offen offset:0 sc1\n"
"v_mfma_f32_16x16x32_bf16 v[140:143], v[124:127], a[44:47], v[140:143]\n"
"v_mfma_f32_16x16x32_bf16 v[128:131], v[80:83], a[48:51], 0\n"
"v_mfma_f32_16x16x32_bf16 v[132:135], v[84:87], a[48:51], 0\n"
"v_mov_b32 v12, s75\n"
"v_mfma_f32_16x16x32_bf16 v[128:131], v[88:91], a[52:55], v[128:131]\n"
"s_waitcnt vmcnt(2)\n"
"v_cmp_gt_u32 vcc, s75, v9\n"
"v_mfma_f32_16x16x32_bf16 v[132:135], v[92:95], a[52:55], v[132:135]\n"
"v_mfma_f32_16x16x32_bf16 v[128:131], v[96:99], a[56:59], v[128:131]\n"
"s_cmp_eq_u64 vcc, 0\n"
"s_cselect_b32 s77, s46, 234881024\n"
"s_add_u32 m0, s69, s47\n"
"v_mfma_f32_16x16x32_bf16 v[132:135], v[100:103], a[56:59], v[132:135]\n"
"s_mov_b64 exec, vcc\n"
"ds_write_b32 v11, v12 offset:40964\n"
"s_mov_b64 exec, -1\n"
"v_mfma_f32_16x16x32_bf16 v[128:131], v[104:107], a[60:63], v[128:131]\n"
"v_cvt_pk_bf16_f32 v164, v136, v137\n"
"v_mfma_f32_16x16x32_bf16 v[132:135], v[108:111], a[60:63], v[132:135]\n"
"v_cvt_pk_bf16_f32 v165, v138, v139\n"
"v_mfma_f32_16x16x32_bf16 v[128:131], v[112:115], a[64:67], v[128:131]\n"
"v_cvt_pk_bf16_f32 v166, v140, v141\n"
"v_mfma_f32_16x16x32_bf16 v[132:135], v[116:119], a[64:67], v[132:135]\n"
"v_cvt_pk_bf16_f32 v167, v142, v143\n"
"v_mfma_f32_16x16x32_bf16 v[128:131], v[120:123], a[68:71], v[128:131]\n"
"buffer_store_dwordx4 v[164:167], v10, s[36:39], s74 offen offset:256 sc1\n"
"v_mfma_f32_16x16x32_bf16 v[132:135], v[124:127], a[68:71], v[132:135]\n"
"s_add_u32 s74, s48, 262144\n"
"v_mfma_f32_16x16x32_bf16 v[136:139], v[80:83], a[72:75], 0\n"
"buffer_load_dwordx4 v0, s[36:39], s77 offen offset:0 sc1 lds\n"
"v_mfma_f32_16x16x32_bf16 v[140:143], v[84:87], a[72:75], 0\n"
"v_mfma_f32_16x16x32_bf16 v[136:139], v[88:91], a[76:79], v[136:139]\n"
"buffer_load_dwordx4 v0, s[36:39], s77 offen offset:1024 sc1 lds\n"
"v_mfma_f32_16x16x32_bf16 v[140:143], v[92:95], a[76:79], v[140:143]\n"
"v_mfma_f32_16x16x32_bf16 v[136:139], v[96:99], a[80:83], v[136:139]\n"
"buffer_load_dwordx4 v0, s[36:39], s77 offen offset:2048 sc1 lds\n"
"v_mfma_f32_16x16x32_bf16 v[140:143], v[100:103], a[80:83], v[140:143]\n"
"v_mfma_f32_16x16x32_bf16 v[136:139], v[104:107], a[84:87], v[136:139]\n"
"v_cvt_pk_bf16_f32 v160, v128, v129\n"
"v_mfma_f32_16x16x32_bf16 v[140:143], v[108:111], a[84:87], v[140:143]\n"
"v_cvt_pk_bf16_f32 v161, v130, v131\n"
"v_mfma_f32_16x16x32_bf16 v[136:139], v[112:115], a[88:91], v[136:139]\n"
"v_cvt_pk_bf16_f32 v162, v132, v133\n"
"v_mfma_f32_16x16x32_bf16 v[140:143], v[116:119], a[88:91], v[140:143]\n"
"v_cvt_pk_bf16_f32 v163, v134, v135\n"
"v_mfma_f32_16x16x32_bf16 v[136:139], v[120:123], a[92:95], v[136:139]\n"
"buffer_store_dwordx4 v[160:163], v10, s[36:39], s74 offen offset:0 sc1\n"
"v_mfma_f32_16x16x32_bf16 v[140:143], v[124:127], a[92:95], v[140:143]\n"
"v_mfma_f32_16x16x32_bf16 v[128:131], v[80:83], a[96:99], 0\n"
"v_mov_b32 v6, s73\n"
"v_mfma_f32_16x16x32_bf16 v[132:135], v[84:87], a[96:99], 0\n"
"s_waitcnt vmcnt(6)\n"
"buffer_store_dword v6, v4, s[36:39], 0 offen sc1\n"
"v_mfma_f32_16x16x32_bf16 v[128:131], v[88:91], a[100:103], v[128:131]\n"
"v_mfma_f32_16x16x32_bf16 v[132:135], v[92:95], a[100:103], v[132:135]\n"
"v_mfma_f32_16x16x32_bf16 v[128:131], v[96:99], a[104:107], v[128:131]\n"
"v_mfma_f32_16x16x32_bf16 v[132:135], v[100:103], a[104:107], v[132:135]\n"
"v_mfma_f32_16x16x32_bf16 v[128:131], v[104:107], a[108:111], v[128:131]\n"
"v_cvt_pk_bf16_f32 v164, v136, v137\n"
"v_mfma_f32_16x16x32_bf16 v[132:135], v[108:111], a[108:111], v[132:135]\n"
"v_cvt_pk_bf16_f32 v165, v138, v139\n"
"v_mfma_f32_16x16x32_bf16 v[128:131], v[112:115], a[112:115], v[128:131]\n"
"v_cvt_pk_bf16_f32 v166, v140, v141\n"
"v_mfma_f32_16x16x32_bf16 v[132:135], v[116:119], a[112:115], v[132:135]\n"
"v_cvt_pk_bf16_f32 v167, v142, v143\n"
"v_mfma_f32_16x16x32_bf16 v[128:131], v[120:123], a[116:119], v[128:131]\n"
"buffer_store_dwordx4 v[164:167], v10, s[36:39], s74 offen offset:256 sc1\n"
"v_mfma_f32_16x16x32_bf16 v[132:135], v[124:127], a[116:119], v[132:135]\n"
"s_add_u32 s74, s48, 524288\n"
"v_mfma_f32_16x16x32_bf16 v[136:139], v[80:83], a[120:123], 0\n"
"v_mfma_f32_16x16x32_bf16 v[140:143], v[84:87], a[120:123], 0\n"
"v_mfma_f32_16x16x32_bf16 v[136:139], v[88:91], a[124:127], v[136:139]\n"
"v_mfma_f32_16x16x32_bf16 v[140:143], v[92:95], a[124:127], v[140:143]\n"
"v_mfma_f32_16x16x32_bf16 v[136:139], v[96:99], a[128:131], v[136:139]\n"
"v_mfma_f32_16x16x32_bf16 v[140:143], v[100:103], a[128:131], v[140:143]\n"
"v_mfma_f32_16x16x32_bf16 v[136:139], v[104:107], a[132:135], v[136:139]\n"
"v_cvt_pk_bf16_f32 v160, v128, v129\n"
"v_mfma_f32_16x16x32_bf16 v[140:143], v[108:111], a[132:135], v[140:143]\n"
"v_cvt_pk_bf16_f32 v161, v130, v131\n"
"v_mfma_f32_16x16x32_bf16 v[136:139], v[112:115], a[136:139], v[136:139]\n"
"v_cvt_pk_bf16_f32 v162, v132, v133\n"
"v_mfma_f32_16x16x32_bf16 v[140:143], v[116:119], a[136:139], v[140:143]\n"
"v_cvt_pk_bf16_f32 v163, v134, v135\n"
"v_mfma_f32_16x16x32_bf16 v[136:139], v[120:123], a[140:143], v[136:139]\n"
"buffer_store_dwordx4 v[160:163], v10, s[36:39], s74 offen offset:0 sc1\n"
"v_mfma_f32_16x16x32_bf16 v[140:143], v[124:127], a[140:143], v[140:143]\n"
"v_mfma_f32_16x16x32_bf16 v[128:131], v[80:83], a[144:147], 0\n"
"v_mfma_f32_16x16x32_bf16 v[132:135], v[84:87], a[144:147], 0\n"
"v_mfma_f32_16x16x32_bf16 v[128:131], v[88:91], a[148:151], v[128:131]\n"
"s_add_u32 s70, s70, 12288\n"
"s_cmp_eq_u32 s70, 36864\n"
"s_cselect_b32 s70, 0, s70\n"
"v_mfma_f32_16x16x32_bf16 v[132:135], v[92:95], a[148:151], v[132:135]\n"
"v_add_u32 v5, s70, v3\n"
"v_mfma_f32_16x16x32_bf16 v[128:131], v[96:99], a[152:155], v[128:131]\n"
"v_mfma_f32_16x16x32_bf16 v[132:135], v[100:103], a[152:155], v[132:135]\n"
"v_mfma_f32_16x16x32_bf16 v[128:131], v[104:107], a[156:159], v[128:131]\n"
"v_cvt_pk_bf16_f32 v164, v136, v137\n"
"v_mfma_f32_16x16x32_bf16 v[132:135], v[108:111], a[156:159], v[132:135]\n"
"v_cvt_pk_bf16_f32 v165, v138, v139\n"
"v_mfma_f32_16x16x32_bf16 v[128:131], v[112:115], a[160:163], v[128:131]\n"
"v_cvt_pk_bf16_f32 v166, v140, v141\n"
"v_mfma_f32_16x16x32_bf16 v[132:135], v[116:119], a[160:163], v[132:135]\n"
"v_cvt_pk_bf16_f32 v167, v142, v143\n"
"v_mfma_f32_16x16x32_bf16 v[128:131], v[120:123], a[164:167], v[128:131]\n"
"buffer_store_dwordx4 v[164:167], v10, s[36:39], s74 offen offset:256 sc1\n"
"v_mfma_f32_16x16x32_bf16 v[132:135], v[124:127], a[164:167], v[132:135]\n"
"s_add_u32 s74, s48, 786432\n"
"s_waitcnt vmcnt(5)\n"
"s_waitcnt lgkmcnt(0)\n"
"s_barrier\n"
"v_mfma_f32_16x16x32_bf16 v[136:139], v[80:83], a[168:171], 0\n"
"ds_read_b128 v[32:35], v5 offset:0\n"
"v_mfma_f32_16x16x32_bf16 v[140:143], v[84:87], a[168:171], 0\n"
"v_mfma_f32_16x16x32_bf16 v[136:139], v[88:91], a[172:175], v[136:139]\n"
"ds_read_b128 v[36:39], v5 offset:128\n"
"v_mfma_f32_16x16x32_bf16 v[140:143], v[92:95], a[172:175], v[140:143]\n"
"s_add_u32 s73, s73, 1\n"
"v_mfma_f32_16x16x32_bf16 v[136:139], v[96:99], a[176:179], v[136:139]\n"
"ds_read_b128 v[40:43], v5 offset:2048\n"
"v_mfma_f32_16x16x32_bf16 v[140:143], v[100:103], a[176:179], v[140:143]\n"
"s_mov_b64 exec, vcc\n"
"v_mov_b32 v9, 0\n"
"buffer_atomic_add v9, v7, s[36:39], 0 offen sc0 sc1\n"
"s_mov_b64 exec, -1\n"
"v_mfma_f32_16x16x32_bf16 v[136:139], v[104:107], a[180:183], v[136:139]\n"
"ds_read_b128 v[44:47], v5 offset:2176\n"
"v_cvt_pk_bf16_f32 v160, v128, v129\n"
"v_mfma_f32_16x16x32_bf16 v[140:143], v[108:111], a[180:183], v[140:143]\n"
"v_cvt_pk_bf16_f32 v161, v130, v131\n"
"v_mfma_f32_16x16x32_bf16 v[136:139], v[112:115], a[184:187], v[136:139]\n"
"ds_read_b128 v[48:51], v5 offset:4096\n"
"v_cvt_pk_bf16_f32 v162, v132, v133\n"
"v_mfma_f32_16x16x32_bf16 v[140:143], v[116:119], a[184:187], v[140:143]\n"
"v_cvt_pk_bf16_f32 v163, v134, v135\n"
"v_mfma_f32_16x16x32_bf16 v[136:139], v[120:123], a[188:191], v[136:139]\n"
"buffer_store_dwordx4 v[160:163], v10, s[36:39], s74 offen offset:0 sc1\n"
"v_mfma_f32_16x16x32_bf16 v[140:143], v[124:127], a[188:191], v[140:143]\n"
"ds_read_b128 v[52:55], v5 offset:4224\n"
"v_mfma_f32_16x16x32_bf16 v[128:131], v[80:83], a[192:195], 0\n"
"ds_read_b128 v[56:59], v5 offset:6144\n"
"v_mfma_f32_16x16x32_bf16 v[132:135], v[84:87], a[192:195], 0\n"
"v_mfma_f32_16x16x32_bf16 v[128:131], v[88:91], a[196:199], v[128:131]\n"
"ds_read_b128 v[60:63], v5 offset:6272\n"
"v_mfma_f32_16x16x32_bf16 v[132:135], v[92:95], a[196:199], v[132:135]\n"
"v_mfma_f32_16x16x32_bf16 v[128:131], v[96:99], a[200:203], v[128:131]\n"
"ds_read_b128 v[64:67], v5 offset:8192\n"
"v_mfma_f32_16x16x32_bf16 v[132:135], v[100:103], a[200:203], v[132:135]\n"
"v_mfma_f32_16x16x32_bf16 v[128:131], v[104:107], a[204:207], v[128:131]\n"
"ds_read_b128 v[68:71], v5 offset:8320\n"
"v_cvt_pk_bf16_f32 v164, v136, v137\n"
"v_mfma_f32_16x16x32_bf16 v[132:135], v[108:111], a[204:207], v[132:135]\n"
"v_cvt_pk_bf16_f32 v165, v138, v139\n"
"v_mfma_f32_16x16x32_bf16 v[128:131], v[112:115], a[208:211], v[128:131]\n"
"ds_read_b128 v[72:75], v5 offset:10240\n"
"v_cvt_pk_bf16_f32 v166, v140, v141\n"
"v_mfma_f32_16x16x32_bf16 v[132:135], v[116:119], a[208:211], v[132:135]\n"
"v_cvt_pk_bf16_f32 v167, v142, v143\n"
"v_mfma_f32_16x16x32_bf16 v[128:131], v[120:123], a[212:215], v[128:131]\n"
"buffer_store_dwordx4 v[164:167], v10, s[36:39], s74 offen offset:256 sc1\n"
"v_mfma_f32_16x16x32_bf16 v[132:135], v[124:127], a[212:215], v[132:135]\n"
"ds_read_b128 v[76:79], v5 offset:10368\n"
"s_add_u32 s74, s48, 1048576\n"
"v_mfma_f32_16x16x32_bf16 v[136:139], v[80:83], a[216:219], 0\n"
"v_mfma_f32_16x16x32_bf16 v[140:143], v[84:87], a[216:219], 0\n"
"ds_read_b32 v13, v11 offset:40964\n"
"v_mfma_f32_16x16x32_bf16 v[136:139], v[88:91], a[220:223], v[136:139]\n"
"v_mfma_f32_16x16x32_bf16 v[140:143], v[92:95], a[220:223], v[140:143]\n"
"v_mfma_f32_16x16x32_bf16 v[136:139], v[96:99], a[224:227], v[136:139]\n"
"s_add_u32 s48, s48, 1310720\n"
"v_mfma_f32_16x16x32_bf16 v[140:143], v[100:103], a[224:227], v[140:143]\n"
"v_mfma_f32_16x16x32_bf16 v[136:139], v[104:107], a[228:231], v[136:139]\n"
"v_cvt_pk_bf16_f32 v160, v128, v129\n"
"v_mfma_f32_16x16x32_bf16 v[140:143], v[108:111], a[228:231], v[140:143]\n"
"v_cvt_pk_bf16_f32 v161, v130, v131\n"
"v_mfma_f32_16x16x32_bf16 v[136:139], v[112:115], a[232:235], v[136:139]\n"
"v_cvt_pk_bf16_f32 v162, v132, v133\n"
"s_waitcnt lgkmcnt(0)\n"
"v_cmp_eq_u32 vcc, s75, v13\n"
"v_mfma_f32_16x16x32_bf16 v[140:143], v[116:119], a[232:235], v[140:143]\n"
"v_cvt_pk_bf16_f32 v163, v134, v135\n"
"v_mfma_f32_16x16x32_bf16 v[136:139], v[120:123], a[236:239], v[136:139]\n"
"buffer_store_dwordx4 v[160:163], v10, s[36:39], s74 offen offset:0 sc1\n"
"v_mfma_f32_16x16x32_bf16 v[140:143], v[124:127], a[236:239], v[140:143]\n"
"s_cbranch_vccz W11_%=\n"
"s_waitcnt vmcnt(3)\n"
"v_cmp_gt_u32 vcc, s75, v9\n"
"s_nop 2\n"
"v_cvt_pk_bf16_f32 v164, v136, v137\n"
"v_cvt_pk_bf16_f32 v165, v138, v139\n"
"v_cvt_pk_bf16_f32 v166, v140, v141\n"
"v_cvt_pk_bf16_f32 v167, v142, v143\n"
"buffer_store_dwordx4 v[164:167], v10, s[36:39], s74 offen offset:256 sc1\n"
"s_cbranch_vccz W12_%=\n"
"s_waitcnt vmcnt(0)\n"
"v_mov_b32 v6, s73\n"
"buffer_store_dword v6, v4, s[36:39], 0 offen sc1\n"
"s_mov_b32 s76, 131072\n"
"s_mov_b32 exec_lo, 7\n"
"s_mov_b32 exec_hi, 0\n"
"s_mov_b32 s79, 0\n"
"W13_%=:\n"
"s_sleep 1\n"
"v_mov_b32 v9, 0\n"
"buffer_atomic_add v9, v7, s[36:39], s79 offen sc0 sc1\n"
"s_sub_u32 s76, s76, 1\n"
"s_cbranch_scc1 WABORT_%=\n"
"s_waitcnt vmcnt(0)\n"
"v_cmp_gt_u32 vcc, s75, v9\n"
"s_nop 0\n"
"s_cbranch_vccnz W13_%=\n"
"s_mov_b64 exec, -1\n"
"W12_%=:\n"
"buffer_load_dwordx4 v0, s[36:39], s46 offen offset:0 sc1 lds\n"
"buffer_load_dwordx4 v0, s[36:39], s46 offen offset:1024 sc1 lds\n"
"buffer_load_dwordx4 v0, s[36:39], s46 offen offset:2048 sc1 lds\n"
"s_waitcnt vmcnt(3)\n"
"v_mov_b32 v6, s73\n"
"buffer_store_dword v6, v4, s[36:39], 0 offen sc1\n"
"s_waitcnt vmcnt(1)\n"
"s_barrier\n"
"ds_read_b128 v[32:35], v5 offset:0\n"
"ds_read_b128 v[36:39], v5 offset:128\n"
"ds_read_b128 v[40:43], v5 offset:2048\n"
"ds_read_b128 v[44:47], v5 offset:2176\n"
"ds_read_b128 v[48:51], v5 offset:4096\n"
"ds_read_b128 v[52:55], v5 offset:4224\n"
"ds_read_b128 v[56:59], v5 offset:6144\n"
"ds_read_b128 v[60:63], v5 offset:6272\n"
"ds_read_b128 v[64:67], v5 offset:8192\n"
"ds_read_b128 v[68:71], v5 offset:8320\n"
"ds_read_b128 v[72:75], v5 offset:10240\n"
"ds_read_b128 v[76:79], v5 offset:10368\n"
"s_waitcnt lgkmcnt(0)\n"
"W11_%=:\n"
"s_add_u32 s46, s46, 393216\n"
"s_add_u32 s69, s69, 12288\n"
"s_cmp_eq_u32 s69, 36864\n"
"s_cselect_b32 s69, 0, s69\n"
"s_add_u32 s75, s75, 1\n"
"s_min_u32 s75, s75, 64\n"
"s_cmp_lt_u32 s73, 64\n"
"s_cbranch_scc1 W7_%=\n"
"v_cvt_pk_bf16_f32 v164, v136, v137\n"
"v_cvt_pk_bf16_f32 v165, v138, v139\n"
"v_cvt_pk_bf16_f32 v166, v140, v141\n"
"v_cvt_pk_bf16_f32 v167, v142, v143\n"
"buffer_store_dwordx4 v[164:167], v10, s[36:39], s74 offen offset:256 sc1\n"
"s_waitcnt vmcnt(0)\n"
"v_mov_b32 v6, s73\n"
"buffer_store_dword v6, v4, s[36:39], 0 offen sc1\n"
"s_endpgm\n"
"WABORT_%=:\n"
"s_mov_b64 exec, -1\n"
"s_nop 7\n"
"s_nop 7\n"
"v_mov_b32 v0, 46268544\n"
"v_mov_b32 v1, 1\n"
"buffer_store_dword v1, v0, s[36:39], 0 offen sc1\n"
"s_endpgm\n");
}

DI void role_o(const Ctx& c, int team, int t0, int nt) {
    SETUP
    setsrd(sa, 4, P->wd, FF * DM * 2); setsrd(sa, 8, P->x, NSEG * 32 * DM * 2); setsrd(sa, 12, P->out, NSEG * 32 * DM * 2);
    sb[0] = w * 384 * (DM * 2) + t0 * 64; sb[1] = w * 12288; sb[2] = team; sb[4] = 3; sb[3] = nt == 3 ? 0 : WS_SPAN;
    sb[7] = O_HF + w * 12 * 2048; sb[8] = O_VW + t0 * 65536 + w * 8 * 2048; sb[9] = O_PTX + w * 8 * 2048; sb[10] = O_PTM + w * 8 * 512;
    sb[11] = t0 * 2; sb[12] = c.par; sb[13] = O_U + w * 2048;
    va[1] = (lane >> 2) * (DM * 2) + p * 16;
    va[2] = w * 12288 + (4 * (g >> 1) + q) * 64 + (g & 1) * 32 + p * 8;
    va[4] = lane < 32 ? F_W + fw(32 * w + lane) : F_F + fw(12 * w + lane % 12); va[11] = F_U + (8 * w + c8) * 128;
    va[5] = F_PT + (8 * w + c8) * 128;
    va[6] = (g & 1) * 16384 + (g >> 1) * 256 + n * 16;
    va[7] = (8 * w + r8) * (DM * 2) + t0 * 64 + c8 * 16;
    const int t2 = (lane >> 2) & 7;
    va[3] = (lane < 32 && nt != 2) ? (8 * w + t2) * (DM * 2) + (t0 + nt - 1) * 64 + p * 16 : 0x7ffffff0u;
    va[8] = w * 12 * 1088 + lane * 16; va[9] = c8 * 1088 + (8 * w + r8) * 16; va[10] = (8 + p) * 1088 + (8 * w + t2) * 16;
    sb[5] = nt;
    RUN("s_cmp_eq_u32 s57, 1\n"
"s_cbranch_scc1 TGO_%=\n"
"s_nop 4\n"
"s_add_u32 m0, s53, 0\n"
"s_add_u32 s72, s52, 0\n"
"buffer_load_dwordx4 v1, s[40:43], s72 offen lds\n"
"s_add_u32 m0, s53, 1024\n"
"s_add_u32 s72, s52, 64\n"
"buffer_load_dwordx4 v1, s[40:43], s72 offen lds\n"
"s_add_u32 m0, s53, 2048\n"
"s_add_u32 s72, s52, 128\n"
"buffer_load_dwordx4 v1, s[40:43], s72 offen lds\n"
"s_add_u32 m0, s53, 3072\n"
"s_add_u32 s72, s52, 20480\n"
"buffer_load_dwordx4 v1, s[40:43], s72 offen lds\n"
"s_add_u32 m0, s53, 4096\n"
"s_add_u32 s72, s52, 20544\n"
"buffer_load_dwordx4 v1, s[40:43], s72 offen lds\n"
"s_add_u32 m0, s53, 5120\n"
"s_add_u32 s72, s52, 20608\n"
"buffer_load_dwordx4 v1, s[40:43], s72 offen lds\n"
"s_add_u32 m0, s53, 6144\n"
"s_add_u32 s72, s52, 40960\n"
"buffer_load_dwordx4 v1, s[40:43], s72 offen lds\n"
"s_add_u32 m0, s53, 7168\n"
"s_add_u32 s72, s52, 41024\n"
"buffer_load_dwordx4 v1, s[40:43], s72 offen lds\n"
"s_add_u32 m0, s53, 8192\n"
"s_add_u32 s72, s52, 41088\n"
"buffer_load_dwordx4 v1, s[40:43], s72 offen lds\n"
"s_add_u32 m0, s53, 9216\n"
"s_add_u32 s72, s52, 61440\n"
"buffer_load_dwordx4 v1, s[40:43], s72 offen lds\n"
"s_add_u32 m0, s53, 10240\n"
"s_add_u32 s72, s52, 61504\n"
"buffer_load_dwordx4 v1, s[40:43], s72 offen lds\n"
"s_add_u32 m0, s53, 11264\n"
"s_add_u32 s72, s52, 61568\n"
"buffer_load_dwordx4 v1, s[40:43], s72 offen lds\n"
"s_add_u32 m0, s53, 49152\n"
"s_add_u32 s72, s52, 81920\n"
"buffer_load_dwordx4 v1, s[40:43], s72 offen lds\n"
"s_add_u32 m0, s53, 50176\n"
"s_add_u32 s72, s52, 81984\n"
"buffer_load_dwordx4 v1, s[40:43], s72 offen lds\n"
"s_add_u32 m0, s53, 51200\n"
"s_add_u32 s72, s52, 82048\n"
"buffer_load_dwordx4 v1, s[40:43], s72 offen lds\n"
"s_add_u32 m0, s53, 52224\n"
"s_add_u32 s72, s52, 102400\n"
"buffer_load_dwordx4 v1, s[40:43], s72 offen lds\n"
"s_add_u32 m0, s53, 53248\n"
"s_add_u32 s72, s52, 102464\n"
"buffer_load_dwordx4 v1, s[40:43], s72 offen lds\n"
"s_add_u32 m0, s53, 54272\n"
"s_add_u32 s72, s52, 102528\n"
"buffer_load_dwordx4 v1, s[40:43], s72 offen lds\n"
"s_add_u32 m0, s53, 55296\n"
"s_add_u32 s72, s52, 122880\n"
"buffer_load_dwordx4 v1, s[40:43], s72 offen lds\n"
"s_add_u32 m0, s53, 56320\n"
"s_add_u32 s72, s52, 122944\n"
"buffer_load_dwordx4 v1, s[40:43], s72 offen lds\n"
"s_add_u32 m0, s53, 57344\n"
"s_add_u32 s72, s52, 123008\n"
"buffer_load_dwordx4 v1, s[40:43], s72 offen lds\n"
"s_add_u32 m0, s53, 58368\n"
"s_add_u32 s72, s52, 143360\n"
"buffer_load_dwordx4 v1, s[40:43], s72 offen lds\n"
"s_add_u32 m0, s53, 59392\n"
"s_add_u32 s72, s52, 143424\n"
"buffer_load_dwordx4 v1, s[40:43], s72 offen lds\n"
"s_add_u32 m0, s53, 60416\n"
"s_add_u32 s72, s52, 143488\n"
"buffer_load_dwordx4 v1, s[40:43], s72 offen lds\n"
"s_waitcnt vmcnt(12)\n"
"s_waitcnt lgkmcnt(0)\n"
"s_barrier\n"
"s_add_u32 m0, s53, 98304\n"
"s_add_u32 s72, s52, 163840\n"
"buffer_load_dwordx4 v1, s[40:43], s72 offen lds\n"
"s_add_u32 m0, s53, 99328\n"
"s_add_u32 s72, s52, 163904\n"
"buffer_load_dwordx4 v1, s[40:43], s72 offen lds\n"
"s_add_u32 m0, s53, 100352\n"
"s_add_u32 s72, s52, 163968\n"
"buffer_load_dwordx4 v1, s[40:43], s72 offen lds\n"
"s_add_u32 m0, s53, 101376\n"
"s_add_u32 s72, s52, 184320\n"
"buffer_load_dwordx4 v1, s[40:43], s72 offen lds\n"
"s_add_u32 m0, s53, 102400\n"
"s_add_u32 s72, s52, 184384\n"
"buffer_load_dwordx4 v1, s[40:43], s72 offen lds\n"
"s_add_u32 m0, s53, 103424\n"
"s_add_u32 s72, s52, 184448\n"
"buffer_load_dwordx4 v1, s[40:43], s72 offen lds\n"
"s_add_u32 m0, s53, 104448\n"
"s_add_u32 s72, s52, 204800\n"
"buffer_load_dwordx4 v1, s[40:43], s72 offen lds\n"
"s_add_u32 m0, s53, 105472\n"
"s_add_u32 s72, s52, 204864\n"
"buffer_load_dwordx4 v1, s[40:43], s72 offen lds\n"
"s_add_u32 m0, s53, 106496\n"
"s_add_u32 s72, s52, 204928\n"
"buffer_load_dwordx4 v1, s[40:43], s72 offen lds\n"
"s_add_u32 m0, s53, 107520\n"
"s_add_u32 s72, s52, 225280\n"
"buffer_load_dwordx4 v1, s[40:43], s72 offen lds\n"
"s_add_u32 m0, s53, 108544\n"
"s_add_u32 s72, s52, 225344\n"
"buffer_load_dwordx4 v1, s[40:43], s72 offen lds\n"
"s_add_u32 m0, s53, 109568\n"
"s_add_u32 s72, s52, 225408\n"
"buffer_load_dwordx4 v1, s[40:43], s72 offen lds\n"
"v_add_u32 v12, 0, v2\n"
"ds_read_b64_tr_b16 v[224:225], v12 offset:0\n"
"ds_read_b64_tr_b16 v[226:227], v12 offset:512\n"
"ds_read_b64_tr_b16 v[228:229], v12 offset:1024\n"
"ds_read_b64_tr_b16 v[230:231], v12 offset:1536\n"
"ds_read_b64_tr_b16 v[232:233], v12 offset:2048\n"
"ds_read_b64_tr_b16 v[234:235], v12 offset:2560\n"
"ds_read_b64_tr_b16 v[236:237], v12 offset:3072\n"
"ds_read_b64_tr_b16 v[238:239], v12 offset:3584\n"
"ds_read_b64_tr_b16 v[240:241], v12 offset:4096\n"
"ds_read_b64_tr_b16 v[242:243], v12 offset:4608\n"
"ds_read_b64_tr_b16 v[244:245], v12 offset:5120\n"
"ds_read_b64_tr_b16 v[246:247], v12 offset:5632\n"
"ds_read_b64_tr_b16 v[248:249], v12 offset:6144\n"
"ds_read_b64_tr_b16 v[250:251], v12 offset:6656\n"
"ds_read_b64_tr_b16 v[252:253], v12 offset:7168\n"
"ds_read_b64_tr_b16 v[254:255], v12 offset:7680\n"
"ds_read_b64_tr_b16 a[0:1], v12 offset:8192\n"
"ds_read_b64_tr_b16 a[2:3], v12 offset:8704\n"
"ds_read_b64_tr_b16 a[4:5], v12 offset:9216\n"
"ds_read_b64_tr_b16 a[6:7], v12 offset:9728\n"
"ds_read_b64_tr_b16 a[8:9], v12 offset:10240\n"
"ds_read_b64_tr_b16 a[10:11], v12 offset:10752\n"
"ds_read_b64_tr_b16 a[12:13], v12 offset:11264\n"
"ds_read_b64_tr_b16 a[14:15], v12 offset:11776\n"
"s_waitcnt vmcnt(12)\n"
"s_waitcnt lgkmcnt(0)\n"
"s_barrier\n"
"s_add_u32 m0, s53, 0\n"
"s_add_u32 s72, s52, 245760\n"
"buffer_load_dwordx4 v1, s[40:43], s72 offen lds\n"
"s_add_u32 m0, s53, 1024\n"
"s_add_u32 s72, s52, 245824\n"
"buffer_load_dwordx4 v1, s[40:43], s72 offen lds\n"
"s_add_u32 m0, s53, 2048\n"
"s_add_u32 s72, s52, 245888\n"
"buffer_load_dwordx4 v1, s[40:43], s72 offen lds\n"
"s_add_u32 m0, s53, 3072\n"
"s_add_u32 s72, s52, 266240\n"
"buffer_load_dwordx4 v1, s[40:43], s72 offen lds\n"
"s_add_u32 m0, s53, 4096\n"
"s_add_u32 s72, s52, 266304\n"
"buffer_load_dwordx4 v1, s[40:43], s72 offen lds\n"
"s_add_u32 m0, s53, 5120\n"
"s_add_u32 s72, s52, 266368\n"
"buffer_load_dwordx4 v1, s[40:43], s72 offen lds\n"
"s_add_u32 m0, s53, 6144\n"
"s_add_u32 s72, s52, 286720\n"
"buffer_load_dwordx4 v1, s[40:43], s72 offen lds\n"
"s_add_u32 m0, s53, 7168\n"
"s_add_u32 s72, s52, 286784\n"
"buffer_load_dwordx4 v1, s[40:43], s72 offen lds\n"
"s_add_u32 m0, s53, 8192\n"
"s_add_u32 s72, s52, 286848\n"
"buffer_load_dwordx4 v1, s[40:43], s72 offen lds\n"
"s_add_u32 m0, s53, 9216\n"
"s_add_u32 s72, s52, 307200\n"
"buffer_load_dwordx4 v1, s[40:43], s72 offen lds\n"
"s_add_u32 m0, s53, 10240\n"
"s_add_u32 s72, s52, 307264\n"
"buffer_load_dwordx4 v1, s[40:43], s72 offen lds\n"
"s_add_u32 m0, s53, 11264\n"
"s_add_u32 s72, s52, 307328\n"
"buffer_load_dwordx4 v1, s[40:43], s72 offen lds\n"
"v_add_u32 v12, 49152, v2\n"
"ds_read_b64_tr_b16 a[16:17], v12 offset:0\n"
"ds_read_b64_tr_b16 a[18:19], v12 offset:512\n"
"ds_read_b64_tr_b16 a[20:21], v12 offset:1024\n"
"ds_read_b64_tr_b16 a[22:23], v12 offset:1536\n"
"ds_read_b64_tr_b16 a[24:25], v12 offset:2048\n"
"ds_read_b64_tr_b16 a[26:27], v12 offset:2560\n"
"ds_read_b64_tr_b16 a[28:29], v12 offset:3072\n"
"ds_read_b64_tr_b16 a[30:31], v12 offset:3584\n"
"ds_read_b64_tr_b16 a[32:33], v12 offset:4096\n"
"ds_read_b64_tr_b16 a[34:35], v12 offset:4608\n"
"ds_read_b64_tr_b16 a[36:37], v12 offset:5120\n"
"ds_read_b64_tr_b16 a[38:39], v12 offset:5632\n"
"ds_read_b64_tr_b16 a[40:41], v12 offset:6144\n"
"ds_read_b64_tr_b16 a[42:43], v12 offset:6656\n"
"ds_read_b64_tr_b16 a[44:45], v12 offset:7168\n"
"ds_read_b64_tr_b16 a[46:47], v12 offset:7680\n"
"ds_read_b64_tr_b16 a[48:49], v12 offset:8192\n"
"ds_read_b64_tr_b16 a[50:51], v12 offset:8704\n"
"ds_read_b64_tr_b16 a[52:53], v12 offset:9216\n"
"ds_read_b64_tr_b16 a[54:55], v12 offset:9728\n"
"ds_read_b64_tr_b16 a[56:57], v12 offset:10240\n"
"ds_read_b64_tr_b16 a[58:59], v12 offset:10752\n"
"ds_read_b64_tr_b16 a[60:61], v12 offset:11264\n"
"ds_read_b64_tr_b16 a[62:63], v12 offset:11776\n"
"s_waitcnt vmcnt(12)\n"
"s_waitcnt lgkmcnt(0)\n"
"s_barrier\n"
"s_add_u32 m0, s53, 49152\n"
"s_add_u32 s72, s52, 327680\n"
"buffer_load_dwordx4 v1, s[40:43], s72 offen lds\n"
"s_add_u32 m0, s53, 50176\n"
"s_add_u32 s72, s52, 327744\n"
"buffer_load_dwordx4 v1, s[40:43], s72 offen lds\n"
"s_add_u32 m0, s53, 51200\n"
"s_add_u32 s72, s52, 327808\n"
"buffer_load_dwordx4 v1, s[40:43], s72 offen lds\n"
"s_add_u32 m0, s53, 52224\n"
"s_add_u32 s72, s52, 348160\n"
"buffer_load_dwordx4 v1, s[40:43], s72 offen lds\n"
"s_add_u32 m0, s53, 53248\n"
"s_add_u32 s72, s52, 348224\n"
"buffer_load_dwordx4 v1, s[40:43], s72 offen lds\n"
"s_add_u32 m0, s53, 54272\n"
"s_add_u32 s72, s52, 348288\n"
"buffer_load_dwordx4 v1, s[40:43], s72 offen lds\n"
"s_add_u32 m0, s53, 55296\n"
"s_add_u32 s72, s52, 368640\n"
"buffer_load_dwordx4 v1, s[40:43], s72 offen lds\n"
"s_add_u32 m0, s53, 56320\n"
"s_add_u32 s72, s52, 368704\n"
"buffer_load_dwordx4 v1, s[40:43], s72 offen lds\n"
"s_add_u32 m0, s53, 57344\n"
"s_add_u32 s72, s52, 368768\n"
"buffer_load_dwordx4 v1, s[40:43], s72 offen lds\n"
"s_add_u32 m0, s53, 58368\n"
"s_add_u32 s72, s52, 389120\n"
"buffer_load_dwordx4 v1, s[40:43], s72 offen lds\n"
"s_add_u32 m0, s53, 59392\n"
"s_add_u32 s72, s52, 389184\n"
"buffer_load_dwordx4 v1, s[40:43], s72 offen lds\n"
"s_add_u32 m0, s53, 60416\n"
"s_add_u32 s72, s52, 389248\n"
"buffer_load_dwordx4 v1, s[40:43], s72 offen lds\n"
"v_add_u32 v12, 98304, v2\n"
"ds_read_b64_tr_b16 a[64:65], v12 offset:0\n"
"ds_read_b64_tr_b16 a[66:67], v12 offset:512\n"
"ds_read_b64_tr_b16 a[68:69], v12 offset:1024\n"
"ds_read_b64_tr_b16 a[70:71], v12 offset:1536\n"
"ds_read_b64_tr_b16 a[72:73], v12 offset:2048\n"
"ds_read_b64_tr_b16 a[74:75], v12 offset:2560\n"
"ds_read_b64_tr_b16 a[76:77], v12 offset:3072\n"
"ds_read_b64_tr_b16 a[78:79], v12 offset:3584\n"
"ds_read_b64_tr_b16 a[80:81], v12 offset:4096\n"
"ds_read_b64_tr_b16 a[82:83], v12 offset:4608\n"
"ds_read_b64_tr_b16 a[84:85], v12 offset:5120\n"
"ds_read_b64_tr_b16 a[86:87], v12 offset:5632\n"
"ds_read_b64_tr_b16 a[88:89], v12 offset:6144\n"
"ds_read_b64_tr_b16 a[90:91], v12 offset:6656\n"
"ds_read_b64_tr_b16 a[92:93], v12 offset:7168\n"
"ds_read_b64_tr_b16 a[94:95], v12 offset:7680\n"
"ds_read_b64_tr_b16 a[96:97], v12 offset:8192\n"
"ds_read_b64_tr_b16 a[98:99], v12 offset:8704\n"
"ds_read_b64_tr_b16 a[100:101], v12 offset:9216\n"
"ds_read_b64_tr_b16 a[102:103], v12 offset:9728\n"
"ds_read_b64_tr_b16 a[104:105], v12 offset:10240\n"
"ds_read_b64_tr_b16 a[106:107], v12 offset:10752\n"
"ds_read_b64_tr_b16 a[108:109], v12 offset:11264\n"
"ds_read_b64_tr_b16 a[110:111], v12 offset:11776\n"
"s_waitcnt vmcnt(12)\n"
"s_waitcnt lgkmcnt(0)\n"
"s_barrier\n"
"s_add_u32 m0, s53, 98304\n"
"s_add_u32 s72, s52, 409600\n"
"buffer_load_dwordx4 v1, s[40:43], s72 offen lds\n"
"s_add_u32 m0, s53, 99328\n"
"s_add_u32 s72, s52, 409664\n"
"buffer_load_dwordx4 v1, s[40:43], s72 offen lds\n"
"s_add_u32 m0, s53, 100352\n"
"s_add_u32 s72, s52, 409728\n"
"buffer_load_dwordx4 v1, s[40:43], s72 offen lds\n"
"s_add_u32 m0, s53, 101376\n"
"s_add_u32 s72, s52, 430080\n"
"buffer_load_dwordx4 v1, s[40:43], s72 offen lds\n"
"s_add_u32 m0, s53, 102400\n"
"s_add_u32 s72, s52, 430144\n"
"buffer_load_dwordx4 v1, s[40:43], s72 offen lds\n"
"s_add_u32 m0, s53, 103424\n"
"s_add_u32 s72, s52, 430208\n"
"buffer_load_dwordx4 v1, s[40:43], s72 offen lds\n"
"s_add_u32 m0, s53, 104448\n"
"s_add_u32 s72, s52, 450560\n"
"buffer_load_dwordx4 v1, s[40:43], s72 offen lds\n"
"s_add_u32 m0, s53, 105472\n"
"s_add_u32 s72, s52, 450624\n"
"buffer_load_dwordx4 v1, s[40:43], s72 offen lds\n"
"s_add_u32 m0, s53, 106496\n"
"s_add_u32 s72, s52, 450688\n"
"buffer_load_dwordx4 v1, s[40:43], s72 offen lds\n"
"s_add_u32 m0, s53, 107520\n"
"s_add_u32 s72, s52, 471040\n"
"buffer_load_dwordx4 v1, s[40:43], s72 offen lds\n"
"s_add_u32 m0, s53, 108544\n"
"s_add_u32 s72, s52, 471104\n"
"buffer_load_dwordx4 v1, s[40:43], s72 offen lds\n"
"s_add_u32 m0, s53, 109568\n"
"s_add_u32 s72, s52, 471168\n"
"buffer_load_dwordx4 v1, s[40:43], s72 offen lds\n"
"v_add_u32 v12, 0, v2\n"
"ds_read_b64_tr_b16 a[112:113], v12 offset:0\n"
"ds_read_b64_tr_b16 a[114:115], v12 offset:512\n"
"ds_read_b64_tr_b16 a[116:117], v12 offset:1024\n"
"ds_read_b64_tr_b16 a[118:119], v12 offset:1536\n"
"ds_read_b64_tr_b16 a[120:121], v12 offset:2048\n"
"ds_read_b64_tr_b16 a[122:123], v12 offset:2560\n"
"ds_read_b64_tr_b16 a[124:125], v12 offset:3072\n"
"ds_read_b64_tr_b16 a[126:127], v12 offset:3584\n"
"ds_read_b64_tr_b16 a[128:129], v12 offset:4096\n"
"ds_read_b64_tr_b16 a[130:131], v12 offset:4608\n"
"ds_read_b64_tr_b16 a[132:133], v12 offset:5120\n"
"ds_read_b64_tr_b16 a[134:135], v12 offset:5632\n"
"ds_read_b64_tr_b16 a[136:137], v12 offset:6144\n"
"ds_read_b64_tr_b16 a[138:139], v12 offset:6656\n"
"ds_read_b64_tr_b16 a[140:141], v12 offset:7168\n"
"ds_read_b64_tr_b16 a[142:143], v12 offset:7680\n"
"ds_read_b64_tr_b16 a[144:145], v12 offset:8192\n"
"ds_read_b64_tr_b16 a[146:147], v12 offset:8704\n"
"ds_read_b64_tr_b16 a[148:149], v12 offset:9216\n"
"ds_read_b64_tr_b16 a[150:151], v12 offset:9728\n"
"ds_read_b64_tr_b16 a[152:153], v12 offset:10240\n"
"ds_read_b64_tr_b16 a[154:155], v12 offset:10752\n"
"ds_read_b64_tr_b16 a[156:157], v12 offset:11264\n"
"ds_read_b64_tr_b16 a[158:159], v12 offset:11776\n"
"s_waitcnt vmcnt(12)\n"
"s_waitcnt lgkmcnt(0)\n"
"s_barrier\n"
"v_add_u32 v12, 49152, v2\n"
"ds_read_b64_tr_b16 a[160:161], v12 offset:0\n"
"ds_read_b64_tr_b16 a[162:163], v12 offset:512\n"
"ds_read_b64_tr_b16 a[164:165], v12 offset:1024\n"
"ds_read_b64_tr_b16 a[166:167], v12 offset:1536\n"
"ds_read_b64_tr_b16 a[168:169], v12 offset:2048\n"
"ds_read_b64_tr_b16 a[170:171], v12 offset:2560\n"
"ds_read_b64_tr_b16 a[172:173], v12 offset:3072\n"
"ds_read_b64_tr_b16 a[174:175], v12 offset:3584\n"
"ds_read_b64_tr_b16 a[176:177], v12 offset:4096\n"
"ds_read_b64_tr_b16 a[178:179], v12 offset:4608\n"
"ds_read_b64_tr_b16 a[180:181], v12 offset:5120\n"
"ds_read_b64_tr_b16 a[182:183], v12 offset:5632\n"
"ds_read_b64_tr_b16 a[184:185], v12 offset:6144\n"
"ds_read_b64_tr_b16 a[186:187], v12 offset:6656\n"
"ds_read_b64_tr_b16 a[188:189], v12 offset:7168\n"
"ds_read_b64_tr_b16 a[190:191], v12 offset:7680\n"
"ds_read_b64_tr_b16 a[192:193], v12 offset:8192\n"
"ds_read_b64_tr_b16 a[194:195], v12 offset:8704\n"
"ds_read_b64_tr_b16 a[196:197], v12 offset:9216\n"
"ds_read_b64_tr_b16 a[198:199], v12 offset:9728\n"
"ds_read_b64_tr_b16 a[200:201], v12 offset:10240\n"
"ds_read_b64_tr_b16 a[202:203], v12 offset:10752\n"
"ds_read_b64_tr_b16 a[204:205], v12 offset:11264\n"
"ds_read_b64_tr_b16 a[206:207], v12 offset:11776\n"
"s_waitcnt vmcnt(0)\n"
"s_waitcnt lgkmcnt(0)\n"
"s_barrier\n"
"v_add_u32 v12, 98304, v2\n"
"ds_read_b64_tr_b16 a[208:209], v12 offset:0\n"
"ds_read_b64_tr_b16 a[210:211], v12 offset:512\n"
"ds_read_b64_tr_b16 a[212:213], v12 offset:1024\n"
"ds_read_b64_tr_b16 a[214:215], v12 offset:1536\n"
"ds_read_b64_tr_b16 a[216:217], v12 offset:2048\n"
"ds_read_b64_tr_b16 a[218:219], v12 offset:2560\n"
"ds_read_b64_tr_b16 a[220:221], v12 offset:3072\n"
"ds_read_b64_tr_b16 a[222:223], v12 offset:3584\n"
"ds_read_b64_tr_b16 a[224:225], v12 offset:4096\n"
"ds_read_b64_tr_b16 a[226:227], v12 offset:4608\n"
"ds_read_b64_tr_b16 a[228:229], v12 offset:5120\n"
"ds_read_b64_tr_b16 a[230:231], v12 offset:5632\n"
"ds_read_b64_tr_b16 a[232:233], v12 offset:6144\n"
"ds_read_b64_tr_b16 a[234:235], v12 offset:6656\n"
"ds_read_b64_tr_b16 a[236:237], v12 offset:7168\n"
"ds_read_b64_tr_b16 a[238:239], v12 offset:7680\n"
"ds_read_b64_tr_b16 a[240:241], v12 offset:8192\n"
"ds_read_b64_tr_b16 a[242:243], v12 offset:8704\n"
"ds_read_b64_tr_b16 a[244:245], v12 offset:9216\n"
"ds_read_b64_tr_b16 a[246:247], v12 offset:9728\n"
"ds_read_b64_tr_b16 a[248:249], v12 offset:10240\n"
"ds_read_b64_tr_b16 a[250:251], v12 offset:10752\n"
"ds_read_b64_tr_b16 a[252:253], v12 offset:11264\n"
"ds_read_b64_tr_b16 a[254:255], v12 offset:11776\n"
"s_waitcnt lgkmcnt(0)\n"
"s_barrier\n"
"s_mov_b32 s68, s54\n"
"v_mov_b32 v1, 0\n"
"s_nop 4\n"
"O1_%=:\n"
"s_add_u32 s69, s68, 1\n"
"s_and_b32 s83, s68, 63\n"
"s_mul_i32 s70, s83, 98304\n"
"s_add_u32 s70, s70, s59\n"
"v_cmp_gt_u32 vcc, s69, v1\n"
"s_nop 0\n"
"s_cbranch_vccz O2_%=\n"
"s_mov_b32 s76, 131072\n"
"s_branch O4_%=\n"
"O3_%=:\n"
"s_sleep 2\n"
"O4_%=:\n"
"buffer_load_dword v1, v4, s[36:39], 0 offen sc1\n"
"s_sub_u32 s76, s76, 1\n"
"s_cbranch_scc1 OABORT_%=\n"
"s_waitcnt vmcnt(0)\n"
"v_cmp_gt_u32 vcc, s69, v1\n"
"s_nop 0\n"
"s_cbranch_vccnz O3_%=\n"
"s_mov_b32 s71, s70\n"
"buffer_load_dwordx4 v[172:175], v0, s[36:39], s71 offen offset:0 sc1\n"
"buffer_load_dwordx4 v[176:179], v0, s[36:39], s71 offen offset:1024 sc1\n"
"buffer_load_dwordx4 v[180:183], v0, s[36:39], s71 offen offset:2048 sc1\n"
"buffer_load_dwordx4 v[184:187], v0, s[36:39], s71 offen offset:3072 sc1\n"
"s_add_u32 s71, s71, 4096\n"
"buffer_load_dwordx4 v[188:191], v0, s[36:39], s71 offen offset:0 sc1\n"
"buffer_load_dwordx4 v[192:195], v0, s[36:39], s71 offen offset:1024 sc1\n"
"buffer_load_dwordx4 v[196:199], v0, s[36:39], s71 offen offset:2048 sc1\n"
"buffer_load_dwordx4 v[200:203], v0, s[36:39], s71 offen offset:3072 sc1\n"
"s_add_u32 s71, s71, 4096\n"
"buffer_load_dwordx4 v[204:207], v0, s[36:39], s71 offen offset:0 sc1\n"
"buffer_load_dwordx4 v[208:211], v0, s[36:39], s71 offen offset:1024 sc1\n"
"buffer_load_dwordx4 v[212:215], v0, s[36:39], s71 offen offset:2048 sc1\n"
"buffer_load_dwordx4 v[216:219], v0, s[36:39], s71 offen offset:3072 sc1\n"
"buffer_load_dword v2, v0, s[36:39], s38 offen\n"
"buffer_load_dword v2, v0, s[36:39], s38 offen\n"
"O2_%=:\n"
"s_mul_i32 s71, s83, 1310720\n"
"s_add_u32 s71, s71, s60\n"
"s_add_u32 s72, s71, 65536\n"
"s_add_u32 s73, s72, 65536\n"
"s_mul_i32 s74, s83, 65536\n"
"s_add_u32 s74, s74, s61\n"
"s_mul_i32 s75, s83, 16384\n"
"s_add_u32 s75, s75, s62\n"
"s_mul_i32 s78, s83, 40\n"
"s_add_u32 s78, s78, s63\n"
"s_add_u32 s78, s78, 0\n"
"s_lshr_b32 s79, s78, 7\n"
"s_xor_b32 s79, s79, s64\n"
"s_and_b32 s79, s79, 1\n"
"s_lshl_b32 s79, s79, 13\n"
"s_lshl_b32 s78, s78, 14\n"
"s_add_u32 s78, s78, s79\n"
"s_add_u32 s80, s78, s65\n"
"s_mul_i32 s78, s83, 40\n"
"s_add_u32 s78, s78, s63\n"
"s_add_u32 s78, s78, 2\n"
"s_lshr_b32 s79, s78, 7\n"
"s_xor_b32 s79, s79, s64\n"
"s_and_b32 s79, s79, 1\n"
"s_lshl_b32 s79, s79, 13\n"
"s_lshl_b32 s78, s78, 14\n"
"s_add_u32 s78, s78, s79\n"
"s_add_u32 s81, s78, s65\n"
"s_mul_i32 s78, s83, 40\n"
"s_add_u32 s78, s78, s63\n"
"s_add_u32 s78, s78, 4\n"
"s_lshr_b32 s79, s78, 7\n"
"s_xor_b32 s79, s79, s64\n"
"s_and_b32 s79, s79, 1\n"
"s_lshl_b32 s79, s79, 13\n"
"s_lshl_b32 s78, s78, 14\n"
"s_add_u32 s78, s78, s79\n"
"s_add_u32 s82, s78, s65\n"
"s_max_u32 s73, s73, s55\n"
"s_max_u32 s82, s82, s55\n"
"s_mul_i32 s77, s68, 40960\n"
"s_add_u32 s70, s70, 12288\n"
"buffer_load_dwordx4 v[64:67], v0, s[36:39], s70 offen offset:0 sc1\n"
"buffer_load_dwordx4 v[68:71], v0, s[36:39], s70 offen offset:1024 sc1\n"
"s_waitcnt vmcnt(15)\n"
"v_mfma_f32_32x32x16_bf16 v[16:31], v[224:227], v[172:175], 0\n"
"buffer_load_dwordx4 v[72:75], v0, s[36:39], s70 offen offset:2048 sc1\n"
"v_mfma_f32_32x32x16_bf16 v[32:47], v[228:231], v[172:175], 0\n"
"buffer_load_dwordx4 v[76:79], v0, s[36:39], s70 offen offset:3072 sc1\n"
"v_mfma_f32_32x32x16_bf16 v[48:63], v[232:235], v[172:175], 0\n"
"s_waitcnt vmcnt(16)\n"
"v_mfma_f32_32x32x16_bf16 v[16:31], v[236:239], v[176:179], v[16:31]\n"
"s_add_u32 s70, s70, 4096\n"
"buffer_load_dwordx4 v[80:83], v0, s[36:39], s70 offen offset:0 sc1\n"
"v_mfma_f32_32x32x16_bf16 v[32:47], v[240:243], v[176:179], v[32:47]\n"
"buffer_load_dwordx4 v[84:87], v0, s[36:39], s70 offen offset:1024 sc1\n"
"v_mfma_f32_32x32x16_bf16 v[48:63], v[244:247], v[176:179], v[48:63]\n"
"s_waitcnt vmcnt(17)\n"
"v_mfma_f32_32x32x16_bf16 v[16:31], v[248:251], v[180:183], v[16:31]\n"
"buffer_load_dwordx4 v[88:91], v0, s[36:39], s70 offen offset:2048 sc1\n"
"v_mfma_f32_32x32x16_bf16 v[32:47], v[252:255], v[180:183], v[32:47]\n"
"buffer_load_dwordx4 v[92:95], v0, s[36:39], s70 offen offset:3072 sc1\n"
"v_mfma_f32_32x32x16_bf16 v[48:63], a[0:3], v[180:183], v[48:63]\n"
"s_waitcnt vmcnt(18)\n"
"v_mfma_f32_32x32x16_bf16 v[16:31], a[4:7], v[184:187], v[16:31]\n"
"s_add_u32 s70, s70, 4096\n"
"buffer_load_dwordx4 v[96:99], v0, s[36:39], s70 offen offset:0 sc1\n"
"v_mfma_f32_32x32x16_bf16 v[32:47], a[8:11], v[184:187], v[32:47]\n"
"buffer_load_dwordx4 v[100:103], v0, s[36:39], s70 offen offset:1024 sc1\n"
"v_mfma_f32_32x32x16_bf16 v[48:63], a[12:15], v[184:187], v[48:63]\n"
"s_waitcnt vmcnt(19)\n"
"v_mfma_f32_32x32x16_bf16 v[16:31], a[16:19], v[188:191], v[16:31]\n"
"buffer_load_dwordx4 v[104:107], v0, s[36:39], s70 offen offset:2048 sc1\n"
"v_mfma_f32_32x32x16_bf16 v[32:47], a[20:23], v[188:191], v[32:47]\n"
"buffer_load_dwordx4 v[108:111], v0, s[36:39], s70 offen offset:3072 sc1\n"
"v_mfma_f32_32x32x16_bf16 v[48:63], a[24:27], v[188:191], v[48:63]\n"
"s_waitcnt vmcnt(20)\n"
"v_mfma_f32_32x32x16_bf16 v[16:31], a[28:31], v[192:195], v[16:31]\n"
"buffer_load_dwordx4 v[112:115], v0, s[36:39], s71 offen offset:0 sc1\n"
"v_mfma_f32_32x32x16_bf16 v[32:47], a[32:35], v[192:195], v[32:47]\n"
"buffer_load_dwordx4 v[116:119], v0, s[36:39], s72 offen offset:0 sc1\n"
"v_mfma_f32_32x32x16_bf16 v[48:63], a[36:39], v[192:195], v[48:63]\n"
"s_waitcnt vmcnt(21)\n"
"v_mfma_f32_32x32x16_bf16 v[16:31], a[40:43], v[196:199], v[16:31]\n"
"buffer_load_dwordx4 v[120:123], v0, s[36:39], s73 offen offset:0 sc1\n"
"v_mfma_f32_32x32x16_bf16 v[32:47], a[44:47], v[196:199], v[32:47]\n"
"buffer_load_dwordx4 v[124:127], v0, s[36:39], s71 offen offset:1024 sc1\n"
"v_mfma_f32_32x32x16_bf16 v[48:63], a[48:51], v[196:199], v[48:63]\n"
"s_waitcnt vmcnt(22)\n"
"v_mfma_f32_32x32x16_bf16 v[16:31], a[52:55], v[200:203], v[16:31]\n"
"buffer_load_dwordx4 v[128:131], v0, s[36:39], s72 offen offset:1024 sc1\n"
"v_mfma_f32_32x32x16_bf16 v[32:47], a[56:59], v[200:203], v[32:47]\n"
"buffer_load_dwordx4 v[132:135], v0, s[36:39], s73 offen offset:1024 sc1\n"
"v_mfma_f32_32x32x16_bf16 v[48:63], a[60:63], v[200:203], v[48:63]\n"
"s_waitcnt vmcnt(23)\n"
"v_mfma_f32_32x32x16_bf16 v[16:31], a[64:67], v[204:207], v[16:31]\n"
"buffer_load_dwordx4 v[136:139], v0, s[36:39], s71 offen offset:2048 sc1\n"
"v_mfma_f32_32x32x16_bf16 v[32:47], a[68:71], v[204:207], v[32:47]\n"
"buffer_load_dwordx4 v[140:143], v0, s[36:39], s72 offen offset:2048 sc1\n"
"v_mfma_f32_32x32x16_bf16 v[48:63], a[72:75], v[204:207], v[48:63]\n"
"s_waitcnt vmcnt(24)\n"
"v_mfma_f32_32x32x16_bf16 v[16:31], a[76:79], v[208:211], v[16:31]\n"
"buffer_load_dwordx4 v[144:147], v0, s[36:39], s73 offen offset:2048 sc1\n"
"v_mfma_f32_32x32x16_bf16 v[32:47], a[80:83], v[208:211], v[32:47]\n"
"buffer_load_dwordx4 v[148:151], v0, s[36:39], s71 offen offset:3072 sc1\n"
"v_mfma_f32_32x32x16_bf16 v[48:63], a[84:87], v[208:211], v[48:63]\n"
"s_waitcnt vmcnt(25)\n"
"v_mfma_f32_32x32x16_bf16 v[16:31], a[88:91], v[212:215], v[16:31]\n"
"buffer_load_dwordx4 v[152:155], v0, s[36:39], s72 offen offset:3072 sc1\n"
"v_mfma_f32_32x32x16_bf16 v[32:47], a[92:95], v[212:215], v[32:47]\n"
"buffer_load_dwordx4 v[156:159], v0, s[36:39], s73 offen offset:3072 sc1\n"
"v_mfma_f32_32x32x16_bf16 v[48:63], a[96:99], v[212:215], v[48:63]\n"
"s_waitcnt vmcnt(26)\n"
"v_mfma_f32_32x32x16_bf16 v[16:31], a[100:103], v[216:219], v[16:31]\n"
"s_add_u32 s71, s71, 4096\n"
"buffer_load_dwordx4 v[160:163], v0, s[36:39], s71 offen offset:0 sc1\n"
"v_mfma_f32_32x32x16_bf16 v[32:47], a[104:107], v[216:219], v[32:47]\n"
"s_add_u32 s72, s72, 4096\n"
"buffer_load_dwordx4 v[164:167], v0, s[36:39], s72 offen offset:0 sc1\n"
"v_mfma_f32_32x32x16_bf16 v[48:63], a[108:111], v[216:219], v[48:63]\n"
"s_waitcnt vmcnt(25)\n"
"v_mfma_f32_32x32x16_bf16 v[16:31], a[112:115], v[64:67], v[16:31]\n"
"s_add_u32 s73, s73, 4096\n"
"buffer_load_dwordx4 v[168:171], v0, s[36:39], s73 offen offset:0 sc1\n"
"v_mfma_f32_32x32x16_bf16 v[32:47], a[116:119], v[64:67], v[32:47]\n"
"buffer_load_dwordx4 v[172:175], v0, s[36:39], s71 offen offset:1024 sc1\n"
"v_mfma_f32_32x32x16_bf16 v[48:63], a[120:123], v[64:67], v[48:63]\n"
"s_waitcnt vmcnt(26)\n"
"v_mfma_f32_32x32x16_bf16 v[16:31], a[124:127], v[68:71], v[16:31]\n"
"buffer_load_dwordx4 v[176:179], v0, s[36:39], s72 offen offset:1024 sc1\n"
"v_mfma_f32_32x32x16_bf16 v[32:47], a[128:131], v[68:71], v[32:47]\n"
"buffer_load_dwordx4 v[180:183], v0, s[36:39], s73 offen offset:1024 sc1\n"
"v_mfma_f32_32x32x16_bf16 v[48:63], a[132:135], v[68:71], v[48:63]\n"
"s_waitcnt vmcnt(27)\n"
"v_mfma_f32_32x32x16_bf16 v[16:31], a[136:139], v[72:75], v[16:31]\n"
"buffer_load_dwordx4 v[184:187], v0, s[36:39], s71 offen offset:2048 sc1\n"
"v_mfma_f32_32x32x16_bf16 v[32:47], a[140:143], v[72:75], v[32:47]\n"
"buffer_load_dwordx4 v[188:191], v0, s[36:39], s72 offen offset:2048 sc1\n"
"v_mfma_f32_32x32x16_bf16 v[48:63], a[144:147], v[72:75], v[48:63]\n"
"s_waitcnt vmcnt(28)\n"
"v_mfma_f32_32x32x16_bf16 v[16:31], a[148:151], v[76:79], v[16:31]\n"
"buffer_load_dwordx4 v[192:195], v0, s[36:39], s73 offen offset:2048 sc1\n"
"v_mfma_f32_32x32x16_bf16 v[32:47], a[152:155], v[76:79], v[32:47]\n"
"buffer_load_dwordx4 v[196:199], v0, s[36:39], s71 offen offset:3072 sc1\n"
"v_mfma_f32_32x32x16_bf16 v[48:63], a[156:159], v[76:79], v[48:63]\n"
"s_waitcnt vmcnt(29)\n"
"v_mfma_f32_32x32x16_bf16 v[16:31], a[160:163], v[80:83], v[16:31]\n"
"buffer_load_dwordx4 v[200:203], v0, s[36:39], s72 offen offset:3072 sc1\n"
"v_mfma_f32_32x32x16_bf16 v[32:47], a[164:167], v[80:83], v[32:47]\n"
"buffer_load_dwordx4 v[204:207], v0, s[36:39], s73 offen offset:3072 sc1\n"
"v_mfma_f32_32x32x16_bf16 v[48:63], a[168:171], v[80:83], v[48:63]\n"
"s_waitcnt vmcnt(30)\n"
"v_mfma_f32_32x32x16_bf16 v[16:31], a[172:175], v[84:87], v[16:31]\n"
"s_add_u32 s71, s71, 4096\n"
"buffer_load_dwordx4 v[208:211], v0, s[36:39], s71 offen offset:0 sc1\n"
"v_mfma_f32_32x32x16_bf16 v[32:47], a[176:179], v[84:87], v[32:47]\n"
"s_add_u32 s72, s72, 4096\n"
"buffer_load_dwordx4 v[212:215], v0, s[36:39], s72 offen offset:0 sc1\n"
"v_mfma_f32_32x32x16_bf16 v[48:63], a[180:183], v[84:87], v[48:63]\n"
"s_waitcnt vmcnt(31)\n"
"v_mfma_f32_32x32x16_bf16 v[16:31], a[184:187], v[88:91], v[16:31]\n"
"s_add_u32 s73, s73, 4096\n"
"buffer_load_dwordx4 v[216:219], v0, s[36:39], s73 offen offset:0 sc1\n"
"v_mfma_f32_32x32x16_bf16 v[32:47], a[188:191], v[88:91], v[32:47]\n"
"v_mfma_f32_32x32x16_bf16 v[48:63], a[192:195], v[88:91], v[48:63]\n"
"v_mov_b32 v2, -1\n"
"s_mov_b64 exec, 0xff\n"
"v_mov_b32 v2, 0\n"
"buffer_atomic_add v2, v5, s[36:39], 0 offen sc0 sc1\n"
"s_mov_b64 exec, -1\n"
"s_waitcnt vmcnt(32)\n"
"v_mfma_f32_32x32x16_bf16 v[16:31], a[196:199], v[92:95], v[16:31]\n"
"v_mfma_f32_32x32x16_bf16 v[32:47], a[200:203], v[92:95], v[32:47]\n"
"v_mfma_f32_32x32x16_bf16 v[48:63], a[204:207], v[92:95], v[48:63]\n"
"s_waitcnt vmcnt(31)\n"
"v_mfma_f32_32x32x16_bf16 v[16:31], a[208:211], v[96:99], v[16:31]\n"
"v_mfma_f32_32x32x16_bf16 v[32:47], a[212:215], v[96:99], v[32:47]\n"
"v_mfma_f32_32x32x16_bf16 v[48:63], a[216:219], v[96:99], v[48:63]\n"
"s_waitcnt vmcnt(30)\n"
"v_mfma_f32_32x32x16_bf16 v[16:31], a[220:223], v[100:103], v[16:31]\n"
"v_mfma_f32_32x32x16_bf16 v[32:47], a[224:227], v[100:103], v[32:47]\n"
"v_mfma_f32_32x32x16_bf16 v[48:63], a[228:231], v[100:103], v[48:63]\n"
"s_waitcnt vmcnt(29)\n"
"v_mfma_f32_32x32x16_bf16 v[16:31], a[232:235], v[104:107], v[16:31]\n"
"v_mfma_f32_32x32x16_bf16 v[32:47], a[236:239], v[104:107], v[32:47]\n"
"v_mfma_f32_32x32x16_bf16 v[48:63], a[240:243], v[104:107], v[48:63]\n"
"s_waitcnt vmcnt(28)\n"
"v_mfma_f32_32x32x16_bf16 v[16:31], a[244:247], v[108:111], v[16:31]\n"
"v_mfma_f32_32x32x16_bf16 v[32:47], a[248:251], v[108:111], v[32:47]\n"
"v_mfma_f32_32x32x16_bf16 v[48:63], a[252:255], v[108:111], v[48:63]\n"
"s_cmp_gt_u32 s68, 60\n"
"s_cbranch_scc1 O5_%=\n"
"s_waitcnt vmcnt(0)\n"
"v_cmp_gt_u32 vcc, s69, v2\n"
"s_nop 0\n"
"s_cbranch_vccz O7_%=\n"
"s_mov_b32 s76, 131072\n"
"s_mov_b32 exec_lo, 255\n"
"s_mov_b32 exec_hi, 0\n"
"s_mov_b32 s79, 0\n"
"O8_%=:\n"
"s_sleep 1\n"
"v_mov_b32 v2, 0\n"
"buffer_atomic_add v2, v5, s[36:39], s79 offen sc0 sc1\n"
"s_sub_u32 s76, s76, 1\n"
"s_cbranch_scc1 OABORT_%=\n"
"s_waitcnt vmcnt(0)\n"
"v_cmp_gt_u32 vcc, s69, v2\n"
"s_nop 0\n"
"s_cbranch_vccnz O8_%=\n"
"s_mov_b64 exec, -1\n"
"O7_%=:\n"
"buffer_load_dwordx4 v[64:67], v0, s[36:39], s74 offen offset:0 sc1\n"
"buffer_load_dwordx4 v[68:71], v0, s[36:39], s74 offen offset:1024 sc1\n"
"buffer_load_dwordx4 v[72:75], v0, s[36:39], s74 offen offset:2048 sc1\n"
"buffer_load_dwordx4 v[76:79], v0, s[36:39], s74 offen offset:3072 sc1\n"
"s_add_u32 s74, s74, 4096\n"
"buffer_load_dwordx4 v[80:83], v0, s[36:39], s74 offen offset:0 sc1\n"
"buffer_load_dwordx4 v[84:87], v0, s[36:39], s74 offen offset:1024 sc1\n"
"buffer_load_dwordx4 v[88:91], v0, s[36:39], s74 offen offset:2048 sc1\n"
"buffer_load_dwordx4 v[92:95], v0, s[36:39], s74 offen offset:3072 sc1\n"
"s_add_u32 s74, s74, 4096\n"
"buffer_load_dwordx4 v[96:99], v0, s[36:39], s74 offen offset:0 sc1\n"
"buffer_load_dwordx4 v[100:103], v0, s[36:39], s74 offen offset:1024 sc1\n"
"s_waitcnt vmcnt(9)\n"
"v_mfma_f32_32x32x16_bf16 v[16:31], v[112:115], v[64:67], v[16:31]\n"
"buffer_load_dwordx4 v[104:107], v0, s[36:39], s71 offen offset:1024 sc1\n"
"v_mfma_f32_32x32x16_bf16 v[32:47], v[116:119], v[64:67], v[32:47]\n"
"buffer_load_dwordx4 v[108:111], v0, s[36:39], s72 offen offset:1024 sc1\n"
"v_mfma_f32_32x32x16_bf16 v[48:63], v[120:123], v[64:67], v[48:63]\n"
"buffer_load_dwordx4 v[112:115], v0, s[36:39], s73 offen offset:1024 sc1\n"
"s_waitcnt vmcnt(11)\n"
"v_mfma_f32_32x32x16_bf16 v[16:31], v[124:127], v[68:71], v[16:31]\n"
"buffer_load_dwordx4 v[116:119], v0, s[36:39], s74 offen offset:2048 sc1\n"
"v_mfma_f32_32x32x16_bf16 v[32:47], v[128:131], v[68:71], v[32:47]\n"
"buffer_load_dwordx4 v[120:123], v0, s[36:39], s71 offen offset:2048 sc1\n"
"v_mfma_f32_32x32x16_bf16 v[48:63], v[132:135], v[68:71], v[48:63]\n"
"buffer_load_dwordx4 v[64:67], v0, s[36:39], s72 offen offset:2048 sc1\n"
"s_waitcnt vmcnt(13)\n"
"v_mfma_f32_32x32x16_bf16 v[16:31], v[136:139], v[72:75], v[16:31]\n"
"buffer_load_dwordx4 v[124:127], v0, s[36:39], s73 offen offset:2048 sc1\n"
"v_mfma_f32_32x32x16_bf16 v[32:47], v[140:143], v[72:75], v[32:47]\n"
"buffer_load_dwordx4 v[128:131], v0, s[36:39], s74 offen offset:3072 sc1\n"
"v_mfma_f32_32x32x16_bf16 v[48:63], v[144:147], v[72:75], v[48:63]\n"
"buffer_load_dwordx4 v[132:135], v0, s[36:39], s71 offen offset:3072 sc1\n"
"s_waitcnt vmcnt(15)\n"
"v_mfma_f32_32x32x16_bf16 v[16:31], v[148:151], v[76:79], v[16:31]\n"
"buffer_load_dwordx4 v[68:71], v0, s[36:39], s72 offen offset:3072 sc1\n"
"v_mfma_f32_32x32x16_bf16 v[32:47], v[152:155], v[76:79], v[32:47]\n"
"buffer_load_dwordx4 v[136:139], v0, s[36:39], s73 offen offset:3072 sc1\n"
"v_mfma_f32_32x32x16_bf16 v[48:63], v[156:159], v[76:79], v[48:63]\n"
"s_add_u32 s74, s74, 4096\n"
"buffer_load_dwordx4 v[140:143], v0, s[36:39], s74 offen offset:0 sc1\n"
"s_waitcnt vmcnt(17)\n"
"v_mfma_f32_32x32x16_bf16 v[16:31], v[160:163], v[80:83], v[16:31]\n"
"s_add_u32 s71, s71, 4096\n"
"buffer_load_dwordx4 v[144:147], v0, s[36:39], s71 offen offset:0 sc1\n"
"v_mfma_f32_32x32x16_bf16 v[32:47], v[164:167], v[80:83], v[32:47]\n"
"s_add_u32 s72, s72, 4096\n"
"buffer_load_dwordx4 v[72:75], v0, s[36:39], s72 offen offset:0 sc1\n"
"v_mfma_f32_32x32x16_bf16 v[48:63], v[168:171], v[80:83], v[48:63]\n"
"s_add_u32 s73, s73, 4096\n"
"buffer_load_dwordx4 v[148:151], v0, s[36:39], s73 offen offset:0 sc1\n"
"s_waitcnt vmcnt(19)\n"
"v_mfma_f32_32x32x16_bf16 v[16:31], v[172:175], v[84:87], v[16:31]\n"
"buffer_load_dwordx4 v[152:155], v0, s[36:39], s74 offen offset:1024 sc1\n"
"v_mfma_f32_32x32x16_bf16 v[32:47], v[176:179], v[84:87], v[32:47]\n"
"buffer_load_dwordx4 v[156:159], v0, s[36:39], s71 offen offset:1024 sc1\n"
"v_mfma_f32_32x32x16_bf16 v[48:63], v[180:183], v[84:87], v[48:63]\n"
"buffer_load_dwordx4 v[76:79], v0, s[36:39], s72 offen offset:1024 sc1\n"
"s_waitcnt vmcnt(21)\n"
"v_mfma_f32_32x32x16_bf16 v[16:31], v[184:187], v[88:91], v[16:31]\n"
"buffer_load_dwordx4 v[160:163], v0, s[36:39], s73 offen offset:1024 sc1\n"
"v_mfma_f32_32x32x16_bf16 v[32:47], v[188:191], v[88:91], v[32:47]\n"
"buffer_load_dwordx4 v[164:167], v0, s[36:39], s74 offen offset:2048 sc1\n"
"v_mfma_f32_32x32x16_bf16 v[48:63], v[192:195], v[88:91], v[48:63]\n"
"buffer_load_dwordx4 v[168:171], v0, s[36:39], s71 offen offset:2048 sc1\n"
"s_waitcnt vmcnt(23)\n"
"v_mfma_f32_32x32x16_bf16 v[16:31], v[196:199], v[92:95], v[16:31]\n"
"buffer_load_dwordx4 v[80:83], v0, s[36:39], s72 offen offset:2048 sc1\n"
"v_mfma_f32_32x32x16_bf16 v[32:47], v[200:203], v[92:95], v[32:47]\n"
"buffer_load_dwordx4 v[172:175], v0, s[36:39], s73 offen offset:2048 sc1\n"
"v_mfma_f32_32x32x16_bf16 v[48:63], v[204:207], v[92:95], v[48:63]\n"
"buffer_load_dwordx4 v[176:179], v0, s[36:39], s74 offen offset:3072 sc1\n"
"s_waitcnt vmcnt(25)\n"
"v_mfma_f32_32x32x16_bf16 v[16:31], v[208:211], v[96:99], v[16:31]\n"
"buffer_load_dwordx4 v[180:183], v0, s[36:39], s71 offen offset:3072 sc1\n"
"v_mfma_f32_32x32x16_bf16 v[32:47], v[212:215], v[96:99], v[32:47]\n"
"buffer_load_dwordx4 v[84:87], v0, s[36:39], s72 offen offset:3072 sc1\n"
"v_mfma_f32_32x32x16_bf16 v[48:63], v[216:219], v[96:99], v[48:63]\n"
"buffer_load_dwordx4 v[184:187], v0, s[36:39], s73 offen offset:3072 sc1\n"
"s_waitcnt vmcnt(27)\n"
"s_waitcnt vmcnt(26)\n"
"v_mfma_f32_32x32x16_bf16 v[16:31], v[104:107], v[100:103], v[16:31]\n"
"buffer_load_dwordx4 v[188:191], v0, s[36:39], s75 offen offset:0 sc1\n"
"s_waitcnt vmcnt(26)\n"
"v_mfma_f32_32x32x16_bf16 v[32:47], v[108:111], v[100:103], v[32:47]\n"
"buffer_load_dwordx4 v[192:195], v6, s[36:39], s80 offen offset:0 sc1\n"
"s_waitcnt vmcnt(26)\n"
"v_mfma_f32_32x32x16_bf16 v[48:63], v[112:115], v[100:103], v[48:63]\n"
"buffer_load_dwordx4 v[88:91], v6, s[36:39], s81 offen offset:0 sc1\n"
"s_waitcnt vmcnt(26)\n"
"s_waitcnt vmcnt(25)\n"
"v_mfma_f32_32x32x16_bf16 v[16:31], v[120:123], v[116:119], v[16:31]\n"
"buffer_load_dwordx4 v[196:199], v6, s[36:39], s82 offen offset:0 sc1\n"
"s_waitcnt vmcnt(25)\n"
"v_mfma_f32_32x32x16_bf16 v[32:47], v[64:67], v[116:119], v[32:47]\n"
"buffer_load_dwordx4 v[200:203], v0, s[36:39], s75 offen offset:1024 sc1\n"
"s_waitcnt vmcnt(25)\n"
"v_mfma_f32_32x32x16_bf16 v[48:63], v[124:127], v[116:119], v[48:63]\n"
"buffer_load_dwordx4 v[204:207], v6, s[36:39], s80 offen offset:512 sc1\n"
"s_waitcnt vmcnt(25)\n"
"s_waitcnt vmcnt(24)\n"
"v_mfma_f32_32x32x16_bf16 v[16:31], v[132:135], v[128:131], v[16:31]\n"
"buffer_load_dwordx4 v[92:95], v6, s[36:39], s81 offen offset:512 sc1\n"
"s_waitcnt vmcnt(24)\n"
"v_mfma_f32_32x32x16_bf16 v[32:47], v[68:71], v[128:131], v[32:47]\n"
"buffer_load_dwordx4 v[208:211], v6, s[36:39], s82 offen offset:512 sc1\n"
"s_waitcnt vmcnt(24)\n"
"v_mfma_f32_32x32x16_bf16 v[48:63], v[136:139], v[128:131], v[48:63]\n"
"buffer_load_dwordx4 v[212:215], v0, s[36:39], s75 offen offset:2048 sc1\n"
"s_waitcnt vmcnt(24)\n"
"s_waitcnt vmcnt(23)\n"
"v_mfma_f32_32x32x16_bf16 v[16:31], v[144:147], v[140:143], v[16:31]\n"
"buffer_load_dwordx4 v[216:219], v6, s[36:39], s80 offen offset:1024 sc1\n"
"s_waitcnt vmcnt(23)\n"
"v_mfma_f32_32x32x16_bf16 v[32:47], v[72:75], v[140:143], v[32:47]\n"
"buffer_load_dwordx4 v[96:99], v6, s[36:39], s81 offen offset:1024 sc1\n"
"s_waitcnt vmcnt(23)\n"
"v_mfma_f32_32x32x16_bf16 v[48:63], v[148:151], v[140:143], v[48:63]\n"
"buffer_load_dwordx4 v[104:107], v6, s[36:39], s82 offen offset:1024 sc1\n"
"s_waitcnt vmcnt(23)\n"
"s_waitcnt vmcnt(22)\n"
"v_mfma_f32_32x32x16_bf16 v[16:31], v[156:159], v[152:155], v[16:31]\n"
"buffer_load_dwordx4 v[108:111], v0, s[36:39], s75 offen offset:3072 sc1\n"
"s_waitcnt vmcnt(22)\n"
"v_mfma_f32_32x32x16_bf16 v[32:47], v[76:79], v[152:155], v[32:47]\n"
"buffer_load_dwordx4 v[112:115], v6, s[36:39], s80 offen offset:1536 sc1\n"
"s_waitcnt vmcnt(22)\n"
"v_mfma_f32_32x32x16_bf16 v[48:63], v[160:163], v[152:155], v[48:63]\n"
"buffer_load_dwordx4 v[100:103], v6, s[36:39], s81 offen offset:1536 sc1\n"
"s_waitcnt vmcnt(22)\n"
"s_waitcnt vmcnt(21)\n"
"v_mfma_f32_32x32x16_bf16 v[16:31], v[168:171], v[164:167], v[16:31]\n"
"buffer_load_dwordx4 v[120:123], v6, s[36:39], s82 offen offset:1536 sc1\n"
"s_waitcnt vmcnt(21)\n"
"v_mfma_f32_32x32x16_bf16 v[32:47], v[80:83], v[164:167], v[32:47]\n"
"s_waitcnt vmcnt(20)\n"
"v_mfma_f32_32x32x16_bf16 v[48:63], v[172:175], v[164:167], v[48:63]\n"
"s_waitcnt vmcnt(19)\n"
"s_waitcnt vmcnt(18)\n"
"v_mfma_f32_32x32x16_bf16 v[16:31], v[180:183], v[176:179], v[16:31]\n"
"s_waitcnt vmcnt(17)\n"
"v_mfma_f32_32x32x16_bf16 v[32:47], v[84:87], v[176:179], v[32:47]\n"
"s_waitcnt vmcnt(16)\n"
"v_mfma_f32_32x32x16_bf16 v[48:63], v[184:187], v[176:179], v[48:63]\n"
"buffer_load_dwordx4 v[12:15], v7, s[44:47], s77 offen\n"
"buffer_load_dwordx4 v[220:223], v3, s[44:47], s77 offen\n"
"s_waitcnt vmcnt(17)\n"
"s_waitcnt vmcnt(16)\n"
"v_mfma_f32_32x32x16_bf16 v[16:31], v[192:195], v[188:191], v[16:31]\n"
"s_waitcnt vmcnt(15)\n"
"v_mfma_f32_32x32x16_bf16 v[32:47], v[88:91], v[188:191], v[32:47]\n"
"s_waitcnt vmcnt(14)\n"
"v_mfma_f32_32x32x16_bf16 v[48:63], v[196:199], v[188:191], v[48:63]\n"
"s_waitcnt vmcnt(13)\n"
"s_waitcnt vmcnt(12)\n"
"v_mfma_f32_32x32x16_bf16 v[16:31], v[204:207], v[200:203], v[16:31]\n"
"s_waitcnt vmcnt(11)\n"
"v_mfma_f32_32x32x16_bf16 v[32:47], v[92:95], v[200:203], v[32:47]\n"
"s_waitcnt vmcnt(10)\n"
"v_mfma_f32_32x32x16_bf16 v[48:63], v[208:211], v[200:203], v[48:63]\n"
"s_waitcnt vmcnt(9)\n"
"s_waitcnt vmcnt(8)\n"
"v_mfma_f32_32x32x16_bf16 v[16:31], v[216:219], v[212:215], v[16:31]\n"
"s_waitcnt vmcnt(7)\n"
"v_mfma_f32_32x32x16_bf16 v[32:47], v[96:99], v[212:215], v[32:47]\n"
"s_waitcnt vmcnt(6)\n"
"v_mfma_f32_32x32x16_bf16 v[48:63], v[104:107], v[212:215], v[48:63]\n"
"s_waitcnt vmcnt(5)\n"
"s_waitcnt vmcnt(4)\n"
"v_mfma_f32_32x32x16_bf16 v[16:31], v[112:115], v[108:111], v[16:31]\n"
"s_waitcnt vmcnt(3)\n"
"v_mfma_f32_32x32x16_bf16 v[32:47], v[100:103], v[108:111], v[32:47]\n"
"s_waitcnt vmcnt(2)\n"
"v_mfma_f32_32x32x16_bf16 v[48:63], v[120:123], v[108:111], v[48:63]\n"
"s_barrier\n"
"s_nop 6\n"
"ds_write_b128 v8, v[16:19] offset:0\n"
"ds_write_b128 v8, v[20:23] offset:1088\n"
"ds_write_b128 v8, v[24:27] offset:2176\n"
"ds_write_b128 v8, v[28:31] offset:3264\n"
"ds_write_b128 v8, v[32:35] offset:4352\n"
"ds_write_b128 v8, v[36:39] offset:5440\n"
"ds_write_b128 v8, v[40:43] offset:6528\n"
"ds_write_b128 v8, v[44:47] offset:7616\n"
"ds_write_b128 v8, v[48:51] offset:8704\n"
"ds_write_b128 v8, v[52:55] offset:9792\n"
"ds_write_b128 v8, v[56:59] offset:10880\n"
"ds_write_b128 v8, v[60:63] offset:11968\n"
"s_branch O6_%=\n"
"O5_%=:\n"
"s_nop 7\n"
"s_waitcnt vmcnt(0)\n"
"buffer_load_dwordx4 v[64:67], v0, s[36:39], s71 offen offset:1024 sc1\n"
"buffer_load_dwordx4 v[68:71], v0, s[36:39], s72 offen offset:1024 sc1\n"
"buffer_load_dwordx4 v[72:75], v0, s[36:39], s73 offen offset:1024 sc1\n"
"buffer_load_dwordx4 v[76:79], v0, s[36:39], s71 offen offset:2048 sc1\n"
"buffer_load_dwordx4 v[80:83], v0, s[36:39], s72 offen offset:2048 sc1\n"
"buffer_load_dwordx4 v[84:87], v0, s[36:39], s73 offen offset:2048 sc1\n"
"buffer_load_dwordx4 v[88:91], v0, s[36:39], s71 offen offset:3072 sc1\n"
"buffer_load_dwordx4 v[92:95], v0, s[36:39], s72 offen offset:3072 sc1\n"
"buffer_load_dwordx4 v[96:99], v0, s[36:39], s73 offen offset:3072 sc1\n"
"s_add_u32 s71, s71, 4096\n"
"buffer_load_dwordx4 v[100:103], v0, s[36:39], s71 offen offset:0 sc1\n"
"s_add_u32 s72, s72, 4096\n"
"buffer_load_dwordx4 v[104:107], v0, s[36:39], s72 offen offset:0 sc1\n"
"s_add_u32 s73, s73, 4096\n"
"buffer_load_dwordx4 v[108:111], v0, s[36:39], s73 offen offset:0 sc1\n"
"buffer_load_dwordx4 v[224:227], v0, s[36:39], s71 offen offset:1024 sc1\n"
"buffer_load_dwordx4 v[228:231], v0, s[36:39], s72 offen offset:1024 sc1\n"
"buffer_load_dwordx4 v[232:235], v0, s[36:39], s73 offen offset:1024 sc1\n"
"buffer_load_dwordx4 v[236:239], v0, s[36:39], s71 offen offset:2048 sc1\n"
"buffer_load_dwordx4 v[240:243], v0, s[36:39], s72 offen offset:2048 sc1\n"
"buffer_load_dwordx4 v[244:247], v0, s[36:39], s73 offen offset:2048 sc1\n"
"buffer_load_dwordx4 v[248:251], v0, s[36:39], s71 offen offset:3072 sc1\n"
"buffer_load_dwordx4 v[252:255], v0, s[36:39], s72 offen offset:3072 sc1\n"
"buffer_load_dwordx4 a[0:3], v0, s[36:39], s73 offen offset:3072 sc1\n"
"buffer_load_dwordx4 v[12:15], v7, s[44:47], s77 offen\n"
"buffer_load_dwordx4 v[220:223], v3, s[44:47], s77 offen\n"
"s_mov_b32 s70, 58\n"
"s_mov_b32 s76, 131072\n"
"s_mov_b32 exec_lo, 1\n"
"s_mov_b32 exec_hi, 0\n"
"s_mov_b32 s79, 0\n"
"O9_%=:\n"
"s_sleep 60\n"
"v_mov_b32 v2, 0\n"
"buffer_atomic_add v2, v5, s[36:39], s79 offen sc0 sc1\n"
"s_sub_u32 s76, s76, 1\n"
"s_cbranch_scc1 OABORT_%=\n"
"s_waitcnt vmcnt(0)\n"
"v_cmp_gt_u32 vcc, s70, v2\n"
"s_nop 0\n"
"s_cbranch_vccnz O9_%=\n"
"s_mov_b64 exec, -1\n"
"s_mov_b32 s76, 131072\n"
"s_mov_b32 exec_lo, 255\n"
"s_mov_b32 exec_hi, 0\n"
"s_mov_b32 s79, 0\n"
"O10_%=:\n"
"s_sleep 1\n"
"v_mov_b32 v2, 0\n"
"buffer_atomic_add v2, v11, s[36:39], s79 offen sc0 sc1\n"
"s_sub_u32 s76, s76, 1\n"
"s_cbranch_scc1 OABORT_%=\n"
"s_waitcnt vmcnt(0)\n"
"v_cmp_gt_u32 vcc, s69, v2\n"
"s_nop 0\n"
"s_cbranch_vccnz O10_%=\n"
"s_mov_b64 exec, -1\n"
"buffer_load_dwordx4 a[4:7], v6, s[36:39], s80 offen offset:0 sc1\n"
"buffer_load_dwordx4 a[8:11], v6, s[36:39], s81 offen offset:0 sc1\n"
"buffer_load_dwordx4 a[12:15], v6, s[36:39], s82 offen offset:0 sc1\n"
"buffer_load_dwordx4 a[16:19], v6, s[36:39], s80 offen offset:512 sc1\n"
"buffer_load_dwordx4 a[20:23], v6, s[36:39], s81 offen offset:512 sc1\n"
"buffer_load_dwordx4 a[24:27], v6, s[36:39], s82 offen offset:512 sc1\n"
"buffer_load_dwordx4 a[28:31], v6, s[36:39], s80 offen offset:1024 sc1\n"
"buffer_load_dwordx4 a[32:35], v6, s[36:39], s81 offen offset:1024 sc1\n"
"buffer_load_dwordx4 a[36:39], v6, s[36:39], s82 offen offset:1024 sc1\n"
"buffer_load_dwordx4 a[40:43], v6, s[36:39], s80 offen offset:1536 sc1\n"
"buffer_load_dwordx4 a[44:47], v6, s[36:39], s81 offen offset:1536 sc1\n"
"buffer_load_dwordx4 a[48:51], v6, s[36:39], s82 offen offset:1536 sc1\n"
"s_barrier\n"
"s_mov_b32 s76, 131072\n"
"s_mov_b32 exec_lo, 255\n"
"s_mov_b32 exec_hi, 0\n"
"s_mov_b32 s79, 0\n"
"O11_%=:\n"
"s_sleep 1\n"
"v_mov_b32 v2, 0\n"
"buffer_atomic_add v2, v5, s[36:39], s79 offen sc0 sc1\n"
"s_sub_u32 s76, s76, 1\n"
"s_cbranch_scc1 OABORT_%=\n"
"s_waitcnt vmcnt(0)\n"
"v_cmp_gt_u32 vcc, s69, v2\n"
"s_nop 0\n"
"s_cbranch_vccnz O11_%=\n"
"s_mov_b64 exec, -1\n"
"buffer_load_dwordx4 a[52:55], v0, s[36:39], s74 offen offset:0 sc1\n"
"buffer_load_dwordx4 a[56:59], v0, s[36:39], s74 offen offset:1024 sc1\n"
"buffer_load_dwordx4 a[60:63], v0, s[36:39], s74 offen offset:2048 sc1\n"
"buffer_load_dwordx4 a[64:67], v0, s[36:39], s74 offen offset:3072 sc1\n"
"s_add_u32 s74, s74, 4096\n"
"buffer_load_dwordx4 a[68:71], v0, s[36:39], s74 offen offset:0 sc1\n"
"buffer_load_dwordx4 a[72:75], v0, s[36:39], s74 offen offset:1024 sc1\n"
"buffer_load_dwordx4 a[76:79], v0, s[36:39], s74 offen offset:2048 sc1\n"
"buffer_load_dwordx4 a[80:83], v0, s[36:39], s74 offen offset:3072 sc1\n"
"s_waitcnt vmcnt(7)\n"
"v_mfma_f32_32x32x16_bf16 v[16:31], v[112:115], a[52:55], v[16:31]\n"
"s_add_u32 s74, s74, 4096\n"
"buffer_load_dwordx4 a[84:87], v0, s[36:39], s74 offen offset:0 sc1\n"
"v_mfma_f32_32x32x16_bf16 v[32:47], v[116:119], a[52:55], v[32:47]\n"
"s_waitcnt vmcnt(7)\n"
"v_mfma_f32_32x32x16_bf16 v[16:31], v[124:127], a[56:59], v[16:31]\n"
"buffer_load_dwordx4 a[88:91], v0, s[36:39], s74 offen offset:1024 sc1\n"
"v_mfma_f32_32x32x16_bf16 v[32:47], v[128:131], a[56:59], v[32:47]\n"
"s_waitcnt vmcnt(7)\n"
"v_mfma_f32_32x32x16_bf16 v[16:31], v[136:139], a[60:63], v[16:31]\n"
"buffer_load_dwordx4 a[92:95], v0, s[36:39], s74 offen offset:2048 sc1\n"
"v_mfma_f32_32x32x16_bf16 v[32:47], v[140:143], a[60:63], v[32:47]\n"
"s_waitcnt vmcnt(7)\n"
"v_mfma_f32_32x32x16_bf16 v[16:31], v[148:151], a[64:67], v[16:31]\n"
"buffer_load_dwordx4 a[96:99], v0, s[36:39], s74 offen offset:3072 sc1\n"
"v_mfma_f32_32x32x16_bf16 v[32:47], v[152:155], a[64:67], v[32:47]\n"
"s_waitcnt vmcnt(7)\n"
"v_mfma_f32_32x32x16_bf16 v[16:31], v[160:163], a[68:71], v[16:31]\n"
"s_add_u32 s74, s74, 4096\n"
"buffer_load_dwordx4 a[100:103], v0, s[36:39], s74 offen offset:0 sc1\n"
"v_mfma_f32_32x32x16_bf16 v[32:47], v[164:167], a[68:71], v[32:47]\n"
"s_waitcnt vmcnt(7)\n"
"v_mfma_f32_32x32x16_bf16 v[16:31], v[172:175], a[72:75], v[16:31]\n"
"buffer_load_dwordx4 a[104:107], v0, s[36:39], s74 offen offset:1024 sc1\n"
"v_mfma_f32_32x32x16_bf16 v[32:47], v[176:179], a[72:75], v[32:47]\n"
"s_waitcnt vmcnt(7)\n"
"v_mfma_f32_32x32x16_bf16 v[16:31], v[184:187], a[76:79], v[16:31]\n"
"buffer_load_dwordx4 a[108:111], v0, s[36:39], s74 offen offset:2048 sc1\n"
"v_mfma_f32_32x32x16_bf16 v[32:47], v[188:191], a[76:79], v[32:47]\n"
"s_waitcnt vmcnt(7)\n"
"v_mfma_f32_32x32x16_bf16 v[16:31], v[196:199], a[80:83], v[16:31]\n"
"buffer_load_dwordx4 a[112:115], v0, s[36:39], s74 offen offset:3072 sc1\n"
"v_mfma_f32_32x32x16_bf16 v[32:47], v[200:203], a[80:83], v[32:47]\n"
"s_waitcnt vmcnt(7)\n"
"v_mfma_f32_32x32x16_bf16 v[16:31], v[208:211], a[84:87], v[16:31]\n"
"buffer_load_dwordx4 a[116:119], v0, s[36:39], s75 offen offset:0 sc1\n"
"v_mfma_f32_32x32x16_bf16 v[32:47], v[212:215], a[84:87], v[32:47]\n"
"s_waitcnt vmcnt(7)\n"
"v_mfma_f32_32x32x16_bf16 v[16:31], v[64:67], a[88:91], v[16:31]\n"
"buffer_load_dwordx4 a[120:123], v0, s[36:39], s75 offen offset:1024 sc1\n"
"v_mfma_f32_32x32x16_bf16 v[32:47], v[68:71], a[88:91], v[32:47]\n"
"s_waitcnt vmcnt(7)\n"
"v_mfma_f32_32x32x16_bf16 v[16:31], v[76:79], a[92:95], v[16:31]\n"
"buffer_load_dwordx4 a[124:127], v0, s[36:39], s75 offen offset:2048 sc1\n"
"v_mfma_f32_32x32x16_bf16 v[32:47], v[80:83], a[92:95], v[32:47]\n"
"s_waitcnt vmcnt(7)\n"
"v_mfma_f32_32x32x16_bf16 v[16:31], v[88:91], a[96:99], v[16:31]\n"
"buffer_load_dwordx4 a[128:131], v0, s[36:39], s75 offen offset:3072 sc1\n"
"v_mfma_f32_32x32x16_bf16 v[32:47], v[92:95], a[96:99], v[32:47]\n"
"s_waitcnt vmcnt(7)\n"
"v_mfma_f32_32x32x16_bf16 v[16:31], v[100:103], a[100:103], v[16:31]\n"
"v_mfma_f32_32x32x16_bf16 v[32:47], v[104:107], a[100:103], v[32:47]\n"
"s_waitcnt vmcnt(6)\n"
"v_mfma_f32_32x32x16_bf16 v[16:31], v[224:227], a[104:107], v[16:31]\n"
"v_mfma_f32_32x32x16_bf16 v[32:47], v[228:231], a[104:107], v[32:47]\n"
"s_waitcnt vmcnt(5)\n"
"v_mfma_f32_32x32x16_bf16 v[16:31], v[236:239], a[108:111], v[16:31]\n"
"v_mfma_f32_32x32x16_bf16 v[32:47], v[240:243], a[108:111], v[32:47]\n"
"s_waitcnt vmcnt(4)\n"
"v_mfma_f32_32x32x16_bf16 v[16:31], v[248:251], a[112:115], v[16:31]\n"
"v_mfma_f32_32x32x16_bf16 v[32:47], v[252:255], a[112:115], v[32:47]\n"
"s_waitcnt vmcnt(3)\n"
"v_mfma_f32_32x32x16_bf16 v[16:31], a[4:7], a[116:119], v[16:31]\n"
"v_mfma_f32_32x32x16_bf16 v[32:47], a[8:11], a[116:119], v[32:47]\n"
"s_waitcnt vmcnt(2)\n"
"v_mfma_f32_32x32x16_bf16 v[16:31], a[16:19], a[120:123], v[16:31]\n"
"v_mfma_f32_32x32x16_bf16 v[32:47], a[20:23], a[120:123], v[32:47]\n"
"s_waitcnt vmcnt(1)\n"
"v_mfma_f32_32x32x16_bf16 v[16:31], a[28:31], a[124:127], v[16:31]\n"
"v_mfma_f32_32x32x16_bf16 v[32:47], a[32:35], a[124:127], v[32:47]\n"
"s_waitcnt vmcnt(0)\n"
"v_mfma_f32_32x32x16_bf16 v[16:31], a[40:43], a[128:131], v[16:31]\n"
"v_mfma_f32_32x32x16_bf16 v[32:47], a[44:47], a[128:131], v[32:47]\n"
"v_mfma_f32_32x32x16_bf16 v[48:63], v[120:123], a[52:55], v[48:63]\n"
"v_mfma_f32_32x32x16_bf16 v[48:63], v[132:135], a[56:59], v[48:63]\n"
"v_mfma_f32_32x32x16_bf16 v[48:63], v[144:147], a[60:63], v[48:63]\n"
"v_mfma_f32_32x32x16_bf16 v[48:63], v[156:159], a[64:67], v[48:63]\n"
"v_mfma_f32_32x32x16_bf16 v[48:63], v[168:171], a[68:71], v[48:63]\n"
"s_nop 5\n"
"ds_write_b128 v8, v[16:19] offset:0\n"
"v_mfma_f32_32x32x16_bf16 v[48:63], v[180:183], a[72:75], v[48:63]\n"
"v_mfma_f32_32x32x16_bf16 v[48:63], v[192:195], a[76:79], v[48:63]\n"
"ds_write_b128 v8, v[20:23] offset:1088\n"
"v_mfma_f32_32x32x16_bf16 v[48:63], v[204:207], a[80:83], v[48:63]\n"
"v_mfma_f32_32x32x16_bf16 v[48:63], v[216:219], a[84:87], v[48:63]\n"
"ds_write_b128 v8, v[24:27] offset:2176\n"
"v_mfma_f32_32x32x16_bf16 v[48:63], v[72:75], a[88:91], v[48:63]\n"
"v_mfma_f32_32x32x16_bf16 v[48:63], v[84:87], a[92:95], v[48:63]\n"
"ds_write_b128 v8, v[28:31] offset:3264\n"
"v_mfma_f32_32x32x16_bf16 v[48:63], v[96:99], a[96:99], v[48:63]\n"
"v_mfma_f32_32x32x16_bf16 v[48:63], v[108:111], a[100:103], v[48:63]\n"
"ds_write_b128 v8, v[32:35] offset:4352\n"
"v_mfma_f32_32x32x16_bf16 v[48:63], v[232:235], a[104:107], v[48:63]\n"
"v_mfma_f32_32x32x16_bf16 v[48:63], v[244:247], a[108:111], v[48:63]\n"
"ds_write_b128 v8, v[36:39] offset:5440\n"
"v_mfma_f32_32x32x16_bf16 v[48:63], a[0:3], a[112:115], v[48:63]\n"
"v_mfma_f32_32x32x16_bf16 v[48:63], a[12:15], a[116:119], v[48:63]\n"
"ds_write_b128 v8, v[40:43] offset:6528\n"
"v_mfma_f32_32x32x16_bf16 v[48:63], a[24:27], a[120:123], v[48:63]\n"
"v_mfma_f32_32x32x16_bf16 v[48:63], a[36:39], a[124:127], v[48:63]\n"
"ds_write_b128 v8, v[44:47] offset:7616\n"
"v_mfma_f32_32x32x16_bf16 v[48:63], a[48:51], a[128:131], v[48:63]\n"
"s_nop 11\n"
"ds_write_b128 v8, v[48:51] offset:8704\n"
"ds_write_b128 v8, v[52:55] offset:9792\n"
"ds_write_b128 v8, v[56:59] offset:10880\n"
"ds_write_b128 v8, v[60:63] offset:11968\n"
"O6_%=:\n"
"s_add_u32 s71, s68, s56\n"
"s_add_u32 s72, s71, 1\n"
"v_cmp_gt_u32 vcc, s72, v1\n"
"s_and_b32 s71, s71, 63\n"
"s_mul_i32 s71, s71, 98304\n"
"s_add_u32 s71, s71, s59\n"
"s_cmp_eq_u64 vcc, 0\n"
"s_cselect_b32 s71, s71, s38\n"
"buffer_load_dwordx4 v[172:175], v0, s[36:39], s71 offen offset:0 sc1\n"
"buffer_load_dwordx4 v[176:179], v0, s[36:39], s71 offen offset:1024 sc1\n"
"buffer_load_dwordx4 v[180:183], v0, s[36:39], s71 offen offset:2048 sc1\n"
"buffer_load_dwordx4 v[184:187], v0, s[36:39], s71 offen offset:3072 sc1\n"
"s_add_u32 s71, s71, 4096\n"
"buffer_load_dwordx4 v[188:191], v0, s[36:39], s71 offen offset:0 sc1\n"
"buffer_load_dwordx4 v[192:195], v0, s[36:39], s71 offen offset:1024 sc1\n"
"buffer_load_dwordx4 v[196:199], v0, s[36:39], s71 offen offset:2048 sc1\n"
"buffer_load_dwordx4 v[200:203], v0, s[36:39], s71 offen offset:3072 sc1\n"
"s_add_u32 s71, s71, 4096\n"
"buffer_load_dwordx4 v[204:207], v0, s[36:39], s71 offen offset:0 sc1\n"
"buffer_load_dwordx4 v[208:211], v0, s[36:39], s71 offen offset:1024 sc1\n"
"buffer_load_dwordx4 v[212:215], v0, s[36:39], s71 offen offset:2048 sc1\n"
"buffer_load_dwordx4 v[216:219], v0, s[36:39], s71 offen offset:3072 sc1\n"
"s_waitcnt lgkmcnt(0)\n"
"s_barrier\n"
"ds_read_b128 v[64:67], v9 offset:0\n"
"ds_read_b128 v[68:71], v9 offset:512\n"
"ds_read_b128 v[72:75], v9 offset:13056\n"
"ds_read_b128 v[76:79], v9 offset:13568\n"
"ds_read_b128 v[80:83], v9 offset:26112\n"
"ds_read_b128 v[84:87], v9 offset:26624\n"
"ds_read_b128 v[88:91], v9 offset:39168\n"
"ds_read_b128 v[92:95], v9 offset:39680\n"
"ds_read_b128 v[96:99], v10 offset:0\n"
"ds_read_b128 v[100:103], v10 offset:512\n"
"ds_read_b128 v[104:107], v10 offset:13056\n"
"ds_read_b128 v[108:111], v10 offset:13568\n"
"ds_read_b128 v[112:115], v10 offset:26112\n"
"ds_read_b128 v[116:119], v10 offset:26624\n"
"ds_read_b128 v[120:123], v10 offset:39168\n"
"ds_read_b128 v[124:127], v10 offset:39680\n"
"s_waitcnt vmcnt(12)\n"
"v_lshlrev_b32 v16, 16, v12\n"
"v_and_b32 v17, 0xffff0000, v12\n"
"v_lshlrev_b32 v18, 16, v13\n"
"v_and_b32 v19, 0xffff0000, v13\n"
"v_lshlrev_b32 v20, 16, v14\n"
"v_and_b32 v21, 0xffff0000, v14\n"
"v_lshlrev_b32 v22, 16, v15\n"
"v_and_b32 v23, 0xffff0000, v15\n"
"s_waitcnt lgkmcnt(8)\n"
"v_add_f32 v16, v16, v64\n"
"v_add_f32 v17, v17, v65\n"
"v_add_f32 v18, v18, v66\n"
"v_add_f32 v19, v19, v67\n"
"v_add_f32 v20, v20, v68\n"
"v_add_f32 v21, v21, v69\n"
"v_add_f32 v22, v22, v70\n"
"v_add_f32 v23, v23, v71\n"
"v_add_f32 v16, v16, v72\n"
"v_add_f32 v17, v17, v73\n"
"v_add_f32 v18, v18, v74\n"
"v_add_f32 v19, v19, v75\n"
"v_add_f32 v20, v20, v76\n"
"v_add_f32 v21, v21, v77\n"
"v_add_f32 v22, v22, v78\n"
"v_add_f32 v23, v23, v79\n"
"v_add_f32 v16, v16, v80\n"
"v_add_f32 v17, v17, v81\n"
"v_add_f32 v18, v18, v82\n"
"v_add_f32 v19, v19, v83\n"
"v_add_f32 v20, v20, v84\n"
"v_add_f32 v21, v21, v85\n"
"v_add_f32 v22, v22, v86\n"
"v_add_f32 v23, v23, v87\n"
"v_add_f32 v16, v16, v88\n"
"v_add_f32 v17, v17, v89\n"
"v_add_f32 v18, v18, v90\n"
"v_add_f32 v19, v19, v91\n"
"v_add_f32 v20, v20, v92\n"
"v_add_f32 v21, v21, v93\n"
"v_add_f32 v22, v22, v94\n"
"v_add_f32 v23, v23, v95\n"
"v_cvt_pk_bf16_f32 v24, v16, v17\n"
"v_cvt_pk_bf16_f32 v25, v18, v19\n"
"v_cvt_pk_bf16_f32 v26, v20, v21\n"
"v_cvt_pk_bf16_f32 v27, v22, v23\n"
"buffer_store_dwordx4 v[24:27], v7, s[48:51], s77 offen\n"
"v_lshlrev_b32 v32, 16, v220\n"
"v_and_b32 v33, 0xffff0000, v220\n"
"v_lshlrev_b32 v34, 16, v221\n"
"v_and_b32 v35, 0xffff0000, v221\n"
"v_lshlrev_b32 v36, 16, v222\n"
"v_and_b32 v37, 0xffff0000, v222\n"
"v_lshlrev_b32 v38, 16, v223\n"
"v_and_b32 v39, 0xffff0000, v223\n"
"s_waitcnt lgkmcnt(0)\n"
"v_add_f32 v32, v32, v96\n"
"v_add_f32 v33, v33, v97\n"
"v_add_f32 v34, v34, v98\n"
"v_add_f32 v35, v35, v99\n"
"v_add_f32 v36, v36, v100\n"
"v_add_f32 v37, v37, v101\n"
"v_add_f32 v38, v38, v102\n"
"v_add_f32 v39, v39, v103\n"
"v_add_f32 v32, v32, v104\n"
"v_add_f32 v33, v33, v105\n"
"v_add_f32 v34, v34, v106\n"
"v_add_f32 v35, v35, v107\n"
"v_add_f32 v36, v36, v108\n"
"v_add_f32 v37, v37, v109\n"
"v_add_f32 v38, v38, v110\n"
"v_add_f32 v39, v39, v111\n"
"v_add_f32 v32, v32, v112\n"
"v_add_f32 v33, v33, v113\n"
"v_add_f32 v34, v34, v114\n"
"v_add_f32 v35, v35, v115\n"
"v_add_f32 v36, v36, v116\n"
"v_add_f32 v37, v37, v117\n"
"v_add_f32 v38, v38, v118\n"
"v_add_f32 v39, v39, v119\n"
"v_add_f32 v32, v32, v120\n"
"v_add_f32 v33, v33, v121\n"
"v_add_f32 v34, v34, v122\n"
"v_add_f32 v35, v35, v123\n"
"v_add_f32 v36, v36, v124\n"
"v_add_f32 v37, v37, v125\n"
"v_add_f32 v38, v38, v126\n"
"v_add_f32 v39, v39, v127\n"
"v_cvt_pk_bf16_f32 v40, v32, v33\n"
"v_cvt_pk_bf16_f32 v41, v34, v35\n"
"v_cvt_pk_bf16_f32 v42, v36, v37\n"
"v_cvt_pk_bf16_f32 v43, v38, v39\n"
"buffer_store_dwordx4 v[40:43], v3, s[48:51], s77 offen\n"
"s_add_u32 s68, s68, s56\n"
"s_cmp_lt_u32 s68, 61\n"
"s_cbranch_scc1 O1_%=\n"
"s_endpgm\n"
"OABORT_%=:\n"
"s_mov_b64 exec, -1\n"
"s_nop 7\n"
"s_nop 7\n"
"v_mov_b32 v0, 46268544\n"
"v_mov_b32 v1, 1\n"
"buffer_store_dword v1, v0, s[36:39], 0 offen sc1\n"
"s_endpgm\n"
"TGO_%=:\n"
"s_nop 4\n"
"s_add_u32 m0, s53, 0\n"
"s_add_u32 s72, s52, 0\n"
"buffer_load_dwordx4 v1, s[40:43], s72 offen lds\n"
"s_add_u32 m0, s53, 1024\n"
"s_add_u32 s72, s52, 20480\n"
"buffer_load_dwordx4 v1, s[40:43], s72 offen lds\n"
"s_add_u32 m0, s53, 2048\n"
"s_add_u32 s72, s52, 40960\n"
"buffer_load_dwordx4 v1, s[40:43], s72 offen lds\n"
"s_add_u32 m0, s53, 3072\n"
"s_add_u32 s72, s52, 61440\n"
"buffer_load_dwordx4 v1, s[40:43], s72 offen lds\n"
"s_add_u32 m0, s53, 4096\n"
"s_add_u32 s72, s52, 81920\n"
"buffer_load_dwordx4 v1, s[40:43], s72 offen lds\n"
"s_add_u32 m0, s53, 5120\n"
"s_add_u32 s72, s52, 102400\n"
"buffer_load_dwordx4 v1, s[40:43], s72 offen lds\n"
"s_add_u32 m0, s53, 6144\n"
"s_add_u32 s72, s52, 122880\n"
"buffer_load_dwordx4 v1, s[40:43], s72 offen lds\n"
"s_add_u32 m0, s53, 7168\n"
"s_add_u32 s72, s52, 143360\n"
"buffer_load_dwordx4 v1, s[40:43], s72 offen lds\n"
"s_add_u32 m0, s53, 8192\n"
"s_add_u32 s72, s52, 163840\n"
"buffer_load_dwordx4 v1, s[40:43], s72 offen lds\n"
"s_add_u32 m0, s53, 9216\n"
"s_add_u32 s72, s52, 184320\n"
"buffer_load_dwordx4 v1, s[40:43], s72 offen lds\n"
"s_add_u32 m0, s53, 10240\n"
"s_add_u32 s72, s52, 204800\n"
"buffer_load_dwordx4 v1, s[40:43], s72 offen lds\n"
"s_add_u32 m0, s53, 11264\n"
"s_add_u32 s72, s52, 225280\n"
"buffer_load_dwordx4 v1, s[40:43], s72 offen lds\n"
"s_add_u32 m0, s53, 49152\n"
"s_add_u32 s72, s52, 245760\n"
"buffer_load_dwordx4 v1, s[40:43], s72 offen lds\n"
"s_add_u32 m0, s53, 50176\n"
"s_add_u32 s72, s52, 266240\n"
"buffer_load_dwordx4 v1, s[40:43], s72 offen lds\n"
"s_add_u32 m0, s53, 51200\n"
"s_add_u32 s72, s52, 286720\n"
"buffer_load_dwordx4 v1, s[40:43], s72 offen lds\n"
"s_add_u32 m0, s53, 52224\n"
"s_add_u32 s72, s52, 307200\n"
"buffer_load_dwordx4 v1, s[40:43], s72 offen lds\n"
"s_add_u32 m0, s53, 53248\n"
"s_add_u32 s72, s52, 327680\n"
"buffer_load_dwordx4 v1, s[40:43], s72 offen lds\n"
"s_add_u32 m0, s53, 54272\n"
"s_add_u32 s72, s52, 348160\n"
"buffer_load_dwordx4 v1, s[40:43], s72 offen lds\n"
"s_add_u32 m0, s53, 55296\n"
"s_add_u32 s72, s52, 368640\n"
"buffer_load_dwordx4 v1, s[40:43], s72 offen lds\n"
"s_add_u32 m0, s53, 56320\n"
"s_add_u32 s72, s52, 389120\n"
"buffer_load_dwordx4 v1, s[40:43], s72 offen lds\n"
"s_add_u32 m0, s53, 57344\n"
"s_add_u32 s72, s52, 409600\n"
"buffer_load_dwordx4 v1, s[40:43], s72 offen lds\n"
"s_add_u32 m0, s53, 58368\n"
"s_add_u32 s72, s52, 430080\n"
"buffer_load_dwordx4 v1, s[40:43], s72 offen lds\n"
"s_add_u32 m0, s53, 59392\n"
"s_add_u32 s72, s52, 450560\n"
"buffer_load_dwordx4 v1, s[40:43], s72 offen lds\n"
"s_add_u32 m0, s53, 60416\n"
"s_add_u32 s72, s52, 471040\n"
"buffer_load_dwordx4 v1, s[40:43], s72 offen lds\n"
"s_waitcnt vmcnt(12)\n"
"s_waitcnt lgkmcnt(0)\n"
"s_barrier\n"
"v_add_u32 v12, 0, v2\n"
"ds_read_b64_tr_b16 a[0:1], v12 offset:0\n"
"ds_read_b64_tr_b16 a[2:3], v12 offset:512\n"
"ds_read_b64_tr_b16 a[4:5], v12 offset:1024\n"
"ds_read_b64_tr_b16 a[6:7], v12 offset:1536\n"
"ds_read_b64_tr_b16 a[8:9], v12 offset:2048\n"
"ds_read_b64_tr_b16 a[10:11], v12 offset:2560\n"
"ds_read_b64_tr_b16 a[12:13], v12 offset:3072\n"
"ds_read_b64_tr_b16 a[14:15], v12 offset:3584\n"
"ds_read_b64_tr_b16 a[16:17], v12 offset:4096\n"
"ds_read_b64_tr_b16 a[18:19], v12 offset:4608\n"
"ds_read_b64_tr_b16 a[20:21], v12 offset:5120\n"
"ds_read_b64_tr_b16 a[22:23], v12 offset:5632\n"
"ds_read_b64_tr_b16 a[24:25], v12 offset:6144\n"
"ds_read_b64_tr_b16 a[26:27], v12 offset:6656\n"
"ds_read_b64_tr_b16 a[28:29], v12 offset:7168\n"
"ds_read_b64_tr_b16 a[30:31], v12 offset:7680\n"
"ds_read_b64_tr_b16 a[32:33], v12 offset:8192\n"
"ds_read_b64_tr_b16 a[34:35], v12 offset:8704\n"
"ds_read_b64_tr_b16 a[36:37], v12 offset:9216\n"
"ds_read_b64_tr_b16 a[38:39], v12 offset:9728\n"
"ds_read_b64_tr_b16 a[40:41], v12 offset:10240\n"
"ds_read_b64_tr_b16 a[42:43], v12 offset:10752\n"
"ds_read_b64_tr_b16 a[44:45], v12 offset:11264\n"
"ds_read_b64_tr_b16 a[46:47], v12 offset:11776\n"
"s_waitcnt vmcnt(0)\n"
"s_waitcnt lgkmcnt(0)\n"
"s_barrier\n"
"v_add_u32 v12, 49152, v2\n"
"ds_read_b64_tr_b16 a[48:49], v12 offset:0\n"
"ds_read_b64_tr_b16 a[50:51], v12 offset:512\n"
"ds_read_b64_tr_b16 a[52:53], v12 offset:1024\n"
"ds_read_b64_tr_b16 a[54:55], v12 offset:1536\n"
"ds_read_b64_tr_b16 a[56:57], v12 offset:2048\n"
"ds_read_b64_tr_b16 a[58:59], v12 offset:2560\n"
"ds_read_b64_tr_b16 a[60:61], v12 offset:3072\n"
"ds_read_b64_tr_b16 a[62:63], v12 offset:3584\n"
"ds_read_b64_tr_b16 a[64:65], v12 offset:4096\n"
"ds_read_b64_tr_b16 a[66:67], v12 offset:4608\n"
"ds_read_b64_tr_b16 a[68:69], v12 offset:5120\n"
"ds_read_b64_tr_b16 a[70:71], v12 offset:5632\n"
"ds_read_b64_tr_b16 a[72:73], v12 offset:6144\n"
"ds_read_b64_tr_b16 a[74:75], v12 offset:6656\n"
"ds_read_b64_tr_b16 a[76:77], v12 offset:7168\n"
"ds_read_b64_tr_b16 a[78:79], v12 offset:7680\n"
"ds_read_b64_tr_b16 a[80:81], v12 offset:8192\n"
"ds_read_b64_tr_b16 a[82:83], v12 offset:8704\n"
"ds_read_b64_tr_b16 a[84:85], v12 offset:9216\n"
"ds_read_b64_tr_b16 a[86:87], v12 offset:9728\n"
"ds_read_b64_tr_b16 a[88:89], v12 offset:10240\n"
"ds_read_b64_tr_b16 a[90:91], v12 offset:10752\n"
"ds_read_b64_tr_b16 a[92:93], v12 offset:11264\n"
"ds_read_b64_tr_b16 a[94:95], v12 offset:11776\n"
"s_waitcnt lgkmcnt(0)\n"
"s_barrier\n"
"s_add_u32 s69, s54, 1\n"
"s_and_b32 s83, s54, 63\n"
"s_mov_b32 s76, 131072\n"
"s_branch T2_%=\n"
"T1_%=:\n"
"s_sleep 2\n"
"T2_%=:\n"
"buffer_load_dword v1, v4, s[36:39], 0 offen sc1\n"
"s_sub_u32 s76, s76, 1\n"
"s_cbranch_scc1 TABORT_%=\n"
"s_waitcnt vmcnt(0)\n"
"v_cmp_gt_u32 vcc, s69, v1\n"
"s_nop 0\n"
"s_cbranch_vccnz T1_%=\n"
"s_mul_i32 s70, s83, 98304\n"
"s_add_u32 s70, s70, s59\n"
"buffer_load_dwordx4 v[48:51], v0, s[36:39], s70 offen offset:0 sc1\n"
"buffer_load_dwordx4 v[52:55], v0, s[36:39], s70 offen offset:1024 sc1\n"
"buffer_load_dwordx4 v[56:59], v0, s[36:39], s70 offen offset:2048 sc1\n"
"buffer_load_dwordx4 v[60:63], v0, s[36:39], s70 offen offset:3072 sc1\n"
"s_add_u32 s70, s70, 4096\n"
"buffer_load_dwordx4 v[64:67], v0, s[36:39], s70 offen offset:0 sc1\n"
"buffer_load_dwordx4 v[68:71], v0, s[36:39], s70 offen offset:1024 sc1\n"
"buffer_load_dwordx4 v[72:75], v0, s[36:39], s70 offen offset:2048 sc1\n"
"buffer_load_dwordx4 v[76:79], v0, s[36:39], s70 offen offset:3072 sc1\n"
"s_add_u32 s70, s70, 4096\n"
"buffer_load_dwordx4 v[80:83], v0, s[36:39], s70 offen offset:0 sc1\n"
"buffer_load_dwordx4 v[84:87], v0, s[36:39], s70 offen offset:1024 sc1\n"
"buffer_load_dwordx4 v[88:91], v0, s[36:39], s70 offen offset:2048 sc1\n"
"buffer_load_dwordx4 v[92:95], v0, s[36:39], s70 offen offset:3072 sc1\n"
"s_add_u32 s70, s70, 4096\n"
"buffer_load_dwordx4 v[96:99], v0, s[36:39], s70 offen offset:0 sc1\n"
"buffer_load_dwordx4 v[100:103], v0, s[36:39], s70 offen offset:1024 sc1\n"
"buffer_load_dwordx4 v[104:107], v0, s[36:39], s70 offen offset:2048 sc1\n"
"buffer_load_dwordx4 v[108:111], v0, s[36:39], s70 offen offset:3072 sc1\n"
"s_add_u32 s70, s70, 4096\n"
"buffer_load_dwordx4 v[112:115], v0, s[36:39], s70 offen offset:0 sc1\n"
"buffer_load_dwordx4 v[116:119], v0, s[36:39], s70 offen offset:1024 sc1\n"
"buffer_load_dwordx4 v[120:123], v0, s[36:39], s70 offen offset:2048 sc1\n"
"buffer_load_dwordx4 v[124:127], v0, s[36:39], s70 offen offset:3072 sc1\n"
"s_add_u32 s70, s70, 4096\n"
"buffer_load_dwordx4 v[128:131], v0, s[36:39], s70 offen offset:0 sc1\n"
"buffer_load_dwordx4 v[132:135], v0, s[36:39], s70 offen offset:1024 sc1\n"
"buffer_load_dwordx4 v[136:139], v0, s[36:39], s70 offen offset:2048 sc1\n"
"buffer_load_dwordx4 v[140:143], v0, s[36:39], s70 offen offset:3072 sc1\n"
"s_mul_i32 s70, s83, 1310720\n"
"s_add_u32 s70, s70, s60\n"
"buffer_load_dwordx4 a[96:99], v0, s[36:39], s70 offen offset:0 sc1\n"
"buffer_load_dwordx4 a[100:103], v0, s[36:39], s70 offen offset:1024 sc1\n"
"buffer_load_dwordx4 a[104:107], v0, s[36:39], s70 offen offset:2048 sc1\n"
"buffer_load_dwordx4 a[108:111], v0, s[36:39], s70 offen offset:3072 sc1\n"
"s_add_u32 s70, s70, 4096\n"
"buffer_load_dwordx4 a[112:115], v0, s[36:39], s70 offen offset:0 sc1\n"
"buffer_load_dwordx4 a[116:119], v0, s[36:39], s70 offen offset:1024 sc1\n"
"buffer_load_dwordx4 a[120:123], v0, s[36:39], s70 offen offset:2048 sc1\n"
"buffer_load_dwordx4 a[124:127], v0, s[36:39], s70 offen offset:3072 sc1\n"
"s_add_u32 s70, s70, 4096\n"
"buffer_load_dwordx4 a[128:131], v0, s[36:39], s70 offen offset:0 sc1\n"
"buffer_load_dwordx4 a[132:135], v0, s[36:39], s70 offen offset:1024 sc1\n"
"buffer_load_dwordx4 a[136:139], v0, s[36:39], s70 offen offset:2048 sc1\n"
"buffer_load_dwordx4 a[140:143], v0, s[36:39], s70 offen offset:3072 sc1\n"
"s_add_u32 s70, s70, 4096\n"
"buffer_load_dwordx4 a[144:147], v0, s[36:39], s70 offen offset:0 sc1\n"
"buffer_load_dwordx4 a[148:151], v0, s[36:39], s70 offen offset:1024 sc1\n"
"buffer_load_dwordx4 a[152:155], v0, s[36:39], s70 offen offset:2048 sc1\n"
"buffer_load_dwordx4 a[156:159], v0, s[36:39], s70 offen offset:3072 sc1\n"
"s_mul_i32 s77, s54, 40960\n"
"buffer_load_dwordx4 v[12:15], v3, s[44:47], s77 offen\n"
"s_waitcnt vmcnt(40)\n"
"v_mfma_f32_32x32x16_bf16 v[16:31], a[0:3], v[48:51], 0\n"
"s_waitcnt vmcnt(39)\n"
"v_mfma_f32_32x32x16_bf16 v[16:31], a[4:7], v[52:55], v[16:31]\n"
"s_waitcnt vmcnt(38)\n"
"v_mfma_f32_32x32x16_bf16 v[16:31], a[8:11], v[56:59], v[16:31]\n"
"s_waitcnt vmcnt(37)\n"
"v_mfma_f32_32x32x16_bf16 v[16:31], a[12:15], v[60:63], v[16:31]\n"
"s_waitcnt vmcnt(36)\n"
"v_mfma_f32_32x32x16_bf16 v[16:31], a[16:19], v[64:67], v[16:31]\n"
"s_waitcnt vmcnt(35)\n"
"v_mfma_f32_32x32x16_bf16 v[16:31], a[20:23], v[68:71], v[16:31]\n"
"s_waitcnt vmcnt(34)\n"
"v_mfma_f32_32x32x16_bf16 v[16:31], a[24:27], v[72:75], v[16:31]\n"
"s_waitcnt vmcnt(33)\n"
"v_mfma_f32_32x32x16_bf16 v[16:31], a[28:31], v[76:79], v[16:31]\n"
"s_waitcnt vmcnt(32)\n"
"v_mfma_f32_32x32x16_bf16 v[16:31], a[32:35], v[80:83], v[16:31]\n"
"s_waitcnt vmcnt(31)\n"
"v_mfma_f32_32x32x16_bf16 v[16:31], a[36:39], v[84:87], v[16:31]\n"
"s_waitcnt vmcnt(30)\n"
"v_mfma_f32_32x32x16_bf16 v[16:31], a[40:43], v[88:91], v[16:31]\n"
"s_waitcnt vmcnt(29)\n"
"v_mfma_f32_32x32x16_bf16 v[16:31], a[44:47], v[92:95], v[16:31]\n"
"s_waitcnt vmcnt(28)\n"
"v_mfma_f32_32x32x16_bf16 v[16:31], a[48:51], v[96:99], v[16:31]\n"
"s_waitcnt vmcnt(27)\n"
"v_mfma_f32_32x32x16_bf16 v[16:31], a[52:55], v[100:103], v[16:31]\n"
"s_waitcnt vmcnt(26)\n"
"v_mfma_f32_32x32x16_bf16 v[16:31], a[56:59], v[104:107], v[16:31]\n"
"s_waitcnt vmcnt(25)\n"
"v_mfma_f32_32x32x16_bf16 v[16:31], a[60:63], v[108:111], v[16:31]\n"
"s_waitcnt vmcnt(24)\n"
"v_mfma_f32_32x32x16_bf16 v[16:31], a[64:67], v[112:115], v[16:31]\n"
"s_waitcnt vmcnt(23)\n"
"v_mfma_f32_32x32x16_bf16 v[16:31], a[68:71], v[116:119], v[16:31]\n"
"s_waitcnt vmcnt(22)\n"
"v_mfma_f32_32x32x16_bf16 v[16:31], a[72:75], v[120:123], v[16:31]\n"
"s_waitcnt vmcnt(21)\n"
"v_mfma_f32_32x32x16_bf16 v[16:31], a[76:79], v[124:127], v[16:31]\n"
"s_waitcnt vmcnt(20)\n"
"v_mfma_f32_32x32x16_bf16 v[16:31], a[80:83], v[128:131], v[16:31]\n"
"s_waitcnt vmcnt(19)\n"
"v_mfma_f32_32x32x16_bf16 v[16:31], a[84:87], v[132:135], v[16:31]\n"
"s_waitcnt vmcnt(18)\n"
"v_mfma_f32_32x32x16_bf16 v[16:31], a[88:91], v[136:139], v[16:31]\n"
"s_waitcnt vmcnt(17)\n"
"v_mfma_f32_32x32x16_bf16 v[16:31], a[92:95], v[140:143], v[16:31]\n"
"s_mov_b32 s68, 0\n"
"s_mov_b32 s55, 234881024\n"
"s_nop 9\n"
"T3_%=:\n"
"s_sub_u32 s70, s68, 4\n"
"s_max_i32 s70, s70, 0\n"
"s_mov_b32 s76, 131072\n"
"s_mov_b32 exec_lo, 1\n"
"s_mov_b32 exec_hi, 0\n"
"s_mov_b32 s79, 0\n"
"T4_%=:\n"
"s_sleep 60\n"
"v_mov_b32 v2, 0\n"
"buffer_atomic_add v2, v5, s[36:39], s79 offen sc0 sc1\n"
"s_sub_u32 s76, s76, 1\n"
"s_cbranch_scc1 TABORT_%=\n"
"s_waitcnt vmcnt(0)\n"
"v_cmp_gt_u32 vcc, s70, v2\n"
"s_nop 0\n"
"s_cbranch_vccnz T4_%=\n"
"s_mov_b64 exec, -1\n"
"s_mov_b32 s76, 131072\n"
"s_mov_b32 exec_lo, 255\n"
"s_mov_b32 exec_hi, 0\n"
"s_mov_b32 s79, 0\n"
"T5_%=:\n"
"s_sleep 1\n"
"v_mov_b32 v2, 0\n"
"buffer_atomic_add v2, v11, s[36:39], s79 offen sc0 sc1\n"
"s_sub_u32 s76, s76, 1\n"
"s_cbranch_scc1 TABORT_%=\n"
"s_waitcnt vmcnt(0)\n"
"v_cmp_gt_u32 vcc, s68, v2\n"
"s_nop 0\n"
"s_cbranch_vccnz T5_%=\n"
"s_mov_b64 exec, -1\n"
"s_mul_i32 s78, s83, 40\n"
"s_add_u32 s78, s78, s63\n"
"s_lshr_b32 s79, s78, 7\n"
"s_xor_b32 s79, s79, s64\n"
"s_and_b32 s79, s79, 1\n"
"s_lshl_b32 s79, s79, 13\n"
"s_lshl_b32 s78, s78, 14\n"
"s_add_u32 s78, s78, s79\n"
"s_add_u32 s78, s78, s65\n"
"s_max_u32 s78, s78, s55\n"
"buffer_load_dwordx4 a[160:163], v6, s[36:39], s78 offen offset:0 sc1\n"
"buffer_load_dwordx4 a[164:167], v6, s[36:39], s78 offen offset:512 sc1\n"
"buffer_load_dwordx4 a[168:171], v6, s[36:39], s78 offen offset:1024 sc1\n"
"buffer_load_dwordx4 a[172:175], v6, s[36:39], s78 offen offset:1536 sc1\n"
"s_mul_i32 s74, s83, 65536\n"
"s_add_u32 s74, s74, s61\n"
"s_max_u32 s74, s74, s55\n"
"s_mul_i32 s75, s83, 16384\n"
"s_add_u32 s75, s75, s62\n"
"s_max_u32 s75, s75, s55\n"
"s_max_u32 s73, s77, s55\n"
"v_lshlrev_b32 v160, 16, v12\n"
"v_and_b32 v161, 0xffff0000, v12\n"
"v_lshlrev_b32 v162, 16, v13\n"
"v_and_b32 v163, 0xffff0000, v13\n"
"v_lshlrev_b32 v164, 16, v14\n"
"v_and_b32 v165, 0xffff0000, v14\n"
"v_lshlrev_b32 v166, 16, v15\n"
"v_and_b32 v167, 0xffff0000, v15\n"
"s_mov_b32 s76, 131072\n"
"s_mov_b32 exec_lo, 255\n"
"s_mov_b32 exec_hi, 0\n"
"s_mov_b32 s79, 4096\n"
"T6_%=:\n"
"s_sleep 1\n"
"v_mov_b32 v2, 0\n"
"buffer_atomic_add v2, v5, s[36:39], s79 offen sc0 sc1\n"
"s_sub_u32 s76, s76, 1\n"
"s_cbranch_scc1 TABORT_%=\n"
"s_waitcnt vmcnt(0)\n"
"v_cmp_gt_u32 vcc, s68, v2\n"
"s_nop 0\n"
"s_cbranch_vccnz T6_%=\n"
"s_mov_b64 exec, -1\n"
"s_mov_b32 s76, 131072\n"
"T7_%=:\n"
"s_mov_b32 s71, s74\n"
"buffer_load_dwordx4 v[48:51], v0, s[36:39], s71 offen offset:0 sc1\n"
"buffer_load_dwordx4 v[52:55], v0, s[36:39], s71 offen offset:1024 sc1\n"
"buffer_load_dwordx4 v[56:59], v0, s[36:39], s71 offen offset:2048 sc1\n"
"buffer_load_dwordx4 v[60:63], v0, s[36:39], s71 offen offset:3072 sc1\n"
"s_add_u32 s71, s71, 4096\n"
"buffer_load_dwordx4 v[64:67], v0, s[36:39], s71 offen offset:0 sc1\n"
"buffer_load_dwordx4 v[68:71], v0, s[36:39], s71 offen offset:1024 sc1\n"
"buffer_load_dwordx4 v[72:75], v0, s[36:39], s71 offen offset:2048 sc1\n"
"buffer_load_dwordx4 v[76:79], v0, s[36:39], s71 offen offset:3072 sc1\n"
"s_add_u32 s71, s71, 4096\n"
"buffer_load_dwordx4 v[80:83], v0, s[36:39], s71 offen offset:0 sc1\n"
"buffer_load_dwordx4 v[84:87], v0, s[36:39], s71 offen offset:1024 sc1\n"
"buffer_load_dwordx4 v[88:91], v0, s[36:39], s71 offen offset:2048 sc1\n"
"buffer_load_dwordx4 v[92:95], v0, s[36:39], s71 offen offset:3072 sc1\n"
"s_add_u32 s71, s71, 4096\n"
"buffer_load_dwordx4 v[96:99], v0, s[36:39], s71 offen offset:0 sc1\n"
"buffer_load_dwordx4 v[100:103], v0, s[36:39], s71 offen offset:1024 sc1\n"
"buffer_load_dwordx4 v[104:107], v0, s[36:39], s71 offen offset:2048 sc1\n"
"buffer_load_dwordx4 v[108:111], v0, s[36:39], s71 offen offset:3072 sc1\n"
"buffer_load_dwordx4 v[112:115], v0, s[36:39], s75 offen offset:0 sc1\n"
"buffer_load_dwordx4 v[116:119], v0, s[36:39], s75 offen offset:1024 sc1\n"
"buffer_load_dwordx4 v[120:123], v0, s[36:39], s75 offen offset:2048 sc1\n"
"buffer_load_dwordx4 v[124:127], v0, s[36:39], s75 offen offset:3072 sc1\n"
"s_waitcnt vmcnt(19)\n"
"v_mfma_f32_32x32x16_bf16 v[32:47], a[96:99], v[48:51], v[16:31]\n"
"v_max3_u32 v2, v48, v49, 0\n"
"v_max3_u32 v2, v50, v51, v2\n"
"s_waitcnt vmcnt(18)\n"
"v_mfma_f32_32x32x16_bf16 v[32:47], a[100:103], v[52:55], v[32:47]\n"
"v_max3_u32 v2, v52, v53, v2\n"
"v_max3_u32 v2, v54, v55, v2\n"
"s_waitcnt vmcnt(17)\n"
"v_mfma_f32_32x32x16_bf16 v[32:47], a[104:107], v[56:59], v[32:47]\n"
"v_max3_u32 v2, v56, v57, v2\n"
"v_max3_u32 v2, v58, v59, v2\n"
"s_waitcnt vmcnt(16)\n"
"v_mfma_f32_32x32x16_bf16 v[32:47], a[108:111], v[60:63], v[32:47]\n"
"v_max3_u32 v2, v60, v61, v2\n"
"v_max3_u32 v2, v62, v63, v2\n"
"s_waitcnt vmcnt(15)\n"
"v_mfma_f32_32x32x16_bf16 v[32:47], a[112:115], v[64:67], v[32:47]\n"
"v_max3_u32 v2, v64, v65, v2\n"
"v_max3_u32 v2, v66, v67, v2\n"
"s_waitcnt vmcnt(14)\n"
"v_mfma_f32_32x32x16_bf16 v[32:47], a[116:119], v[68:71], v[32:47]\n"
"v_max3_u32 v2, v68, v69, v2\n"
"v_max3_u32 v2, v70, v71, v2\n"
"s_waitcnt vmcnt(13)\n"
"v_mfma_f32_32x32x16_bf16 v[32:47], a[120:123], v[72:75], v[32:47]\n"
"v_max3_u32 v2, v72, v73, v2\n"
"v_max3_u32 v2, v74, v75, v2\n"
"s_waitcnt vmcnt(12)\n"
"v_mfma_f32_32x32x16_bf16 v[32:47], a[124:127], v[76:79], v[32:47]\n"
"v_max3_u32 v2, v76, v77, v2\n"
"v_max3_u32 v2, v78, v79, v2\n"
"s_waitcnt vmcnt(11)\n"
"v_mfma_f32_32x32x16_bf16 v[32:47], a[128:131], v[80:83], v[32:47]\n"
"v_max3_u32 v2, v80, v81, v2\n"
"v_max3_u32 v2, v82, v83, v2\n"
"s_waitcnt vmcnt(10)\n"
"v_mfma_f32_32x32x16_bf16 v[32:47], a[132:135], v[84:87], v[32:47]\n"
"v_max3_u32 v2, v84, v85, v2\n"
"v_max3_u32 v2, v86, v87, v2\n"
"s_waitcnt vmcnt(9)\n"
"v_mfma_f32_32x32x16_bf16 v[32:47], a[136:139], v[88:91], v[32:47]\n"
"v_max3_u32 v2, v88, v89, v2\n"
"v_max3_u32 v2, v90, v91, v2\n"
"s_waitcnt vmcnt(8)\n"
"v_mfma_f32_32x32x16_bf16 v[32:47], a[140:143], v[92:95], v[32:47]\n"
"v_max3_u32 v2, v92, v93, v2\n"
"v_max3_u32 v2, v94, v95, v2\n"
"s_waitcnt vmcnt(7)\n"
"v_mfma_f32_32x32x16_bf16 v[32:47], a[144:147], v[96:99], v[32:47]\n"
"v_max3_u32 v2, v96, v97, v2\n"
"v_max3_u32 v2, v98, v99, v2\n"
"s_waitcnt vmcnt(6)\n"
"v_mfma_f32_32x32x16_bf16 v[32:47], a[148:151], v[100:103], v[32:47]\n"
"v_max3_u32 v2, v100, v101, v2\n"
"v_max3_u32 v2, v102, v103, v2\n"
"s_waitcnt vmcnt(5)\n"
"v_mfma_f32_32x32x16_bf16 v[32:47], a[152:155], v[104:107], v[32:47]\n"
"v_max3_u32 v2, v104, v105, v2\n"
"v_max3_u32 v2, v106, v107, v2\n"
"s_waitcnt vmcnt(4)\n"
"v_mfma_f32_32x32x16_bf16 v[32:47], a[156:159], v[108:111], v[32:47]\n"
"v_max3_u32 v2, v108, v109, v2\n"
"v_max3_u32 v2, v110, v111, v2\n"
"s_waitcnt vmcnt(3)\n"
"v_mfma_f32_32x32x16_bf16 v[32:47], a[160:163], v[112:115], v[32:47]\n"
"v_max3_u32 v2, v112, v113, v2\n"
"v_max3_u32 v2, v114, v115, v2\n"
"s_waitcnt vmcnt(2)\n"
"v_mfma_f32_32x32x16_bf16 v[32:47], a[164:167], v[116:119], v[32:47]\n"
"v_max3_u32 v2, v116, v117, v2\n"
"v_max3_u32 v2, v118, v119, v2\n"
"s_waitcnt vmcnt(1)\n"
"v_mfma_f32_32x32x16_bf16 v[32:47], a[168:171], v[120:123], v[32:47]\n"
"v_max3_u32 v2, v120, v121, v2\n"
"v_max3_u32 v2, v122, v123, v2\n"
"s_waitcnt vmcnt(0)\n"
"v_mfma_f32_32x32x16_bf16 v[32:47], a[172:175], v[124:127], v[32:47]\n"
"v_max3_u32 v2, v124, v125, v2\n"
"v_max3_u32 v2, v126, v127, v2\n"
"v_cmp_eq_u32 vcc, -1, v2\n"
"s_nop 0\n"
"s_cbranch_vccz T8_%=\n"
"s_sub_u32 s76, s76, 1\n"
"s_cbranch_scc0 T7_%=\n"
"s_branch TABORT_%=\n"
"T8_%=:\n"
"s_nop 7\n"
"ds_write_b128 v8, v[32:35] offset:8704\n"
"ds_write_b128 v8, v[36:39] offset:9792\n"
"ds_write_b128 v8, v[40:43] offset:10880\n"
"ds_write_b128 v8, v[44:47] offset:11968\n"
"s_waitcnt lgkmcnt(0)\n"
"s_barrier\n"
"ds_read_b128 v[128:131], v10 offset:0\n"
"ds_read_b128 v[132:135], v10 offset:512\n"
"ds_read_b128 v[136:139], v10 offset:13056\n"
"ds_read_b128 v[140:143], v10 offset:13568\n"
"ds_read_b128 v[144:147], v10 offset:26112\n"
"ds_read_b128 v[148:151], v10 offset:26624\n"
"ds_read_b128 v[152:155], v10 offset:39168\n"
"ds_read_b128 v[156:159], v10 offset:39680\n"
"s_waitcnt lgkmcnt(6)\n"
"v_add_f32 v160, v160, v128\n"
"v_add_f32 v161, v161, v129\n"
"v_add_f32 v162, v162, v130\n"
"v_add_f32 v163, v163, v131\n"
"v_add_f32 v164, v164, v132\n"
"v_add_f32 v165, v165, v133\n"
"v_add_f32 v166, v166, v134\n"
"v_add_f32 v167, v167, v135\n"
"s_waitcnt lgkmcnt(4)\n"
"v_add_f32 v160, v160, v136\n"
"v_add_f32 v161, v161, v137\n"
"v_add_f32 v162, v162, v138\n"
"v_add_f32 v163, v163, v139\n"
"v_add_f32 v164, v164, v140\n"
"v_add_f32 v165, v165, v141\n"
"v_add_f32 v166, v166, v142\n"
"v_add_f32 v167, v167, v143\n"
"s_waitcnt lgkmcnt(2)\n"
"v_add_f32 v160, v160, v144\n"
"v_add_f32 v161, v161, v145\n"
"v_add_f32 v162, v162, v146\n"
"v_add_f32 v163, v163, v147\n"
"v_add_f32 v164, v164, v148\n"
"v_add_f32 v165, v165, v149\n"
"v_add_f32 v166, v166, v150\n"
"v_add_f32 v167, v167, v151\n"
"s_waitcnt lgkmcnt(0)\n"
"v_add_f32 v160, v160, v152\n"
"v_add_f32 v161, v161, v153\n"
"v_add_f32 v162, v162, v154\n"
"v_add_f32 v163, v163, v155\n"
"v_add_f32 v164, v164, v156\n"
"v_add_f32 v165, v165, v157\n"
"v_add_f32 v166, v166, v158\n"
"v_add_f32 v167, v167, v159\n"
"v_cvt_pk_bf16_f32 v168, v160, v161\n"
"v_cvt_pk_bf16_f32 v169, v162, v163\n"
"v_cvt_pk_bf16_f32 v170, v164, v165\n"
"v_cvt_pk_bf16_f32 v171, v166, v167\n"
"buffer_store_dwordx4 v[168:171], v3, s[48:51], s73 offen\n"
"s_barrier\n"
"s_cmp_eq_u32 s68, 0\n"
"s_mov_b32 s68, s69\n"
"s_mov_b32 s55, 0\n"
"s_cbranch_scc1 T3_%=\n"
"s_endpgm\n"
"TABORT_%=:\n"
"s_mov_b64 exec, -1\n"
"s_nop 7\n"
"s_nop 7\n"
"v_mov_b32 v0, 46268544\n"
"v_mov_b32 v1, 1\n"
"buffer_store_dword v1, v0, s[36:39], 0 offen sc1\n"
"s_endpgm\n");
}

DI void role_s(const Ctx& c, int h) {
    SETUP
    setsrd(sa, 4, w < 2 ? P->Wq : P->Wk, DM * NH * HD * 2);
    sa[8] = (w & 1) * 32 * (NH * HD * 2) + h * HD * 2; sa[9] = (w >> 1) * 24576 + (w & 1) * 4096;
    sa[10] = O_KX + h * 12288 + (w & 1) * 3 * 2048; sa[11] = w * 3072; sa[12] = O_KM + h * 3072 + w * 1024; sa[13] = O_COMM + (c.par << 13) + (RHO << 14);
    va[1] = r8 * (NH * HD * 2) + ((c8 ^ ((r8 >> 2) << 1)) * 16); va[2] = va[1] ^ 64;
    const int row0 = 16 * (g >> 1) + 8 * (q >> 1) + 4 * (g & 1) + (q & 1), key = (row0 >> 2) & 3;
#pragma unroll
    for (int t = 0; t < 3; ++t) { const int T = 4 * t + w; va[3 + t] = (T >> 2) * 8192 + row0 * 128 + (((2 * (T & 3) + (p >> 1)) ^ (key << 1)) * 16) + (p & 1) * 8; }
    va[6] = (lane & 2) ? 0xEEEE : 0x4444;
    va[7] = L_QK + ((n >> 2) & 1) * 256 + (2 * (n >> 3) + (n & 1)) * 32 + g * 8 + ((n >> 1) & 1) * 4;
    va[8] = L_QK + (g >> 1) * 512 + (g & 1) * 128 + n * 8;
    va[9] = (2 * g + (p >> 1)) * 128 + (2 * q + (p & 1)) * 16;
    sb[11] = P->tune & 0xffffffu; sb[12] = h * 64;
    const unsigned px = (n >> 3) * 256 + g * 64 + c8 * 8, pm = (n & 1) * 0x70000000u + 512 + (2 * g + (p >> 1)) * 16 + q * 4;
    va[10] = (h >> 3) * (NREP * 16384) + (h & 7) * 640 + px + (w >> 1) * (pm - px);
    va[11] = g * 512 + (16 * (n >> 3) + 8 * (w >> 1) + c8) * 16; vb[3] = 20480 + g * 256 + (8 * (n >> 3) + 4 * (n & 1) + 2 * ((n >> 2) & 1) + ((n >> 1) & 1)) * 16;
    va[12] = F_K + fw(3 * h + lane % 3); va[13] = one(lane, F_KM + h * 128);
    va[15] = L_RES + w * 24576 + lane * 16;
    RUN("s_nop 4\n"
"s_mov_b32 s68, s49\n"
"s_mov_b32 s73, 0\n"
"s_mov_b32 s69, 0\n"
"s_mov_b32 s50, 20480\n"
"s_mov_b32 s82, 0xcccccccc\n"
"s_mov_b32 s83, 0xcccccccc\n"
"s_mov_b32 s93, 0\n"
"s_mov_b32 s21, s64\n"
"s_and_b32 s20, s49, 8192\n"
"s_xor_b32 s20, s20, 0\n"
"s_add_u32 s20, s20, 100663296\n"
"s_mov_b32 s74, 0x3dd53b94\n"
"s_mov_b32 s81, 0x42800000\n"
"v_mov_b32 v28, 0\n"
"s_cmp_eq_u32 s66, 1\n"
"s_cbranch_scc1 S2_%=\n"
"s_cmp_eq_u32 s66, 2\n"
"s_cbranch_scc1 S3_%=\n"
"s_cmp_eq_u32 s66, 3\n"
"s_cbranch_scc1 S4_%=\n"
"S1_%=:\n"
"s_mov_b32 s22, 46301184\n"
"s_mov_b32 s23, 46317568\n"
"s_cmp_lg_u32 s66, 0\n"
"s_cbranch_scc1 S6_%=\n"
"v_mov_b32 v24, 0\n"
"s_mov_b64 exec, 1\n"
"buffer_atomic_add v24, v26, s[36:39], s23 offen sc0 sc1\n"
"s_mov_b64 exec, -1\n"
"S6_%=:\n"
"s_add_u32 m0, s45, 0\n"
"s_add_u32 s72, s44, 0\n"
"buffer_load_dwordx4 v1, s[40:43], s72 offen lds\n"
"s_add_u32 m0, s45, 1024\n"
"s_add_u32 s72, s44, 98304\n"
"buffer_load_dwordx4 v2, s[40:43], s72 offen lds\n"
"s_add_u32 m0, s45, 2048\n"
"s_add_u32 s72, s44, 196608\n"
"buffer_load_dwordx4 v1, s[40:43], s72 offen lds\n"
"s_add_u32 m0, s45, 3072\n"
"s_add_u32 s72, s44, 294912\n"
"buffer_load_dwordx4 v2, s[40:43], s72 offen lds\n"
"s_add_u32 m0, s45, 8192\n"
"s_add_u32 s72, s44, 128\n"
"buffer_load_dwordx4 v1, s[40:43], s72 offen lds\n"
"s_add_u32 m0, s45, 9216\n"
"s_add_u32 s72, s44, 98432\n"
"buffer_load_dwordx4 v2, s[40:43], s72 offen lds\n"
"s_add_u32 m0, s45, 10240\n"
"s_add_u32 s72, s44, 196736\n"
"buffer_load_dwordx4 v1, s[40:43], s72 offen lds\n"
"s_add_u32 m0, s45, 11264\n"
"s_add_u32 s72, s44, 295040\n"
"buffer_load_dwordx4 v2, s[40:43], s72 offen lds\n"
"s_add_u32 m0, s45, 16384\n"
"s_add_u32 s72, s44, 256\n"
"buffer_load_dwordx4 v1, s[40:43], s72 offen lds\n"
"s_add_u32 m0, s45, 17408\n"
"s_add_u32 s72, s44, 98560\n"
"buffer_load_dwordx4 v2, s[40:43], s72 offen lds\n"
"s_add_u32 m0, s45, 18432\n"
"s_add_u32 s72, s44, 196864\n"
"buffer_load_dwordx4 v1, s[40:43], s72 offen lds\n"
"s_add_u32 m0, s45, 19456\n"
"s_add_u32 s72, s44, 295168\n"
"buffer_load_dwordx4 v2, s[40:43], s72 offen lds\n"
"s_add_u32 m0, s45, 49152\n"
"s_add_u32 s72, s44, 786432\n"
"buffer_load_dwordx4 v1, s[40:43], s72 offen lds\n"
"s_add_u32 m0, s45, 50176\n"
"s_add_u32 s72, s44, 884736\n"
"buffer_load_dwordx4 v2, s[40:43], s72 offen lds\n"
"s_add_u32 m0, s45, 51200\n"
"s_add_u32 s72, s44, 983040\n"
"buffer_load_dwordx4 v1, s[40:43], s72 offen lds\n"
"s_add_u32 m0, s45, 52224\n"
"s_add_u32 s72, s44, 1081344\n"
"buffer_load_dwordx4 v2, s[40:43], s72 offen lds\n"
"s_add_u32 m0, s45, 57344\n"
"s_add_u32 s72, s44, 786560\n"
"buffer_load_dwordx4 v1, s[40:43], s72 offen lds\n"
"s_add_u32 m0, s45, 58368\n"
"s_add_u32 s72, s44, 884864\n"
"buffer_load_dwordx4 v2, s[40:43], s72 offen lds\n"
"s_add_u32 m0, s45, 59392\n"
"s_add_u32 s72, s44, 983168\n"
"buffer_load_dwordx4 v1, s[40:43], s72 offen lds\n"
"s_add_u32 m0, s45, 60416\n"
"s_add_u32 s72, s44, 1081472\n"
"buffer_load_dwordx4 v2, s[40:43], s72 offen lds\n"
"s_add_u32 m0, s45, 65536\n"
"s_add_u32 s72, s44, 786688\n"
"buffer_load_dwordx4 v1, s[40:43], s72 offen lds\n"
"s_add_u32 m0, s45, 66560\n"
"s_add_u32 s72, s44, 884992\n"
"buffer_load_dwordx4 v2, s[40:43], s72 offen lds\n"
"s_add_u32 m0, s45, 67584\n"
"s_add_u32 s72, s44, 983296\n"
"buffer_load_dwordx4 v1, s[40:43], s72 offen lds\n"
"s_add_u32 m0, s45, 68608\n"
"s_add_u32 s72, s44, 1081600\n"
"buffer_load_dwordx4 v2, s[40:43], s72 offen lds\n"
"s_waitcnt vmcnt(12)\n"
"s_waitcnt lgkmcnt(0)\n"
"s_barrier\n"
"s_cmp_lg_u32 s66, 0\n"
"s_cbranch_scc1 S7_%=\n"
"v_or_b32 v24, 1, v24\n"
"v_add_u32 v24, 1, v24\n"
"buffer_store_dword v24, v26, s[36:39], s22 offen sc1\n"
"S7_%=:\n"
"s_add_u32 m0, s45, 98304\n"
"s_add_u32 s72, s44, 1572864\n"
"buffer_load_dwordx4 v1, s[40:43], s72 offen lds\n"
"s_add_u32 m0, s45, 99328\n"
"s_add_u32 s72, s44, 1671168\n"
"buffer_load_dwordx4 v2, s[40:43], s72 offen lds\n"
"s_add_u32 m0, s45, 100352\n"
"s_add_u32 s72, s44, 1769472\n"
"buffer_load_dwordx4 v1, s[40:43], s72 offen lds\n"
"s_add_u32 m0, s45, 101376\n"
"s_add_u32 s72, s44, 1867776\n"
"buffer_load_dwordx4 v2, s[40:43], s72 offen lds\n"
"s_add_u32 m0, s45, 106496\n"
"s_add_u32 s72, s44, 1572992\n"
"buffer_load_dwordx4 v1, s[40:43], s72 offen lds\n"
"s_add_u32 m0, s45, 107520\n"
"s_add_u32 s72, s44, 1671296\n"
"buffer_load_dwordx4 v2, s[40:43], s72 offen lds\n"
"s_add_u32 m0, s45, 108544\n"
"s_add_u32 s72, s44, 1769600\n"
"buffer_load_dwordx4 v1, s[40:43], s72 offen lds\n"
"s_add_u32 m0, s45, 109568\n"
"s_add_u32 s72, s44, 1867904\n"
"buffer_load_dwordx4 v2, s[40:43], s72 offen lds\n"
"s_add_u32 m0, s45, 114688\n"
"s_add_u32 s72, s44, 1573120\n"
"buffer_load_dwordx4 v1, s[40:43], s72 offen lds\n"
"s_add_u32 m0, s45, 115712\n"
"s_add_u32 s72, s44, 1671424\n"
"buffer_load_dwordx4 v2, s[40:43], s72 offen lds\n"
"s_add_u32 m0, s45, 116736\n"
"s_add_u32 s72, s44, 1769728\n"
"buffer_load_dwordx4 v1, s[40:43], s72 offen lds\n"
"s_add_u32 m0, s45, 117760\n"
"s_add_u32 s72, s44, 1868032\n"
"buffer_load_dwordx4 v2, s[40:43], s72 offen lds\n"
"v_add_u32 v20, 0, v3\n"
"v_add_u32 v21, 0, v4\n"
"v_add_u32 v22, 0, v5\n"
"ds_read_b64_tr_b16 v[32:33], v20 offset:0\n"
"ds_read_b64_tr_b16 v[34:35], v20 offset:256\n"
"ds_read_b64_tr_b16 v[36:37], v20 offset:4096\n"
"ds_read_b64_tr_b16 v[38:39], v20 offset:4352\n"
"ds_read_b64_tr_b16 v[128:129], v21 offset:0\n"
"ds_read_b64_tr_b16 v[130:131], v21 offset:256\n"
"ds_read_b64_tr_b16 v[132:133], v21 offset:4096\n"
"ds_read_b64_tr_b16 v[134:135], v21 offset:4352\n"
"ds_read_b64_tr_b16 v[136:137], v22 offset:0\n"
"ds_read_b64_tr_b16 v[138:139], v22 offset:256\n"
"ds_read_b64_tr_b16 v[140:141], v22 offset:4096\n"
"ds_read_b64_tr_b16 v[142:143], v22 offset:4352\n"
"ds_read_b64_tr_b16 v[40:41], v20 offset:24576\n"
"ds_read_b64_tr_b16 v[42:43], v20 offset:24832\n"
"ds_read_b64_tr_b16 v[44:45], v20 offset:28672\n"
"ds_read_b64_tr_b16 v[46:47], v20 offset:28928\n"
"ds_read_b64_tr_b16 v[48:49], v21 offset:24576\n"
"ds_read_b64_tr_b16 v[50:51], v21 offset:24832\n"
"ds_read_b64_tr_b16 v[52:53], v21 offset:28672\n"
"ds_read_b64_tr_b16 v[54:55], v21 offset:28928\n"
"ds_read_b64_tr_b16 a[176:177], v22 offset:24576\n"
"ds_read_b64_tr_b16 a[178:179], v22 offset:24832\n"
"ds_read_b64_tr_b16 a[180:181], v22 offset:28672\n"
"ds_read_b64_tr_b16 a[182:183], v22 offset:28928\n"
"s_waitcnt vmcnt(12)\n"
"s_waitcnt lgkmcnt(0)\n"
"s_barrier\n"
"s_add_u32 m0, s45, 0\n"
"s_add_u32 s72, s44, 2359296\n"
"buffer_load_dwordx4 v1, s[40:43], s72 offen lds\n"
"s_add_u32 m0, s45, 1024\n"
"s_add_u32 s72, s44, 2457600\n"
"buffer_load_dwordx4 v2, s[40:43], s72 offen lds\n"
"s_add_u32 m0, s45, 2048\n"
"s_add_u32 s72, s44, 2555904\n"
"buffer_load_dwordx4 v1, s[40:43], s72 offen lds\n"
"s_add_u32 m0, s45, 3072\n"
"s_add_u32 s72, s44, 2654208\n"
"buffer_load_dwordx4 v2, s[40:43], s72 offen lds\n"
"s_add_u32 m0, s45, 8192\n"
"s_add_u32 s72, s44, 2359424\n"
"buffer_load_dwordx4 v1, s[40:43], s72 offen lds\n"
"s_add_u32 m0, s45, 9216\n"
"s_add_u32 s72, s44, 2457728\n"
"buffer_load_dwordx4 v2, s[40:43], s72 offen lds\n"
"s_add_u32 m0, s45, 10240\n"
"s_add_u32 s72, s44, 2556032\n"
"buffer_load_dwordx4 v1, s[40:43], s72 offen lds\n"
"s_add_u32 m0, s45, 11264\n"
"s_add_u32 s72, s44, 2654336\n"
"buffer_load_dwordx4 v2, s[40:43], s72 offen lds\n"
"s_add_u32 m0, s45, 16384\n"
"s_add_u32 s72, s44, 2359552\n"
"buffer_load_dwordx4 v1, s[40:43], s72 offen lds\n"
"s_add_u32 m0, s45, 17408\n"
"s_add_u32 s72, s44, 2457856\n"
"buffer_load_dwordx4 v2, s[40:43], s72 offen lds\n"
"s_add_u32 m0, s45, 18432\n"
"s_add_u32 s72, s44, 2556160\n"
"buffer_load_dwordx4 v1, s[40:43], s72 offen lds\n"
"s_add_u32 m0, s45, 19456\n"
"s_add_u32 s72, s44, 2654464\n"
"buffer_load_dwordx4 v2, s[40:43], s72 offen lds\n"
"v_add_u32 v20, 49152, v3\n"
"v_add_u32 v21, 49152, v4\n"
"v_add_u32 v22, 49152, v5\n"
"ds_read_b64_tr_b16 v[56:57], v20 offset:0\n"
"ds_read_b64_tr_b16 v[58:59], v20 offset:256\n"
"ds_read_b64_tr_b16 v[60:61], v20 offset:4096\n"
"ds_read_b64_tr_b16 v[62:63], v20 offset:4352\n"
"ds_read_b64_tr_b16 v[152:153], v21 offset:0\n"
"ds_read_b64_tr_b16 v[154:155], v21 offset:256\n"
"ds_read_b64_tr_b16 v[156:157], v21 offset:4096\n"
"ds_read_b64_tr_b16 v[158:159], v21 offset:4352\n"
"ds_read_b64_tr_b16 v[160:161], v22 offset:0\n"
"ds_read_b64_tr_b16 v[162:163], v22 offset:256\n"
"ds_read_b64_tr_b16 v[164:165], v22 offset:4096\n"
"ds_read_b64_tr_b16 v[166:167], v22 offset:4352\n"
"ds_read_b64_tr_b16 v[144:145], v20 offset:24576\n"
"ds_read_b64_tr_b16 v[146:147], v20 offset:24832\n"
"ds_read_b64_tr_b16 v[148:149], v20 offset:28672\n"
"ds_read_b64_tr_b16 v[150:151], v20 offset:28928\n"
"ds_read_b64_tr_b16 v[64:65], v21 offset:24576\n"
"ds_read_b64_tr_b16 v[66:67], v21 offset:24832\n"
"ds_read_b64_tr_b16 v[68:69], v21 offset:28672\n"
"ds_read_b64_tr_b16 v[70:71], v21 offset:28928\n"
"ds_read_b64_tr_b16 a[184:185], v22 offset:24576\n"
"ds_read_b64_tr_b16 a[186:187], v22 offset:24832\n"
"ds_read_b64_tr_b16 a[188:189], v22 offset:28672\n"
"ds_read_b64_tr_b16 a[190:191], v22 offset:28928\n"
"s_waitcnt vmcnt(12)\n"
"s_waitcnt lgkmcnt(0)\n"
"s_barrier\n"
"s_add_u32 m0, s45, 49152\n"
"s_add_u32 s72, s44, 3145728\n"
"buffer_load_dwordx4 v1, s[40:43], s72 offen lds\n"
"s_add_u32 m0, s45, 50176\n"
"s_add_u32 s72, s44, 3244032\n"
"buffer_load_dwordx4 v2, s[40:43], s72 offen lds\n"
"s_add_u32 m0, s45, 51200\n"
"s_add_u32 s72, s44, 3342336\n"
"buffer_load_dwordx4 v1, s[40:43], s72 offen lds\n"
"s_add_u32 m0, s45, 52224\n"
"s_add_u32 s72, s44, 3440640\n"
"buffer_load_dwordx4 v2, s[40:43], s72 offen lds\n"
"s_add_u32 m0, s45, 57344\n"
"s_add_u32 s72, s44, 3145856\n"
"buffer_load_dwordx4 v1, s[40:43], s72 offen lds\n"
"s_add_u32 m0, s45, 58368\n"
"s_add_u32 s72, s44, 3244160\n"
"buffer_load_dwordx4 v2, s[40:43], s72 offen lds\n"
"s_add_u32 m0, s45, 59392\n"
"s_add_u32 s72, s44, 3342464\n"
"buffer_load_dwordx4 v1, s[40:43], s72 offen lds\n"
"s_add_u32 m0, s45, 60416\n"
"s_add_u32 s72, s44, 3440768\n"
"buffer_load_dwordx4 v2, s[40:43], s72 offen lds\n"
"s_add_u32 m0, s45, 65536\n"
"s_add_u32 s72, s44, 3145984\n"
"buffer_load_dwordx4 v1, s[40:43], s72 offen lds\n"
"s_add_u32 m0, s45, 66560\n"
"s_add_u32 s72, s44, 3244288\n"
"buffer_load_dwordx4 v2, s[40:43], s72 offen lds\n"
"s_add_u32 m0, s45, 67584\n"
"s_add_u32 s72, s44, 3342592\n"
"buffer_load_dwordx4 v1, s[40:43], s72 offen lds\n"
"s_add_u32 m0, s45, 68608\n"
"s_add_u32 s72, s44, 3440896\n"
"buffer_load_dwordx4 v2, s[40:43], s72 offen lds\n"
"v_add_u32 v20, 98304, v3\n"
"v_add_u32 v21, 98304, v4\n"
"v_add_u32 v22, 98304, v5\n"
"ds_read_b64_tr_b16 v[168:169], v20 offset:0\n"
"ds_read_b64_tr_b16 v[170:171], v20 offset:256\n"
"ds_read_b64_tr_b16 v[172:173], v20 offset:4096\n"
"ds_read_b64_tr_b16 v[174:175], v20 offset:4352\n"
"ds_read_b64_tr_b16 v[184:185], v21 offset:0\n"
"ds_read_b64_tr_b16 v[186:187], v21 offset:256\n"
"ds_read_b64_tr_b16 v[188:189], v21 offset:4096\n"
"ds_read_b64_tr_b16 v[190:191], v21 offset:4352\n"
"ds_read_b64_tr_b16 v[192:193], v22 offset:0\n"
"ds_read_b64_tr_b16 v[194:195], v22 offset:256\n"
"ds_read_b64_tr_b16 v[196:197], v22 offset:4096\n"
"ds_read_b64_tr_b16 v[198:199], v22 offset:4352\n"
"ds_read_b64_tr_b16 v[176:177], v20 offset:24576\n"
"ds_read_b64_tr_b16 v[178:179], v20 offset:24832\n"
"ds_read_b64_tr_b16 v[180:181], v20 offset:28672\n"
"ds_read_b64_tr_b16 v[182:183], v20 offset:28928\n"
"ds_read_b64_tr_b16 v[72:73], v21 offset:24576\n"
"ds_read_b64_tr_b16 v[74:75], v21 offset:24832\n"
"ds_read_b64_tr_b16 v[76:77], v21 offset:28672\n"
"ds_read_b64_tr_b16 v[78:79], v21 offset:28928\n"
"ds_read_b64_tr_b16 a[192:193], v22 offset:24576\n"
"ds_read_b64_tr_b16 a[194:195], v22 offset:24832\n"
"ds_read_b64_tr_b16 a[196:197], v22 offset:28672\n"
"ds_read_b64_tr_b16 a[198:199], v22 offset:28928\n"
"s_waitcnt vmcnt(12)\n"
"s_waitcnt lgkmcnt(0)\n"
"s_barrier\n"
"s_add_u32 m0, s45, 98304\n"
"s_add_u32 s72, s44, 3932160\n"
"buffer_load_dwordx4 v1, s[40:43], s72 offen lds\n"
"s_add_u32 m0, s45, 99328\n"
"s_add_u32 s72, s44, 4030464\n"
"buffer_load_dwordx4 v2, s[40:43], s72 offen lds\n"
"s_add_u32 m0, s45, 100352\n"
"s_add_u32 s72, s44, 4128768\n"
"buffer_load_dwordx4 v1, s[40:43], s72 offen lds\n"
"s_add_u32 m0, s45, 101376\n"
"s_add_u32 s72, s44, 4227072\n"
"buffer_load_dwordx4 v2, s[40:43], s72 offen lds\n"
"s_add_u32 m0, s45, 106496\n"
"s_add_u32 s72, s44, 3932288\n"
"buffer_load_dwordx4 v1, s[40:43], s72 offen lds\n"
"s_add_u32 m0, s45, 107520\n"
"s_add_u32 s72, s44, 4030592\n"
"buffer_load_dwordx4 v2, s[40:43], s72 offen lds\n"
"s_add_u32 m0, s45, 108544\n"
"s_add_u32 s72, s44, 4128896\n"
"buffer_load_dwordx4 v1, s[40:43], s72 offen lds\n"
"s_add_u32 m0, s45, 109568\n"
"s_add_u32 s72, s44, 4227200\n"
"buffer_load_dwordx4 v2, s[40:43], s72 offen lds\n"
"s_add_u32 m0, s45, 114688\n"
"s_add_u32 s72, s44, 3932416\n"
"buffer_load_dwordx4 v1, s[40:43], s72 offen lds\n"
"s_add_u32 m0, s45, 115712\n"
"s_add_u32 s72, s44, 4030720\n"
"buffer_load_dwordx4 v2, s[40:43], s72 offen lds\n"
"s_add_u32 m0, s45, 116736\n"
"s_add_u32 s72, s44, 4129024\n"
"buffer_load_dwordx4 v1, s[40:43], s72 offen lds\n"
"s_add_u32 m0, s45, 117760\n"
"s_add_u32 s72, s44, 4227328\n"
"buffer_load_dwordx4 v2, s[40:43], s72 offen lds\n"
"v_add_u32 v20, 0, v3\n"
"v_add_u32 v21, 0, v4\n"
"v_add_u32 v22, 0, v5\n"
"ds_read_b64_tr_b16 v[200:201], v20 offset:0\n"
"ds_read_b64_tr_b16 v[202:203], v20 offset:256\n"
"ds_read_b64_tr_b16 v[204:205], v20 offset:4096\n"
"ds_read_b64_tr_b16 v[206:207], v20 offset:4352\n"
"ds_read_b64_tr_b16 v[208:209], v21 offset:0\n"
"ds_read_b64_tr_b16 v[210:211], v21 offset:256\n"
"ds_read_b64_tr_b16 v[212:213], v21 offset:4096\n"
"ds_read_b64_tr_b16 v[214:215], v21 offset:4352\n"
"ds_read_b64_tr_b16 a[40:41], v22 offset:0\n"
"ds_read_b64_tr_b16 a[42:43], v22 offset:256\n"
"ds_read_b64_tr_b16 a[44:45], v22 offset:4096\n"
"ds_read_b64_tr_b16 a[46:47], v22 offset:4352\n"
"ds_read_b64_tr_b16 a[80:81], v20 offset:24576\n"
"ds_read_b64_tr_b16 a[82:83], v20 offset:24832\n"
"ds_read_b64_tr_b16 a[84:85], v20 offset:28672\n"
"ds_read_b64_tr_b16 a[86:87], v20 offset:28928\n"
"ds_read_b64_tr_b16 a[120:121], v21 offset:24576\n"
"ds_read_b64_tr_b16 a[122:123], v21 offset:24832\n"
"ds_read_b64_tr_b16 a[124:125], v21 offset:28672\n"
"ds_read_b64_tr_b16 a[126:127], v21 offset:28928\n"
"ds_read_b64_tr_b16 a[200:201], v22 offset:24576\n"
"ds_read_b64_tr_b16 a[202:203], v22 offset:24832\n"
"ds_read_b64_tr_b16 a[204:205], v22 offset:28672\n"
"ds_read_b64_tr_b16 a[206:207], v22 offset:28928\n"
"s_waitcnt vmcnt(12)\n"
"s_waitcnt lgkmcnt(0)\n"
"s_barrier\n"
"s_add_u32 m0, s45, 0\n"
"s_add_u32 s72, s44, 4718592\n"
"buffer_load_dwordx4 v1, s[40:43], s72 offen lds\n"
"s_add_u32 m0, s45, 1024\n"
"s_add_u32 s72, s44, 4816896\n"
"buffer_load_dwordx4 v2, s[40:43], s72 offen lds\n"
"s_add_u32 m0, s45, 2048\n"
"s_add_u32 s72, s44, 4915200\n"
"buffer_load_dwordx4 v1, s[40:43], s72 offen lds\n"
"s_add_u32 m0, s45, 3072\n"
"s_add_u32 s72, s44, 5013504\n"
"buffer_load_dwordx4 v2, s[40:43], s72 offen lds\n"
"s_add_u32 m0, s45, 8192\n"
"s_add_u32 s72, s44, 4718720\n"
"buffer_load_dwordx4 v1, s[40:43], s72 offen lds\n"
"s_add_u32 m0, s45, 9216\n"
"s_add_u32 s72, s44, 4817024\n"
"buffer_load_dwordx4 v2, s[40:43], s72 offen lds\n"
"s_add_u32 m0, s45, 10240\n"
"s_add_u32 s72, s44, 4915328\n"
"buffer_load_dwordx4 v1, s[40:43], s72 offen lds\n"
"s_add_u32 m0, s45, 11264\n"
"s_add_u32 s72, s44, 5013632\n"
"buffer_load_dwordx4 v2, s[40:43], s72 offen lds\n"
"s_add_u32 m0, s45, 16384\n"
"s_add_u32 s72, s44, 4718848\n"
"buffer_load_dwordx4 v1, s[40:43], s72 offen lds\n"
"s_add_u32 m0, s45, 17408\n"
"s_add_u32 s72, s44, 4817152\n"
"buffer_load_dwordx4 v2, s[40:43], s72 offen lds\n"
"s_add_u32 m0, s45, 18432\n"
"s_add_u32 s72, s44, 4915456\n"
"buffer_load_dwordx4 v1, s[40:43], s72 offen lds\n"
"s_add_u32 m0, s45, 19456\n"
"s_add_u32 s72, s44, 5013760\n"
"buffer_load_dwordx4 v2, s[40:43], s72 offen lds\n"
"v_add_u32 v20, 49152, v3\n"
"v_add_u32 v21, 49152, v4\n"
"v_add_u32 v22, 49152, v5\n"
"ds_read_b64_tr_b16 v[216:217], v20 offset:0\n"
"ds_read_b64_tr_b16 v[218:219], v20 offset:256\n"
"ds_read_b64_tr_b16 v[220:221], v20 offset:4096\n"
"ds_read_b64_tr_b16 v[222:223], v20 offset:4352\n"
"ds_read_b64_tr_b16 v[224:225], v21 offset:0\n"
"ds_read_b64_tr_b16 v[226:227], v21 offset:256\n"
"ds_read_b64_tr_b16 v[228:229], v21 offset:4096\n"
"ds_read_b64_tr_b16 v[230:231], v21 offset:4352\n"
"ds_read_b64_tr_b16 a[48:49], v22 offset:0\n"
"ds_read_b64_tr_b16 a[50:51], v22 offset:256\n"
"ds_read_b64_tr_b16 a[52:53], v22 offset:4096\n"
"ds_read_b64_tr_b16 a[54:55], v22 offset:4352\n"
"ds_read_b64_tr_b16 a[88:89], v20 offset:24576\n"
"ds_read_b64_tr_b16 a[90:91], v20 offset:24832\n"
"ds_read_b64_tr_b16 a[92:93], v20 offset:28672\n"
"ds_read_b64_tr_b16 a[94:95], v20 offset:28928\n"
"ds_read_b64_tr_b16 a[128:129], v21 offset:24576\n"
"ds_read_b64_tr_b16 a[130:131], v21 offset:24832\n"
"ds_read_b64_tr_b16 a[132:133], v21 offset:28672\n"
"ds_read_b64_tr_b16 a[134:135], v21 offset:28928\n"
"ds_read_b64_tr_b16 a[208:209], v22 offset:24576\n"
"ds_read_b64_tr_b16 a[210:211], v22 offset:24832\n"
"ds_read_b64_tr_b16 a[212:213], v22 offset:28672\n"
"ds_read_b64_tr_b16 a[214:215], v22 offset:28928\n"
"s_waitcnt vmcnt(12)\n"
"s_waitcnt lgkmcnt(0)\n"
"s_barrier\n"
"s_add_u32 m0, s45, 49152\n"
"s_add_u32 s72, s44, 5505024\n"
"buffer_load_dwordx4 v1, s[40:43], s72 offen lds\n"
"s_add_u32 m0, s45, 50176\n"
"s_add_u32 s72, s44, 5603328\n"
"buffer_load_dwordx4 v2, s[40:43], s72 offen lds\n"
"s_add_u32 m0, s45, 51200\n"
"s_add_u32 s72, s44, 5701632\n"
"buffer_load_dwordx4 v1, s[40:43], s72 offen lds\n"
"s_add_u32 m0, s45, 52224\n"
"s_add_u32 s72, s44, 5799936\n"
"buffer_load_dwordx4 v2, s[40:43], s72 offen lds\n"
"s_add_u32 m0, s45, 57344\n"
"s_add_u32 s72, s44, 5505152\n"
"buffer_load_dwordx4 v1, s[40:43], s72 offen lds\n"
"s_add_u32 m0, s45, 58368\n"
"s_add_u32 s72, s44, 5603456\n"
"buffer_load_dwordx4 v2, s[40:43], s72 offen lds\n"
"s_add_u32 m0, s45, 59392\n"
"s_add_u32 s72, s44, 5701760\n"
"buffer_load_dwordx4 v1, s[40:43], s72 offen lds\n"
"s_add_u32 m0, s45, 60416\n"
"s_add_u32 s72, s44, 5800064\n"
"buffer_load_dwordx4 v2, s[40:43], s72 offen lds\n"
"s_add_u32 m0, s45, 65536\n"
"s_add_u32 s72, s44, 5505280\n"
"buffer_load_dwordx4 v1, s[40:43], s72 offen lds\n"
"s_add_u32 m0, s45, 66560\n"
"s_add_u32 s72, s44, 5603584\n"
"buffer_load_dwordx4 v2, s[40:43], s72 offen lds\n"
"s_add_u32 m0, s45, 67584\n"
"s_add_u32 s72, s44, 5701888\n"
"buffer_load_dwordx4 v1, s[40:43], s72 offen lds\n"
"s_add_u32 m0, s45, 68608\n"
"s_add_u32 s72, s44, 5800192\n"
"buffer_load_dwordx4 v2, s[40:43], s72 offen lds\n"
"v_add_u32 v20, 98304, v3\n"
"v_add_u32 v21, 98304, v4\n"
"v_add_u32 v22, 98304, v5\n"
"ds_read_b64_tr_b16 v[232:233], v20 offset:0\n"
"ds_read_b64_tr_b16 v[234:235], v20 offset:256\n"
"ds_read_b64_tr_b16 v[236:237], v20 offset:4096\n"
"ds_read_b64_tr_b16 v[238:239], v20 offset:4352\n"
"ds_read_b64_tr_b16 v[240:241], v21 offset:0\n"
"ds_read_b64_tr_b16 v[242:243], v21 offset:256\n"
"ds_read_b64_tr_b16 v[244:245], v21 offset:4096\n"
"ds_read_b64_tr_b16 v[246:247], v21 offset:4352\n"
"ds_read_b64_tr_b16 v[96:97], v22 offset:0\n"
"ds_read_b64_tr_b16 v[98:99], v22 offset:256\n"
"ds_read_b64_tr_b16 v[100:101], v22 offset:4096\n"
"ds_read_b64_tr_b16 v[102:103], v22 offset:4352\n"
"ds_read_b64_tr_b16 v[112:113], v20 offset:24576\n"
"ds_read_b64_tr_b16 v[114:115], v20 offset:24832\n"
"ds_read_b64_tr_b16 v[116:117], v20 offset:28672\n"
"ds_read_b64_tr_b16 v[118:119], v20 offset:28928\n"
"ds_read_b64_tr_b16 a[136:137], v21 offset:24576\n"
"ds_read_b64_tr_b16 a[138:139], v21 offset:24832\n"
"ds_read_b64_tr_b16 a[140:141], v21 offset:28672\n"
"ds_read_b64_tr_b16 a[142:143], v21 offset:28928\n"
"ds_read_b64_tr_b16 a[216:217], v22 offset:24576\n"
"ds_read_b64_tr_b16 a[218:219], v22 offset:24832\n"
"ds_read_b64_tr_b16 a[220:221], v22 offset:28672\n"
"ds_read_b64_tr_b16 a[222:223], v22 offset:28928\n"
"s_waitcnt vmcnt(12)\n"
"s_waitcnt lgkmcnt(0)\n"
"s_barrier\n"
"s_add_u32 m0, s45, 98304\n"
"s_add_u32 s72, s44, 6291456\n"
"buffer_load_dwordx4 v1, s[40:43], s72 offen lds\n"
"s_add_u32 m0, s45, 99328\n"
"s_add_u32 s72, s44, 6389760\n"
"buffer_load_dwordx4 v2, s[40:43], s72 offen lds\n"
"s_add_u32 m0, s45, 100352\n"
"s_add_u32 s72, s44, 6488064\n"
"buffer_load_dwordx4 v1, s[40:43], s72 offen lds\n"
"s_add_u32 m0, s45, 101376\n"
"s_add_u32 s72, s44, 6586368\n"
"buffer_load_dwordx4 v2, s[40:43], s72 offen lds\n"
"s_add_u32 m0, s45, 106496\n"
"s_add_u32 s72, s44, 6291584\n"
"buffer_load_dwordx4 v1, s[40:43], s72 offen lds\n"
"s_add_u32 m0, s45, 107520\n"
"s_add_u32 s72, s44, 6389888\n"
"buffer_load_dwordx4 v2, s[40:43], s72 offen lds\n"
"s_add_u32 m0, s45, 108544\n"
"s_add_u32 s72, s44, 6488192\n"
"buffer_load_dwordx4 v1, s[40:43], s72 offen lds\n"
"s_add_u32 m0, s45, 109568\n"
"s_add_u32 s72, s44, 6586496\n"
"buffer_load_dwordx4 v2, s[40:43], s72 offen lds\n"
"s_add_u32 m0, s45, 114688\n"
"s_add_u32 s72, s44, 6291712\n"
"buffer_load_dwordx4 v1, s[40:43], s72 offen lds\n"
"s_add_u32 m0, s45, 115712\n"
"s_add_u32 s72, s44, 6390016\n"
"buffer_load_dwordx4 v2, s[40:43], s72 offen lds\n"
"s_add_u32 m0, s45, 116736\n"
"s_add_u32 s72, s44, 6488320\n"
"buffer_load_dwordx4 v1, s[40:43], s72 offen lds\n"
"s_add_u32 m0, s45, 117760\n"
"s_add_u32 s72, s44, 6586624\n"
"buffer_load_dwordx4 v2, s[40:43], s72 offen lds\n"
"v_add_u32 v20, 0, v3\n"
"v_add_u32 v21, 0, v4\n"
"v_add_u32 v22, 0, v5\n"
"ds_read_b64_tr_b16 v[248:249], v20 offset:0\n"
"ds_read_b64_tr_b16 v[250:251], v20 offset:256\n"
"ds_read_b64_tr_b16 v[252:253], v20 offset:4096\n"
"ds_read_b64_tr_b16 v[254:255], v20 offset:4352\n"
"ds_read_b64_tr_b16 a[0:1], v21 offset:0\n"
"ds_read_b64_tr_b16 a[2:3], v21 offset:256\n"
"ds_read_b64_tr_b16 a[4:5], v21 offset:4096\n"
"ds_read_b64_tr_b16 a[6:7], v21 offset:4352\n"
"ds_read_b64_tr_b16 a[56:57], v22 offset:0\n"
"ds_read_b64_tr_b16 a[58:59], v22 offset:256\n"
"ds_read_b64_tr_b16 a[60:61], v22 offset:4096\n"
"ds_read_b64_tr_b16 a[62:63], v22 offset:4352\n"
"ds_read_b64_tr_b16 a[96:97], v20 offset:24576\n"
"ds_read_b64_tr_b16 a[98:99], v20 offset:24832\n"
"ds_read_b64_tr_b16 a[100:101], v20 offset:28672\n"
"ds_read_b64_tr_b16 a[102:103], v20 offset:28928\n"
"ds_read_b64_tr_b16 a[144:145], v21 offset:24576\n"
"ds_read_b64_tr_b16 a[146:147], v21 offset:24832\n"
"ds_read_b64_tr_b16 a[148:149], v21 offset:28672\n"
"ds_read_b64_tr_b16 a[150:151], v21 offset:28928\n"
"ds_read_b64_tr_b16 a[224:225], v22 offset:24576\n"
"ds_read_b64_tr_b16 a[226:227], v22 offset:24832\n"
"ds_read_b64_tr_b16 a[228:229], v22 offset:28672\n"
"ds_read_b64_tr_b16 a[230:231], v22 offset:28928\n"
"s_waitcnt vmcnt(12)\n"
"s_waitcnt lgkmcnt(0)\n"
"s_barrier\n"
"s_add_u32 m0, s45, 0\n"
"s_add_u32 s72, s44, 7077888\n"
"buffer_load_dwordx4 v1, s[40:43], s72 offen lds\n"
"s_add_u32 m0, s45, 1024\n"
"s_add_u32 s72, s44, 7176192\n"
"buffer_load_dwordx4 v2, s[40:43], s72 offen lds\n"
"s_add_u32 m0, s45, 2048\n"
"s_add_u32 s72, s44, 7274496\n"
"buffer_load_dwordx4 v1, s[40:43], s72 offen lds\n"
"s_add_u32 m0, s45, 3072\n"
"s_add_u32 s72, s44, 7372800\n"
"buffer_load_dwordx4 v2, s[40:43], s72 offen lds\n"
"s_add_u32 m0, s45, 8192\n"
"s_add_u32 s72, s44, 7078016\n"
"buffer_load_dwordx4 v1, s[40:43], s72 offen lds\n"
"s_add_u32 m0, s45, 9216\n"
"s_add_u32 s72, s44, 7176320\n"
"buffer_load_dwordx4 v2, s[40:43], s72 offen lds\n"
"s_add_u32 m0, s45, 10240\n"
"s_add_u32 s72, s44, 7274624\n"
"buffer_load_dwordx4 v1, s[40:43], s72 offen lds\n"
"s_add_u32 m0, s45, 11264\n"
"s_add_u32 s72, s44, 7372928\n"
"buffer_load_dwordx4 v2, s[40:43], s72 offen lds\n"
"s_add_u32 m0, s45, 16384\n"
"s_add_u32 s72, s44, 7078144\n"
"buffer_load_dwordx4 v1, s[40:43], s72 offen lds\n"
"s_add_u32 m0, s45, 17408\n"
"s_add_u32 s72, s44, 7176448\n"
"buffer_load_dwordx4 v2, s[40:43], s72 offen lds\n"
"s_add_u32 m0, s45, 18432\n"
"s_add_u32 s72, s44, 7274752\n"
"buffer_load_dwordx4 v1, s[40:43], s72 offen lds\n"
"s_add_u32 m0, s45, 19456\n"
"s_add_u32 s72, s44, 7373056\n"
"buffer_load_dwordx4 v2, s[40:43], s72 offen lds\n"
"v_add_u32 v20, 49152, v3\n"
"v_add_u32 v21, 49152, v4\n"
"v_add_u32 v22, 49152, v5\n"
"ds_read_b64_tr_b16 v[80:81], v20 offset:0\n"
"ds_read_b64_tr_b16 v[82:83], v20 offset:256\n"
"ds_read_b64_tr_b16 v[84:85], v20 offset:4096\n"
"ds_read_b64_tr_b16 v[86:87], v20 offset:4352\n"
"ds_read_b64_tr_b16 a[8:9], v21 offset:0\n"
"ds_read_b64_tr_b16 a[10:11], v21 offset:256\n"
"ds_read_b64_tr_b16 a[12:13], v21 offset:4096\n"
"ds_read_b64_tr_b16 a[14:15], v21 offset:4352\n"
"ds_read_b64_tr_b16 a[64:65], v22 offset:0\n"
"ds_read_b64_tr_b16 a[66:67], v22 offset:256\n"
"ds_read_b64_tr_b16 a[68:69], v22 offset:4096\n"
"ds_read_b64_tr_b16 a[70:71], v22 offset:4352\n"
"ds_read_b64_tr_b16 a[104:105], v20 offset:24576\n"
"ds_read_b64_tr_b16 a[106:107], v20 offset:24832\n"
"ds_read_b64_tr_b16 a[108:109], v20 offset:28672\n"
"ds_read_b64_tr_b16 a[110:111], v20 offset:28928\n"
"ds_read_b64_tr_b16 a[152:153], v21 offset:24576\n"
"ds_read_b64_tr_b16 a[154:155], v21 offset:24832\n"
"ds_read_b64_tr_b16 a[156:157], v21 offset:28672\n"
"ds_read_b64_tr_b16 a[158:159], v21 offset:28928\n"
"ds_read_b64_tr_b16 a[232:233], v22 offset:24576\n"
"ds_read_b64_tr_b16 a[234:235], v22 offset:24832\n"
"ds_read_b64_tr_b16 a[236:237], v22 offset:28672\n"
"ds_read_b64_tr_b16 a[238:239], v22 offset:28928\n"
"s_cmp_lg_u32 s66, 0\n"
"s_cbranch_scc1 S8_%=\n"
"s_mov_b32 s76, 131072\n"
"s_mov_b64 exec, 1\n"
"s_branch S10_%=\n"
"S9_%=:\n"
"s_sleep 4\n"
"S10_%=:\n"
"v_mov_b32 v29, 0\n"
"buffer_atomic_add v29, v26, s[36:39], s23 offen sc0 sc1\n"
"s_sub_u32 s76, s76, 1\n"
"s_cbranch_scc1 SABORT_%=\n"
"s_waitcnt vmcnt(0)\n"
"v_xor_b32 v29, v29, v24\n"
"v_cmp_lt_u32 vcc, 1, v29\n"
"s_nop 0\n"
"s_cbranch_vccnz S9_%=\n"
"v_mov_b32 v28, 153600\n"
"ds_write_b32 v28, v29\n"
"v_mov_b32 v24, 1\n"
"s_add_u32 s22, s23, 16384\n"
"buffer_store_dword v24, v26, s[36:39], s22 offen sc1\n"
"s_mov_b64 exec, -1\n"
"S8_%=:\n"
"s_waitcnt lgkmcnt(0)\n"
"s_barrier\n"
"v_mov_b32 v28, 153600\n"
"ds_read_b32 v28, v28\n"
"s_waitcnt lgkmcnt(0)\n"
"v_readfirstlane_b32 s22, v28\n"
"s_nop 0\n"
"s_lshl_b32 s22, s22, 13\n"
"v_xor_b32 v12, s22, v12\n"
"v_xor_b32 v13, s22, v13\n"
"s_xor_b32 s68, s68, s22\n"
"s_xor_b32 s20, s20, s22\n"
"s_waitcnt vmcnt(12)\n"
"s_waitcnt lgkmcnt(0)\n"
"s_barrier\n"
"v_add_u32 v20, 98304, v3\n"
"v_add_u32 v21, 98304, v4\n"
"v_add_u32 v22, 98304, v5\n"
"ds_read_b64_tr_b16 a[16:17], v20 offset:0\n"
"ds_read_b64_tr_b16 a[18:19], v20 offset:256\n"
"ds_read_b64_tr_b16 a[20:21], v20 offset:4096\n"
"ds_read_b64_tr_b16 a[22:23], v20 offset:4352\n"
"ds_read_b64_tr_b16 a[24:25], v21 offset:0\n"
"ds_read_b64_tr_b16 a[26:27], v21 offset:256\n"
"ds_read_b64_tr_b16 a[28:29], v21 offset:4096\n"
"ds_read_b64_tr_b16 a[30:31], v21 offset:4352\n"
"ds_read_b64_tr_b16 a[72:73], v22 offset:0\n"
"ds_read_b64_tr_b16 a[74:75], v22 offset:256\n"
"ds_read_b64_tr_b16 a[76:77], v22 offset:4096\n"
"ds_read_b64_tr_b16 a[78:79], v22 offset:4352\n"
"ds_read_b64_tr_b16 v[120:121], v20 offset:24576\n"
"ds_read_b64_tr_b16 v[122:123], v20 offset:24832\n"
"ds_read_b64_tr_b16 v[124:125], v20 offset:28672\n"
"ds_read_b64_tr_b16 v[126:127], v20 offset:28928\n"
"ds_read_b64_tr_b16 a[160:161], v21 offset:24576\n"
"ds_read_b64_tr_b16 a[162:163], v21 offset:24832\n"
"ds_read_b64_tr_b16 a[164:165], v21 offset:28672\n"
"ds_read_b64_tr_b16 a[166:167], v21 offset:28928\n"
"ds_read_b64_tr_b16 a[240:241], v22 offset:24576\n"
"ds_read_b64_tr_b16 a[242:243], v22 offset:24832\n"
"ds_read_b64_tr_b16 a[244:245], v22 offset:28672\n"
"ds_read_b64_tr_b16 a[246:247], v22 offset:28928\n"
"s_waitcnt vmcnt(0)\n"
"s_waitcnt lgkmcnt(0)\n"
"s_barrier\n"
"v_add_u32 v20, 0, v3\n"
"v_add_u32 v21, 0, v4\n"
"v_add_u32 v22, 0, v5\n"
"ds_read_b64_tr_b16 v[88:89], v20 offset:0\n"
"ds_read_b64_tr_b16 v[90:91], v20 offset:256\n"
"ds_read_b64_tr_b16 v[92:93], v20 offset:4096\n"
"ds_read_b64_tr_b16 v[94:95], v20 offset:4352\n"
"ds_read_b64_tr_b16 a[32:33], v21 offset:0\n"
"ds_read_b64_tr_b16 a[34:35], v21 offset:256\n"
"ds_read_b64_tr_b16 a[36:37], v21 offset:4096\n"
"ds_read_b64_tr_b16 a[38:39], v21 offset:4352\n"
"ds_read_b64_tr_b16 v[104:105], v22 offset:0\n"
"ds_read_b64_tr_b16 v[106:107], v22 offset:256\n"
"ds_read_b64_tr_b16 v[108:109], v22 offset:4096\n"
"ds_read_b64_tr_b16 v[110:111], v22 offset:4352\n"
"ds_read_b64_tr_b16 a[112:113], v20 offset:24576\n"
"ds_read_b64_tr_b16 a[114:115], v20 offset:24832\n"
"ds_read_b64_tr_b16 a[116:117], v20 offset:28672\n"
"ds_read_b64_tr_b16 a[118:119], v20 offset:28928\n"
"ds_read_b64_tr_b16 a[168:169], v21 offset:24576\n"
"ds_read_b64_tr_b16 a[170:171], v21 offset:24832\n"
"ds_read_b64_tr_b16 a[172:173], v21 offset:28672\n"
"ds_read_b64_tr_b16 a[174:175], v21 offset:28928\n"
"ds_read_b64_tr_b16 a[248:249], v22 offset:24576\n"
"ds_read_b64_tr_b16 a[250:251], v22 offset:24832\n"
"ds_read_b64_tr_b16 a[252:253], v22 offset:28672\n"
"ds_read_b64_tr_b16 a[254:255], v22 offset:28928\n"
"s_waitcnt lgkmcnt(0)\n"
"s_barrier\n"
"buffer_load_dword v28, v12, s[36:39], 0 offen sc1\n"
"ds_write_b128 v15, v[32:35] offset:0\n"
"ds_write_b128 v15, v[36:39] offset:1024\n"
"ds_write_b128 v15, v[40:43] offset:2048\n"
"ds_write_b128 v15, v[44:47] offset:3072\n"
"ds_write_b128 v15, v[48:51] offset:4096\n"
"ds_write_b128 v15, v[52:55] offset:5120\n"
"ds_write_b128 v15, v[56:59] offset:6144\n"
"ds_write_b128 v15, v[60:63] offset:7168\n"
"ds_write_b128 v15, v[64:67] offset:8192\n"
"ds_write_b128 v15, v[68:71] offset:9216\n"
"ds_write_b128 v15, v[72:75] offset:10240\n"
"ds_write_b128 v15, v[76:79] offset:11264\n"
"ds_write_b128 v15, v[80:83] offset:12288\n"
"ds_write_b128 v15, v[84:87] offset:13312\n"
"ds_write_b128 v15, v[88:91] offset:14336\n"
"ds_write_b128 v15, v[92:95] offset:15360\n"
"ds_write_b128 v15, v[96:99] offset:16384\n"
"ds_write_b128 v15, v[100:103] offset:17408\n"
"ds_write_b128 v15, v[104:107] offset:18432\n"
"ds_write_b128 v15, v[108:111] offset:19456\n"
"ds_write_b128 v15, v[112:115] offset:20480\n"
"ds_write_b128 v15, v[116:119] offset:21504\n"
"ds_write_b128 v15, v[120:123] offset:22528\n"
"ds_write_b128 v15, v[124:127] offset:23552\n"
"s_waitcnt lgkmcnt(0)\n"
"ds_read_b128 v[96:99], v15 offset:0\n"
"ds_read_b128 v[100:103], v15 offset:1024\n"
"ds_read_b128 v[72:75], v15 offset:2048\n"
"ds_read_b128 v[76:79], v15 offset:3072\n"
"ds_read_b128 v[80:83], v15 offset:4096\n"
"ds_read_b128 v[84:87], v15 offset:5120\n"
"ds_read_b128 v[88:91], v15 offset:6144\n"
"ds_read_b128 v[92:95], v15 offset:7168\n"
"ds_read_b128 v[104:107], v15 offset:8192\n"
"ds_read_b128 v[108:111], v15 offset:9216\n"
"ds_read_b128 v[112:115], v15 offset:10240\n"
"ds_read_b128 v[116:119], v15 offset:11264\n"
"v_mov_b32 v32, 0\n"
"v_mov_b32 v33, 0\n"
"v_mov_b32 v34, 0\n"
"v_mov_b32 v35, 0\n"
"v_mov_b32 v36, 0\n"
"v_mov_b32 v37, 0\n"
"v_mov_b32 v38, 0\n"
"v_mov_b32 v39, 0\n"
"v_mov_b32 v40, 0\n"
"v_mov_b32 v41, 0\n"
"v_mov_b32 v42, 0\n"
"v_mov_b32 v43, 0\n"
"v_mov_b32 v44, 0\n"
"v_mov_b32 v45, 0\n"
"v_mov_b32 v46, 0\n"
"v_mov_b32 v47, 0\n"
"v_mov_b32 v48, 0\n"
"v_mov_b32 v49, 0\n"
"v_mov_b32 v50, 0\n"
"v_mov_b32 v51, 0\n"
"v_mov_b32 v52, 0\n"
"v_mov_b32 v53, 0\n"
"v_mov_b32 v54, 0\n"
"v_mov_b32 v55, 0\n"
"v_mov_b32 v120, 0\n"
"v_mov_b32 v121, 0\n"
"v_mov_b32 v122, 0\n"
"v_mov_b32 v123, 0\n"
"v_mov_b32 v124, 0\n"
"v_mov_b32 v125, 0\n"
"v_mov_b32 v126, 0\n"
"v_mov_b32 v127, 0\n"
"s_waitcnt vmcnt(0)\n"
"s_nop 3\n"
"S12_%=:\n"
"s_mov_b32 s45, 0\n"
"s_cmp_eq_u32 s73, 63\n"
"s_cselect_b32 s49, 234881024, s68\n"
"s_xor_b32 s49, s49, 0\n"
"s_cmp_gt_u32 s69, s73\n"
"s_cbranch_scc1 S13_%=\n"
"s_add_u32 s75, s73, 1\n"
"v_cmp_gt_u32 vcc, s75, v28\n"
"s_nop 0\n"
"s_cbranch_vccz S17_%=\n"
"s_mov_b32 s76, 131072\n"
"s_branch S19_%=\n"
"S18_%=:\n"
"s_sleep 2\n"
"S19_%=:\n"
"buffer_load_dword v28, v12, s[36:39], 0 offen sc1\n"
"s_sub_u32 s76, s76, 1\n"
"s_cbranch_scc1 SABORT_%=\n"
"s_waitcnt vmcnt(0)\n"
"v_cmp_gt_u32 vcc, s75, v28\n"
"s_nop 0\n"
"s_cbranch_vccnz S18_%=\n"
"S17_%=:\n"
"s_add_u32 s51, s50, s47\n"
"s_add_u32 m0, s51, 0\n"
"s_add_u32 s72, s46, 0\n"
"buffer_load_dwordx4 v11, s[36:39], s72 offen sc1 lds\n"
"s_add_u32 m0, s51, 1024\n"
"s_add_u32 s72, s46, 2048\n"
"buffer_load_dwordx4 v11, s[36:39], s72 offen sc1 lds\n"
"s_add_u32 m0, s51, 2048\n"
"s_add_u32 s72, s46, 4096\n"
"buffer_load_dwordx4 v11, s[36:39], s72 offen sc1 lds\n"
"s_add_u32 s46, s46, 393216\n"
"s_sub_u32 s50, 53248, s50\n"
"s_add_u32 s69, s69, 1\n"
"S13_%=:\n"
"s_add_u32 s75, s73, 2\n"
"s_min_u32 s75, s75, 64\n"
"v_cmp_gt_u32 vcc, s75, v28\n"
"s_nop 0\n"
"s_cmp_eq_u64 vcc, 0\n"
"s_cselect_b32 s70, s46, 234881024\n"
"s_cselect_b32 s71, 393216, 0\n"
"s_cselect_b32 s72, 1, 0\n"
"s_cselect_b32 s75, 53248, 0\n"
"s_add_u32 m0, s50, s47\n"
"s_add_u32 s46, s46, s71\n"
"s_add_u32 s69, s69, s72\n"
"s_xor_b32 s50, s50, s75\n"
"s_add_u32 s71, s70, 1024\n"
"s_add_u32 s72, s70, 2048\n"
"s_add_u32 s60, s68, 5120\n"
"s_add_u32 s61, s68, 6144\n"
"s_add_u32 s62, s68, 7168\n"
"s_waitcnt lgkmcnt(0)\n"
"s_barrier\n"
"buffer_load_dwordx4 v[56:59], v0, s[36:39], s60 offen offset:0 sc1\n"
"s_nop 12\n"
"buffer_load_dwordx4 v[60:63], v0, s[36:39], s61 offen offset:0 sc1\n"
"s_nop 12\n"
"buffer_load_dwordx4 v[64:67], v0, s[36:39], s62 offen offset:0 sc1\n"
"buffer_load_dword v30, v0, s[36:39], s38 offen\n"
"S20_%=:\n"
"s_waitcnt vmcnt(3)\n"
"v_smfmac_f32_16x16x64_bf16 v[32:35], v[56:59], v[96:103], v6\n"
"ds_write_b128 v0, v[56:59] offset:0\n"
"v_smfmac_f32_16x16x64_bf16 v[44:47], v[56:59], v[72:79], v6\n"
"v_smfmac_f32_16x16x64_bf16 v[36:39], v[56:59], v[128:135], v6\n"
"v_smfmac_f32_16x16x64_bf16 v[48:51], v[56:59], v[80:87], v6\n"
"s_waitcnt vmcnt(2)\n"
"ds_write_b128 v0, v[60:63] offset:1024\n"
"v_smfmac_f32_16x16x64_bf16 v[40:43], v[56:59], v[136:143], v6\n"
"v_max_u32 v29, v59, v63\n"
"v_smfmac_f32_16x16x64_bf16 v[32:35], v[60:63], v[88:95], v6\n"
"v_smfmac_f32_16x16x64_bf16 v[44:47], v[60:63], v[144:151], v6\n"
"s_waitcnt vmcnt(1)\n"
"ds_write_b128 v0, v[64:67] offset:2048\n"
"v_smfmac_f32_16x16x64_bf16 v[36:39], v[60:63], v[152:159], v6\n"
"v_max_u32 v29, v29, v67\n"
"v_smfmac_f32_16x16x64_bf16 v[48:51], v[60:63], v[104:111], v6\n"
"v_cmp_eq_u32 vcc, -1, v29\n"
"v_smfmac_f32_16x16x64_bf16 v[40:43], v[60:63], v[160:167], v6\n"
"s_cbranch_vccnz S14_%=\n"
"v_smfmac_f32_16x16x64_bf16 v[32:35], v[64:67], v[168:175], v6\n"
"s_waitcnt lgkmcnt(0)\n"
"s_barrier\n"
"ds_read_b128 v[68:71], v0 offset:3072\n"
"v_smfmac_f32_16x16x64_bf16 v[44:47], v[64:67], v[176:183], v6\n"
"ds_read_b128 v[72:75], v0 offset:4096\n"
"v_smfmac_f32_16x16x64_bf16 v[36:39], v[64:67], v[184:191], v6\n"
"ds_read_b128 v[76:79], v0 offset:5120\n"
"v_smfmac_f32_16x16x64_bf16 v[48:51], v[64:67], v[112:119], v6\n"
"v_smfmac_f32_16x16x64_bf16 v[40:43], v[64:67], v[192:199], v6\n"
"ds_read_b128 v[96:99], v15 offset:12288\n"
"s_waitcnt lgkmcnt(3)\n"
"v_smfmac_f32_16x16x64_bf16 v[32:35], v[68:71], v[200:207], v6\n"
"ds_read_b128 v[80:83], v0 offset:6144\n"
"v_smfmac_f32_16x16x64_bf16 v[36:39], v[68:71], v[208:215], v6\n"
"ds_read_b128 v[100:103], v15 offset:13312\n"
"s_waitcnt lgkmcnt(4)\n"
"v_smfmac_f32_16x16x64_bf16 v[32:35], v[72:75], v[216:223], v6\n"
"ds_read_b128 v[84:87], v0 offset:7168\n"
"v_smfmac_f32_16x16x64_bf16 v[36:39], v[72:75], v[224:231], v6\n"
"ds_read_b128 v[104:107], v15 offset:14336\n"
"s_waitcnt lgkmcnt(5)\n"
"v_smfmac_f32_16x16x64_bf16 v[32:35], v[76:79], v[232:239], v6\n"
"ds_read_b128 v[88:91], v0 offset:8192\n"
"v_smfmac_f32_16x16x64_bf16 v[36:39], v[76:79], v[240:247], v6\n"
"ds_read_b128 v[108:111], v15 offset:15360\n"
"s_waitcnt lgkmcnt(5)\n"
"v_smfmac_f32_16x16x64_bf16 v[32:35], v[80:83], v[248:255], v6\n"
"ds_read_b128 v[92:95], v0 offset:9216\n"
"v_smfmac_f32_16x16x64_bf16 v[36:39], v[80:83], a[0:7], v6\n"
"ds_read_b128 v[112:115], v15 offset:16384\n"
"s_waitcnt lgkmcnt(5)\n"
"v_smfmac_f32_16x16x64_bf16 v[32:35], v[84:87], v[96:103], v6\n"
"v_smfmac_f32_16x16x64_bf16 v[36:39], v[84:87], a[8:15], v6\n"
"ds_read_b128 v[116:119], v15 offset:17408\n"
"s_waitcnt lgkmcnt(4)\n"
"v_smfmac_f32_16x16x64_bf16 v[32:35], v[88:91], a[16:23], v6\n"
"v_smfmac_f32_16x16x64_bf16 v[36:39], v[88:91], a[24:31], v6\n"
"ds_read_b128 v[96:99], v15 offset:18432\n"
"s_waitcnt lgkmcnt(3)\n"
"v_smfmac_f32_16x16x64_bf16 v[32:35], v[92:95], v[104:111], v6\n"
"ds_read_b128 v[100:103], v15 offset:19456\n"
"v_smfmac_f32_16x16x64_bf16 v[36:39], v[92:95], a[32:39], v6\n"
"v_smfmac_f32_16x16x64_bf16 v[40:43], v[68:71], a[40:47], v6\n"
"ds_read_b128 v[104:107], v15 offset:20480\n"
"v_smfmac_f32_16x16x64_bf16 v[40:43], v[72:75], a[48:55], v6\n"
"ds_read_b128 v[108:111], v15 offset:21504\n"
"s_waitcnt lgkmcnt(4)\n"
"v_smfmac_f32_16x16x64_bf16 v[40:43], v[76:79], v[112:119], v6\n"
"v_add_f32 v32, v32, v34\n"
"v_smfmac_f32_16x16x64_bf16 v[40:43], v[80:83], a[56:63], v6\n"
"ds_read_b128 v[112:115], v15 offset:22528\n"
"v_add_f32 v33, v33, v35\n"
"v_smfmac_f32_16x16x64_bf16 v[40:43], v[84:87], a[64:71], v6\n"
"v_cvt_pk_bf16_f32 v32, v32, v33\n"
"v_smfmac_f32_16x16x64_bf16 v[40:43], v[88:91], a[72:79], v6\n"
"ds_read_b128 v[116:119], v15 offset:23552\n"
"v_add_f32 v36, v36, v38\n"
"s_waitcnt lgkmcnt(4)\n"
"v_smfmac_f32_16x16x64_bf16 v[40:43], v[92:95], v[96:103], v6\n"
"ds_write_b32 v7, v32 offset:0\n"
"v_add_f32 v37, v37, v39\n"
"v_smfmac_f32_16x16x64_bf16 v[44:47], v[68:71], a[80:87], v6\n"
"ds_read_b128 v[96:99], v19 offset:0\n"
"v_cvt_pk_bf16_f32 v36, v36, v37\n"
"v_smfmac_f32_16x16x64_bf16 v[44:47], v[72:75], a[88:95], v6\n"
"ds_read_b128 v[100:103], v19 offset:1024\n"
"s_waitcnt lgkmcnt(5)\n"
"v_smfmac_f32_16x16x64_bf16 v[44:47], v[76:79], v[104:111], v6\n"
"ds_write_b32 v7, v36 offset:1024\n"
"v_smfmac_f32_16x16x64_bf16 v[44:47], v[80:83], a[96:103], v6\n"
"ds_read_b128 v[104:107], v19 offset:6144\n"
"v_add_f32 v40, v40, v42\n"
"v_smfmac_f32_16x16x64_bf16 v[44:47], v[84:87], a[104:111], v6\n"
"ds_read_b128 v[108:111], v19 offset:7168\n"
"v_add_f32 v41, v41, v43\n"
"s_waitcnt lgkmcnt(6)\n"
"v_smfmac_f32_16x16x64_bf16 v[44:47], v[88:91], v[112:119], v6\n"
"v_cvt_pk_bf16_f32 v40, v40, v41\n"
"v_smfmac_f32_16x16x64_bf16 v[44:47], v[92:95], a[112:119], v6\n"
"ds_write_b32 v7, v40 offset:2048\n"
"v_smfmac_f32_16x16x64_bf16 v[48:51], v[68:71], a[120:127], v6\n"
"ds_read_b128 v[112:115], v19 offset:2048\n"
"v_smfmac_f32_16x16x64_bf16 v[48:51], v[72:75], a[128:135], v6\n"
"ds_read_b128 v[116:119], v19 offset:3072\n"
"v_smfmac_f32_16x16x64_bf16 v[48:51], v[76:79], a[136:143], v6\n"
"buffer_load_dwordx4 v11, s[36:39], s70 offen offset:0 sc1 lds\n"
"s_nop 0\n"
"v_add_f32 v44, v44, v46\n"
"v_smfmac_f32_16x16x64_bf16 v[48:51], v[80:83], a[144:151], v6\n"
"s_waitcnt lgkmcnt(0)\n"
"s_barrier\n"
"ds_read_b64_tr_b16 v[32:33], v8 offset:0\n"
"v_add_f32 v45, v45, v47\n"
"v_smfmac_f32_16x16x64_bf16 v[48:51], v[84:87], a[152:159], v6\n"
"ds_read_b64_tr_b16 v[34:35], v8 offset:256\n"
"ds_read_b64_tr_b16 v[36:37], v8 offset:1024\n"
"v_smfmac_f32_16x16x64_bf16 v[48:51], v[88:91], a[160:167], v6\n"
"ds_read_b64_tr_b16 v[38:39], v8 offset:1280\n"
"v_cvt_pk_bf16_f32 v44, v44, v45\n"
"v_smfmac_f32_16x16x64_bf16 v[48:51], v[92:95], a[168:175], v6\n"
"buffer_load_dwordx4 v11, s[36:39], s71 offen offset:1024 sc1 lds\n"
"ds_write_b32 v7, v44 offset:3072\n"
"v_smfmac_f32_16x16x64_bf16 v[52:55], v[56:59], a[176:183], v6\n"
"ds_read_b64_tr_b16 v[40:41], v8 offset:2048\n"
"ds_read_b64_tr_b16 v[42:43], v8 offset:2304\n"
"v_smfmac_f32_16x16x64_bf16 v[52:55], v[60:63], a[184:191], v6\n"
"ds_read_b128 v[56:59], v19 offset:8192\n"
"v_smfmac_f32_16x16x64_bf16 v[52:55], v[64:67], a[192:199], v6\n"
"ds_read_b128 v[60:63], v19 offset:9216\n"
"v_add_f32 v48, v48, v50\n"
"v_add_f32 v49, v49, v51\n"
"v_smfmac_f32_16x16x64_bf16 v[52:55], v[68:71], a[200:207], v6\n"
"buffer_load_dwordx4 v11, s[36:39], s72 offen offset:2048 sc1 lds\n"
"ds_read_b128 v[64:67], v19 offset:4096\n"
"v_cvt_pk_bf16_f32 v48, v48, v49\n"
"v_smfmac_f32_16x16x64_bf16 v[52:55], v[72:75], a[208:215], v6\n"
"ds_write_b32 v7, v48 offset:4096\n"
"v_smfmac_f32_16x16x64_bf16 v[52:55], v[76:79], a[216:223], v6\n"
"ds_read_b128 v[68:71], v19 offset:5120\n"
"v_smfmac_f32_16x16x64_bf16 v[52:55], v[80:83], a[224:231], v6\n"
"ds_read_b128 v[72:75], v19 offset:10240\n"
"v_smfmac_f32_16x16x64_bf16 v[52:55], v[84:87], a[232:239], v6\n"
"ds_read_b128 v[76:79], v19 offset:11264\n"
"v_smfmac_f32_16x16x64_bf16 v[52:55], v[88:91], a[240:247], v6\n"
"s_waitcnt lgkmcnt(3)\n"
"s_barrier\n"
"ds_read_b64_tr_b16 v[80:81], v8 offset:3072\n"
"ds_read_b64_tr_b16 v[82:83], v8 offset:3328\n"
"v_smfmac_f32_16x16x64_bf16 v[52:55], v[92:95], a[248:255], v6\n"
"ds_read_b64_tr_b16 v[84:85], v8 offset:4096\n"
"ds_read_b64_tr_b16 v[86:87], v8 offset:4352\n"
"v_smfmac_f32_16x16x64_bf16 v[120:123], v[32:35], v[96:103], v6\n"
"v_smfmac_f32_16x16x64_bf16 v[124:127], v[32:35], v[104:111], v6\n"
"s_nop 3\n"
"v_add_f32 v52, v52, v54\n"
"v_add_f32 v53, v53, v55\n"
"v_cvt_pk_bf16_f32 v52, v52, v53\n"
"ds_write_b32 v7, v52 offset:5120\n"
"v_smfmac_f32_16x16x64_bf16 v[120:123], v[36:39], v[112:119], v6\n"
"v_smfmac_f32_16x16x64_bf16 v[124:127], v[36:39], v[56:63], v6\n"
"s_waitcnt lgkmcnt(7)\n"
"v_smfmac_f32_16x16x64_bf16 v[120:123], v[40:43], v[64:71], v6\n"
"s_waitcnt lgkmcnt(5)\n"
"v_smfmac_f32_16x16x64_bf16 v[124:127], v[40:43], v[72:79], v6\n"
"s_mov_b64 vcc, s[82:83]\n"
"s_waitcnt lgkmcnt(0)\n"
"s_barrier\n"
"ds_read_b64_tr_b16 v[88:89], v8 offset:5120\n"
"ds_read_b64_tr_b16 v[90:91], v8 offset:5376\n"
"v_mfma_f32_16x16x32_bf16 v[20:23], v[32:35], v[80:83], 0\n"
"v_add_f32 v120, v120, v122\n"
"v_add_f32 v121, v121, v123\n"
"v_mfma_f32_16x16x32_bf16 v[20:23], v[36:39], v[84:87], v[20:23]\n"
"v_add_f32 v124, v124, v126\n"
"v_add_f32 v125, v125, v127\n"
"v_cndmask_b32_dpp v97, v120, v121, vcc quad_perm:[2,3,2,3] row_mask:0xf bank_mask:0xf\n"
"s_nop 0\n"
"v_cndmask_b32_dpp v99, v124, v125, vcc quad_perm:[2,3,2,3] row_mask:0xf bank_mask:0xf\n"
"s_waitcnt lgkmcnt(0)\n"
"v_mfma_f32_16x16x32_bf16 v[20:23], v[40:43], v[88:91], v[20:23]\n"
"s_not_b64 vcc, s[82:83]\n"
"v_cndmask_b32_dpp v96, v121, v120, vcc quad_perm:[0,1,0,1] row_mask:0xf bank_mask:0xf\n"
"v_cndmask_b32_dpp v98, v125, v124, vcc quad_perm:[0,1,0,1] row_mask:0xf bank_mask:0xf\n"
"v_max_f32 v24, v97, v99\n"
"v_max3_f32 v24, v24, v96, v98\n"
"s_nop 1\n"
"v_max_f32_dpp v24, v24, v24 quad_perm:[1,0,3,2] row_mask:0xf bank_mask:0xf\n"
"v_add_f32_dpp v3, v22, v20 quad_perm:[2,3,2,3] row_mask:0xf bank_mask:0xf\n"
"v_add_f32_dpp v4, v21, v23 quad_perm:[0,1,0,1] row_mask:0xf bank_mask:0xf\n"
"v_max_f32_dpp v24, v24, v24 row_ror:4 row_mask:0xf bank_mask:0xf\n"
"v_cndmask_b32 v100, v4, v3, vcc\n"
"s_nop 0\n"
"v_max_f32_dpp v24, v24, v24 row_ror:8 row_mask:0xf bank_mask:0xf\n"
"v_mul_f32 v25, s74, v24\n"
"S16_%=:\n"
"v_fma_f32 v102, v96, s74, -v25\n"
"v_fma_f32 v103, v97, s74, -v25\n"
"v_fma_f32 v104, v98, s74, -v25\n"
"v_fma_f32 v105, v99, s74, -v25\n"
"v_fma_f32 v3, v100, s74, -v25\n"
"v_exp_f32 v102, v102\n"
"v_exp_f32 v103, v103\n"
"v_exp_f32 v104, v104\n"
"v_exp_f32 v105, v105\n"
"v_exp_f32 v5, v3\n"
"v_add_f32 v26, v102, v103\n"
"v_add_f32 v4, v104, v105\n"
"v_add_f32 v26, v26, v5\n"
"v_add_f32 v26, v26, v4\n"
"v_cmp_nge_f32 vcc, s81, v3\n"
"s_nop 0\n"
"v_add_f32_dpp v26, v26, v26 quad_perm:[1,0,3,2] row_mask:0xf bank_mask:0xf\n"
"s_nop 1\n"
"v_add_f32_dpp v26, v26, v26 row_ror:4 row_mask:0xf bank_mask:0xf\n"
"s_cbranch_vccnz S15_%=\n"
"S15ON_%=:\n"
"s_nop 0\n"
"v_add_f32_dpp v26, v26, v26 row_ror:8 row_mask:0xf bank_mask:0xf\n"
"v_rcp_f32 v26, v26\n"
"s_nop 0\n"
"v_mul_f32 v102, v102, v26\n"
"v_mul_f32 v103, v103, v26\n"
"v_mul_f32 v104, v104, v26\n"
"v_mul_f32 v105, v105, v26\n"
"v_cvt_pk_bf16_f32 v2, v102, v103\n"
"v_cvt_pk_bf16_f32 v3, v104, v105\n"
"v_min_u32 v2, -2, v2\n"
"v_min_u32 v3, -2, v3\n"
"buffer_store_dwordx2 v[2:3], v10, s[36:39], s49 offen sc1\n"
"s_nop 3\n"
"s_add_u32 s75, s73, 3\n"
"s_min_u32 s75, s75, 64\n"
"v_cmp_gt_u32 vcc, s75, v28\n"
"s_nop 0\n"
"s_mov_b64 exec, vcc\n"
"buffer_load_dword v28, v12, s[36:39], 0 offen sc1\n"
"s_mov_b64 exec, -1\n"
"s_mov_b64 vcc, s[82:83]\n"
"v_cndmask_b32_dpp v57, v80, v82, vcc quad_perm:[2,3,0,1] row_mask:0xf bank_mask:0xf\n"
"v_cndmask_b32_dpp v59, v81, v83, vcc quad_perm:[2,3,0,1] row_mask:0xf bank_mask:0xf\n"
"s_not_b64 vcc, s[82:83]\n"
"v_cndmask_b32_dpp v56, v82, v80, vcc quad_perm:[2,3,0,1] row_mask:0xf bank_mask:0xf\n"
"v_cndmask_b32_dpp v58, v83, v81, vcc quad_perm:[2,3,0,1] row_mask:0xf bank_mask:0xf\n"
"v_min_u32 v56, -2, v56\n"
"v_min_u32 v57, -2, v57\n"
"v_min_u32 v58, -2, v58\n"
"v_min_u32 v59, -2, v59\n"
"buffer_store_dwordx4 v[56:59], v9, s[36:39], s48 offen sc1\n"
"s_add_u32 s48, s48, 98304\n"
"s_add_u32 s73, s73, 1\n"
"s_add_u32 s79, s68, 131072\n"
"s_and_b32 s79, s79, 0x3fffff\n"
"s_xor_b32 s68, s79, s68\n"
"s_lshr_b32 s68, s68, 8\n"
"s_and_b32 s68, s68, 8192\n"
"s_xor_b32 s68, s68, s79\n"
"v_xor_b32 v19, 53248, v19\n"
"s_cmp_lt_u32 s73, 64\n"
"s_cbranch_scc0 S11_%=\n"
"ds_read_b128 v[96:99], v15 offset:0\n"
"ds_read_b128 v[100:103], v15 offset:1024\n"
"ds_read_b128 v[72:75], v15 offset:2048\n"
"ds_read_b128 v[76:79], v15 offset:3072\n"
"ds_read_b128 v[80:83], v15 offset:4096\n"
"ds_read_b128 v[84:87], v15 offset:5120\n"
"ds_read_b128 v[88:91], v15 offset:6144\n"
"ds_read_b128 v[92:95], v15 offset:7168\n"
"ds_read_b128 v[104:107], v15 offset:8192\n"
"ds_read_b128 v[108:111], v15 offset:9216\n"
"ds_read_b128 v[112:115], v15 offset:10240\n"
"ds_read_b128 v[116:119], v15 offset:11264\n"
"v_mov_b32 v32, 0\n"
"v_mov_b32 v33, 0\n"
"v_mov_b32 v34, 0\n"
"v_mov_b32 v35, 0\n"
"v_mov_b32 v36, 0\n"
"v_mov_b32 v37, 0\n"
"v_mov_b32 v38, 0\n"
"v_mov_b32 v39, 0\n"
"v_mov_b32 v40, 0\n"
"v_mov_b32 v41, 0\n"
"v_mov_b32 v42, 0\n"
"v_mov_b32 v43, 0\n"
"v_mov_b32 v44, 0\n"
"v_mov_b32 v45, 0\n"
"v_mov_b32 v46, 0\n"
"v_mov_b32 v47, 0\n"
"v_mov_b32 v48, 0\n"
"v_mov_b32 v49, 0\n"
"v_mov_b32 v50, 0\n"
"v_mov_b32 v51, 0\n"
"v_mov_b32 v52, 0\n"
"v_mov_b32 v53, 0\n"
"v_mov_b32 v54, 0\n"
"v_mov_b32 v55, 0\n"
"v_mov_b32 v120, 0\n"
"v_mov_b32 v121, 0\n"
"v_mov_b32 v122, 0\n"
"v_mov_b32 v123, 0\n"
"v_mov_b32 v124, 0\n"
"v_mov_b32 v125, 0\n"
"v_mov_b32 v126, 0\n"
"v_mov_b32 v127, 0\n"
"s_waitcnt vmcnt(1)\n"
"s_cmp_lg_u32 s73, 63\n"
"s_cbranch_scc1 S12_%=\n"
"s_waitcnt vmcnt(0)\n"
"s_branch S12_%=\n"
"S14_%=:\n"
"s_waitcnt lgkmcnt(0)\n"
"s_mov_b32 s76, 131072\n"
"S21_%=:\n"
"s_memrealtime s[4:5]\n"
"buffer_load_dwordx4 v[56:59], v0, s[36:39], s60 offen offset:0 sc1\n"
"buffer_load_dwordx4 v[60:63], v0, s[36:39], s61 offen offset:0 sc1\n"
"buffer_load_dwordx4 v[64:67], v0, s[36:39], s62 offen offset:0 sc1\n"
"v_mov_b32 v29, 0\n"
"ds_read_b32 v31, v29 offset:16896\n"
"s_lshl_b32 s77, s73, 14\n"
"s_add_u32 s77, s77, s20\n"
"s_add_u32 s77, s77, s21\n"
"s_add_u32 s77, s77, 4096\n"
"v_mov_b32 v32, 0\n"
"v_mov_b32 v33, 0\n"
"v_mov_b32 v34, 0\n"
"v_mov_b32 v35, 0\n"
"v_mov_b32 v36, 0\n"
"v_mov_b32 v37, 0\n"
"v_mov_b32 v38, 0\n"
"v_mov_b32 v39, 0\n"
"v_mov_b32 v40, 0\n"
"v_mov_b32 v41, 0\n"
"v_mov_b32 v42, 0\n"
"v_mov_b32 v43, 0\n"
"v_mov_b32 v44, 0\n"
"v_mov_b32 v45, 0\n"
"v_mov_b32 v46, 0\n"
"v_mov_b32 v47, 0\n"
"v_mov_b32 v48, 0\n"
"v_mov_b32 v49, 0\n"
"v_mov_b32 v50, 0\n"
"v_mov_b32 v51, 0\n"
"v_mov_b32 v52, 0\n"
"v_mov_b32 v53, 0\n"
"v_mov_b32 v54, 0\n"
"v_mov_b32 v55, 0\n"
"s_sub_u32 s76, s76, 1\n"
"s_cbranch_scc1 SABORT_%=\n"
"s_waitcnt vmcnt(2)\n"
"v_max3_u32 v29, v56, v57, 0\n"
"v_max3_u32 v29, v58, v59, v29\n"
"s_waitcnt vmcnt(1)\n"
"v_max3_u32 v29, v60, v61, v29\n"
"v_max3_u32 v29, v62, v63, v29\n"
"s_waitcnt vmcnt(0)\n"
"v_max3_u32 v29, v64, v65, v29\n"
"v_max3_u32 v29, v66, v67, v29\n"
"v_cmp_eq_u32 vcc, -1, v29\n"
"s_nop 0\n"
"s_cbranch_vccnz S21_%=\n"
"s_waitcnt lgkmcnt(0)\n"
"v_lshl_add_u32 v31, s4, 3, v31\n"
"v_lshrrev_b32 v30, 2, v0\n"
"s_mov_b64 exec, 0xffff\n"
"buffer_store_dword v31, v30, s[36:39], s77 offen sc1\n"
"s_mov_b64 exec, -1\n"
"s_branch S20_%=\n"
"S15_%=:\n"
"s_cmp_lg_u32 s45, 0\n"
"s_cbranch_scc1 S22_%=\n"
"v_mov_b32 v3, 0\n"
"v_and_b32 v4, 0x7fff, v80\n"
"v_max_u32 v3, v3, v4\n"
"v_bfe_u32 v4, v80, 16, 15\n"
"v_max_u32 v3, v3, v4\n"
"v_and_b32 v4, 0x7fff, v81\n"
"v_max_u32 v3, v3, v4\n"
"v_bfe_u32 v4, v81, 16, 15\n"
"v_max_u32 v3, v3, v4\n"
"v_and_b32 v4, 0x7fff, v82\n"
"v_max_u32 v3, v3, v4\n"
"v_bfe_u32 v4, v82, 16, 15\n"
"v_max_u32 v3, v3, v4\n"
"v_and_b32 v4, 0x7fff, v83\n"
"v_max_u32 v3, v3, v4\n"
"v_bfe_u32 v4, v83, 16, 15\n"
"v_max_u32 v3, v3, v4\n"
"v_and_b32 v4, 0x7fff, v84\n"
"v_max_u32 v3, v3, v4\n"
"v_bfe_u32 v4, v84, 16, 15\n"
"v_max_u32 v3, v3, v4\n"
"v_and_b32 v4, 0x7fff, v85\n"
"v_max_u32 v3, v3, v4\n"
"v_bfe_u32 v4, v85, 16, 15\n"
"v_max_u32 v3, v3, v4\n"
"v_and_b32 v4, 0x7fff, v86\n"
"v_max_u32 v3, v3, v4\n"
"v_bfe_u32 v4, v86, 16, 15\n"
"v_max_u32 v3, v3, v4\n"
"v_and_b32 v4, 0x7fff, v87\n"
"v_max_u32 v3, v3, v4\n"
"v_bfe_u32 v4, v87, 16, 15\n"
"v_max_u32 v3, v3, v4\n"
"v_and_b32 v4, 0x7fff, v88\n"
"v_max_u32 v3, v3, v4\n"
"v_bfe_u32 v4, v88, 16, 15\n"
"v_max_u32 v3, v3, v4\n"
"v_and_b32 v4, 0x7fff, v89\n"
"v_max_u32 v3, v3, v4\n"
"v_bfe_u32 v4, v89, 16, 15\n"
"v_max_u32 v3, v3, v4\n"
"v_and_b32 v4, 0x7fff, v90\n"
"v_max_u32 v3, v3, v4\n"
"v_bfe_u32 v4, v90, 16, 15\n"
"v_max_u32 v3, v3, v4\n"
"v_and_b32 v4, 0x7fff, v91\n"
"v_max_u32 v3, v3, v4\n"
"v_bfe_u32 v4, v91, 16, 15\n"
"v_max_u32 v3, v3, v4\n"
"v_cmp_lt_u32 vcc, 0x7f80, v3\n"
"s_nop 0\n"
"s_cbranch_vccz S22_%=\n"
"s_mov_b32 s45, 1\n"
"ds_read_b128 v[96:99], v15 offset:0\n"
"ds_read_b128 v[100:103], v15 offset:1024\n"
"ds_read_b128 v[72:75], v15 offset:2048\n"
"ds_read_b128 v[76:79], v15 offset:3072\n"
"ds_read_b128 v[80:83], v15 offset:4096\n"
"ds_read_b128 v[84:87], v15 offset:5120\n"
"ds_read_b128 v[88:91], v15 offset:6144\n"
"ds_read_b128 v[92:95], v15 offset:7168\n"
"ds_read_b128 v[104:107], v15 offset:8192\n"
"ds_read_b128 v[108:111], v15 offset:9216\n"
"ds_read_b128 v[112:115], v15 offset:10240\n"
"ds_read_b128 v[116:119], v15 offset:11264\n"
"v_mov_b32 v32, 0\n"
"v_mov_b32 v33, 0\n"
"v_mov_b32 v34, 0\n"
"v_mov_b32 v35, 0\n"
"v_mov_b32 v36, 0\n"
"v_mov_b32 v37, 0\n"
"v_mov_b32 v38, 0\n"
"v_mov_b32 v39, 0\n"
"v_mov_b32 v40, 0\n"
"v_mov_b32 v41, 0\n"
"v_mov_b32 v42, 0\n"
"v_mov_b32 v43, 0\n"
"v_mov_b32 v44, 0\n"
"v_mov_b32 v45, 0\n"
"v_mov_b32 v46, 0\n"
"v_mov_b32 v47, 0\n"
"v_mov_b32 v48, 0\n"
"v_mov_b32 v49, 0\n"
"v_mov_b32 v50, 0\n"
"v_mov_b32 v51, 0\n"
"v_mov_b32 v52, 0\n"
"v_mov_b32 v53, 0\n"
"v_mov_b32 v54, 0\n"
"v_mov_b32 v55, 0\n"
"v_mov_b32 v120, 0\n"
"v_mov_b32 v121, 0\n"
"v_mov_b32 v122, 0\n"
"v_mov_b32 v123, 0\n"
"v_mov_b32 v124, 0\n"
"v_mov_b32 v125, 0\n"
"v_mov_b32 v126, 0\n"
"v_mov_b32 v127, 0\n"
"s_waitcnt lgkmcnt(0)\n"
"s_barrier\n"
"s_branch S14_%=\n"
"S22_%=:\n"
"s_cmp_eq_u32 s45, 2\n"
"s_cbranch_scc1 S23_%=\n"
"s_mov_b32 s45, 2\n"
"v_max_f32 v24, v24, v100\n"
"s_nop 1\n"
"v_max_f32_dpp v24, v24, v24 quad_perm:[1,0,3,2] row_mask:0xf bank_mask:0xf\n"
"s_nop 1\n"
"v_max_f32_dpp v24, v24, v24 row_ror:4 row_mask:0xf bank_mask:0xf\n"
"s_nop 1\n"
"v_max_f32_dpp v24, v24, v24 row_ror:8 row_mask:0xf bank_mask:0xf\n"
"v_mul_f32 v25, s74, v24\n"
"s_branch S16_%=\n"
"S23_%=:\n"
"s_branch S15ON_%=\n"
"S11_%=:\n"
"s_waitcnt vmcnt(0)\n"
"s_barrier\n"
"s_branch S5_%=\n"
"S2_%=:\n"
"s_mov_b32 s22, 46301184\n"
"s_mov_b32 s23, 46317568\n"
"s_cmp_lg_u32 s66, 0\n"
"s_cbranch_scc1 S24_%=\n"
"v_mov_b32 v24, 0\n"
"s_mov_b64 exec, 1\n"
"buffer_atomic_add v24, v26, s[36:39], s23 offen sc0 sc1\n"
"s_mov_b64 exec, -1\n"
"S24_%=:\n"
"s_add_u32 m0, s45, 0\n"
"s_add_u32 s72, s44, 0\n"
"buffer_load_dwordx4 v1, s[40:43], s72 offen lds\n"
"s_add_u32 m0, s45, 1024\n"
"s_add_u32 s72, s44, 98304\n"
"buffer_load_dwordx4 v2, s[40:43], s72 offen lds\n"
"s_add_u32 m0, s45, 2048\n"
"s_add_u32 s72, s44, 196608\n"
"buffer_load_dwordx4 v1, s[40:43], s72 offen lds\n"
"s_add_u32 m0, s45, 3072\n"
"s_add_u32 s72, s44, 294912\n"
"buffer_load_dwordx4 v2, s[40:43], s72 offen lds\n"
"s_add_u32 m0, s45, 8192\n"
"s_add_u32 s72, s44, 128\n"
"buffer_load_dwordx4 v1, s[40:43], s72 offen lds\n"
"s_add_u32 m0, s45, 9216\n"
"s_add_u32 s72, s44, 98432\n"
"buffer_load_dwordx4 v2, s[40:43], s72 offen lds\n"
"s_add_u32 m0, s45, 10240\n"
"s_add_u32 s72, s44, 196736\n"
"buffer_load_dwordx4 v1, s[40:43], s72 offen lds\n"
"s_add_u32 m0, s45, 11264\n"
"s_add_u32 s72, s44, 295040\n"
"buffer_load_dwordx4 v2, s[40:43], s72 offen lds\n"
"s_add_u32 m0, s45, 16384\n"
"s_add_u32 s72, s44, 256\n"
"buffer_load_dwordx4 v1, s[40:43], s72 offen lds\n"
"s_add_u32 m0, s45, 17408\n"
"s_add_u32 s72, s44, 98560\n"
"buffer_load_dwordx4 v2, s[40:43], s72 offen lds\n"
"s_add_u32 m0, s45, 18432\n"
"s_add_u32 s72, s44, 196864\n"
"buffer_load_dwordx4 v1, s[40:43], s72 offen lds\n"
"s_add_u32 m0, s45, 19456\n"
"s_add_u32 s72, s44, 295168\n"
"buffer_load_dwordx4 v2, s[40:43], s72 offen lds\n"
"s_add_u32 m0, s45, 49152\n"
"s_add_u32 s72, s44, 786432\n"
"buffer_load_dwordx4 v1, s[40:43], s72 offen lds\n"
"s_add_u32 m0, s45, 50176\n"
"s_add_u32 s72, s44, 884736\n"
"buffer_load_dwordx4 v2, s[40:43], s72 offen lds\n"
"s_add_u32 m0, s45, 51200\n"
"s_add_u32 s72, s44, 983040\n"
"buffer_load_dwordx4 v1, s[40:43], s72 offen lds\n"
"s_add_u32 m0, s45, 52224\n"
"s_add_u32 s72, s44, 1081344\n"
"buffer_load_dwordx4 v2, s[40:43], s72 offen lds\n"
"s_add_u32 m0, s45, 57344\n"
"s_add_u32 s72, s44, 786560\n"
"buffer_load_dwordx4 v1, s[40:43], s72 offen lds\n"
"s_add_u32 m0, s45, 58368\n"
"s_add_u32 s72, s44, 884864\n"
"buffer_load_dwordx4 v2, s[40:43], s72 offen lds\n"
"s_add_u32 m0, s45, 59392\n"
"s_add_u32 s72, s44, 983168\n"
"buffer_load_dwordx4 v1, s[40:43], s72 offen lds\n"
"s_add_u32 m0, s45, 60416\n"
"s_add_u32 s72, s44, 1081472\n"
"buffer_load_dwordx4 v2, s[40:43], s72 offen lds\n"
"s_add_u32 m0, s45, 65536\n"
"s_add_u32 s72, s44, 786688\n"
"buffer_load_dwordx4 v1, s[40:43], s72 offen lds\n"
"s_add_u32 m0, s45, 66560\n"
"s_add_u32 s72, s44, 884992\n"
"buffer_load_dwordx4 v2, s[40:43], s72 offen lds\n"
"s_add_u32 m0, s45, 67584\n"
"s_add_u32 s72, s44, 983296\n"
"buffer_load_dwordx4 v1, s[40:43], s72 offen lds\n"
"s_add_u32 m0, s45, 68608\n"
"s_add_u32 s72, s44, 1081600\n"
"buffer_load_dwordx4 v2, s[40:43], s72 offen lds\n"
"s_waitcnt vmcnt(12)\n"
"s_waitcnt lgkmcnt(0)\n"
"s_barrier\n"
"s_cmp_lg_u32 s66, 0\n"
"s_cbranch_scc1 S25_%=\n"
"v_or_b32 v24, 1, v24\n"
"v_add_u32 v24, 1, v24\n"
"buffer_store_dword v24, v26, s[36:39], s22 offen sc1\n"
"S25_%=:\n"
"s_add_u32 m0, s45, 98304\n"
"s_add_u32 s72, s44, 1572864\n"
"buffer_load_dwordx4 v1, s[40:43], s72 offen lds\n"
"s_add_u32 m0, s45, 99328\n"
"s_add_u32 s72, s44, 1671168\n"
"buffer_load_dwordx4 v2, s[40:43], s72 offen lds\n"
"s_add_u32 m0, s45, 100352\n"
"s_add_u32 s72, s44, 1769472\n"
"buffer_load_dwordx4 v1, s[40:43], s72 offen lds\n"
"s_add_u32 m0, s45, 101376\n"
"s_add_u32 s72, s44, 1867776\n"
"buffer_load_dwordx4 v2, s[40:43], s72 offen lds\n"
"s_add_u32 m0, s45, 106496\n"
"s_add_u32 s72, s44, 1572992\n"
"buffer_load_dwordx4 v1, s[40:43], s72 offen lds\n"
"s_add_u32 m0, s45, 107520\n"
"s_add_u32 s72, s44, 1671296\n"
"buffer_load_dwordx4 v2, s[40:43], s72 offen lds\n"
"s_add_u32 m0, s45, 108544\n"
"s_add_u32 s72, s44, 1769600\n"
"buffer_load_dwordx4 v1, s[40:43], s72 offen lds\n"
"s_add_u32 m0, s45, 109568\n"
"s_add_u32 s72, s44, 1867904\n"
"buffer_load_dwordx4 v2, s[40:43], s72 offen lds\n"
"s_add_u32 m0, s45, 114688\n"
"s_add_u32 s72, s44, 1573120\n"
"buffer_load_dwordx4 v1, s[40:43], s72 offen lds\n"
"s_add_u32 m0, s45, 115712\n"
"s_add_u32 s72, s44, 1671424\n"
"buffer_load_dwordx4 v2, s[40:43], s72 offen lds\n"
"s_add_u32 m0, s45, 116736\n"
"s_add_u32 s72, s44, 1769728\n"
"buffer_load_dwordx4 v1, s[40:43], s72 offen lds\n"
"s_add_u32 m0, s45, 117760\n"
"s_add_u32 s72, s44, 1868032\n"
"buffer_load_dwordx4 v2, s[40:43], s72 offen lds\n"
"v_add_u32 v20, 0, v3\n"
"v_add_u32 v21, 0, v4\n"
"v_add_u32 v22, 0, v5\n"
"ds_read_b64_tr_b16 v[80:81], v20 offset:0\n"
"ds_read_b64_tr_b16 v[82:83], v20 offset:256\n"
"ds_read_b64_tr_b16 v[84:85], v20 offset:4096\n"
"ds_read_b64_tr_b16 v[86:87], v20 offset:4352\n"
"ds_read_b64_tr_b16 a[8:9], v21 offset:0\n"
"ds_read_b64_tr_b16 a[10:11], v21 offset:256\n"
"ds_read_b64_tr_b16 a[12:13], v21 offset:4096\n"
"ds_read_b64_tr_b16 a[14:15], v21 offset:4352\n"
"ds_read_b64_tr_b16 a[64:65], v22 offset:0\n"
"ds_read_b64_tr_b16 a[66:67], v22 offset:256\n"
"ds_read_b64_tr_b16 a[68:69], v22 offset:4096\n"
"ds_read_b64_tr_b16 a[70:71], v22 offset:4352\n"
"ds_read_b64_tr_b16 a[104:105], v20 offset:24576\n"
"ds_read_b64_tr_b16 a[106:107], v20 offset:24832\n"
"ds_read_b64_tr_b16 a[108:109], v20 offset:28672\n"
"ds_read_b64_tr_b16 a[110:111], v20 offset:28928\n"
"ds_read_b64_tr_b16 a[152:153], v21 offset:24576\n"
"ds_read_b64_tr_b16 a[154:155], v21 offset:24832\n"
"ds_read_b64_tr_b16 a[156:157], v21 offset:28672\n"
"ds_read_b64_tr_b16 a[158:159], v21 offset:28928\n"
"ds_read_b64_tr_b16 a[232:233], v22 offset:24576\n"
"ds_read_b64_tr_b16 a[234:235], v22 offset:24832\n"
"ds_read_b64_tr_b16 a[236:237], v22 offset:28672\n"
"ds_read_b64_tr_b16 a[238:239], v22 offset:28928\n"
"s_waitcnt vmcnt(12)\n"
"s_waitcnt lgkmcnt(0)\n"
"s_barrier\n"
"s_add_u32 m0, s45, 0\n"
"s_add_u32 s72, s44, 2359296\n"
"buffer_load_dwordx4 v1, s[40:43], s72 offen lds\n"
"s_add_u32 m0, s45, 1024\n"
"s_add_u32 s72, s44, 2457600\n"
"buffer_load_dwordx4 v2, s[40:43], s72 offen lds\n"
"s_add_u32 m0, s45, 2048\n"
"s_add_u32 s72, s44, 2555904\n"
"buffer_load_dwordx4 v1, s[40:43], s72 offen lds\n"
"s_add_u32 m0, s45, 3072\n"
"s_add_u32 s72, s44, 2654208\n"
"buffer_load_dwordx4 v2, s[40:43], s72 offen lds\n"
"s_add_u32 m0, s45, 8192\n"
"s_add_u32 s72, s44, 2359424\n"
"buffer_load_dwordx4 v1, s[40:43], s72 offen lds\n"
"s_add_u32 m0, s45, 9216\n"
"s_add_u32 s72, s44, 2457728\n"
"buffer_load_dwordx4 v2, s[40:43], s72 offen lds\n"
"s_add_u32 m0, s45, 10240\n"
"s_add_u32 s72, s44, 2556032\n"
"buffer_load_dwordx4 v1, s[40:43], s72 offen lds\n"
"s_add_u32 m0, s45, 11264\n"
"s_add_u32 s72, s44, 2654336\n"
"buffer_load_dwordx4 v2, s[40:43], s72 offen lds\n"
"s_add_u32 m0, s45, 16384\n"
"s_add_u32 s72, s44, 2359552\n"
"buffer_load_dwordx4 v1, s[40:43], s72 offen lds\n"
"s_add_u32 m0, s45, 17408\n"
"s_add_u32 s72, s44, 2457856\n"
"buffer_load_dwordx4 v2, s[40:43], s72 offen lds\n"
"s_add_u32 m0, s45, 18432\n"
"s_add_u32 s72, s44, 2556160\n"
"buffer_load_dwordx4 v1, s[40:43], s72 offen lds\n"
"s_add_u32 m0, s45, 19456\n"
"s_add_u32 s72, s44, 2654464\n"
"buffer_load_dwordx4 v2, s[40:43], s72 offen lds\n"
"v_add_u32 v20, 49152, v3\n"
"v_add_u32 v21, 49152, v4\n"
"v_add_u32 v22, 49152, v5\n"
"ds_read_b64_tr_b16 a[16:17], v20 offset:0\n"
"ds_read_b64_tr_b16 a[18:19], v20 offset:256\n"
"ds_read_b64_tr_b16 a[20:21], v20 offset:4096\n"
"ds_read_b64_tr_b16 a[22:23], v20 offset:4352\n"
"ds_read_b64_tr_b16 a[24:25], v21 offset:0\n"
"ds_read_b64_tr_b16 a[26:27], v21 offset:256\n"
"ds_read_b64_tr_b16 a[28:29], v21 offset:4096\n"
"ds_read_b64_tr_b16 a[30:31], v21 offset:4352\n"
"ds_read_b64_tr_b16 a[72:73], v22 offset:0\n"
"ds_read_b64_tr_b16 a[74:75], v22 offset:256\n"
"ds_read_b64_tr_b16 a[76:77], v22 offset:4096\n"
"ds_read_b64_tr_b16 a[78:79], v22 offset:4352\n"
"ds_read_b64_tr_b16 v[120:121], v20 offset:24576\n"
"ds_read_b64_tr_b16 v[122:123], v20 offset:24832\n"
"ds_read_b64_tr_b16 v[124:125], v20 offset:28672\n"
"ds_read_b64_tr_b16 v[126:127], v20 offset:28928\n"
"ds_read_b64_tr_b16 a[160:161], v21 offset:24576\n"
"ds_read_b64_tr_b16 a[162:163], v21 offset:24832\n"
"ds_read_b64_tr_b16 a[164:165], v21 offset:28672\n"
"ds_read_b64_tr_b16 a[166:167], v21 offset:28928\n"
"ds_read_b64_tr_b16 a[240:241], v22 offset:24576\n"
"ds_read_b64_tr_b16 a[242:243], v22 offset:24832\n"
"ds_read_b64_tr_b16 a[244:245], v22 offset:28672\n"
"ds_read_b64_tr_b16 a[246:247], v22 offset:28928\n"
"s_waitcnt vmcnt(12)\n"
"s_waitcnt lgkmcnt(0)\n"
"s_barrier\n"
"s_add_u32 m0, s45, 49152\n"
"s_add_u32 s72, s44, 3145728\n"
"buffer_load_dwordx4 v1, s[40:43], s72 offen lds\n"
"s_add_u32 m0, s45, 50176\n"
"s_add_u32 s72, s44, 3244032\n"
"buffer_load_dwordx4 v2, s[40:43], s72 offen lds\n"
"s_add_u32 m0, s45, 51200\n"
"s_add_u32 s72, s44, 3342336\n"
"buffer_load_dwordx4 v1, s[40:43], s72 offen lds\n"
"s_add_u32 m0, s45, 52224\n"
"s_add_u32 s72, s44, 3440640\n"
"buffer_load_dwordx4 v2, s[40:43], s72 offen lds\n"
"s_add_u32 m0, s45, 57344\n"
"s_add_u32 s72, s44, 3145856\n"
"buffer_load_dwordx4 v1, s[40:43], s72 offen lds\n"
"s_add_u32 m0, s45, 58368\n"
"s_add_u32 s72, s44, 3244160\n"
"buffer_load_dwordx4 v2, s[40:43], s72 offen lds\n"
"s_add_u32 m0, s45, 59392\n"
"s_add_u32 s72, s44, 3342464\n"
"buffer_load_dwordx4 v1, s[40:43], s72 offen lds\n"
"s_add_u32 m0, s45, 60416\n"
"s_add_u32 s72, s44, 3440768\n"
"buffer_load_dwordx4 v2, s[40:43], s72 offen lds\n"
"s_add_u32 m0, s45, 65536\n"
"s_add_u32 s72, s44, 3145984\n"
"buffer_load_dwordx4 v1, s[40:43], s72 offen lds\n"
"s_add_u32 m0, s45, 66560\n"
"s_add_u32 s72, s44, 3244288\n"
"buffer_load_dwordx4 v2, s[40:43], s72 offen lds\n"
"s_add_u32 m0, s45, 67584\n"
"s_add_u32 s72, s44, 3342592\n"
"buffer_load_dwordx4 v1, s[40:43], s72 offen lds\n"
"s_add_u32 m0, s45, 68608\n"
"s_add_u32 s72, s44, 3440896\n"
"buffer_load_dwordx4 v2, s[40:43], s72 offen lds\n"
"v_add_u32 v20, 98304, v3\n"
"v_add_u32 v21, 98304, v4\n"
"v_add_u32 v22, 98304, v5\n"
"ds_read_b64_tr_b16 v[88:89], v20 offset:0\n"
"ds_read_b64_tr_b16 v[90:91], v20 offset:256\n"
"ds_read_b64_tr_b16 v[92:93], v20 offset:4096\n"
"ds_read_b64_tr_b16 v[94:95], v20 offset:4352\n"
"ds_read_b64_tr_b16 a[32:33], v21 offset:0\n"
"ds_read_b64_tr_b16 a[34:35], v21 offset:256\n"
"ds_read_b64_tr_b16 a[36:37], v21 offset:4096\n"
"ds_read_b64_tr_b16 a[38:39], v21 offset:4352\n"
"ds_read_b64_tr_b16 v[104:105], v22 offset:0\n"
"ds_read_b64_tr_b16 v[106:107], v22 offset:256\n"
"ds_read_b64_tr_b16 v[108:109], v22 offset:4096\n"
"ds_read_b64_tr_b16 v[110:111], v22 offset:4352\n"
"ds_read_b64_tr_b16 a[112:113], v20 offset:24576\n"
"ds_read_b64_tr_b16 a[114:115], v20 offset:24832\n"
"ds_read_b64_tr_b16 a[116:117], v20 offset:28672\n"
"ds_read_b64_tr_b16 a[118:119], v20 offset:28928\n"
"ds_read_b64_tr_b16 a[168:169], v21 offset:24576\n"
"ds_read_b64_tr_b16 a[170:171], v21 offset:24832\n"
"ds_read_b64_tr_b16 a[172:173], v21 offset:28672\n"
"ds_read_b64_tr_b16 a[174:175], v21 offset:28928\n"
"ds_read_b64_tr_b16 a[248:249], v22 offset:24576\n"
"ds_read_b64_tr_b16 a[250:251], v22 offset:24832\n"
"ds_read_b64_tr_b16 a[252:253], v22 offset:28672\n"
"ds_read_b64_tr_b16 a[254:255], v22 offset:28928\n"
"s_waitcnt vmcnt(12)\n"
"s_waitcnt lgkmcnt(0)\n"
"s_barrier\n"
"s_add_u32 m0, s45, 98304\n"
"s_add_u32 s72, s44, 3932160\n"
"buffer_load_dwordx4 v1, s[40:43], s72 offen lds\n"
"s_add_u32 m0, s45, 99328\n"
"s_add_u32 s72, s44, 4030464\n"
"buffer_load_dwordx4 v2, s[40:43], s72 offen lds\n"
"s_add_u32 m0, s45, 100352\n"
"s_add_u32 s72, s44, 4128768\n"
"buffer_load_dwordx4 v1, s[40:43], s72 offen lds\n"
"s_add_u32 m0, s45, 101376\n"
"s_add_u32 s72, s44, 4227072\n"
"buffer_load_dwordx4 v2, s[40:43], s72 offen lds\n"
"s_add_u32 m0, s45, 106496\n"
"s_add_u32 s72, s44, 3932288\n"
"buffer_load_dwordx4 v1, s[40:43], s72 offen lds\n"
"s_add_u32 m0, s45, 107520\n"
"s_add_u32 s72, s44, 4030592\n"
"buffer_load_dwordx4 v2, s[40:43], s72 offen lds\n"
"s_add_u32 m0, s45, 108544\n"
"s_add_u32 s72, s44, 4128896\n"
"buffer_load_dwordx4 v1, s[40:43], s72 offen lds\n"
"s_add_u32 m0, s45, 109568\n"
"s_add_u32 s72, s44, 4227200\n"
"buffer_load_dwordx4 v2, s[40:43], s72 offen lds\n"
"s_add_u32 m0, s45, 114688\n"
"s_add_u32 s72, s44, 3932416\n"
"buffer_load_dwordx4 v1, s[40:43], s72 offen lds\n"
"s_add_u32 m0, s45, 115712\n"
"s_add_u32 s72, s44, 4030720\n"
"buffer_load_dwordx4 v2, s[40:43], s72 offen lds\n"
"s_add_u32 m0, s45, 116736\n"
"s_add_u32 s72, s44, 4129024\n"
"buffer_load_dwordx4 v1, s[40:43], s72 offen lds\n"
"s_add_u32 m0, s45, 117760\n"
"s_add_u32 s72, s44, 4227328\n"
"buffer_load_dwordx4 v2, s[40:43], s72 offen lds\n"
"v_add_u32 v20, 0, v3\n"
"v_add_u32 v21, 0, v4\n"
"v_add_u32 v22, 0, v5\n"
"ds_read_b64_tr_b16 v[32:33], v20 offset:0\n"
"ds_read_b64_tr_b16 v[34:35], v20 offset:256\n"
"ds_read_b64_tr_b16 v[36:37], v20 offset:4096\n"
"ds_read_b64_tr_b16 v[38:39], v20 offset:4352\n"
"ds_read_b64_tr_b16 v[128:129], v21 offset:0\n"
"ds_read_b64_tr_b16 v[130:131], v21 offset:256\n"
"ds_read_b64_tr_b16 v[132:133], v21 offset:4096\n"
"ds_read_b64_tr_b16 v[134:135], v21 offset:4352\n"
"ds_read_b64_tr_b16 v[136:137], v22 offset:0\n"
"ds_read_b64_tr_b16 v[138:139], v22 offset:256\n"
"ds_read_b64_tr_b16 v[140:141], v22 offset:4096\n"
"ds_read_b64_tr_b16 v[142:143], v22 offset:4352\n"
"ds_read_b64_tr_b16 v[40:41], v20 offset:24576\n"
"ds_read_b64_tr_b16 v[42:43], v20 offset:24832\n"
"ds_read_b64_tr_b16 v[44:45], v20 offset:28672\n"
"ds_read_b64_tr_b16 v[46:47], v20 offset:28928\n"
"ds_read_b64_tr_b16 v[48:49], v21 offset:24576\n"
"ds_read_b64_tr_b16 v[50:51], v21 offset:24832\n"
"ds_read_b64_tr_b16 v[52:53], v21 offset:28672\n"
"ds_read_b64_tr_b16 v[54:55], v21 offset:28928\n"
"ds_read_b64_tr_b16 a[176:177], v22 offset:24576\n"
"ds_read_b64_tr_b16 a[178:179], v22 offset:24832\n"
"ds_read_b64_tr_b16 a[180:181], v22 offset:28672\n"
"ds_read_b64_tr_b16 a[182:183], v22 offset:28928\n"
"s_waitcnt vmcnt(12)\n"
"s_waitcnt lgkmcnt(0)\n"
"s_barrier\n"
"s_add_u32 m0, s45, 0\n"
"s_add_u32 s72, s44, 4718592\n"
"buffer_load_dwordx4 v1, s[40:43], s72 offen lds\n"
"s_add_u32 m0, s45, 1024\n"
"s_add_u32 s72, s44, 4816896\n"
"buffer_load_dwordx4 v2, s[40:43], s72 offen lds\n"
"s_add_u32 m0, s45, 2048\n"
"s_add_u32 s72, s44, 4915200\n"
"buffer_load_dwordx4 v1, s[40:43], s72 offen lds\n"
"s_add_u32 m0, s45, 3072\n"
"s_add_u32 s72, s44, 5013504\n"
"buffer_load_dwordx4 v2, s[40:43], s72 offen lds\n"
"s_add_u32 m0, s45, 8192\n"
"s_add_u32 s72, s44, 4718720\n"
"buffer_load_dwordx4 v1, s[40:43], s72 offen lds\n"
"s_add_u32 m0, s45, 9216\n"
"s_add_u32 s72, s44, 4817024\n"
"buffer_load_dwordx4 v2, s[40:43], s72 offen lds\n"
"s_add_u32 m0, s45, 10240\n"
"s_add_u32 s72, s44, 4915328\n"
"buffer_load_dwordx4 v1, s[40:43], s72 offen lds\n"
"s_add_u32 m0, s45, 11264\n"
"s_add_u32 s72, s44, 5013632\n"
"buffer_load_dwordx4 v2, s[40:43], s72 offen lds\n"
"s_add_u32 m0, s45, 16384\n"
"s_add_u32 s72, s44, 4718848\n"
"buffer_load_dwordx4 v1, s[40:43], s72 offen lds\n"
"s_add_u32 m0, s45, 17408\n"
"s_add_u32 s72, s44, 4817152\n"
"buffer_load_dwordx4 v2, s[40:43], s72 offen lds\n"
"s_add_u32 m0, s45, 18432\n"
"s_add_u32 s72, s44, 4915456\n"
"buffer_load_dwordx4 v1, s[40:43], s72 offen lds\n"
"s_add_u32 m0, s45, 19456\n"
"s_add_u32 s72, s44, 5013760\n"
"buffer_load_dwordx4 v2, s[40:43], s72 offen lds\n"
"v_add_u32 v20, 49152, v3\n"
"v_add_u32 v21, 49152, v4\n"
"v_add_u32 v22, 49152, v5\n"
"ds_read_b64_tr_b16 v[56:57], v20 offset:0\n"
"ds_read_b64_tr_b16 v[58:59], v20 offset:256\n"
"ds_read_b64_tr_b16 v[60:61], v20 offset:4096\n"
"ds_read_b64_tr_b16 v[62:63], v20 offset:4352\n"
"ds_read_b64_tr_b16 v[152:153], v21 offset:0\n"
"ds_read_b64_tr_b16 v[154:155], v21 offset:256\n"
"ds_read_b64_tr_b16 v[156:157], v21 offset:4096\n"
"ds_read_b64_tr_b16 v[158:159], v21 offset:4352\n"
"ds_read_b64_tr_b16 v[160:161], v22 offset:0\n"
"ds_read_b64_tr_b16 v[162:163], v22 offset:256\n"
"ds_read_b64_tr_b16 v[164:165], v22 offset:4096\n"
"ds_read_b64_tr_b16 v[166:167], v22 offset:4352\n"
"ds_read_b64_tr_b16 v[144:145], v20 offset:24576\n"
"ds_read_b64_tr_b16 v[146:147], v20 offset:24832\n"
"ds_read_b64_tr_b16 v[148:149], v20 offset:28672\n"
"ds_read_b64_tr_b16 v[150:151], v20 offset:28928\n"
"ds_read_b64_tr_b16 v[64:65], v21 offset:24576\n"
"ds_read_b64_tr_b16 v[66:67], v21 offset:24832\n"
"ds_read_b64_tr_b16 v[68:69], v21 offset:28672\n"
"ds_read_b64_tr_b16 v[70:71], v21 offset:28928\n"
"ds_read_b64_tr_b16 a[184:185], v22 offset:24576\n"
"ds_read_b64_tr_b16 a[186:187], v22 offset:24832\n"
"ds_read_b64_tr_b16 a[188:189], v22 offset:28672\n"
"ds_read_b64_tr_b16 a[190:191], v22 offset:28928\n"
"s_waitcnt vmcnt(12)\n"
"s_waitcnt lgkmcnt(0)\n"
"s_barrier\n"
"s_add_u32 m0, s45, 49152\n"
"s_add_u32 s72, s44, 5505024\n"
"buffer_load_dwordx4 v1, s[40:43], s72 offen lds\n"
"s_add_u32 m0, s45, 50176\n"
"s_add_u32 s72, s44, 5603328\n"
"buffer_load_dwordx4 v2, s[40:43], s72 offen lds\n"
"s_add_u32 m0, s45, 51200\n"
"s_add_u32 s72, s44, 5701632\n"
"buffer_load_dwordx4 v1, s[40:43], s72 offen lds\n"
"s_add_u32 m0, s45, 52224\n"
"s_add_u32 s72, s44, 5799936\n"
"buffer_load_dwordx4 v2, s[40:43], s72 offen lds\n"
"s_add_u32 m0, s45, 57344\n"
"s_add_u32 s72, s44, 5505152\n"
"buffer_load_dwordx4 v1, s[40:43], s72 offen lds\n"
"s_add_u32 m0, s45, 58368\n"
"s_add_u32 s72, s44, 5603456\n"
"buffer_load_dwordx4 v2, s[40:43], s72 offen lds\n"
"s_add_u32 m0, s45, 59392\n"
"s_add_u32 s72, s44, 5701760\n"
"buffer_load_dwordx4 v1, s[40:43], s72 offen lds\n"
"s_add_u32 m0, s45, 60416\n"
"s_add_u32 s72, s44, 5800064\n"
"buffer_load_dwordx4 v2, s[40:43], s72 offen lds\n"
"s_add_u32 m0, s45, 65536\n"
"s_add_u32 s72, s44, 5505280\n"
"buffer_load_dwordx4 v1, s[40:43], s72 offen lds\n"
"s_add_u32 m0, s45, 66560\n"
"s_add_u32 s72, s44, 5603584\n"
"buffer_load_dwordx4 v2, s[40:43], s72 offen lds\n"
"s_add_u32 m0, s45, 67584\n"
"s_add_u32 s72, s44, 5701888\n"
"buffer_load_dwordx4 v1, s[40:43], s72 offen lds\n"
"s_add_u32 m0, s45, 68608\n"
"s_add_u32 s72, s44, 5800192\n"
"buffer_load_dwordx4 v2, s[40:43], s72 offen lds\n"
"v_add_u32 v20, 98304, v3\n"
"v_add_u32 v21, 98304, v4\n"
"v_add_u32 v22, 98304, v5\n"
"ds_read_b64_tr_b16 v[168:169], v20 offset:0\n"
"ds_read_b64_tr_b16 v[170:171], v20 offset:256\n"
"ds_read_b64_tr_b16 v[172:173], v20 offset:4096\n"
"ds_read_b64_tr_b16 v[174:175], v20 offset:4352\n"
"ds_read_b64_tr_b16 v[184:185], v21 offset:0\n"
"ds_read_b64_tr_b16 v[186:187], v21 offset:256\n"
"ds_read_b64_tr_b16 v[188:189], v21 offset:4096\n"
"ds_read_b64_tr_b16 v[190:191], v21 offset:4352\n"
"ds_read_b64_tr_b16 v[192:193], v22 offset:0\n"
"ds_read_b64_tr_b16 v[194:195], v22 offset:256\n"
"ds_read_b64_tr_b16 v[196:197], v22 offset:4096\n"
"ds_read_b64_tr_b16 v[198:199], v22 offset:4352\n"
"ds_read_b64_tr_b16 v[176:177], v20 offset:24576\n"
"ds_read_b64_tr_b16 v[178:179], v20 offset:24832\n"
"ds_read_b64_tr_b16 v[180:181], v20 offset:28672\n"
"ds_read_b64_tr_b16 v[182:183], v20 offset:28928\n"
"ds_read_b64_tr_b16 v[72:73], v21 offset:24576\n"
"ds_read_b64_tr_b16 v[74:75], v21 offset:24832\n"
"ds_read_b64_tr_b16 v[76:77], v21 offset:28672\n"
"ds_read_b64_tr_b16 v[78:79], v21 offset:28928\n"
"ds_read_b64_tr_b16 a[192:193], v22 offset:24576\n"
"ds_read_b64_tr_b16 a[194:195], v22 offset:24832\n"
"ds_read_b64_tr_b16 a[196:197], v22 offset:28672\n"
"ds_read_b64_tr_b16 a[198:199], v22 offset:28928\n"
"s_waitcnt vmcnt(12)\n"
"s_waitcnt lgkmcnt(0)\n"
"s_barrier\n"
"s_add_u32 m0, s45, 98304\n"
"s_add_u32 s72, s44, 6291456\n"
"buffer_load_dwordx4 v1, s[40:43], s72 offen lds\n"
"s_add_u32 m0, s45, 99328\n"
"s_add_u32 s72, s44, 6389760\n"
"buffer_load_dwordx4 v2, s[40:43], s72 offen lds\n"
"s_add_u32 m0, s45, 100352\n"
"s_add_u32 s72, s44, 6488064\n"
"buffer_load_dwordx4 v1, s[40:43], s72 offen lds\n"
"s_add_u32 m0, s45, 101376\n"
"s_add_u32 s72, s44, 6586368\n"
"buffer_load_dwordx4 v2, s[40:43], s72 offen lds\n"
"s_add_u32 m0, s45, 106496\n"
"s_add_u32 s72, s44, 6291584\n"
"buffer_load_dwordx4 v1, s[40:43], s72 offen lds\n"
"s_add_u32 m0, s45, 107520\n"
"s_add_u32 s72, s44, 6389888\n"
"buffer_load_dwordx4 v2, s[40:43], s72 offen lds\n"
"s_add_u32 m0, s45, 108544\n"
"s_add_u32 s72, s44, 6488192\n"
"buffer_load_dwordx4 v1, s[40:43], s72 offen lds\n"
"s_add_u32 m0, s45, 109568\n"
"s_add_u32 s72, s44, 6586496\n"
"buffer_load_dwordx4 v2, s[40:43], s72 offen lds\n"
"s_add_u32 m0, s45, 114688\n"
"s_add_u32 s72, s44, 6291712\n"
"buffer_load_dwordx4 v1, s[40:43], s72 offen lds\n"
"s_add_u32 m0, s45, 115712\n"
"s_add_u32 s72, s44, 6390016\n"
"buffer_load_dwordx4 v2, s[40:43], s72 offen lds\n"
"s_add_u32 m0, s45, 116736\n"
"s_add_u32 s72, s44, 6488320\n"
"buffer_load_dwordx4 v1, s[40:43], s72 offen lds\n"
"s_add_u32 m0, s45, 117760\n"
"s_add_u32 s72, s44, 6586624\n"
"buffer_load_dwordx4 v2, s[40:43], s72 offen lds\n"
"v_add_u32 v20, 0, v3\n"
"v_add_u32 v21, 0, v4\n"
"v_add_u32 v22, 0, v5\n"
"ds_read_b64_tr_b16 v[200:201], v20 offset:0\n"
"ds_read_b64_tr_b16 v[202:203], v20 offset:256\n"
"ds_read_b64_tr_b16 v[204:205], v20 offset:4096\n"
"ds_read_b64_tr_b16 v[206:207], v20 offset:4352\n"
"ds_read_b64_tr_b16 v[208:209], v21 offset:0\n"
"ds_read_b64_tr_b16 v[210:211], v21 offset:256\n"
"ds_read_b64_tr_b16 v[212:213], v21 offset:4096\n"
"ds_read_b64_tr_b16 v[214:215], v21 offset:4352\n"
"ds_read_b64_tr_b16 a[40:41], v22 offset:0\n"
"ds_read_b64_tr_b16 a[42:43], v22 offset:256\n"
"ds_read_b64_tr_b16 a[44:45], v22 offset:4096\n"
"ds_read_b64_tr_b16 a[46:47], v22 offset:4352\n"
"ds_read_b64_tr_b16 a[80:81], v20 offset:24576\n"
"ds_read_b64_tr_b16 a[82:83], v20 offset:24832\n"
"ds_read_b64_tr_b16 a[84:85], v20 offset:28672\n"
"ds_read_b64_tr_b16 a[86:87], v20 offset:28928\n"
"ds_read_b64_tr_b16 a[120:121], v21 offset:24576\n"
"ds_read_b64_tr_b16 a[122:123], v21 offset:24832\n"
"ds_read_b64_tr_b16 a[124:125], v21 offset:28672\n"
"ds_read_b64_tr_b16 a[126:127], v21 offset:28928\n"
"ds_read_b64_tr_b16 a[200:201], v22 offset:24576\n"
"ds_read_b64_tr_b16 a[202:203], v22 offset:24832\n"
"ds_read_b64_tr_b16 a[204:205], v22 offset:28672\n"
"ds_read_b64_tr_b16 a[206:207], v22 offset:28928\n"
"s_waitcnt vmcnt(12)\n"
"s_waitcnt lgkmcnt(0)\n"
"s_barrier\n"
"s_add_u32 m0, s45, 0\n"
"s_add_u32 s72, s44, 7077888\n"
"buffer_load_dwordx4 v1, s[40:43], s72 offen lds\n"
"s_add_u32 m0, s45, 1024\n"
"s_add_u32 s72, s44, 7176192\n"
"buffer_load_dwordx4 v2, s[40:43], s72 offen lds\n"
"s_add_u32 m0, s45, 2048\n"
"s_add_u32 s72, s44, 7274496\n"
"buffer_load_dwordx4 v1, s[40:43], s72 offen lds\n"
"s_add_u32 m0, s45, 3072\n"
"s_add_u32 s72, s44, 7372800\n"
"buffer_load_dwordx4 v2, s[40:43], s72 offen lds\n"
"s_add_u32 m0, s45, 8192\n"
"s_add_u32 s72, s44, 7078016\n"
"buffer_load_dwordx4 v1, s[40:43], s72 offen lds\n"
"s_add_u32 m0, s45, 9216\n"
"s_add_u32 s72, s44, 7176320\n"
"buffer_load_dwordx4 v2, s[40:43], s72 offen lds\n"
"s_add_u32 m0, s45, 10240\n"
"s_add_u32 s72, s44, 7274624\n"
"buffer_load_dwordx4 v1, s[40:43], s72 offen lds\n"
"s_add_u32 m0, s45, 11264\n"
"s_add_u32 s72, s44, 7372928\n"
"buffer_load_dwordx4 v2, s[40:43], s72 offen lds\n"
"s_add_u32 m0, s45, 16384\n"
"s_add_u32 s72, s44, 7078144\n"
"buffer_load_dwordx4 v1, s[40:43], s72 offen lds\n"
"s_add_u32 m0, s45, 17408\n"
"s_add_u32 s72, s44, 7176448\n"
"buffer_load_dwordx4 v2, s[40:43], s72 offen lds\n"
"s_add_u32 m0, s45, 18432\n"
"s_add_u32 s72, s44, 7274752\n"
"buffer_load_dwordx4 v1, s[40:43], s72 offen lds\n"
"s_add_u32 m0, s45, 19456\n"
"s_add_u32 s72, s44, 7373056\n"
"buffer_load_dwordx4 v2, s[40:43], s72 offen lds\n"
"v_add_u32 v20, 49152, v3\n"
"v_add_u32 v21, 49152, v4\n"
"v_add_u32 v22, 49152, v5\n"
"ds_read_b64_tr_b16 v[216:217], v20 offset:0\n"
"ds_read_b64_tr_b16 v[218:219], v20 offset:256\n"
"ds_read_b64_tr_b16 v[220:221], v20 offset:4096\n"
"ds_read_b64_tr_b16 v[222:223], v20 offset:4352\n"
"ds_read_b64_tr_b16 v[224:225], v21 offset:0\n"
"ds_read_b64_tr_b16 v[226:227], v21 offset:256\n"
"ds_read_b64_tr_b16 v[228:229], v21 offset:4096\n"
"ds_read_b64_tr_b16 v[230:231], v21 offset:4352\n"
"ds_read_b64_tr_b16 a[48:49], v22 offset:0\n"
"ds_read_b64_tr_b16 a[50:51], v22 offset:256\n"
"ds_read_b64_tr_b16 a[52:53], v22 offset:4096\n"
"ds_read_b64_tr_b16 a[54:55], v22 offset:4352\n"
"ds_read_b64_tr_b16 a[88:89], v20 offset:24576\n"
"ds_read_b64_tr_b16 a[90:91], v20 offset:24832\n"
"ds_read_b64_tr_b16 a[92:93], v20 offset:28672\n"
"ds_read_b64_tr_b16 a[94:95], v20 offset:28928\n"
"ds_read_b64_tr_b16 a[128:129], v21 offset:24576\n"
"ds_read_b64_tr_b16 a[130:131], v21 offset:24832\n"
"ds_read_b64_tr_b16 a[132:133], v21 offset:28672\n"
"ds_read_b64_tr_b16 a[134:135], v21 offset:28928\n"
"ds_read_b64_tr_b16 a[208:209], v22 offset:24576\n"
"ds_read_b64_tr_b16 a[210:211], v22 offset:24832\n"
"ds_read_b64_tr_b16 a[212:213], v22 offset:28672\n"
"ds_read_b64_tr_b16 a[214:215], v22 offset:28928\n"
"s_cmp_lg_u32 s66, 0\n"
"s_cbranch_scc1 S26_%=\n"
"s_mov_b32 s76, 131072\n"
"s_mov_b64 exec, 1\n"
"s_branch S28_%=\n"
"S27_%=:\n"
"s_sleep 4\n"
"S28_%=:\n"
"v_mov_b32 v29, 0\n"
"buffer_atomic_add v29, v26, s[36:39], s23 offen sc0 sc1\n"
"s_sub_u32 s76, s76, 1\n"
"s_cbranch_scc1 SABORT_%=\n"
"s_waitcnt vmcnt(0)\n"
"v_xor_b32 v29, v29, v24\n"
"v_cmp_lt_u32 vcc, 1, v29\n"
"s_nop 0\n"
"s_cbranch_vccnz S27_%=\n"
"v_mov_b32 v28, 153600\n"
"ds_write_b32 v28, v29\n"
"v_mov_b32 v24, 1\n"
"s_add_u32 s22, s23, 16384\n"
"buffer_store_dword v24, v26, s[36:39], s22 offen sc1\n"
"s_mov_b64 exec, -1\n"
"S26_%=:\n"
"s_waitcnt lgkmcnt(0)\n"
"s_barrier\n"
"v_mov_b32 v28, 153600\n"
"ds_read_b32 v28, v28\n"
"s_waitcnt lgkmcnt(0)\n"
"v_readfirstlane_b32 s22, v28\n"
"s_nop 0\n"
"s_lshl_b32 s22, s22, 13\n"
"v_xor_b32 v12, s22, v12\n"
"v_xor_b32 v13, s22, v13\n"
"s_xor_b32 s68, s68, s22\n"
"s_xor_b32 s20, s20, s22\n"
"s_waitcnt vmcnt(12)\n"
"s_waitcnt lgkmcnt(0)\n"
"s_barrier\n"
"v_add_u32 v20, 98304, v3\n"
"v_add_u32 v21, 98304, v4\n"
"v_add_u32 v22, 98304, v5\n"
"ds_read_b64_tr_b16 v[232:233], v20 offset:0\n"
"ds_read_b64_tr_b16 v[234:235], v20 offset:256\n"
"ds_read_b64_tr_b16 v[236:237], v20 offset:4096\n"
"ds_read_b64_tr_b16 v[238:239], v20 offset:4352\n"
"ds_read_b64_tr_b16 v[240:241], v21 offset:0\n"
"ds_read_b64_tr_b16 v[242:243], v21 offset:256\n"
"ds_read_b64_tr_b16 v[244:245], v21 offset:4096\n"
"ds_read_b64_tr_b16 v[246:247], v21 offset:4352\n"
"ds_read_b64_tr_b16 v[96:97], v22 offset:0\n"
"ds_read_b64_tr_b16 v[98:99], v22 offset:256\n"
"ds_read_b64_tr_b16 v[100:101], v22 offset:4096\n"
"ds_read_b64_tr_b16 v[102:103], v22 offset:4352\n"
"ds_read_b64_tr_b16 v[112:113], v20 offset:24576\n"
"ds_read_b64_tr_b16 v[114:115], v20 offset:24832\n"
"ds_read_b64_tr_b16 v[116:117], v20 offset:28672\n"
"ds_read_b64_tr_b16 v[118:119], v20 offset:28928\n"
"ds_read_b64_tr_b16 a[136:137], v21 offset:24576\n"
"ds_read_b64_tr_b16 a[138:139], v21 offset:24832\n"
"ds_read_b64_tr_b16 a[140:141], v21 offset:28672\n"
"ds_read_b64_tr_b16 a[142:143], v21 offset:28928\n"
"ds_read_b64_tr_b16 a[216:217], v22 offset:24576\n"
"ds_read_b64_tr_b16 a[218:219], v22 offset:24832\n"
"ds_read_b64_tr_b16 a[220:221], v22 offset:28672\n"
"ds_read_b64_tr_b16 a[222:223], v22 offset:28928\n"
"s_waitcnt vmcnt(0)\n"
"s_waitcnt lgkmcnt(0)\n"
"s_barrier\n"
"v_add_u32 v20, 0, v3\n"
"v_add_u32 v21, 0, v4\n"
"v_add_u32 v22, 0, v5\n"
"ds_read_b64_tr_b16 v[248:249], v20 offset:0\n"
"ds_read_b64_tr_b16 v[250:251], v20 offset:256\n"
"ds_read_b64_tr_b16 v[252:253], v20 offset:4096\n"
"ds_read_b64_tr_b16 v[254:255], v20 offset:4352\n"
"ds_read_b64_tr_b16 a[0:1], v21 offset:0\n"
"ds_read_b64_tr_b16 a[2:3], v21 offset:256\n"
"ds_read_b64_tr_b16 a[4:5], v21 offset:4096\n"
"ds_read_b64_tr_b16 a[6:7], v21 offset:4352\n"
"ds_read_b64_tr_b16 a[56:57], v22 offset:0\n"
"ds_read_b64_tr_b16 a[58:59], v22 offset:256\n"
"ds_read_b64_tr_b16 a[60:61], v22 offset:4096\n"
"ds_read_b64_tr_b16 a[62:63], v22 offset:4352\n"
"ds_read_b64_tr_b16 a[96:97], v20 offset:24576\n"
"ds_read_b64_tr_b16 a[98:99], v20 offset:24832\n"
"ds_read_b64_tr_b16 a[100:101], v20 offset:28672\n"
"ds_read_b64_tr_b16 a[102:103], v20 offset:28928\n"
"ds_read_b64_tr_b16 a[144:145], v21 offset:24576\n"
"ds_read_b64_tr_b16 a[146:147], v21 offset:24832\n"
"ds_read_b64_tr_b16 a[148:149], v21 offset:28672\n"
"ds_read_b64_tr_b16 a[150:151], v21 offset:28928\n"
"ds_read_b64_tr_b16 a[224:225], v22 offset:24576\n"
"ds_read_b64_tr_b16 a[226:227], v22 offset:24832\n"
"ds_read_b64_tr_b16 a[228:229], v22 offset:28672\n"
"ds_read_b64_tr_b16 a[230:231], v22 offset:28928\n"
"s_waitcnt lgkmcnt(0)\n"
"s_barrier\n"
"buffer_load_dword v28, v12, s[36:39], 0 offen sc1\n"
"ds_write_b128 v15, v[32:35] offset:0\n"
"ds_write_b128 v15, v[36:39] offset:1024\n"
"ds_write_b128 v15, v[40:43] offset:2048\n"
"ds_write_b128 v15, v[44:47] offset:3072\n"
"ds_write_b128 v15, v[48:51] offset:4096\n"
"ds_write_b128 v15, v[52:55] offset:5120\n"
"ds_write_b128 v15, v[56:59] offset:6144\n"
"ds_write_b128 v15, v[60:63] offset:7168\n"
"ds_write_b128 v15, v[64:67] offset:8192\n"
"ds_write_b128 v15, v[68:71] offset:9216\n"
"ds_write_b128 v15, v[72:75] offset:10240\n"
"ds_write_b128 v15, v[76:79] offset:11264\n"
"ds_write_b128 v15, v[80:83] offset:12288\n"
"ds_write_b128 v15, v[84:87] offset:13312\n"
"ds_write_b128 v15, v[88:91] offset:14336\n"
"ds_write_b128 v15, v[92:95] offset:15360\n"
"ds_write_b128 v15, v[96:99] offset:16384\n"
"ds_write_b128 v15, v[100:103] offset:17408\n"
"ds_write_b128 v15, v[104:107] offset:18432\n"
"ds_write_b128 v15, v[108:111] offset:19456\n"
"ds_write_b128 v15, v[112:115] offset:20480\n"
"ds_write_b128 v15, v[116:119] offset:21504\n"
"ds_write_b128 v15, v[120:123] offset:22528\n"
"ds_write_b128 v15, v[124:127] offset:23552\n"
"s_waitcnt lgkmcnt(0)\n"
"ds_read_b128 v[96:99], v15 offset:0\n"
"ds_read_b128 v[100:103], v15 offset:1024\n"
"ds_read_b128 v[72:75], v15 offset:2048\n"
"ds_read_b128 v[76:79], v15 offset:3072\n"
"ds_read_b128 v[80:83], v15 offset:4096\n"
"ds_read_b128 v[84:87], v15 offset:5120\n"
"ds_read_b128 v[88:91], v15 offset:6144\n"
"ds_read_b128 v[92:95], v15 offset:7168\n"
"ds_read_b128 v[104:107], v15 offset:8192\n"
"ds_read_b128 v[108:111], v15 offset:9216\n"
"ds_read_b128 v[112:115], v15 offset:10240\n"
"ds_read_b128 v[116:119], v15 offset:11264\n"
"v_mov_b32 v32, 0\n"
"v_mov_b32 v33, 0\n"
"v_mov_b32 v34, 0\n"
"v_mov_b32 v35, 0\n"
"v_mov_b32 v36, 0\n"
"v_mov_b32 v37, 0\n"
"v_mov_b32 v38, 0\n"
"v_mov_b32 v39, 0\n"
"v_mov_b32 v40, 0\n"
"v_mov_b32 v41, 0\n"
"v_mov_b32 v42, 0\n"
"v_mov_b32 v43, 0\n"
"v_mov_b32 v44, 0\n"
"v_mov_b32 v45, 0\n"
"v_mov_b32 v46, 0\n"
"v_mov_b32 v47, 0\n"
"v_mov_b32 v48, 0\n"
"v_mov_b32 v49, 0\n"
"v_mov_b32 v50, 0\n"
"v_mov_b32 v51, 0\n"
"v_mov_b32 v52, 0\n"
"v_mov_b32 v53, 0\n"
"v_mov_b32 v54, 0\n"
"v_mov_b32 v55, 0\n"
"v_mov_b32 v120, 0\n"
"v_mov_b32 v121, 0\n"
"v_mov_b32 v122, 0\n"
"v_mov_b32 v123, 0\n"
"v_mov_b32 v124, 0\n"
"v_mov_b32 v125, 0\n"
"v_mov_b32 v126, 0\n"
"v_mov_b32 v127, 0\n"
"s_waitcnt vmcnt(0)\n"
"s_nop 3\n"
"S30_%=:\n"
"s_mov_b32 s45, 0\n"
"s_cmp_eq_u32 s73, 63\n"
"s_cselect_b32 s49, 234881024, s68\n"
"s_xor_b32 s49, s49, 16384\n"
"s_cmp_gt_u32 s69, s73\n"
"s_cbranch_scc1 S31_%=\n"
"s_add_u32 s75, s73, 1\n"
"v_cmp_gt_u32 vcc, s75, v28\n"
"s_nop 0\n"
"s_cbranch_vccz S35_%=\n"
"s_mov_b32 s76, 131072\n"
"s_branch S37_%=\n"
"S36_%=:\n"
"s_sleep 2\n"
"S37_%=:\n"
"buffer_load_dword v28, v12, s[36:39], 0 offen sc1\n"
"s_sub_u32 s76, s76, 1\n"
"s_cbranch_scc1 SABORT_%=\n"
"s_waitcnt vmcnt(0)\n"
"v_cmp_gt_u32 vcc, s75, v28\n"
"s_nop 0\n"
"s_cbranch_vccnz S36_%=\n"
"S35_%=:\n"
"s_add_u32 s51, s50, s47\n"
"s_add_u32 m0, s51, 0\n"
"s_add_u32 s72, s46, 0\n"
"buffer_load_dwordx4 v11, s[36:39], s72 offen sc1 lds\n"
"s_add_u32 m0, s51, 1024\n"
"s_add_u32 s72, s46, 2048\n"
"buffer_load_dwordx4 v11, s[36:39], s72 offen sc1 lds\n"
"s_add_u32 m0, s51, 2048\n"
"s_add_u32 s72, s46, 4096\n"
"buffer_load_dwordx4 v11, s[36:39], s72 offen sc1 lds\n"
"s_add_u32 s46, s46, 393216\n"
"s_sub_u32 s50, 53248, s50\n"
"s_add_u32 s69, s69, 1\n"
"S31_%=:\n"
"s_add_u32 s75, s73, 2\n"
"s_min_u32 s75, s75, 64\n"
"v_cmp_gt_u32 vcc, s75, v28\n"
"s_nop 0\n"
"s_cmp_eq_u64 vcc, 0\n"
"s_cselect_b32 s70, s46, 234881024\n"
"s_cselect_b32 s71, 393216, 0\n"
"s_cselect_b32 s72, 1, 0\n"
"s_cselect_b32 s75, 53248, 0\n"
"s_add_u32 m0, s50, s47\n"
"s_add_u32 s46, s46, s71\n"
"s_add_u32 s69, s69, s72\n"
"s_xor_b32 s50, s50, s75\n"
"s_add_u32 s71, s70, 1024\n"
"s_add_u32 s72, s70, 2048\n"
"s_add_u32 s60, s68, 37888\n"
"s_add_u32 s61, s68, 38912\n"
"s_add_u32 s62, s68, 39936\n"
"s_waitcnt lgkmcnt(0)\n"
"s_barrier\n"
"buffer_load_dwordx4 v[56:59], v0, s[36:39], s60 offen offset:0 sc1\n"
"s_nop 12\n"
"buffer_load_dwordx4 v[60:63], v0, s[36:39], s61 offen offset:0 sc1\n"
"s_nop 12\n"
"buffer_load_dwordx4 v[64:67], v0, s[36:39], s62 offen offset:0 sc1\n"
"buffer_load_dword v30, v0, s[36:39], s38 offen\n"
"S38_%=:\n"
"s_waitcnt vmcnt(3)\n"
"v_smfmac_f32_16x16x64_bf16 v[32:35], v[56:59], v[96:103], v6\n"
"ds_write_b128 v0, v[56:59] offset:3072\n"
"v_smfmac_f32_16x16x64_bf16 v[44:47], v[56:59], v[72:79], v6\n"
"v_smfmac_f32_16x16x64_bf16 v[36:39], v[56:59], v[128:135], v6\n"
"v_smfmac_f32_16x16x64_bf16 v[48:51], v[56:59], v[80:87], v6\n"
"s_waitcnt vmcnt(2)\n"
"ds_write_b128 v0, v[60:63] offset:4096\n"
"v_smfmac_f32_16x16x64_bf16 v[40:43], v[56:59], v[136:143], v6\n"
"v_max_u32 v29, v59, v63\n"
"v_smfmac_f32_16x16x64_bf16 v[32:35], v[60:63], v[88:95], v6\n"
"v_smfmac_f32_16x16x64_bf16 v[44:47], v[60:63], v[144:151], v6\n"
"s_waitcnt vmcnt(1)\n"
"ds_write_b128 v0, v[64:67] offset:5120\n"
"v_smfmac_f32_16x16x64_bf16 v[36:39], v[60:63], v[152:159], v6\n"
"v_max_u32 v29, v29, v67\n"
"v_smfmac_f32_16x16x64_bf16 v[48:51], v[60:63], v[104:111], v6\n"
"v_cmp_eq_u32 vcc, -1, v29\n"
"v_smfmac_f32_16x16x64_bf16 v[40:43], v[60:63], v[160:167], v6\n"
"s_cbranch_vccnz S32_%=\n"
"v_smfmac_f32_16x16x64_bf16 v[32:35], v[64:67], v[168:175], v6\n"
"s_waitcnt lgkmcnt(0)\n"
"s_barrier\n"
"ds_read_b128 v[68:71], v0 offset:6144\n"
"v_smfmac_f32_16x16x64_bf16 v[44:47], v[64:67], v[176:183], v6\n"
"ds_read_b128 v[72:75], v0 offset:7168\n"
"v_smfmac_f32_16x16x64_bf16 v[36:39], v[64:67], v[184:191], v6\n"
"ds_read_b128 v[76:79], v0 offset:8192\n"
"v_smfmac_f32_16x16x64_bf16 v[48:51], v[64:67], v[112:119], v6\n"
"v_smfmac_f32_16x16x64_bf16 v[40:43], v[64:67], v[192:199], v6\n"
"ds_read_b128 v[96:99], v15 offset:12288\n"
"s_waitcnt lgkmcnt(3)\n"
"v_smfmac_f32_16x16x64_bf16 v[32:35], v[68:71], v[200:207], v6\n"
"ds_read_b128 v[80:83], v0 offset:9216\n"
"v_smfmac_f32_16x16x64_bf16 v[36:39], v[68:71], v[208:215], v6\n"
"ds_read_b128 v[100:103], v15 offset:13312\n"
"s_waitcnt lgkmcnt(4)\n"
"v_smfmac_f32_16x16x64_bf16 v[32:35], v[72:75], v[216:223], v6\n"
"ds_read_b128 v[84:87], v0 offset:0\n"
"v_smfmac_f32_16x16x64_bf16 v[36:39], v[72:75], v[224:231], v6\n"
"ds_read_b128 v[104:107], v15 offset:14336\n"
"s_waitcnt lgkmcnt(5)\n"
"v_smfmac_f32_16x16x64_bf16 v[32:35], v[76:79], v[232:239], v6\n"
"ds_read_b128 v[88:91], v0 offset:1024\n"
"v_smfmac_f32_16x16x64_bf16 v[36:39], v[76:79], v[240:247], v6\n"
"ds_read_b128 v[108:111], v15 offset:15360\n"
"s_waitcnt lgkmcnt(5)\n"
"v_smfmac_f32_16x16x64_bf16 v[32:35], v[80:83], v[248:255], v6\n"
"ds_read_b128 v[92:95], v0 offset:2048\n"
"v_smfmac_f32_16x16x64_bf16 v[36:39], v[80:83], a[0:7], v6\n"
"ds_read_b128 v[112:115], v15 offset:16384\n"
"s_waitcnt lgkmcnt(5)\n"
"v_smfmac_f32_16x16x64_bf16 v[32:35], v[84:87], v[96:103], v6\n"
"v_smfmac_f32_16x16x64_bf16 v[36:39], v[84:87], a[8:15], v6\n"
"ds_read_b128 v[116:119], v15 offset:17408\n"
"s_waitcnt lgkmcnt(4)\n"
"v_smfmac_f32_16x16x64_bf16 v[32:35], v[88:91], a[16:23], v6\n"
"v_smfmac_f32_16x16x64_bf16 v[36:39], v[88:91], a[24:31], v6\n"
"ds_read_b128 v[96:99], v15 offset:18432\n"
"s_waitcnt lgkmcnt(3)\n"
"v_smfmac_f32_16x16x64_bf16 v[32:35], v[92:95], v[104:111], v6\n"
"ds_read_b128 v[100:103], v15 offset:19456\n"
"v_smfmac_f32_16x16x64_bf16 v[36:39], v[92:95], a[32:39], v6\n"
"v_smfmac_f32_16x16x64_bf16 v[40:43], v[68:71], a[40:47], v6\n"
"ds_read_b128 v[104:107], v15 offset:20480\n"
"v_smfmac_f32_16x16x64_bf16 v[40:43], v[72:75], a[48:55], v6\n"
"ds_read_b128 v[108:111], v15 offset:21504\n"
"s_waitcnt lgkmcnt(4)\n"
"v_smfmac_f32_16x16x64_bf16 v[40:43], v[76:79], v[112:119], v6\n"
"v_add_f32 v32, v32, v34\n"
"v_smfmac_f32_16x16x64_bf16 v[40:43], v[80:83], a[56:63], v6\n"
"ds_read_b128 v[112:115], v15 offset:22528\n"
"v_add_f32 v33, v33, v35\n"
"v_smfmac_f32_16x16x64_bf16 v[40:43], v[84:87], a[64:71], v6\n"
"v_cvt_pk_bf16_f32 v32, v32, v33\n"
"v_smfmac_f32_16x16x64_bf16 v[40:43], v[88:91], a[72:79], v6\n"
"ds_read_b128 v[116:119], v15 offset:23552\n"
"v_add_f32 v36, v36, v38\n"
"s_waitcnt lgkmcnt(4)\n"
"v_smfmac_f32_16x16x64_bf16 v[40:43], v[92:95], v[96:103], v6\n"
"ds_write_b32 v7, v32 offset:128\n"
"v_add_f32 v37, v37, v39\n"
"v_smfmac_f32_16x16x64_bf16 v[44:47], v[68:71], a[80:87], v6\n"
"ds_read_b128 v[96:99], v19 offset:0\n"
"v_cvt_pk_bf16_f32 v36, v36, v37\n"
"v_smfmac_f32_16x16x64_bf16 v[44:47], v[72:75], a[88:95], v6\n"
"ds_read_b128 v[100:103], v19 offset:1024\n"
"s_waitcnt lgkmcnt(5)\n"
"v_smfmac_f32_16x16x64_bf16 v[44:47], v[76:79], v[104:111], v6\n"
"ds_write_b32 v7, v36 offset:1152\n"
"v_smfmac_f32_16x16x64_bf16 v[44:47], v[80:83], a[96:103], v6\n"
"ds_read_b128 v[104:107], v19 offset:6144\n"
"v_add_f32 v40, v40, v42\n"
"v_smfmac_f32_16x16x64_bf16 v[44:47], v[84:87], a[104:111], v6\n"
"ds_read_b128 v[108:111], v19 offset:7168\n"
"v_add_f32 v41, v41, v43\n"
"s_waitcnt lgkmcnt(6)\n"
"v_smfmac_f32_16x16x64_bf16 v[44:47], v[88:91], v[112:119], v6\n"
"v_cvt_pk_bf16_f32 v40, v40, v41\n"
"v_smfmac_f32_16x16x64_bf16 v[44:47], v[92:95], a[112:119], v6\n"
"ds_write_b32 v7, v40 offset:2176\n"
"v_smfmac_f32_16x16x64_bf16 v[48:51], v[68:71], a[120:127], v6\n"
"ds_read_b128 v[112:115], v19 offset:2048\n"
"v_smfmac_f32_16x16x64_bf16 v[48:51], v[72:75], a[128:135], v6\n"
"ds_read_b128 v[116:119], v19 offset:3072\n"
"v_smfmac_f32_16x16x64_bf16 v[48:51], v[76:79], a[136:143], v6\n"
"buffer_load_dwordx4 v11, s[36:39], s70 offen offset:0 sc1 lds\n"
"s_nop 0\n"
"v_add_f32 v44, v44, v46\n"
"v_smfmac_f32_16x16x64_bf16 v[48:51], v[80:83], a[144:151], v6\n"
"s_waitcnt lgkmcnt(0)\n"
"s_barrier\n"
"ds_read_b64_tr_b16 v[32:33], v8 offset:0\n"
"v_add_f32 v45, v45, v47\n"
"v_smfmac_f32_16x16x64_bf16 v[48:51], v[84:87], a[152:159], v6\n"
"ds_read_b64_tr_b16 v[34:35], v8 offset:256\n"
"ds_read_b64_tr_b16 v[36:37], v8 offset:1024\n"
"v_smfmac_f32_16x16x64_bf16 v[48:51], v[88:91], a[160:167], v6\n"
"ds_read_b64_tr_b16 v[38:39], v8 offset:1280\n"
"v_cvt_pk_bf16_f32 v44, v44, v45\n"
"v_smfmac_f32_16x16x64_bf16 v[48:51], v[92:95], a[168:175], v6\n"
"buffer_load_dwordx4 v11, s[36:39], s71 offen offset:1024 sc1 lds\n"
"ds_write_b32 v7, v44 offset:3200\n"
"v_smfmac_f32_16x16x64_bf16 v[52:55], v[56:59], a[176:183], v6\n"
"ds_read_b64_tr_b16 v[40:41], v8 offset:2048\n"
"ds_read_b64_tr_b16 v[42:43], v8 offset:2304\n"
"v_smfmac_f32_16x16x64_bf16 v[52:55], v[60:63], a[184:191], v6\n"
"ds_read_b128 v[56:59], v19 offset:8192\n"
"v_smfmac_f32_16x16x64_bf16 v[52:55], v[64:67], a[192:199], v6\n"
"ds_read_b128 v[60:63], v19 offset:9216\n"
"v_add_f32 v48, v48, v50\n"
"v_add_f32 v49, v49, v51\n"
"v_smfmac_f32_16x16x64_bf16 v[52:55], v[68:71], a[200:207], v6\n"
"buffer_load_dwordx4 v11, s[36:39], s72 offen offset:2048 sc1 lds\n"
"ds_read_b128 v[64:67], v19 offset:4096\n"
"v_cvt_pk_bf16_f32 v48, v48, v49\n"
"v_smfmac_f32_16x16x64_bf16 v[52:55], v[72:75], a[208:215], v6\n"
"ds_write_b32 v7, v48 offset:4224\n"
"v_smfmac_f32_16x16x64_bf16 v[52:55], v[76:79], a[216:223], v6\n"
"ds_read_b128 v[68:71], v19 offset:5120\n"
"v_smfmac_f32_16x16x64_bf16 v[52:55], v[80:83], a[224:231], v6\n"
"ds_read_b128 v[72:75], v19 offset:10240\n"
"v_smfmac_f32_16x16x64_bf16 v[52:55], v[84:87], a[232:239], v6\n"
"ds_read_b128 v[76:79], v19 offset:11264\n"
"v_smfmac_f32_16x16x64_bf16 v[52:55], v[88:91], a[240:247], v6\n"
"s_waitcnt lgkmcnt(3)\n"
"s_barrier\n"
"ds_read_b64_tr_b16 v[80:81], v8 offset:3072\n"
"ds_read_b64_tr_b16 v[82:83], v8 offset:3328\n"
"v_smfmac_f32_16x16x64_bf16 v[52:55], v[92:95], a[248:255], v6\n"
"ds_read_b64_tr_b16 v[84:85], v8 offset:4096\n"
"ds_read_b64_tr_b16 v[86:87], v8 offset:4352\n"
"v_smfmac_f32_16x16x64_bf16 v[120:123], v[32:35], v[96:103], v6\n"
"v_smfmac_f32_16x16x64_bf16 v[124:127], v[32:35], v[104:111], v6\n"
"s_nop 3\n"
"v_add_f32 v52, v52, v54\n"
"v_add_f32 v53, v53, v55\n"
"v_cvt_pk_bf16_f32 v52, v52, v53\n"
"ds_write_b32 v7, v52 offset:5248\n"
"v_smfmac_f32_16x16x64_bf16 v[120:123], v[36:39], v[112:119], v6\n"
"v_smfmac_f32_16x16x64_bf16 v[124:127], v[36:39], v[56:63], v6\n"
"s_waitcnt lgkmcnt(7)\n"
"v_smfmac_f32_16x16x64_bf16 v[120:123], v[40:43], v[64:71], v6\n"
"s_waitcnt lgkmcnt(5)\n"
"v_smfmac_f32_16x16x64_bf16 v[124:127], v[40:43], v[72:79], v6\n"
"s_mov_b64 vcc, s[82:83]\n"
"s_waitcnt lgkmcnt(0)\n"
"s_barrier\n"
"ds_read_b64_tr_b16 v[88:89], v8 offset:5120\n"
"ds_read_b64_tr_b16 v[90:91], v8 offset:5376\n"
"v_mfma_f32_16x16x32_bf16 v[20:23], v[32:35], v[80:83], 0\n"
"v_add_f32 v120, v120, v122\n"
"v_add_f32 v121, v121, v123\n"
"v_mfma_f32_16x16x32_bf16 v[20:23], v[36:39], v[84:87], v[20:23]\n"
"v_add_f32 v124, v124, v126\n"
"v_add_f32 v125, v125, v127\n"
"v_cndmask_b32_dpp v97, v120, v121, vcc quad_perm:[2,3,2,3] row_mask:0xf bank_mask:0xf\n"
"s_nop 0\n"
"v_cndmask_b32_dpp v99, v124, v125, vcc quad_perm:[2,3,2,3] row_mask:0xf bank_mask:0xf\n"
"s_waitcnt lgkmcnt(0)\n"
"v_mfma_f32_16x16x32_bf16 v[20:23], v[40:43], v[88:91], v[20:23]\n"
"s_not_b64 vcc, s[82:83]\n"
"v_cndmask_b32_dpp v96, v121, v120, vcc quad_perm:[0,1,0,1] row_mask:0xf bank_mask:0xf\n"
"v_cndmask_b32_dpp v98, v125, v124, vcc quad_perm:[0,1,0,1] row_mask:0xf bank_mask:0xf\n"
"v_max_f32 v24, v97, v99\n"
"v_max3_f32 v24, v24, v96, v98\n"
"s_nop 1\n"
"v_max_f32_dpp v24, v24, v24 quad_perm:[1,0,3,2] row_mask:0xf bank_mask:0xf\n"
"v_add_f32_dpp v3, v22, v20 quad_perm:[2,3,2,3] row_mask:0xf bank_mask:0xf\n"
"v_add_f32_dpp v4, v21, v23 quad_perm:[0,1,0,1] row_mask:0xf bank_mask:0xf\n"
"v_max_f32_dpp v24, v24, v24 row_ror:4 row_mask:0xf bank_mask:0xf\n"
"v_cndmask_b32 v100, v4, v3, vcc\n"
"s_nop 0\n"
"v_max_f32_dpp v24, v24, v24 row_ror:8 row_mask:0xf bank_mask:0xf\n"
"v_mul_f32 v25, s74, v24\n"
"S34_%=:\n"
"v_fma_f32 v102, v96, s74, -v25\n"
"v_fma_f32 v103, v97, s74, -v25\n"
"v_fma_f32 v104, v98, s74, -v25\n"
"v_fma_f32 v105, v99, s74, -v25\n"
"v_fma_f32 v3, v100, s74, -v25\n"
"v_exp_f32 v102, v102\n"
"v_exp_f32 v103, v103\n"
"v_exp_f32 v104, v104\n"
"v_exp_f32 v105, v105\n"
"v_exp_f32 v5, v3\n"
"v_add_f32 v26, v102, v103\n"
"v_add_f32 v4, v104, v105\n"
"v_add_f32 v26, v26, v5\n"
"v_add_f32 v26, v26, v4\n"
"v_cmp_nge_f32 vcc, s81, v3\n"
"s_nop 0\n"
"v_add_f32_dpp v26, v26, v26 quad_perm:[1,0,3,2] row_mask:0xf bank_mask:0xf\n"
"s_nop 1\n"
"v_add_f32_dpp v26, v26, v26 row_ror:4 row_mask:0xf bank_mask:0xf\n"
"s_cbranch_vccnz S33_%=\n"
"S33ON_%=:\n"
"s_nop 0\n"
"v_add_f32_dpp v26, v26, v26 row_ror:8 row_mask:0xf bank_mask:0xf\n"
"v_rcp_f32 v26, v26\n"
"s_nop 0\n"
"v_mul_f32 v102, v102, v26\n"
"v_mul_f32 v103, v103, v26\n"
"v_mul_f32 v104, v104, v26\n"
"v_mul_f32 v105, v105, v26\n"
"v_cvt_pk_bf16_f32 v2, v102, v103\n"
"v_cvt_pk_bf16_f32 v3, v104, v105\n"
"v_min_u32 v2, -2, v2\n"
"v_min_u32 v3, -2, v3\n"
"buffer_store_dwordx2 v[2:3], v10, s[36:39], s49 offen sc1\n"
"s_nop 3\n"
"s_add_u32 s75, s73, 3\n"
"s_min_u32 s75, s75, 64\n"
"v_cmp_gt_u32 vcc, s75, v28\n"
"s_nop 0\n"
"s_mov_b64 exec, vcc\n"
"buffer_load_dword v28, v12, s[36:39], 0 offen sc1\n"
"s_mov_b64 exec, -1\n"
"s_mov_b64 vcc, s[82:83]\n"
"v_cndmask_b32_dpp v57, v84, v86, vcc quad_perm:[2,3,0,1] row_mask:0xf bank_mask:0xf\n"
"v_cndmask_b32_dpp v59, v85, v87, vcc quad_perm:[2,3,0,1] row_mask:0xf bank_mask:0xf\n"
"s_not_b64 vcc, s[82:83]\n"
"v_cndmask_b32_dpp v56, v86, v84, vcc quad_perm:[2,3,0,1] row_mask:0xf bank_mask:0xf\n"
"v_cndmask_b32_dpp v58, v87, v85, vcc quad_perm:[2,3,0,1] row_mask:0xf bank_mask:0xf\n"
"v_min_u32 v56, -2, v56\n"
"v_min_u32 v57, -2, v57\n"
"v_min_u32 v58, -2, v58\n"
"v_min_u32 v59, -2, v59\n"
"buffer_store_dwordx4 v[56:59], v9, s[36:39], s48 offen sc1\n"
"s_add_u32 s48, s48, 98304\n"
"s_add_u32 s73, s73, 1\n"
"s_add_u32 s79, s68, 131072\n"
"s_and_b32 s79, s79, 0x3fffff\n"
"s_xor_b32 s68, s79, s68\n"
"s_lshr_b32 s68, s68, 8\n"
"s_and_b32 s68, s68, 8192\n"
"s_xor_b32 s68, s68, s79\n"
"v_xor_b32 v19, 53248, v19\n"
"s_cmp_lt_u32 s73, 64\n"
"s_cbranch_scc0 S29_%=\n"
"ds_read_b128 v[96:99], v15 offset:0\n"
"ds_read_b128 v[100:103], v15 offset:1024\n"
"ds_read_b128 v[72:75], v15 offset:2048\n"
"ds_read_b128 v[76:79], v15 offset:3072\n"
"ds_read_b128 v[80:83], v15 offset:4096\n"
"ds_read_b128 v[84:87], v15 offset:5120\n"
"ds_read_b128 v[88:91], v15 offset:6144\n"
"ds_read_b128 v[92:95], v15 offset:7168\n"
"ds_read_b128 v[104:107], v15 offset:8192\n"
"ds_read_b128 v[108:111], v15 offset:9216\n"
"ds_read_b128 v[112:115], v15 offset:10240\n"
"ds_read_b128 v[116:119], v15 offset:11264\n"
"v_mov_b32 v32, 0\n"
"v_mov_b32 v33, 0\n"
"v_mov_b32 v34, 0\n"
"v_mov_b32 v35, 0\n"
"v_mov_b32 v36, 0\n"
"v_mov_b32 v37, 0\n"
"v_mov_b32 v38, 0\n"
"v_mov_b32 v39, 0\n"
"v_mov_b32 v40, 0\n"
"v_mov_b32 v41, 0\n"
"v_mov_b32 v42, 0\n"
"v_mov_b32 v43, 0\n"
"v_mov_b32 v44, 0\n"
"v_mov_b32 v45, 0\n"
"v_mov_b32 v46, 0\n"
"v_mov_b32 v47, 0\n"
"v_mov_b32 v48, 0\n"
"v_mov_b32 v49, 0\n"
"v_mov_b32 v50, 0\n"
"v_mov_b32 v51, 0\n"
"v_mov_b32 v52, 0\n"
"v_mov_b32 v53, 0\n"
"v_mov_b32 v54, 0\n"
"v_mov_b32 v55, 0\n"
"v_mov_b32 v120, 0\n"
"v_mov_b32 v121, 0\n"
"v_mov_b32 v122, 0\n"
"v_mov_b32 v123, 0\n"
"v_mov_b32 v124, 0\n"
"v_mov_b32 v125, 0\n"
"v_mov_b32 v126, 0\n"
"v_mov_b32 v127, 0\n"
"s_waitcnt vmcnt(1)\n"
"s_cmp_lg_u32 s73, 63\n"
"s_cbranch_scc1 S30_%=\n"
"s_waitcnt vmcnt(0)\n"
"s_branch S30_%=\n"
"S32_%=:\n"
"s_waitcnt lgkmcnt(0)\n"
"s_mov_b32 s76, 131072\n"
"S39_%=:\n"
"s_memrealtime s[4:5]\n"
"buffer_load_dwordx4 v[56:59], v0, s[36:39], s60 offen offset:0 sc1\n"
"buffer_load_dwordx4 v[60:63], v0, s[36:39], s61 offen offset:0 sc1\n"
"buffer_load_dwordx4 v[64:67], v0, s[36:39], s62 offen offset:0 sc1\n"
"v_mov_b32 v29, 0\n"
"ds_read_b32 v31, v29 offset:16896\n"
"s_lshl_b32 s77, s73, 14\n"
"s_add_u32 s77, s77, s20\n"
"s_add_u32 s77, s77, s21\n"
"s_add_u32 s77, s77, 4096\n"
"v_mov_b32 v32, 0\n"
"v_mov_b32 v33, 0\n"
"v_mov_b32 v34, 0\n"
"v_mov_b32 v35, 0\n"
"v_mov_b32 v36, 0\n"
"v_mov_b32 v37, 0\n"
"v_mov_b32 v38, 0\n"
"v_mov_b32 v39, 0\n"
"v_mov_b32 v40, 0\n"
"v_mov_b32 v41, 0\n"
"v_mov_b32 v42, 0\n"
"v_mov_b32 v43, 0\n"
"v_mov_b32 v44, 0\n"
"v_mov_b32 v45, 0\n"
"v_mov_b32 v46, 0\n"
"v_mov_b32 v47, 0\n"
"v_mov_b32 v48, 0\n"
"v_mov_b32 v49, 0\n"
"v_mov_b32 v50, 0\n"
"v_mov_b32 v51, 0\n"
"v_mov_b32 v52, 0\n"
"v_mov_b32 v53, 0\n"
"v_mov_b32 v54, 0\n"
"v_mov_b32 v55, 0\n"
"s_sub_u32 s76, s76, 1\n"
"s_cbranch_scc1 SABORT_%=\n"
"s_waitcnt vmcnt(2)\n"
"v_max3_u32 v29, v56, v57, 0\n"
"v_max3_u32 v29, v58, v59, v29\n"
"s_waitcnt vmcnt(1)\n"
"v_max3_u32 v29, v60, v61, v29\n"
"v_max3_u32 v29, v62, v63, v29\n"
"s_waitcnt vmcnt(0)\n"
"v_max3_u32 v29, v64, v65, v29\n"
"v_max3_u32 v29, v66, v67, v29\n"
"v_cmp_eq_u32 vcc, -1, v29\n"
"s_nop 0\n"
"s_cbranch_vccnz S39_%=\n"
"s_waitcnt lgkmcnt(0)\n"
"v_lshl_add_u32 v31, s4, 3, v31\n"
"v_lshrrev_b32 v30, 2, v0\n"
"s_mov_b64 exec, 0xffff\n"
"buffer_store_dword v31, v30, s[36:39], s77 offen sc1\n"
"s_mov_b64 exec, -1\n"
"s_branch S38_%=\n"
"S33_%=:\n"
"s_cmp_lg_u32 s45, 0\n"
"s_cbranch_scc1 S40_%=\n"
"v_mov_b32 v3, 0\n"
"v_and_b32 v4, 0x7fff, v80\n"
"v_max_u32 v3, v3, v4\n"
"v_bfe_u32 v4, v80, 16, 15\n"
"v_max_u32 v3, v3, v4\n"
"v_and_b32 v4, 0x7fff, v81\n"
"v_max_u32 v3, v3, v4\n"
"v_bfe_u32 v4, v81, 16, 15\n"
"v_max_u32 v3, v3, v4\n"
"v_and_b32 v4, 0x7fff, v82\n"
"v_max_u32 v3, v3, v4\n"
"v_bfe_u32 v4, v82, 16, 15\n"
"v_max_u32 v3, v3, v4\n"
"v_and_b32 v4, 0x7fff, v83\n"
"v_max_u32 v3, v3, v4\n"
"v_bfe_u32 v4, v83, 16, 15\n"
"v_max_u32 v3, v3, v4\n"
"v_and_b32 v4, 0x7fff, v84\n"
"v_max_u32 v3, v3, v4\n"
"v_bfe_u32 v4, v84, 16, 15\n"
"v_max_u32 v3, v3, v4\n"
"v_and_b32 v4, 0x7fff, v85\n"
"v_max_u32 v3, v3, v4\n"
"v_bfe_u32 v4, v85, 16, 15\n"
"v_max_u32 v3, v3, v4\n"
"v_and_b32 v4, 0x7fff, v86\n"
"v_max_u32 v3, v3, v4\n"
"v_bfe_u32 v4, v86, 16, 15\n"
"v_max_u32 v3, v3, v4\n"
"v_and_b32 v4, 0x7fff, v87\n"
"v_max_u32 v3, v3, v4\n"
"v_bfe_u32 v4, v87, 16, 15\n"
"v_max_u32 v3, v3, v4\n"
"v_and_b32 v4, 0x7fff, v88\n"
"v_max_u32 v3, v3, v4\n"
"v_bfe_u32 v4, v88, 16, 15\n"
"v_max_u32 v3, v3, v4\n"
"v_and_b32 v4, 0x7fff, v89\n"
"v_max_u32 v3, v3, v4\n"
"v_bfe_u32 v4, v89, 16, 15\n"
"v_max_u32 v3, v3, v4\n"
"v_and_b32 v4, 0x7fff, v90\n"
"v_max_u32 v3, v3, v4\n"
"v_bfe_u32 v4, v90, 16, 15\n"
"v_max_u32 v3, v3, v4\n"
"v_and_b32 v4, 0x7fff, v91\n"
"v_max_u32 v3, v3, v4\n"
"v_bfe_u32 v4, v91, 16, 15\n"
"v_max_u32 v3, v3, v4\n"
"v_cmp_lt_u32 vcc, 0x7f80, v3\n"
"s_nop 0\n"
"s_cbranch_vccz S40_%=\n"
"s_mov_b32 s45, 1\n"
"ds_read_b128 v[96:99], v15 offset:0\n"
"ds_read_b128 v[100:103], v15 offset:1024\n"
"ds_read_b128 v[72:75], v15 offset:2048\n"
"ds_read_b128 v[76:79], v15 offset:3072\n"
"ds_read_b128 v[80:83], v15 offset:4096\n"
"ds_read_b128 v[84:87], v15 offset:5120\n"
"ds_read_b128 v[88:91], v15 offset:6144\n"
"ds_read_b128 v[92:95], v15 offset:7168\n"
"ds_read_b128 v[104:107], v15 offset:8192\n"
"ds_read_b128 v[108:111], v15 offset:9216\n"
"ds_read_b128 v[112:115], v15 offset:10240\n"
"ds_read_b128 v[116:119], v15 offset:11264\n"
"v_mov_b32 v32, 0\n"
"v_mov_b32 v33, 0\n"
"v_mov_b32 v34, 0\n"
"v_mov_b32 v35, 0\n"
"v_mov_b32 v36, 0\n"
"v_mov_b32 v37, 0\n"
"v_mov_b32 v38, 0\n"
"v_mov_b32 v39, 0\n"
"v_mov_b32 v40, 0\n"
"v_mov_b32 v41, 0\n"
"v_mov_b32 v42, 0\n"
"v_mov_b32 v43, 0\n"
"v_mov_b32 v44, 0\n"
"v_mov_b32 v45, 0\n"
"v_mov_b32 v46, 0\n"
"v_mov_b32 v47, 0\n"
"v_mov_b32 v48, 0\n"
"v_mov_b32 v49, 0\n"
"v_mov_b32 v50, 0\n"
"v_mov_b32 v51, 0\n"
"v_mov_b32 v52, 0\n"
"v_mov_b32 v53, 0\n"
"v_mov_b32 v54, 0\n"
"v_mov_b32 v55, 0\n"
"v_mov_b32 v120, 0\n"
"v_mov_b32 v121, 0\n"
"v_mov_b32 v122, 0\n"
"v_mov_b32 v123, 0\n"
"v_mov_b32 v124, 0\n"
"v_mov_b32 v125, 0\n"
"v_mov_b32 v126, 0\n"
"v_mov_b32 v127, 0\n"
"s_waitcnt lgkmcnt(0)\n"
"s_barrier\n"
"s_branch S32_%=\n"
"S40_%=:\n"
"s_cmp_eq_u32 s45, 2\n"
"s_cbranch_scc1 S41_%=\n"
"s_mov_b32 s45, 2\n"
"v_max_f32 v24, v24, v100\n"
"s_nop 1\n"
"v_max_f32_dpp v24, v24, v24 quad_perm:[1,0,3,2] row_mask:0xf bank_mask:0xf\n"
"s_nop 1\n"
"v_max_f32_dpp v24, v24, v24 row_ror:4 row_mask:0xf bank_mask:0xf\n"
"s_nop 1\n"
"v_max_f32_dpp v24, v24, v24 row_ror:8 row_mask:0xf bank_mask:0xf\n"
"v_mul_f32 v25, s74, v24\n"
"s_branch S34_%=\n"
"S41_%=:\n"
"s_branch S33ON_%=\n"
"S29_%=:\n"
"s_waitcnt vmcnt(0)\n"
"s_barrier\n"
"s_branch S5_%=\n"
"S3_%=:\n"
"s_mov_b32 s22, 46301184\n"
"s_mov_b32 s23, 46317568\n"
"s_cmp_lg_u32 s66, 0\n"
"s_cbranch_scc1 S42_%=\n"
"v_mov_b32 v24, 0\n"
"s_mov_b64 exec, 1\n"
"buffer_atomic_add v24, v26, s[36:39], s23 offen sc0 sc1\n"
"s_mov_b64 exec, -1\n"
"S42_%=:\n"
"s_add_u32 m0, s45, 0\n"
"s_add_u32 s72, s44, 0\n"
"buffer_load_dwordx4 v1, s[40:43], s72 offen lds\n"
"s_add_u32 m0, s45, 1024\n"
"s_add_u32 s72, s44, 98304\n"
"buffer_load_dwordx4 v2, s[40:43], s72 offen lds\n"
"s_add_u32 m0, s45, 2048\n"
"s_add_u32 s72, s44, 196608\n"
"buffer_load_dwordx4 v1, s[40:43], s72 offen lds\n"
"s_add_u32 m0, s45, 3072\n"
"s_add_u32 s72, s44, 294912\n"
"buffer_load_dwordx4 v2, s[40:43], s72 offen lds\n"
"s_add_u32 m0, s45, 8192\n"
"s_add_u32 s72, s44, 128\n"
"buffer_load_dwordx4 v1, s[40:43], s72 offen lds\n"
"s_add_u32 m0, s45, 9216\n"
"s_add_u32 s72, s44, 98432\n"
"buffer_load_dwordx4 v2, s[40:43], s72 offen lds\n"
"s_add_u32 m0, s45, 10240\n"
"s_add_u32 s72, s44, 196736\n"
"buffer_load_dwordx4 v1, s[40:43], s72 offen lds\n"
"s_add_u32 m0, s45, 11264\n"
"s_add_u32 s72, s44, 295040\n"
"buffer_load_dwordx4 v2, s[40:43], s72 offen lds\n"
"s_add_u32 m0, s45, 16384\n"
"s_add_u32 s72, s44, 256\n"
"buffer_load_dwordx4 v1, s[40:43], s72 offen lds\n"
"s_add_u32 m0, s45, 17408\n"
"s_add_u32 s72, s44, 98560\n"
"buffer_load_dwordx4 v2, s[40:43], s72 offen lds\n"
"s_add_u32 m0, s45, 18432\n"
"s_add_u32 s72, s44, 196864\n"
"buffer_load_dwordx4 v1, s[40:43], s72 offen lds\n"
"s_add_u32 m0, s45, 19456\n"
"s_add_u32 s72, s44, 295168\n"
"buffer_load_dwordx4 v2, s[40:43], s72 offen lds\n"
"s_add_u32 m0, s45, 49152\n"
"s_add_u32 s72, s44, 786432\n"
"buffer_load_dwordx4 v1, s[40:43], s72 offen lds\n"
"s_add_u32 m0, s45, 50176\n"
"s_add_u32 s72, s44, 884736\n"
"buffer_load_dwordx4 v2, s[40:43], s72 offen lds\n"
"s_add_u32 m0, s45, 51200\n"
"s_add_u32 s72, s44, 983040\n"
"buffer_load_dwordx4 v1, s[40:43], s72 offen lds\n"
"s_add_u32 m0, s45, 52224\n"
"s_add_u32 s72, s44, 1081344\n"
"buffer_load_dwordx4 v2, s[40:43], s72 offen lds\n"
"s_add_u32 m0, s45, 57344\n"
"s_add_u32 s72, s44, 786560\n"
"buffer_load_dwordx4 v1, s[40:43], s72 offen lds\n"
"s_add_u32 m0, s45, 58368\n"
"s_add_u32 s72, s44, 884864\n"
"buffer_load_dwordx4 v2, s[40:43], s72 offen lds\n"
"s_add_u32 m0, s45, 59392\n"
"s_add_u32 s72, s44, 983168\n"
"buffer_load_dwordx4 v1, s[40:43], s72 offen lds\n"
"s_add_u32 m0, s45, 60416\n"
"s_add_u32 s72, s44, 1081472\n"
"buffer_load_dwordx4 v2, s[40:43], s72 offen lds\n"
"s_add_u32 m0, s45, 65536\n"
"s_add_u32 s72, s44, 786688\n"
"buffer_load_dwordx4 v1, s[40:43], s72 offen lds\n"
"s_add_u32 m0, s45, 66560\n"
"s_add_u32 s72, s44, 884992\n"
"buffer_load_dwordx4 v2, s[40:43], s72 offen lds\n"
"s_add_u32 m0, s45, 67584\n"
"s_add_u32 s72, s44, 983296\n"
"buffer_load_dwordx4 v1, s[40:43], s72 offen lds\n"
"s_add_u32 m0, s45, 68608\n"
"s_add_u32 s72, s44, 1081600\n"
"buffer_load_dwordx4 v2, s[40:43], s72 offen lds\n"
"s_waitcnt vmcnt(12)\n"
"s_waitcnt lgkmcnt(0)\n"
"s_barrier\n"
"s_cmp_lg_u32 s66, 0\n"
"s_cbranch_scc1 S43_%=\n"
"v_or_b32 v24, 1, v24\n"
"v_add_u32 v24, 1, v24\n"
"buffer_store_dword v24, v26, s[36:39], s22 offen sc1\n"
"S43_%=:\n"
"s_add_u32 m0, s45, 98304\n"
"s_add_u32 s72, s44, 1572864\n"
"buffer_load_dwordx4 v1, s[40:43], s72 offen lds\n"
"s_add_u32 m0, s45, 99328\n"
"s_add_u32 s72, s44, 1671168\n"
"buffer_load_dwordx4 v2, s[40:43], s72 offen lds\n"
"s_add_u32 m0, s45, 100352\n"
"s_add_u32 s72, s44, 1769472\n"
"buffer_load_dwordx4 v1, s[40:43], s72 offen lds\n"
"s_add_u32 m0, s45, 101376\n"
"s_add_u32 s72, s44, 1867776\n"
"buffer_load_dwordx4 v2, s[40:43], s72 offen lds\n"
"s_add_u32 m0, s45, 106496\n"
"s_add_u32 s72, s44, 1572992\n"
"buffer_load_dwordx4 v1, s[40:43], s72 offen lds\n"
"s_add_u32 m0, s45, 107520\n"
"s_add_u32 s72, s44, 1671296\n"
"buffer_load_dwordx4 v2, s[40:43], s72 offen lds\n"
"s_add_u32 m0, s45, 108544\n"
"s_add_u32 s72, s44, 1769600\n"
"buffer_load_dwordx4 v1, s[40:43], s72 offen lds\n"
"s_add_u32 m0, s45, 109568\n"
"s_add_u32 s72, s44, 1867904\n"
"buffer_load_dwordx4 v2, s[40:43], s72 offen lds\n"
"s_add_u32 m0, s45, 114688\n"
"s_add_u32 s72, s44, 1573120\n"
"buffer_load_dwordx4 v1, s[40:43], s72 offen lds\n"
"s_add_u32 m0, s45, 115712\n"
"s_add_u32 s72, s44, 1671424\n"
"buffer_load_dwordx4 v2, s[40:43], s72 offen lds\n"
"s_add_u32 m0, s45, 116736\n"
"s_add_u32 s72, s44, 1769728\n"
"buffer_load_dwordx4 v1, s[40:43], s72 offen lds\n"
"s_add_u32 m0, s45, 117760\n"
"s_add_u32 s72, s44, 1868032\n"
"buffer_load_dwordx4 v2, s[40:43], s72 offen lds\n"
"v_add_u32 v20, 0, v3\n"
"v_add_u32 v21, 0, v4\n"
"v_add_u32 v22, 0, v5\n"
"ds_read_b64_tr_b16 v[232:233], v20 offset:0\n"
"ds_read_b64_tr_b16 v[234:235], v20 offset:256\n"
"ds_read_b64_tr_b16 v[236:237], v20 offset:4096\n"
"ds_read_b64_tr_b16 v[238:239], v20 offset:4352\n"
"ds_read_b64_tr_b16 v[240:241], v21 offset:0\n"
"ds_read_b64_tr_b16 v[242:243], v21 offset:256\n"
"ds_read_b64_tr_b16 v[244:245], v21 offset:4096\n"
"ds_read_b64_tr_b16 v[246:247], v21 offset:4352\n"
"ds_read_b64_tr_b16 v[96:97], v22 offset:0\n"
"ds_read_b64_tr_b16 v[98:99], v22 offset:256\n"
"ds_read_b64_tr_b16 v[100:101], v22 offset:4096\n"
"ds_read_b64_tr_b16 v[102:103], v22 offset:4352\n"
"ds_read_b64_tr_b16 v[112:113], v20 offset:24576\n"
"ds_read_b64_tr_b16 v[114:115], v20 offset:24832\n"
"ds_read_b64_tr_b16 v[116:117], v20 offset:28672\n"
"ds_read_b64_tr_b16 v[118:119], v20 offset:28928\n"
"ds_read_b64_tr_b16 a[136:137], v21 offset:24576\n"
"ds_read_b64_tr_b16 a[138:139], v21 offset:24832\n"
"ds_read_b64_tr_b16 a[140:141], v21 offset:28672\n"
"ds_read_b64_tr_b16 a[142:143], v21 offset:28928\n"
"ds_read_b64_tr_b16 a[216:217], v22 offset:24576\n"
"ds_read_b64_tr_b16 a[218:219], v22 offset:24832\n"
"ds_read_b64_tr_b16 a[220:221], v22 offset:28672\n"
"ds_read_b64_tr_b16 a[222:223], v22 offset:28928\n"
"s_waitcnt vmcnt(12)\n"
"s_waitcnt lgkmcnt(0)\n"
"s_barrier\n"
"s_add_u32 m0, s45, 0\n"
"s_add_u32 s72, s44, 2359296\n"
"buffer_load_dwordx4 v1, s[40:43], s72 offen lds\n"
"s_add_u32 m0, s45, 1024\n"
"s_add_u32 s72, s44, 2457600\n"
"buffer_load_dwordx4 v2, s[40:43], s72 offen lds\n"
"s_add_u32 m0, s45, 2048\n"
"s_add_u32 s72, s44, 2555904\n"
"buffer_load_dwordx4 v1, s[40:43], s72 offen lds\n"
"s_add_u32 m0, s45, 3072\n"
"s_add_u32 s72, s44, 2654208\n"
"buffer_load_dwordx4 v2, s[40:43], s72 offen lds\n"
"s_add_u32 m0, s45, 8192\n"
"s_add_u32 s72, s44, 2359424\n"
"buffer_load_dwordx4 v1, s[40:43], s72 offen lds\n"
"s_add_u32 m0, s45, 9216\n"
"s_add_u32 s72, s44, 2457728\n"
"buffer_load_dwordx4 v2, s[40:43], s72 offen lds\n"
"s_add_u32 m0, s45, 10240\n"
"s_add_u32 s72, s44, 2556032\n"
"buffer_load_dwordx4 v1, s[40:43], s72 offen lds\n"
"s_add_u32 m0, s45, 11264\n"
"s_add_u32 s72, s44, 2654336\n"
"buffer_load_dwordx4 v2, s[40:43], s72 offen lds\n"
"s_add_u32 m0, s45, 16384\n"
"s_add_u32 s72, s44, 2359552\n"
"buffer_load_dwordx4 v1, s[40:43], s72 offen lds\n"
"s_add_u32 m0, s45, 17408\n"
"s_add_u32 s72, s44, 2457856\n"
"buffer_load_dwordx4 v2, s[40:43], s72 offen lds\n"
"s_add_u32 m0, s45, 18432\n"
"s_add_u32 s72, s44, 2556160\n"
"buffer_load_dwordx4 v1, s[40:43], s72 offen lds\n"
"s_add_u32 m0, s45, 19456\n"
"s_add_u32 s72, s44, 2654464\n"
"buffer_load_dwordx4 v2, s[40:43], s72 offen lds\n"
"v_add_u32 v20, 49152, v3\n"
"v_add_u32 v21, 49152, v4\n"
"v_add_u32 v22, 49152, v5\n"
"ds_read_b64_tr_b16 v[248:249], v20 offset:0\n"
"ds_read_b64_tr_b16 v[250:251], v20 offset:256\n"
"ds_read_b64_tr_b16 v[252:253], v20 offset:4096\n"
"ds_read_b64_tr_b16 v[254:255], v20 offset:4352\n"
"ds_read_b64_tr_b16 a[0:1], v21 offset:0\n"
"ds_read_b64_tr_b16 a[2:3], v21 offset:256\n"
"ds_read_b64_tr_b16 a[4:5], v21 offset:4096\n"
"ds_read_b64_tr_b16 a[6:7], v21 offset:4352\n"
"ds_read_b64_tr_b16 a[56:57], v22 offset:0\n"
"ds_read_b64_tr_b16 a[58:59], v22 offset:256\n"
"ds_read_b64_tr_b16 a[60:61], v22 offset:4096\n"
"ds_read_b64_tr_b16 a[62:63], v22 offset:4352\n"
"ds_read_b64_tr_b16 a[96:97], v20 offset:24576\n"
"ds_read_b64_tr_b16 a[98:99], v20 offset:24832\n"
"ds_read_b64_tr_b16 a[100:101], v20 offset:28672\n"
"ds_read_b64_tr_b16 a[102:103], v20 offset:28928\n"
"ds_read_b64_tr_b16 a[144:145], v21 offset:24576\n"
"ds_read_b64_tr_b16 a[146:147], v21 offset:24832\n"
"ds_read_b64_tr_b16 a[148:149], v21 offset:28672\n"
"ds_read_b64_tr_b16 a[150:151], v21 offset:28928\n"
"ds_read_b64_tr_b16 a[224:225], v22 offset:24576\n"
"ds_read_b64_tr_b16 a[226:227], v22 offset:24832\n"
"ds_read_b64_tr_b16 a[228:229], v22 offset:28672\n"
"ds_read_b64_tr_b16 a[230:231], v22 offset:28928\n"
"s_waitcnt vmcnt(12)\n"
"s_waitcnt lgkmcnt(0)\n"
"s_barrier\n"
"s_add_u32 m0, s45, 49152\n"
"s_add_u32 s72, s44, 3145728\n"
"buffer_load_dwordx4 v1, s[40:43], s72 offen lds\n"
"s_add_u32 m0, s45, 50176\n"
"s_add_u32 s72, s44, 3244032\n"
"buffer_load_dwordx4 v2, s[40:43], s72 offen lds\n"
"s_add_u32 m0, s45, 51200\n"
"s_add_u32 s72, s44, 3342336\n"
"buffer_load_dwordx4 v1, s[40:43], s72 offen lds\n"
"s_add_u32 m0, s45, 52224\n"
"s_add_u32 s72, s44, 3440640\n"
"buffer_load_dwordx4 v2, s[40:43], s72 offen lds\n"
"s_add_u32 m0, s45, 57344\n"
"s_add_u32 s72, s44, 3145856\n"
"buffer_load_dwordx4 v1, s[40:43], s72 offen lds\n"
"s_add_u32 m0, s45, 58368\n"
"s_add_u32 s72, s44, 3244160\n"
"buffer_load_dwordx4 v2, s[40:43], s72 offen lds\n"
"s_add_u32 m0, s45, 59392\n"
"s_add_u32 s72, s44, 3342464\n"
"buffer_load_dwordx4 v1, s[40:43], s72 offen lds\n"
"s_add_u32 m0, s45, 60416\n"
"s_add_u32 s72, s44, 3440768\n"
"buffer_load_dwordx4 v2, s[40:43], s72 offen lds\n"
"s_add_u32 m0, s45, 65536\n"
"s_add_u32 s72, s44, 3145984\n"
"buffer_load_dwordx4 v1, s[40:43], s72 offen lds\n"
"s_add_u32 m0, s45, 66560\n"
"s_add_u32 s72, s44, 3244288\n"
"buffer_load_dwordx4 v2, s[40:43], s72 offen lds\n"
"s_add_u32 m0, s45, 67584\n"
"s_add_u32 s72, s44, 3342592\n"
"buffer_load_dwordx4 v1, s[40:43], s72 offen lds\n"
"s_add_u32 m0, s45, 68608\n"
"s_add_u32 s72, s44, 3440896\n"
"buffer_load_dwordx4 v2, s[40:43], s72 offen lds\n"
"v_add_u32 v20, 98304, v3\n"
"v_add_u32 v21, 98304, v4\n"
"v_add_u32 v22, 98304, v5\n"
"ds_read_b64_tr_b16 v[80:81], v20 offset:0\n"
"ds_read_b64_tr_b16 v[82:83], v20 offset:256\n"
"ds_read_b64_tr_b16 v[84:85], v20 offset:4096\n"
"ds_read_b64_tr_b16 v[86:87], v20 offset:4352\n"
"ds_read_b64_tr_b16 a[8:9], v21 offset:0\n"
"ds_read_b64_tr_b16 a[10:11], v21 offset:256\n"
"ds_read_b64_tr_b16 a[12:13], v21 offset:4096\n"
"ds_read_b64_tr_b16 a[14:15], v21 offset:4352\n"
"ds_read_b64_tr_b16 a[64:65], v22 offset:0\n"
"ds_read_b64_tr_b16 a[66:67], v22 offset:256\n"
"ds_read_b64_tr_b16 a[68:69], v22 offset:4096\n"
"ds_read_b64_tr_b16 a[70:71], v22 offset:4352\n"
"ds_read_b64_tr_b16 a[104:105], v20 offset:24576\n"
"ds_read_b64_tr_b16 a[106:107], v20 offset:24832\n"
"ds_read_b64_tr_b16 a[108:109], v20 offset:28672\n"
"ds_read_b64_tr_b16 a[110:111], v20 offset:28928\n"
"ds_read_b64_tr_b16 a[152:153], v21 offset:24576\n"
"ds_read_b64_tr_b16 a[154:155], v21 offset:24832\n"
"ds_read_b64_tr_b16 a[156:157], v21 offset:28672\n"
"ds_read_b64_tr_b16 a[158:159], v21 offset:28928\n"
"ds_read_b64_tr_b16 a[232:233], v22 offset:24576\n"
"ds_read_b64_tr_b16 a[234:235], v22 offset:24832\n"
"ds_read_b64_tr_b16 a[236:237], v22 offset:28672\n"
"ds_read_b64_tr_b16 a[238:239], v22 offset:28928\n"
"s_waitcnt vmcnt(12)\n"
"s_waitcnt lgkmcnt(0)\n"
"s_barrier\n"
"s_add_u32 m0, s45, 98304\n"
"s_add_u32 s72, s44, 3932160\n"
"buffer_load_dwordx4 v1, s[40:43], s72 offen lds\n"
"s_add_u32 m0, s45, 99328\n"
"s_add_u32 s72, s44, 4030464\n"
"buffer_load_dwordx4 v2, s[40:43], s72 offen lds\n"
"s_add_u32 m0, s45, 100352\n"
"s_add_u32 s72, s44, 4128768\n"
"buffer_load_dwordx4 v1, s[40:43], s72 offen lds\n"
"s_add_u32 m0, s45, 101376\n"
"s_add_u32 s72, s44, 4227072\n"
"buffer_load_dwordx4 v2, s[40:43], s72 offen lds\n"
"s_add_u32 m0, s45, 106496\n"
"s_add_u32 s72, s44, 3932288\n"
"buffer_load_dwordx4 v1, s[40:43], s72 offen lds\n"
"s_add_u32 m0, s45, 107520\n"
"s_add_u32 s72, s44, 4030592\n"
"buffer_load_dwordx4 v2, s[40:43], s72 offen lds\n"
"s_add_u32 m0, s45, 108544\n"
"s_add_u32 s72, s44, 4128896\n"
"buffer_load_dwordx4 v1, s[40:43], s72 offen lds\n"
"s_add_u32 m0, s45, 109568\n"
"s_add_u32 s72, s44, 4227200\n"
"buffer_load_dwordx4 v2, s[40:43], s72 offen lds\n"
"s_add_u32 m0, s45, 114688\n"
"s_add_u32 s72, s44, 3932416\n"
"buffer_load_dwordx4 v1, s[40:43], s72 offen lds\n"
"s_add_u32 m0, s45, 115712\n"
"s_add_u32 s72, s44, 4030720\n"
"buffer_load_dwordx4 v2, s[40:43], s72 offen lds\n"
"s_add_u32 m0, s45, 116736\n"
"s_add_u32 s72, s44, 4129024\n"
"buffer_load_dwordx4 v1, s[40:43], s72 offen lds\n"
"s_add_u32 m0, s45, 117760\n"
"s_add_u32 s72, s44, 4227328\n"
"buffer_load_dwordx4 v2, s[40:43], s72 offen lds\n"
"v_add_u32 v20, 0, v3\n"
"v_add_u32 v21, 0, v4\n"
"v_add_u32 v22, 0, v5\n"
"ds_read_b64_tr_b16 a[16:17], v20 offset:0\n"
"ds_read_b64_tr_b16 a[18:19], v20 offset:256\n"
"ds_read_b64_tr_b16 a[20:21], v20 offset:4096\n"
"ds_read_b64_tr_b16 a[22:23], v20 offset:4352\n"
"ds_read_b64_tr_b16 a[24:25], v21 offset:0\n"
"ds_read_b64_tr_b16 a[26:27], v21 offset:256\n"
"ds_read_b64_tr_b16 a[28:29], v21 offset:4096\n"
"ds_read_b64_tr_b16 a[30:31], v21 offset:4352\n"
"ds_read_b64_tr_b16 a[72:73], v22 offset:0\n"
"ds_read_b64_tr_b16 a[74:75], v22 offset:256\n"
"ds_read_b64_tr_b16 a[76:77], v22 offset:4096\n"
"ds_read_b64_tr_b16 a[78:79], v22 offset:4352\n"
"ds_read_b64_tr_b16 v[120:121], v20 offset:24576\n"
"ds_read_b64_tr_b16 v[122:123], v20 offset:24832\n"
"ds_read_b64_tr_b16 v[124:125], v20 offset:28672\n"
"ds_read_b64_tr_b16 v[126:127], v20 offset:28928\n"
"ds_read_b64_tr_b16 a[160:161], v21 offset:24576\n"
"ds_read_b64_tr_b16 a[162:163], v21 offset:24832\n"
"ds_read_b64_tr_b16 a[164:165], v21 offset:28672\n"
"ds_read_b64_tr_b16 a[166:167], v21 offset:28928\n"
"ds_read_b64_tr_b16 a[240:241], v22 offset:24576\n"
"ds_read_b64_tr_b16 a[242:243], v22 offset:24832\n"
"ds_read_b64_tr_b16 a[244:245], v22 offset:28672\n"
"ds_read_b64_tr_b16 a[246:247], v22 offset:28928\n"
"s_waitcnt vmcnt(12)\n"
"s_waitcnt lgkmcnt(0)\n"
"s_barrier\n"
"s_add_u32 m0, s45, 0\n"
"s_add_u32 s72, s44, 4718592\n"
"buffer_load_dwordx4 v1, s[40:43], s72 offen lds\n"
"s_add_u32 m0, s45, 1024\n"
"s_add_u32 s72, s44, 4816896\n"
"buffer_load_dwordx4 v2, s[40:43], s72 offen lds\n"
"s_add_u32 m0, s45, 2048\n"
"s_add_u32 s72, s44, 4915200\n"
"buffer_load_dwordx4 v1, s[40:43], s72 offen lds\n"
"s_add_u32 m0, s45, 3072\n"
"s_add_u32 s72, s44, 5013504\n"
"buffer_load_dwordx4 v2, s[40:43], s72 offen lds\n"
"s_add_u32 m0, s45, 8192\n"
"s_add_u32 s72, s44, 4718720\n"
"buffer_load_dwordx4 v1, s[40:43], s72 offen lds\n"
"s_add_u32 m0, s45, 9216\n"
"s_add_u32 s72, s44, 4817024\n"
"buffer_load_dwordx4 v2, s[40:43], s72 offen lds\n"
"s_add_u32 m0, s45, 10240\n"
"s_add_u32 s72, s44, 4915328\n"
"buffer_load_dwordx4 v1, s[40:43], s72 offen lds\n"
"s_add_u32 m0, s45, 11264\n"
"s_add_u32 s72, s44, 5013632\n"
"buffer_load_dwordx4 v2, s[40:43], s72 offen lds\n"
"s_add_u32 m0, s45, 16384\n"
"s_add_u32 s72, s44, 4718848\n"
"buffer_load_dwordx4 v1, s[40:43], s72 offen lds\n"
"s_add_u32 m0, s45, 17408\n"
"s_add_u32 s72, s44, 4817152\n"
"buffer_load_dwordx4 v2, s[40:43], s72 offen lds\n"
"s_add_u32 m0, s45, 18432\n"
"s_add_u32 s72, s44, 4915456\n"
"buffer_load_dwordx4 v1, s[40:43], s72 offen lds\n"
"s_add_u32 m0, s45, 19456\n"
"s_add_u32 s72, s44, 5013760\n"
"buffer_load_dwordx4 v2, s[40:43], s72 offen lds\n"
"v_add_u32 v20, 49152, v3\n"
"v_add_u32 v21, 49152, v4\n"
"v_add_u32 v22, 49152, v5\n"
"ds_read_b64_tr_b16 v[88:89], v20 offset:0\n"
"ds_read_b64_tr_b16 v[90:91], v20 offset:256\n"
"ds_read_b64_tr_b16 v[92:93], v20 offset:4096\n"
"ds_read_b64_tr_b16 v[94:95], v20 offset:4352\n"
"ds_read_b64_tr_b16 a[32:33], v21 offset:0\n"
"ds_read_b64_tr_b16 a[34:35], v21 offset:256\n"
"ds_read_b64_tr_b16 a[36:37], v21 offset:4096\n"
"ds_read_b64_tr_b16 a[38:39], v21 offset:4352\n"
"ds_read_b64_tr_b16 v[104:105], v22 offset:0\n"
"ds_read_b64_tr_b16 v[106:107], v22 offset:256\n"
"ds_read_b64_tr_b16 v[108:109], v22 offset:4096\n"
"ds_read_b64_tr_b16 v[110:111], v22 offset:4352\n"
"ds_read_b64_tr_b16 a[112:113], v20 offset:24576\n"
"ds_read_b64_tr_b16 a[114:115], v20 offset:24832\n"
"ds_read_b64_tr_b16 a[116:117], v20 offset:28672\n"
"ds_read_b64_tr_b16 a[118:119], v20 offset:28928\n"
"ds_read_b64_tr_b16 a[168:169], v21 offset:24576\n"
"ds_read_b64_tr_b16 a[170:171], v21 offset:24832\n"
"ds_read_b64_tr_b16 a[172:173], v21 offset:28672\n"
"ds_read_b64_tr_b16 a[174:175], v21 offset:28928\n"
"ds_read_b64_tr_b16 a[248:249], v22 offset:24576\n"
"ds_read_b64_tr_b16 a[250:251], v22 offset:24832\n"
"ds_read_b64_tr_b16 a[252:253], v22 offset:28672\n"
"ds_read_b64_tr_b16 a[254:255], v22 offset:28928\n"
"s_waitcnt vmcnt(12)\n"
"s_waitcnt lgkmcnt(0)\n"
"s_barrier\n"
"s_add_u32 m0, s45, 49152\n"
"s_add_u32 s72, s44, 5505024\n"
"buffer_load_dwordx4 v1, s[40:43], s72 offen lds\n"
"s_add_u32 m0, s45, 50176\n"
"s_add_u32 s72, s44, 5603328\n"
"buffer_load_dwordx4 v2, s[40:43], s72 offen lds\n"
"s_add_u32 m0, s45, 51200\n"
"s_add_u32 s72, s44, 5701632\n"
"buffer_load_dwordx4 v1, s[40:43], s72 offen lds\n"
"s_add_u32 m0, s45, 52224\n"
"s_add_u32 s72, s44, 5799936\n"
"buffer_load_dwordx4 v2, s[40:43], s72 offen lds\n"
"s_add_u32 m0, s45, 57344\n"
"s_add_u32 s72, s44, 5505152\n"
"buffer_load_dwordx4 v1, s[40:43], s72 offen lds\n"
"s_add_u32 m0, s45, 58368\n"
"s_add_u32 s72, s44, 5603456\n"
"buffer_load_dwordx4 v2, s[40:43], s72 offen lds\n"
"s_add_u32 m0, s45, 59392\n"
"s_add_u32 s72, s44, 5701760\n"
"buffer_load_dwordx4 v1, s[40:43], s72 offen lds\n"
"s_add_u32 m0, s45, 60416\n"
"s_add_u32 s72, s44, 5800064\n"
"buffer_load_dwordx4 v2, s[40:43], s72 offen lds\n"
"s_add_u32 m0, s45, 65536\n"
"s_add_u32 s72, s44, 5505280\n"
"buffer_load_dwordx4 v1, s[40:43], s72 offen lds\n"
"s_add_u32 m0, s45, 66560\n"
"s_add_u32 s72, s44, 5603584\n"
"buffer_load_dwordx4 v2, s[40:43], s72 offen lds\n"
"s_add_u32 m0, s45, 67584\n"
"s_add_u32 s72, s44, 5701888\n"
"buffer_load_dwordx4 v1, s[40:43], s72 offen lds\n"
"s_add_u32 m0, s45, 68608\n"
"s_add_u32 s72, s44, 5800192\n"
"buffer_load_dwordx4 v2, s[40:43], s72 offen lds\n"
"v_add_u32 v20, 98304, v3\n"
"v_add_u32 v21, 98304, v4\n"
"v_add_u32 v22, 98304, v5\n"
"ds_read_b64_tr_b16 v[168:169], v20 offset:0\n"
"ds_read_b64_tr_b16 v[170:171], v20 offset:256\n"
"ds_read_b64_tr_b16 v[172:173], v20 offset:4096\n"
"ds_read_b64_tr_b16 v[174:175], v20 offset:4352\n"
"ds_read_b64_tr_b16 v[184:185], v21 offset:0\n"
"ds_read_b64_tr_b16 v[186:187], v21 offset:256\n"
"ds_read_b64_tr_b16 v[188:189], v21 offset:4096\n"
"ds_read_b64_tr_b16 v[190:191], v21 offset:4352\n"
"ds_read_b64_tr_b16 v[192:193], v22 offset:0\n"
"ds_read_b64_tr_b16 v[194:195], v22 offset:256\n"
"ds_read_b64_tr_b16 v[196:197], v22 offset:4096\n"
"ds_read_b64_tr_b16 v[198:199], v22 offset:4352\n"
"ds_read_b64_tr_b16 v[176:177], v20 offset:24576\n"
"ds_read_b64_tr_b16 v[178:179], v20 offset:24832\n"
"ds_read_b64_tr_b16 v[180:181], v20 offset:28672\n"
"ds_read_b64_tr_b16 v[182:183], v20 offset:28928\n"
"ds_read_b64_tr_b16 v[72:73], v21 offset:24576\n"
"ds_read_b64_tr_b16 v[74:75], v21 offset:24832\n"
"ds_read_b64_tr_b16 v[76:77], v21 offset:28672\n"
"ds_read_b64_tr_b16 v[78:79], v21 offset:28928\n"
"ds_read_b64_tr_b16 a[192:193], v22 offset:24576\n"
"ds_read_b64_tr_b16 a[194:195], v22 offset:24832\n"
"ds_read_b64_tr_b16 a[196:197], v22 offset:28672\n"
"ds_read_b64_tr_b16 a[198:199], v22 offset:28928\n"
"s_waitcnt vmcnt(12)\n"
"s_waitcnt lgkmcnt(0)\n"
"s_barrier\n"
"s_add_u32 m0, s45, 98304\n"
"s_add_u32 s72, s44, 6291456\n"
"buffer_load_dwordx4 v1, s[40:43], s72 offen lds\n"
"s_add_u32 m0, s45, 99328\n"
"s_add_u32 s72, s44, 6389760\n"
"buffer_load_dwordx4 v2, s[40:43], s72 offen lds\n"
"s_add_u32 m0, s45, 100352\n"
"s_add_u32 s72, s44, 6488064\n"
"buffer_load_dwordx4 v1, s[40:43], s72 offen lds\n"
"s_add_u32 m0, s45, 101376\n"
"s_add_u32 s72, s44, 6586368\n"
"buffer_load_dwordx4 v2, s[40:43], s72 offen lds\n"
"s_add_u32 m0, s45, 106496\n"
"s_add_u32 s72, s44, 6291584\n"
"buffer_load_dwordx4 v1, s[40:43], s72 offen lds\n"
"s_add_u32 m0, s45, 107520\n"
"s_add_u32 s72, s44, 6389888\n"
"buffer_load_dwordx4 v2, s[40:43], s72 offen lds\n"
"s_add_u32 m0, s45, 108544\n"
"s_add_u32 s72, s44, 6488192\n"
"buffer_load_dwordx4 v1, s[40:43], s72 offen lds\n"
"s_add_u32 m0, s45, 109568\n"
"s_add_u32 s72, s44, 6586496\n"
"buffer_load_dwordx4 v2, s[40:43], s72 offen lds\n"
"s_add_u32 m0, s45, 114688\n"
"s_add_u32 s72, s44, 6291712\n"
"buffer_load_dwordx4 v1, s[40:43], s72 offen lds\n"
"s_add_u32 m0, s45, 115712\n"
"s_add_u32 s72, s44, 6390016\n"
"buffer_load_dwordx4 v2, s[40:43], s72 offen lds\n"
"s_add_u32 m0, s45, 116736\n"
"s_add_u32 s72, s44, 6488320\n"
"buffer_load_dwordx4 v1, s[40:43], s72 offen lds\n"
"s_add_u32 m0, s45, 117760\n"
"s_add_u32 s72, s44, 6586624\n"
"buffer_load_dwordx4 v2, s[40:43], s72 offen lds\n"
"v_add_u32 v20, 0, v3\n"
"v_add_u32 v21, 0, v4\n"
"v_add_u32 v22, 0, v5\n"
"ds_read_b64_tr_b16 v[32:33], v20 offset:0\n"
"ds_read_b64_tr_b16 v[34:35], v20 offset:256\n"
"ds_read_b64_tr_b16 v[36:37], v20 offset:4096\n"
"ds_read_b64_tr_b16 v[38:39], v20 offset:4352\n"
"ds_read_b64_tr_b16 v[128:129], v21 offset:0\n"
"ds_read_b64_tr_b16 v[130:131], v21 offset:256\n"
"ds_read_b64_tr_b16 v[132:133], v21 offset:4096\n"
"ds_read_b64_tr_b16 v[134:135], v21 offset:4352\n"
"ds_read_b64_tr_b16 v[136:137], v22 offset:0\n"
"ds_read_b64_tr_b16 v[138:139], v22 offset:256\n"
"ds_read_b64_tr_b16 v[140:141], v22 offset:4096\n"
"ds_read_b64_tr_b16 v[142:143], v22 offset:4352\n"
"ds_read_b64_tr_b16 v[40:41], v20 offset:24576\n"
"ds_read_b64_tr_b16 v[42:43], v20 offset:24832\n"
"ds_read_b64_tr_b16 v[44:45], v20 offset:28672\n"
"ds_read_b64_tr_b16 v[46:47], v20 offset:28928\n"
"ds_read_b64_tr_b16 v[48:49], v21 offset:24576\n"
"ds_read_b64_tr_b16 v[50:51], v21 offset:24832\n"
"ds_read_b64_tr_b16 v[52:53], v21 offset:28672\n"
"ds_read_b64_tr_b16 v[54:55], v21 offset:28928\n"
"ds_read_b64_tr_b16 a[176:177], v22 offset:24576\n"
"ds_read_b64_tr_b16 a[178:179], v22 offset:24832\n"
"ds_read_b64_tr_b16 a[180:181], v22 offset:28672\n"
"ds_read_b64_tr_b16 a[182:183], v22 offset:28928\n"
"s_waitcnt vmcnt(12)\n"
"s_waitcnt lgkmcnt(0)\n"
"s_barrier\n"
"s_add_u32 m0, s45, 0\n"
"s_add_u32 s72, s44, 7077888\n"
"buffer_load_dwordx4 v1, s[40:43], s72 offen lds\n"
"s_add_u32 m0, s45, 1024\n"
"s_add_u32 s72, s44, 7176192\n"
"buffer_load_dwordx4 v2, s[40:43], s72 offen lds\n"
"s_add_u32 m0, s45, 2048\n"
"s_add_u32 s72, s44, 7274496\n"
"buffer_load_dwordx4 v1, s[40:43], s72 offen lds\n"
"s_add_u32 m0, s45, 3072\n"
"s_add_u32 s72, s44, 7372800\n"
"buffer_load_dwordx4 v2, s[40:43], s72 offen lds\n"
"s_add_u32 m0, s45, 8192\n"
"s_add_u32 s72, s44, 7078016\n"
"buffer_load_dwordx4 v1, s[40:43], s72 offen lds\n"
"s_add_u32 m0, s45, 9216\n"
"s_add_u32 s72, s44, 7176320\n"
"buffer_load_dwordx4 v2, s[40:43], s72 offen lds\n"
"s_add_u32 m0, s45, 10240\n"
"s_add_u32 s72, s44, 7274624\n"
"buffer_load_dwordx4 v1, s[40:43], s72 offen lds\n"
"s_add_u32 m0, s45, 11264\n"
"s_add_u32 s72, s44, 7372928\n"
"buffer_load_dwordx4 v2, s[40:43], s72 offen lds\n"
"s_add_u32 m0, s45, 16384\n"
"s_add_u32 s72, s44, 7078144\n"
"buffer_load_dwordx4 v1, s[40:43], s72 offen lds\n"
"s_add_u32 m0, s45, 17408\n"
"s_add_u32 s72, s44, 7176448\n"
"buffer_load_dwordx4 v2, s[40:43], s72 offen lds\n"
"s_add_u32 m0, s45, 18432\n"
"s_add_u32 s72, s44, 7274752\n"
"buffer_load_dwordx4 v1, s[40:43], s72 offen lds\n"
"s_add_u32 m0, s45, 19456\n"
"s_add_u32 s72, s44, 7373056\n"
"buffer_load_dwordx4 v2, s[40:43], s72 offen lds\n"
"v_add_u32 v20, 49152, v3\n"
"v_add_u32 v21, 49152, v4\n"
"v_add_u32 v22, 49152, v5\n"
"ds_read_b64_tr_b16 v[56:57], v20 offset:0\n"
"ds_read_b64_tr_b16 v[58:59], v20 offset:256\n"
"ds_read_b64_tr_b16 v[60:61], v20 offset:4096\n"
"ds_read_b64_tr_b16 v[62:63], v20 offset:4352\n"
"ds_read_b64_tr_b16 v[152:153], v21 offset:0\n"
"ds_read_b64_tr_b16 v[154:155], v21 offset:256\n"
"ds_read_b64_tr_b16 v[156:157], v21 offset:4096\n"
"ds_read_b64_tr_b16 v[158:159], v21 offset:4352\n"
"ds_read_b64_tr_b16 v[160:161], v22 offset:0\n"
"ds_read_b64_tr_b16 v[162:163], v22 offset:256\n"
"ds_read_b64_tr_b16 v[164:165], v22 offset:4096\n"
"ds_read_b64_tr_b16 v[166:167], v22 offset:4352\n"
"ds_read_b64_tr_b16 v[144:145], v20 offset:24576\n"
"ds_read_b64_tr_b16 v[146:147], v20 offset:24832\n"
"ds_read_b64_tr_b16 v[148:149], v20 offset:28672\n"
"ds_read_b64_tr_b16 v[150:151], v20 offset:28928\n"
"ds_read_b64_tr_b16 v[64:65], v21 offset:24576\n"
"ds_read_b64_tr_b16 v[66:67], v21 offset:24832\n"
"ds_read_b64_tr_b16 v[68:69], v21 offset:28672\n"
"ds_read_b64_tr_b16 v[70:71], v21 offset:28928\n"
"ds_read_b64_tr_b16 a[184:185], v22 offset:24576\n"
"ds_read_b64_tr_b16 a[186:187], v22 offset:24832\n"
"ds_read_b64_tr_b16 a[188:189], v22 offset:28672\n"
"ds_read_b64_tr_b16 a[190:191], v22 offset:28928\n"
"s_cmp_lg_u32 s66, 0\n"
"s_cbranch_scc1 S44_%=\n"
"s_mov_b32 s76, 131072\n"
"s_mov_b64 exec, 1\n"
"s_branch S46_%=\n"
"S45_%=:\n"
"s_sleep 4\n"
"S46_%=:\n"
"v_mov_b32 v29, 0\n"
"buffer_atomic_add v29, v26, s[36:39], s23 offen sc0 sc1\n"
"s_sub_u32 s76, s76, 1\n"
"s_cbranch_scc1 SABORT_%=\n"
"s_waitcnt vmcnt(0)\n"
"v_xor_b32 v29, v29, v24\n"
"v_cmp_lt_u32 vcc, 1, v29\n"
"s_nop 0\n"
"s_cbranch_vccnz S45_%=\n"
"v_mov_b32 v28, 153600\n"
"ds_write_b32 v28, v29\n"
"v_mov_b32 v24, 1\n"
"s_add_u32 s22, s23, 16384\n"
"buffer_store_dword v24, v26, s[36:39], s22 offen sc1\n"
"s_mov_b64 exec, -1\n"
"S44_%=:\n"
"s_waitcnt lgkmcnt(0)\n"
"s_barrier\n"
"v_mov_b32 v28, 153600\n"
"ds_read_b32 v28, v28\n"
"s_waitcnt lgkmcnt(0)\n"
"v_readfirstlane_b32 s22, v28\n"
"s_nop 0\n"
"s_lshl_b32 s22, s22, 13\n"
"v_xor_b32 v12, s22, v12\n"
"v_xor_b32 v13, s22, v13\n"
"s_xor_b32 s68, s68, s22\n"
"s_xor_b32 s20, s20, s22\n"
"s_waitcnt vmcnt(12)\n"
"s_waitcnt lgkmcnt(0)\n"
"s_barrier\n"
"v_add_u32 v20, 98304, v3\n"
"v_add_u32 v21, 98304, v4\n"
"v_add_u32 v22, 98304, v5\n"
"ds_read_b64_tr_b16 v[200:201], v20 offset:0\n"
"ds_read_b64_tr_b16 v[202:203], v20 offset:256\n"
"ds_read_b64_tr_b16 v[204:205], v20 offset:4096\n"
"ds_read_b64_tr_b16 v[206:207], v20 offset:4352\n"
"ds_read_b64_tr_b16 v[208:209], v21 offset:0\n"
"ds_read_b64_tr_b16 v[210:211], v21 offset:256\n"
"ds_read_b64_tr_b16 v[212:213], v21 offset:4096\n"
"ds_read_b64_tr_b16 v[214:215], v21 offset:4352\n"
"ds_read_b64_tr_b16 a[40:41], v22 offset:0\n"
"ds_read_b64_tr_b16 a[42:43], v22 offset:256\n"
"ds_read_b64_tr_b16 a[44:45], v22 offset:4096\n"
"ds_read_b64_tr_b16 a[46:47], v22 offset:4352\n"
"ds_read_b64_tr_b16 a[80:81], v20 offset:24576\n"
"ds_read_b64_tr_b16 a[82:83], v20 offset:24832\n"
"ds_read_b64_tr_b16 a[84:85], v20 offset:28672\n"
"ds_read_b64_tr_b16 a[86:87], v20 offset:28928\n"
"ds_read_b64_tr_b16 a[120:121], v21 offset:24576\n"
"ds_read_b64_tr_b16 a[122:123], v21 offset:24832\n"
"ds_read_b64_tr_b16 a[124:125], v21 offset:28672\n"
"ds_read_b64_tr_b16 a[126:127], v21 offset:28928\n"
"ds_read_b64_tr_b16 a[200:201], v22 offset:24576\n"
"ds_read_b64_tr_b16 a[202:203], v22 offset:24832\n"
"ds_read_b64_tr_b16 a[204:205], v22 offset:28672\n"
"ds_read_b64_tr_b16 a[206:207], v22 offset:28928\n"
"s_waitcnt vmcnt(0)\n"
"s_waitcnt lgkmcnt(0)\n"
"s_barrier\n"
"v_add_u32 v20, 0, v3\n"
"v_add_u32 v21, 0, v4\n"
"v_add_u32 v22, 0, v5\n"
"ds_read_b64_tr_b16 v[216:217], v20 offset:0\n"
"ds_read_b64_tr_b16 v[218:219], v20 offset:256\n"
"ds_read_b64_tr_b16 v[220:221], v20 offset:4096\n"
"ds_read_b64_tr_b16 v[222:223], v20 offset:4352\n"
"ds_read_b64_tr_b16 v[224:225], v21 offset:0\n"
"ds_read_b64_tr_b16 v[226:227], v21 offset:256\n"
"ds_read_b64_tr_b16 v[228:229], v21 offset:4096\n"
"ds_read_b64_tr_b16 v[230:231], v21 offset:4352\n"
"ds_read_b64_tr_b16 a[48:49], v22 offset:0\n"
"ds_read_b64_tr_b16 a[50:51], v22 offset:256\n"
"ds_read_b64_tr_b16 a[52:53], v22 offset:4096\n"
"ds_read_b64_tr_b16 a[54:55], v22 offset:4352\n"
"ds_read_b64_tr_b16 a[88:89], v20 offset:24576\n"
"ds_read_b64_tr_b16 a[90:91], v20 offset:24832\n"
"ds_read_b64_tr_b16 a[92:93], v20 offset:28672\n"
"ds_read_b64_tr_b16 a[94:95], v20 offset:28928\n"
"ds_read_b64_tr_b16 a[128:129], v21 offset:24576\n"
"ds_read_b64_tr_b16 a[130:131], v21 offset:24832\n"
"ds_read_b64_tr_b16 a[132:133], v21 offset:28672\n"
"ds_read_b64_tr_b16 a[134:135], v21 offset:28928\n"
"ds_read_b64_tr_b16 a[208:209], v22 offset:24576\n"
"ds_read_b64_tr_b16 a[210:211], v22 offset:24832\n"
"ds_read_b64_tr_b16 a[212:213], v22 offset:28672\n"
"ds_read_b64_tr_b16 a[214:215], v22 offset:28928\n"
"s_waitcnt lgkmcnt(0)\n"
"s_barrier\n"
"buffer_load_dword v28, v12, s[36:39], 0 offen sc1\n"
"ds_write_b128 v15, v[32:35] offset:0\n"
"ds_write_b128 v15, v[36:39] offset:1024\n"
"ds_write_b128 v15, v[40:43] offset:2048\n"
"ds_write_b128 v15, v[44:47] offset:3072\n"
"ds_write_b128 v15, v[48:51] offset:4096\n"
"ds_write_b128 v15, v[52:55] offset:5120\n"
"ds_write_b128 v15, v[56:59] offset:6144\n"
"ds_write_b128 v15, v[60:63] offset:7168\n"
"ds_write_b128 v15, v[64:67] offset:8192\n"
"ds_write_b128 v15, v[68:71] offset:9216\n"
"ds_write_b128 v15, v[72:75] offset:10240\n"
"ds_write_b128 v15, v[76:79] offset:11264\n"
"ds_write_b128 v15, v[80:83] offset:12288\n"
"ds_write_b128 v15, v[84:87] offset:13312\n"
"ds_write_b128 v15, v[88:91] offset:14336\n"
"ds_write_b128 v15, v[92:95] offset:15360\n"
"ds_write_b128 v15, v[96:99] offset:16384\n"
"ds_write_b128 v15, v[100:103] offset:17408\n"
"ds_write_b128 v15, v[104:107] offset:18432\n"
"ds_write_b128 v15, v[108:111] offset:19456\n"
"ds_write_b128 v15, v[112:115] offset:20480\n"
"ds_write_b128 v15, v[116:119] offset:21504\n"
"ds_write_b128 v15, v[120:123] offset:22528\n"
"ds_write_b128 v15, v[124:127] offset:23552\n"
"s_waitcnt lgkmcnt(0)\n"
"ds_read_b128 v[96:99], v15 offset:0\n"
"ds_read_b128 v[100:103], v15 offset:1024\n"
"ds_read_b128 v[72:75], v15 offset:2048\n"
"ds_read_b128 v[76:79], v15 offset:3072\n"
"ds_read_b128 v[80:83], v15 offset:4096\n"
"ds_read_b128 v[84:87], v15 offset:5120\n"
"ds_read_b128 v[88:91], v15 offset:6144\n"
"ds_read_b128 v[92:95], v15 offset:7168\n"
"ds_read_b128 v[104:107], v15 offset:8192\n"
"ds_read_b128 v[108:111], v15 offset:9216\n"
"ds_read_b128 v[112:115], v15 offset:10240\n"
"ds_read_b128 v[116:119], v15 offset:11264\n"
"v_mov_b32 v32, 0\n"
"v_mov_b32 v33, 0\n"
"v_mov_b32 v34, 0\n"
"v_mov_b32 v35, 0\n"
"v_mov_b32 v36, 0\n"
"v_mov_b32 v37, 0\n"
"v_mov_b32 v38, 0\n"
"v_mov_b32 v39, 0\n"
"v_mov_b32 v40, 0\n"
"v_mov_b32 v41, 0\n"
"v_mov_b32 v42, 0\n"
"v_mov_b32 v43, 0\n"
"v_mov_b32 v44, 0\n"
"v_mov_b32 v45, 0\n"
"v_mov_b32 v46, 0\n"
"v_mov_b32 v47, 0\n"
"v_mov_b32 v48, 0\n"
"v_mov_b32 v49, 0\n"
"v_mov_b32 v50, 0\n"
"v_mov_b32 v51, 0\n"
"v_mov_b32 v52, 0\n"
"v_mov_b32 v53, 0\n"
"v_mov_b32 v54, 0\n"
"v_mov_b32 v55, 0\n"
"v_mov_b32 v120, 0\n"
"v_mov_b32 v121, 0\n"
"v_mov_b32 v122, 0\n"
"v_mov_b32 v123, 0\n"
"v_mov_b32 v124, 0\n"
"v_mov_b32 v125, 0\n"
"v_mov_b32 v126, 0\n"
"v_mov_b32 v127, 0\n"
"s_waitcnt vmcnt(0)\n"
"s_nop 3\n"
"S48_%=:\n"
"s_mov_b32 s45, 0\n"
"s_cmp_eq_u32 s73, 63\n"
"s_cselect_b32 s49, 234881024, s68\n"
"s_xor_b32 s49, s49, 0\n"
"s_cmp_gt_u32 s69, s73\n"
"s_cbranch_scc1 S49_%=\n"
"s_add_u32 s75, s73, 1\n"
"v_cmp_gt_u32 vcc, s75, v28\n"
"s_nop 0\n"
"s_cbranch_vccz S53_%=\n"
"s_mov_b32 s76, 131072\n"
"s_branch S55_%=\n"
"S54_%=:\n"
"s_sleep 2\n"
"S55_%=:\n"
"buffer_load_dword v28, v12, s[36:39], 0 offen sc1\n"
"s_sub_u32 s76, s76, 1\n"
"s_cbranch_scc1 SABORT_%=\n"
"s_waitcnt vmcnt(0)\n"
"v_cmp_gt_u32 vcc, s75, v28\n"
"s_nop 0\n"
"s_cbranch_vccnz S54_%=\n"
"S53_%=:\n"
"s_add_u32 s51, s50, s47\n"
"s_add_u32 m0, s51, 0\n"
"s_add_u32 s72, s46, 0\n"
"buffer_load_dwordx4 v11, s[36:39], s72 offen sc1 lds\n"
"s_add_u32 m0, s51, 1024\n"
"s_add_u32 s72, s46, 2048\n"
"buffer_load_dwordx4 v11, s[36:39], s72 offen sc1 lds\n"
"s_add_u32 m0, s51, 2048\n"
"s_add_u32 s72, s46, 4096\n"
"buffer_load_dwordx4 v11, s[36:39], s72 offen sc1 lds\n"
"s_add_u32 s46, s46, 393216\n"
"s_sub_u32 s50, 53248, s50\n"
"s_add_u32 s69, s69, 1\n"
"S49_%=:\n"
"s_add_u32 s75, s73, 2\n"
"s_min_u32 s75, s75, 64\n"
"v_cmp_gt_u32 vcc, s75, v28\n"
"s_nop 0\n"
"s_cmp_eq_u64 vcc, 0\n"
"s_cselect_b32 s70, s46, 234881024\n"
"s_cselect_b32 s71, 393216, 0\n"
"s_cselect_b32 s72, 1, 0\n"
"s_cselect_b32 s75, 53248, 0\n"
"s_add_u32 m0, s50, s47\n"
"s_add_u32 s46, s46, s71\n"
"s_add_u32 s69, s69, s72\n"
"s_xor_b32 s50, s50, s75\n"
"s_add_u32 s71, s70, 1024\n"
"s_add_u32 s72, s70, 2048\n"
"v_mov_b32 v27, s73\n"
"s_cmp_eq_u32 s73, 63\n"
"s_cselect_b32 s52, 0, 234881024\n"
"s_cselect_b32 s53, 64, s73\n"
"v_mov_b32 v27, s53\n"
"s_add_u32 s60, s68, 70656\n"
"s_add_u32 s61, s68, 71680\n"
"s_add_u32 s62, s68, 39936\n"
"s_waitcnt lgkmcnt(0)\n"
"s_barrier\n"
"buffer_load_dwordx4 v[56:59], v0, s[36:39], s60 offen offset:0 sc1\n"
"s_nop 12\n"
"buffer_load_dwordx4 v[60:63], v0, s[36:39], s61 offen offset:0 sc1\n"
"s_nop 12\n"
"s_nop 8\n"
"buffer_load_dwordx4 v[64:67], v0, s[36:39], s62 offen offset:0 sc1\n"
"s_nop 12\n"
"buffer_store_dword v27, v13, s[36:39], s52 offen sc1\n"
"S56_%=:\n"
"s_waitcnt vmcnt(3)\n"
"v_smfmac_f32_16x16x64_bf16 v[32:35], v[56:59], v[96:103], v6\n"
"ds_write_b128 v0, v[56:59] offset:6144\n"
"v_smfmac_f32_16x16x64_bf16 v[44:47], v[56:59], v[72:79], v6\n"
"v_smfmac_f32_16x16x64_bf16 v[36:39], v[56:59], v[128:135], v6\n"
"v_smfmac_f32_16x16x64_bf16 v[48:51], v[56:59], v[80:87], v6\n"
"s_waitcnt vmcnt(2)\n"
"ds_write_b128 v0, v[60:63] offset:7168\n"
"v_smfmac_f32_16x16x64_bf16 v[40:43], v[56:59], v[136:143], v6\n"
"v_max_u32 v29, v59, v63\n"
"v_smfmac_f32_16x16x64_bf16 v[32:35], v[60:63], v[88:95], v6\n"
"v_smfmac_f32_16x16x64_bf16 v[44:47], v[60:63], v[144:151], v6\n"
"v_smfmac_f32_16x16x64_bf16 v[36:39], v[60:63], v[152:159], v6\n"
"s_waitcnt vmcnt(1)\n"
"v_max_u32 v29, v29, v67\n"
"v_smfmac_f32_16x16x64_bf16 v[48:51], v[60:63], v[104:111], v6\n"
"v_cmp_eq_u32 vcc, -1, v29\n"
"v_smfmac_f32_16x16x64_bf16 v[40:43], v[60:63], v[160:167], v6\n"
"s_cbranch_vccnz S50_%=\n"
"v_smfmac_f32_16x16x64_bf16 v[32:35], v[64:67], v[168:175], v6\n"
"s_waitcnt lgkmcnt(0)\n"
"s_barrier\n"
"ds_read_b128 v[68:71], v0 offset:8192\n"
"v_smfmac_f32_16x16x64_bf16 v[44:47], v[64:67], v[176:183], v6\n"
"ds_read_b128 v[72:75], v0 offset:9216\n"
"v_smfmac_f32_16x16x64_bf16 v[36:39], v[64:67], v[184:191], v6\n"
"ds_read_b128 v[76:79], v0 offset:0\n"
"v_smfmac_f32_16x16x64_bf16 v[48:51], v[64:67], v[112:119], v6\n"
"buffer_store_dword v27, v13, s[36:39], 0 offen sc1\n"
"v_smfmac_f32_16x16x64_bf16 v[40:43], v[64:67], v[192:199], v6\n"
"ds_read_b128 v[96:99], v15 offset:12288\n"
"s_waitcnt lgkmcnt(3)\n"
"v_smfmac_f32_16x16x64_bf16 v[32:35], v[68:71], v[200:207], v6\n"
"ds_read_b128 v[80:83], v0 offset:1024\n"
"v_smfmac_f32_16x16x64_bf16 v[36:39], v[68:71], v[208:215], v6\n"
"ds_read_b128 v[100:103], v15 offset:13312\n"
"s_waitcnt lgkmcnt(4)\n"
"v_smfmac_f32_16x16x64_bf16 v[32:35], v[72:75], v[216:223], v6\n"
"ds_read_b128 v[84:87], v0 offset:2048\n"
"v_smfmac_f32_16x16x64_bf16 v[36:39], v[72:75], v[224:231], v6\n"
"ds_read_b128 v[104:107], v15 offset:14336\n"
"s_waitcnt lgkmcnt(5)\n"
"v_smfmac_f32_16x16x64_bf16 v[32:35], v[76:79], v[232:239], v6\n"
"ds_read_b128 v[88:91], v0 offset:3072\n"
"v_smfmac_f32_16x16x64_bf16 v[36:39], v[76:79], v[240:247], v6\n"
"ds_read_b128 v[108:111], v15 offset:15360\n"
"s_waitcnt lgkmcnt(5)\n"
"v_smfmac_f32_16x16x64_bf16 v[32:35], v[80:83], v[248:255], v6\n"
"ds_read_b128 v[92:95], v0 offset:4096\n"
"v_smfmac_f32_16x16x64_bf16 v[36:39], v[80:83], a[0:7], v6\n"
"ds_read_b128 v[112:115], v15 offset:16384\n"
"s_waitcnt lgkmcnt(5)\n"
"v_smfmac_f32_16x16x64_bf16 v[32:35], v[84:87], v[96:103], v6\n"
"v_smfmac_f32_16x16x64_bf16 v[36:39], v[84:87], a[8:15], v6\n"
"ds_read_b128 v[116:119], v15 offset:17408\n"
"s_waitcnt lgkmcnt(4)\n"
"v_smfmac_f32_16x16x64_bf16 v[32:35], v[88:91], a[16:23], v6\n"
"v_smfmac_f32_16x16x64_bf16 v[36:39], v[88:91], a[24:31], v6\n"
"ds_read_b128 v[96:99], v15 offset:18432\n"
"s_waitcnt lgkmcnt(3)\n"
"v_smfmac_f32_16x16x64_bf16 v[32:35], v[92:95], v[104:111], v6\n"
"ds_read_b128 v[100:103], v15 offset:19456\n"
"v_smfmac_f32_16x16x64_bf16 v[36:39], v[92:95], a[32:39], v6\n"
"v_smfmac_f32_16x16x64_bf16 v[40:43], v[68:71], a[40:47], v6\n"
"ds_read_b128 v[104:107], v15 offset:20480\n"
"v_smfmac_f32_16x16x64_bf16 v[40:43], v[72:75], a[48:55], v6\n"
"ds_read_b128 v[108:111], v15 offset:21504\n"
"s_waitcnt lgkmcnt(4)\n"
"v_smfmac_f32_16x16x64_bf16 v[40:43], v[76:79], v[112:119], v6\n"
"v_add_f32 v32, v32, v34\n"
"v_smfmac_f32_16x16x64_bf16 v[40:43], v[80:83], a[56:63], v6\n"
"ds_read_b128 v[112:115], v15 offset:22528\n"
"v_add_f32 v33, v33, v35\n"
"v_smfmac_f32_16x16x64_bf16 v[40:43], v[84:87], a[64:71], v6\n"
"v_cvt_pk_bf16_f32 v32, v32, v33\n"
"v_smfmac_f32_16x16x64_bf16 v[40:43], v[88:91], a[72:79], v6\n"
"ds_read_b128 v[116:119], v15 offset:23552\n"
"v_add_f32 v36, v36, v38\n"
"s_waitcnt lgkmcnt(4)\n"
"v_smfmac_f32_16x16x64_bf16 v[40:43], v[92:95], v[96:103], v6\n"
"ds_write_b32 v7, v32 offset:512\n"
"v_add_f32 v37, v37, v39\n"
"v_smfmac_f32_16x16x64_bf16 v[44:47], v[68:71], a[80:87], v6\n"
"ds_read_b128 v[96:99], v19 offset:0\n"
"v_cvt_pk_bf16_f32 v36, v36, v37\n"
"v_smfmac_f32_16x16x64_bf16 v[44:47], v[72:75], a[88:95], v6\n"
"ds_read_b128 v[100:103], v19 offset:1024\n"
"s_waitcnt lgkmcnt(5)\n"
"v_smfmac_f32_16x16x64_bf16 v[44:47], v[76:79], v[104:111], v6\n"
"ds_write_b32 v7, v36 offset:1536\n"
"v_smfmac_f32_16x16x64_bf16 v[44:47], v[80:83], a[96:103], v6\n"
"ds_read_b128 v[104:107], v19 offset:6144\n"
"v_add_f32 v40, v40, v42\n"
"v_smfmac_f32_16x16x64_bf16 v[44:47], v[84:87], a[104:111], v6\n"
"ds_read_b128 v[108:111], v19 offset:7168\n"
"v_add_f32 v41, v41, v43\n"
"s_waitcnt lgkmcnt(6)\n"
"v_smfmac_f32_16x16x64_bf16 v[44:47], v[88:91], v[112:119], v6\n"
"v_cvt_pk_bf16_f32 v40, v40, v41\n"
"v_smfmac_f32_16x16x64_bf16 v[44:47], v[92:95], a[112:119], v6\n"
"ds_write_b32 v7, v40 offset:2560\n"
"v_smfmac_f32_16x16x64_bf16 v[48:51], v[68:71], a[120:127], v6\n"
"ds_read_b128 v[112:115], v19 offset:2048\n"
"v_smfmac_f32_16x16x64_bf16 v[48:51], v[72:75], a[128:135], v6\n"
"ds_read_b128 v[116:119], v19 offset:3072\n"
"v_smfmac_f32_16x16x64_bf16 v[48:51], v[76:79], a[136:143], v6\n"
"buffer_load_dwordx4 v11, s[36:39], s70 offen offset:0 sc1 lds\n"
"s_nop 0\n"
"v_add_f32 v44, v44, v46\n"
"v_smfmac_f32_16x16x64_bf16 v[48:51], v[80:83], a[144:151], v6\n"
"s_waitcnt lgkmcnt(0)\n"
"s_barrier\n"
"ds_read_b64_tr_b16 v[32:33], v8 offset:0\n"
"v_add_f32 v45, v45, v47\n"
"v_smfmac_f32_16x16x64_bf16 v[48:51], v[84:87], a[152:159], v6\n"
"ds_read_b64_tr_b16 v[34:35], v8 offset:256\n"
"ds_read_b64_tr_b16 v[36:37], v8 offset:1024\n"
"v_smfmac_f32_16x16x64_bf16 v[48:51], v[88:91], a[160:167], v6\n"
"ds_read_b64_tr_b16 v[38:39], v8 offset:1280\n"
"v_cvt_pk_bf16_f32 v44, v44, v45\n"
"v_smfmac_f32_16x16x64_bf16 v[48:51], v[92:95], a[168:175], v6\n"
"buffer_load_dwordx4 v11, s[36:39], s71 offen offset:1024 sc1 lds\n"
"ds_write_b32 v7, v44 offset:3584\n"
"v_smfmac_f32_16x16x64_bf16 v[52:55], v[56:59], a[176:183], v6\n"
"ds_read_b64_tr_b16 v[40:41], v8 offset:2048\n"
"ds_read_b64_tr_b16 v[42:43], v8 offset:2304\n"
"v_smfmac_f32_16x16x64_bf16 v[52:55], v[60:63], a[184:191], v6\n"
"ds_read_b128 v[56:59], v19 offset:8192\n"
"v_smfmac_f32_16x16x64_bf16 v[52:55], v[64:67], a[192:199], v6\n"
"ds_read_b128 v[60:63], v19 offset:9216\n"
"v_add_f32 v48, v48, v50\n"
"v_add_f32 v49, v49, v51\n"
"v_smfmac_f32_16x16x64_bf16 v[52:55], v[68:71], a[200:207], v6\n"
"buffer_load_dwordx4 v11, s[36:39], s72 offen offset:2048 sc1 lds\n"
"ds_read_b128 v[64:67], v19 offset:4096\n"
"v_cvt_pk_bf16_f32 v48, v48, v49\n"
"v_smfmac_f32_16x16x64_bf16 v[52:55], v[72:75], a[208:215], v6\n"
"ds_write_b32 v7, v48 offset:4608\n"
"v_smfmac_f32_16x16x64_bf16 v[52:55], v[76:79], a[216:223], v6\n"
"ds_read_b128 v[68:71], v19 offset:5120\n"
"v_smfmac_f32_16x16x64_bf16 v[52:55], v[80:83], a[224:231], v6\n"
"ds_read_b128 v[72:75], v19 offset:10240\n"
"v_smfmac_f32_16x16x64_bf16 v[52:55], v[84:87], a[232:239], v6\n"
"ds_read_b128 v[76:79], v19 offset:11264\n"
"v_smfmac_f32_16x16x64_bf16 v[52:55], v[88:91], a[240:247], v6\n"
"s_waitcnt lgkmcnt(3)\n"
"s_barrier\n"
"ds_read_b64_tr_b16 v[80:81], v8 offset:3072\n"
"ds_read_b64_tr_b16 v[82:83], v8 offset:3328\n"
"v_smfmac_f32_16x16x64_bf16 v[52:55], v[92:95], a[248:255], v6\n"
"ds_read_b64_tr_b16 v[84:85], v8 offset:4096\n"
"ds_read_b64_tr_b16 v[86:87], v8 offset:4352\n"
"v_smfmac_f32_16x16x64_bf16 v[120:123], v[32:35], v[96:103], v6\n"
"v_smfmac_f32_16x16x64_bf16 v[124:127], v[32:35], v[104:111], v6\n"
"s_nop 3\n"
"v_add_f32 v52, v52, v54\n"
"v_add_f32 v53, v53, v55\n"
"v_cvt_pk_bf16_f32 v52, v52, v53\n"
"ds_write_b32 v7, v52 offset:5632\n"
"v_smfmac_f32_16x16x64_bf16 v[120:123], v[36:39], v[112:119], v6\n"
"v_smfmac_f32_16x16x64_bf16 v[124:127], v[36:39], v[56:63], v6\n"
"s_waitcnt lgkmcnt(7)\n"
"v_smfmac_f32_16x16x64_bf16 v[120:123], v[40:43], v[64:71], v6\n"
"s_waitcnt lgkmcnt(5)\n"
"v_smfmac_f32_16x16x64_bf16 v[124:127], v[40:43], v[72:79], v6\n"
"s_mov_b64 vcc, s[82:83]\n"
"s_waitcnt lgkmcnt(0)\n"
"s_barrier\n"
"ds_read_b64_tr_b16 v[88:89], v8 offset:5120\n"
"ds_read_b64_tr_b16 v[90:91], v8 offset:5376\n"
"v_mfma_f32_16x16x32_bf16 v[20:23], v[32:35], v[80:83], 0\n"
"v_add_f32 v120, v120, v122\n"
"v_add_f32 v121, v121, v123\n"
"v_mfma_f32_16x16x32_bf16 v[20:23], v[36:39], v[84:87], v[20:23]\n"
"v_add_f32 v124, v124, v126\n"
"v_add_f32 v125, v125, v127\n"
"v_cndmask_b32_dpp v97, v120, v121, vcc quad_perm:[2,3,2,3] row_mask:0xf bank_mask:0xf\n"
"s_nop 0\n"
"v_cndmask_b32_dpp v99, v124, v125, vcc quad_perm:[2,3,2,3] row_mask:0xf bank_mask:0xf\n"
"s_waitcnt lgkmcnt(0)\n"
"v_mfma_f32_16x16x32_bf16 v[20:23], v[40:43], v[88:91], v[20:23]\n"
"s_not_b64 vcc, s[82:83]\n"
"v_cndmask_b32_dpp v96, v121, v120, vcc quad_perm:[0,1,0,1] row_mask:0xf bank_mask:0xf\n"
"v_cndmask_b32_dpp v98, v125, v124, vcc quad_perm:[0,1,0,1] row_mask:0xf bank_mask:0xf\n"
"v_max_f32 v24, v97, v99\n"
"v_max3_f32 v24, v24, v96, v98\n"
"s_nop 1\n"
"v_max_f32_dpp v24, v24, v24 quad_perm:[1,0,3,2] row_mask:0xf bank_mask:0xf\n"
"v_add_f32_dpp v3, v22, v20 quad_perm:[2,3,2,3] row_mask:0xf bank_mask:0xf\n"
"v_add_f32_dpp v4, v21, v23 quad_perm:[0,1,0,1] row_mask:0xf bank_mask:0xf\n"
"v_max_f32_dpp v24, v24, v24 row_ror:4 row_mask:0xf bank_mask:0xf\n"
"v_cndmask_b32 v100, v4, v3, vcc\n"
"s_nop 0\n"
"v_max_f32_dpp v24, v24, v24 row_ror:8 row_mask:0xf bank_mask:0xf\n"
"v_mul_f32 v25, s74, v24\n"
"S52_%=:\n"
"v_fma_f32 v102, v96, s74, -v25\n"
"v_fma_f32 v103, v97, s74, -v25\n"
"v_fma_f32 v104, v98, s74, -v25\n"
"v_fma_f32 v105, v99, s74, -v25\n"
"v_fma_f32 v3, v100, s74, -v25\n"
"v_exp_f32 v102, v102\n"
"v_exp_f32 v103, v103\n"
"v_exp_f32 v104, v104\n"
"v_exp_f32 v105, v105\n"
"v_exp_f32 v5, v3\n"
"v_add_f32 v26, v102, v103\n"
"v_add_f32 v4, v104, v105\n"
"v_add_f32 v26, v26, v5\n"
"v_add_f32 v26, v26, v4\n"
"v_cmp_nge_f32 vcc, s81, v3\n"
"v_mov_b32_dpp v4, v5 quad_perm:[1,1,3,3] row_mask:0xf bank_mask:0xf\n"
"v_add_f32_dpp v26, v26, v26 quad_perm:[1,0,3,2] row_mask:0xf bank_mask:0xf\n"
"s_nop 1\n"
"v_add_f32_dpp v26, v26, v26 row_ror:4 row_mask:0xf bank_mask:0xf\n"
"s_cbranch_vccnz S51_%=\n"
"S51ON_%=:\n"
"s_nop 0\n"
"v_add_f32_dpp v26, v26, v26 row_ror:8 row_mask:0xf bank_mask:0xf\n"
"v_rcp_f32 v26, v26\n"
"s_nop 0\n"
"v_mul_f32 v5, v5, v26\n"
"v_mul_f32 v4, v4, v26\n"
"v_cvt_pk_bf16_f32 v2, v5, v4\n"
"v_min_u32 v2, -2, v2\n"
"buffer_store_dword v2, v10, s[36:39], s49 offen sc1\n"
"s_nop 3\n"
"s_add_u32 s75, s73, 3\n"
"s_min_u32 s75, s75, 64\n"
"v_cmp_gt_u32 vcc, s75, v28\n"
"s_nop 0\n"
"s_mov_b64 exec, vcc\n"
"buffer_load_dword v28, v12, s[36:39], 0 offen sc1\n"
"s_mov_b64 exec, -1\n"
"s_mov_b64 vcc, s[82:83]\n"
"v_cndmask_b32_dpp v57, v88, v90, vcc quad_perm:[2,3,0,1] row_mask:0xf bank_mask:0xf\n"
"v_cndmask_b32_dpp v59, v89, v91, vcc quad_perm:[2,3,0,1] row_mask:0xf bank_mask:0xf\n"
"s_not_b64 vcc, s[82:83]\n"
"v_cndmask_b32_dpp v56, v90, v88, vcc quad_perm:[2,3,0,1] row_mask:0xf bank_mask:0xf\n"
"v_cndmask_b32_dpp v58, v91, v89, vcc quad_perm:[2,3,0,1] row_mask:0xf bank_mask:0xf\n"
"v_min_u32 v56, -2, v56\n"
"v_min_u32 v57, -2, v57\n"
"v_min_u32 v58, -2, v58\n"
"v_min_u32 v59, -2, v59\n"
"buffer_store_dwordx4 v[56:59], v9, s[36:39], s48 offen sc1\n"
"s_add_u32 s48, s48, 98304\n"
"s_add_u32 s73, s73, 1\n"
"s_add_u32 s79, s68, 131072\n"
"s_and_b32 s79, s79, 0x3fffff\n"
"s_xor_b32 s68, s79, s68\n"
"s_lshr_b32 s68, s68, 8\n"
"s_and_b32 s68, s68, 8192\n"
"s_xor_b32 s68, s68, s79\n"
"v_xor_b32 v19, 53248, v19\n"
"s_cmp_lt_u32 s73, 64\n"
"s_cbranch_scc0 S47_%=\n"
"ds_read_b128 v[96:99], v15 offset:0\n"
"ds_read_b128 v[100:103], v15 offset:1024\n"
"ds_read_b128 v[72:75], v15 offset:2048\n"
"ds_read_b128 v[76:79], v15 offset:3072\n"
"ds_read_b128 v[80:83], v15 offset:4096\n"
"ds_read_b128 v[84:87], v15 offset:5120\n"
"ds_read_b128 v[88:91], v15 offset:6144\n"
"ds_read_b128 v[92:95], v15 offset:7168\n"
"ds_read_b128 v[104:107], v15 offset:8192\n"
"ds_read_b128 v[108:111], v15 offset:9216\n"
"ds_read_b128 v[112:115], v15 offset:10240\n"
"ds_read_b128 v[116:119], v15 offset:11264\n"
"v_mov_b32 v32, 0\n"
"v_mov_b32 v33, 0\n"
"v_mov_b32 v34, 0\n"
"v_mov_b32 v35, 0\n"
"v_mov_b32 v36, 0\n"
"v_mov_b32 v37, 0\n"
"v_mov_b32 v38, 0\n"
"v_mov_b32 v39, 0\n"
"v_mov_b32 v40, 0\n"
"v_mov_b32 v41, 0\n"
"v_mov_b32 v42, 0\n"
"v_mov_b32 v43, 0\n"
"v_mov_b32 v44, 0\n"
"v_mov_b32 v45, 0\n"
"v_mov_b32 v46, 0\n"
"v_mov_b32 v47, 0\n"
"v_mov_b32 v48, 0\n"
"v_mov_b32 v49, 0\n"
"v_mov_b32 v50, 0\n"
"v_mov_b32 v51, 0\n"
"v_mov_b32 v52, 0\n"
"v_mov_b32 v53, 0\n"
"v_mov_b32 v54, 0\n"
"v_mov_b32 v55, 0\n"
"v_mov_b32 v120, 0\n"
"v_mov_b32 v121, 0\n"
"v_mov_b32 v122, 0\n"
"v_mov_b32 v123, 0\n"
"v_mov_b32 v124, 0\n"
"v_mov_b32 v125, 0\n"
"v_mov_b32 v126, 0\n"
"v_mov_b32 v127, 0\n"
"s_waitcnt vmcnt(1)\n"
"s_cmp_lg_u32 s73, 63\n"
"s_cbranch_scc1 S48_%=\n"
"s_waitcnt vmcnt(0)\n"
"s_branch S48_%=\n"
"S50_%=:\n"
"s_waitcnt lgkmcnt(0)\n"
"s_mov_b32 s76, 131072\n"
"S57_%=:\n"
"s_memrealtime s[4:5]\n"
"buffer_load_dwordx4 v[56:59], v0, s[36:39], s60 offen offset:0 sc1\n"
"buffer_load_dwordx4 v[60:63], v0, s[36:39], s61 offen offset:0 sc1\n"
"buffer_load_dwordx4 v[64:67], v0, s[36:39], s62 offen offset:0 sc1\n"
"v_mov_b32 v29, 0\n"
"ds_read_b32 v31, v29 offset:16896\n"
"s_lshl_b32 s77, s73, 14\n"
"s_add_u32 s77, s77, s20\n"
"s_add_u32 s77, s77, s21\n"
"s_add_u32 s77, s77, 4096\n"
"v_mov_b32 v32, 0\n"
"v_mov_b32 v33, 0\n"
"v_mov_b32 v34, 0\n"
"v_mov_b32 v35, 0\n"
"v_mov_b32 v36, 0\n"
"v_mov_b32 v37, 0\n"
"v_mov_b32 v38, 0\n"
"v_mov_b32 v39, 0\n"
"v_mov_b32 v40, 0\n"
"v_mov_b32 v41, 0\n"
"v_mov_b32 v42, 0\n"
"v_mov_b32 v43, 0\n"
"v_mov_b32 v44, 0\n"
"v_mov_b32 v45, 0\n"
"v_mov_b32 v46, 0\n"
"v_mov_b32 v47, 0\n"
"v_mov_b32 v48, 0\n"
"v_mov_b32 v49, 0\n"
"v_mov_b32 v50, 0\n"
"v_mov_b32 v51, 0\n"
"v_mov_b32 v52, 0\n"
"v_mov_b32 v53, 0\n"
"v_mov_b32 v54, 0\n"
"v_mov_b32 v55, 0\n"
"s_sub_u32 s76, s76, 1\n"
"s_cbranch_scc1 SABORT_%=\n"
"s_waitcnt vmcnt(2)\n"
"v_max3_u32 v29, v56, v57, 0\n"
"v_max3_u32 v29, v58, v59, v29\n"
"s_waitcnt vmcnt(1)\n"
"v_max3_u32 v29, v60, v61, v29\n"
"v_max3_u32 v29, v62, v63, v29\n"
"s_waitcnt vmcnt(0)\n"
"v_max3_u32 v29, v64, v65, v29\n"
"v_max3_u32 v29, v66, v67, v29\n"
"v_cmp_eq_u32 vcc, -1, v29\n"
"s_nop 0\n"
"s_cbranch_vccnz S57_%=\n"
"s_waitcnt lgkmcnt(0)\n"
"v_lshl_add_u32 v31, s4, 3, v31\n"
"v_lshrrev_b32 v30, 2, v0\n"
"s_mov_b64 exec, 0xffff\n"
"buffer_store_dword v31, v30, s[36:39], s77 offen sc1\n"
"s_mov_b64 exec, -1\n"
"s_branch S56_%=\n"
"S51_%=:\n"
"s_cmp_lg_u32 s45, 0\n"
"s_cbranch_scc1 S58_%=\n"
"v_mov_b32 v3, 0\n"
"v_and_b32 v4, 0x7fff, v80\n"
"v_max_u32 v3, v3, v4\n"
"v_bfe_u32 v4, v80, 16, 15\n"
"v_max_u32 v3, v3, v4\n"
"v_and_b32 v4, 0x7fff, v81\n"
"v_max_u32 v3, v3, v4\n"
"v_bfe_u32 v4, v81, 16, 15\n"
"v_max_u32 v3, v3, v4\n"
"v_and_b32 v4, 0x7fff, v82\n"
"v_max_u32 v3, v3, v4\n"
"v_bfe_u32 v4, v82, 16, 15\n"
"v_max_u32 v3, v3, v4\n"
"v_and_b32 v4, 0x7fff, v83\n"
"v_max_u32 v3, v3, v4\n"
"v_bfe_u32 v4, v83, 16, 15\n"
"v_max_u32 v3, v3, v4\n"
"v_and_b32 v4, 0x7fff, v84\n"
"v_max_u32 v3, v3, v4\n"
"v_bfe_u32 v4, v84, 16, 15\n"
"v_max_u32 v3, v3, v4\n"
"v_and_b32 v4, 0x7fff, v85\n"
"v_max_u32 v3, v3, v4\n"
"v_bfe_u32 v4, v85, 16, 15\n"
"v_max_u32 v3, v3, v4\n"
"v_and_b32 v4, 0x7fff, v86\n"
"v_max_u32 v3, v3, v4\n"
"v_bfe_u32 v4, v86, 16, 15\n"
"v_max_u32 v3, v3, v4\n"
"v_and_b32 v4, 0x7fff, v87\n"
"v_max_u32 v3, v3, v4\n"
"v_bfe_u32 v4, v87, 16, 15\n"
"v_max_u32 v3, v3, v4\n"
"v_and_b32 v4, 0x7fff, v88\n"
"v_max_u32 v3, v3, v4\n"
"v_bfe_u32 v4, v88, 16, 15\n"
"v_max_u32 v3, v3, v4\n"
"v_and_b32 v4, 0x7fff, v89\n"
"v_max_u32 v3, v3, v4\n"
"v_bfe_u32 v4, v89, 16, 15\n"
"v_max_u32 v3, v3, v4\n"
"v_and_b32 v4, 0x7fff, v90\n"
"v_max_u32 v3, v3, v4\n"
"v_bfe_u32 v4, v90, 16, 15\n"
"v_max_u32 v3, v3, v4\n"
"v_and_b32 v4, 0x7fff, v91\n"
"v_max_u32 v3, v3, v4\n"
"v_bfe_u32 v4, v91, 16, 15\n"
"v_max_u32 v3, v3, v4\n"
"v_cmp_lt_u32 vcc, 0x7f80, v3\n"
"s_nop 0\n"
"s_cbranch_vccz S58_%=\n"
"s_mov_b32 s45, 1\n"
"ds_read_b128 v[96:99], v15 offset:0\n"
"ds_read_b128 v[100:103], v15 offset:1024\n"
"ds_read_b128 v[72:75], v15 offset:2048\n"
"ds_read_b128 v[76:79], v15 offset:3072\n"
"ds_read_b128 v[80:83], v15 offset:4096\n"
"ds_read_b128 v[84:87], v15 offset:5120\n"
"ds_read_b128 v[88:91], v15 offset:6144\n"
"ds_read_b128 v[92:95], v15 offset:7168\n"
"ds_read_b128 v[104:107], v15 offset:8192\n"
"ds_read_b128 v[108:111], v15 offset:9216\n"
"ds_read_b128 v[112:115], v15 offset:10240\n"
"ds_read_b128 v[116:119], v15 offset:11264\n"
"v_mov_b32 v32, 0\n"
"v_mov_b32 v33, 0\n"
"v_mov_b32 v34, 0\n"
"v_mov_b32 v35, 0\n"
"v_mov_b32 v36, 0\n"
"v_mov_b32 v37, 0\n"
"v_mov_b32 v38, 0\n"
"v_mov_b32 v39, 0\n"
"v_mov_b32 v40, 0\n"
"v_mov_b32 v41, 0\n"
"v_mov_b32 v42, 0\n"
"v_mov_b32 v43, 0\n"
"v_mov_b32 v44, 0\n"
"v_mov_b32 v45, 0\n"
"v_mov_b32 v46, 0\n"
"v_mov_b32 v47, 0\n"
"v_mov_b32 v48, 0\n"
"v_mov_b32 v49, 0\n"
"v_mov_b32 v50, 0\n"
"v_mov_b32 v51, 0\n"
"v_mov_b32 v52, 0\n"
"v_mov_b32 v53, 0\n"
"v_mov_b32 v54, 0\n"
"v_mov_b32 v55, 0\n"
"v_mov_b32 v120, 0\n"
"v_mov_b32 v121, 0\n"
"v_mov_b32 v122, 0\n"
"v_mov_b32 v123, 0\n"
"v_mov_b32 v124, 0\n"
"v_mov_b32 v125, 0\n"
"v_mov_b32 v126, 0\n"
"v_mov_b32 v127, 0\n"
"s_waitcnt lgkmcnt(0)\n"
"s_barrier\n"
"s_branch S50_%=\n"
"S58_%=:\n"
"s_cmp_eq_u32 s45, 2\n"
"s_cbranch_scc1 S59_%=\n"
"s_mov_b32 s45, 2\n"
"v_max_f32 v24, v24, v100\n"
"s_nop 1\n"
"v_max_f32_dpp v24, v24, v24 quad_perm:[1,0,3,2] row_mask:0xf bank_mask:0xf\n"
"s_nop 1\n"
"v_max_f32_dpp v24, v24, v24 row_ror:4 row_mask:0xf bank_mask:0xf\n"
"s_nop 1\n"
"v_max_f32_dpp v24, v24, v24 row_ror:8 row_mask:0xf bank_mask:0xf\n"
"v_mul_f32 v25, s74, v24\n"
"s_branch S52_%=\n"
"S59_%=:\n"
"s_branch S51ON_%=\n"
"S47_%=:\n"
"s_waitcnt vmcnt(0)\n"
"s_barrier\n"
"v_mov_b32 v29, s73\n"
"buffer_store_dword v29, v13, s[36:39], 0 offen sc1\n"
"s_branch S5_%=\n"
"S4_%=:\n"
"s_mov_b32 s22, 46301184\n"
"s_mov_b32 s23, 46317568\n"
"s_cmp_lg_u32 s66, 0\n"
"s_cbranch_scc1 S60_%=\n"
"v_mov_b32 v24, 0\n"
"s_mov_b64 exec, 1\n"
"buffer_atomic_add v24, v26, s[36:39], s23 offen sc0 sc1\n"
"s_mov_b64 exec, -1\n"
"S60_%=:\n"
"s_add_u32 m0, s45, 0\n"
"s_add_u32 s72, s44, 0\n"
"buffer_load_dwordx4 v1, s[40:43], s72 offen lds\n"
"s_add_u32 m0, s45, 1024\n"
"s_add_u32 s72, s44, 98304\n"
"buffer_load_dwordx4 v2, s[40:43], s72 offen lds\n"
"s_add_u32 m0, s45, 2048\n"
"s_add_u32 s72, s44, 196608\n"
"buffer_load_dwordx4 v1, s[40:43], s72 offen lds\n"
"s_add_u32 m0, s45, 3072\n"
"s_add_u32 s72, s44, 294912\n"
"buffer_load_dwordx4 v2, s[40:43], s72 offen lds\n"
"s_add_u32 m0, s45, 8192\n"
"s_add_u32 s72, s44, 128\n"
"buffer_load_dwordx4 v1, s[40:43], s72 offen lds\n"
"s_add_u32 m0, s45, 9216\n"
"s_add_u32 s72, s44, 98432\n"
"buffer_load_dwordx4 v2, s[40:43], s72 offen lds\n"
"s_add_u32 m0, s45, 10240\n"
"s_add_u32 s72, s44, 196736\n"
"buffer_load_dwordx4 v1, s[40:43], s72 offen lds\n"
"s_add_u32 m0, s45, 11264\n"
"s_add_u32 s72, s44, 295040\n"
"buffer_load_dwordx4 v2, s[40:43], s72 offen lds\n"
"s_add_u32 m0, s45, 16384\n"
"s_add_u32 s72, s44, 256\n"
"buffer_load_dwordx4 v1, s[40:43], s72 offen lds\n"
"s_add_u32 m0, s45, 17408\n"
"s_add_u32 s72, s44, 98560\n"
"buffer_load_dwordx4 v2, s[40:43], s72 offen lds\n"
"s_add_u32 m0, s45, 18432\n"
"s_add_u32 s72, s44, 196864\n"
"buffer_load_dwordx4 v1, s[40:43], s72 offen lds\n"
"s_add_u32 m0, s45, 19456\n"
"s_add_u32 s72, s44, 295168\n"
"buffer_load_dwordx4 v2, s[40:43], s72 offen lds\n"
"s_add_u32 m0, s45, 49152\n"
"s_add_u32 s72, s44, 786432\n"
"buffer_load_dwordx4 v1, s[40:43], s72 offen lds\n"
"s_add_u32 m0, s45, 50176\n"
"s_add_u32 s72, s44, 884736\n"
"buffer_load_dwordx4 v2, s[40:43], s72 offen lds\n"
"s_add_u32 m0, s45, 51200\n"
"s_add_u32 s72, s44, 983040\n"
"buffer_load_dwordx4 v1, s[40:43], s72 offen lds\n"
"s_add_u32 m0, s45, 52224\n"
"s_add_u32 s72, s44, 1081344\n"
"buffer_load_dwordx4 v2, s[40:43], s72 offen lds\n"
"s_add_u32 m0, s45, 57344\n"
"s_add_u32 s72, s44, 786560\n"
"buffer_load_dwordx4 v1, s[40:43], s72 offen lds\n"
"s_add_u32 m0, s45, 58368\n"
"s_add_u32 s72, s44, 884864\n"
"buffer_load_dwordx4 v2, s[40:43], s72 offen lds\n"
"s_add_u32 m0, s45, 59392\n"
"s_add_u32 s72, s44, 983168\n"
"buffer_load_dwordx4 v1, s[40:43], s72 offen lds\n"
"s_add_u32 m0, s45, 60416\n"
"s_add_u32 s72, s44, 1081472\n"
"buffer_load_dwordx4 v2, s[40:43], s72 offen lds\n"
"s_add_u32 m0, s45, 65536\n"
"s_add_u32 s72, s44, 786688\n"
"buffer_load_dwordx4 v1, s[40:43], s72 offen lds\n"
"s_add_u32 m0, s45, 66560\n"
"s_add_u32 s72, s44, 884992\n"
"buffer_load_dwordx4 v2, s[40:43], s72 offen lds\n"
"s_add_u32 m0, s45, 67584\n"
"s_add_u32 s72, s44, 983296\n"
"buffer_load_dwordx4 v1, s[40:43], s72 offen lds\n"
"s_add_u32 m0, s45, 68608\n"
"s_add_u32 s72, s44, 1081600\n"
"buffer_load_dwordx4 v2, s[40:43], s72 offen lds\n"
"s_waitcnt vmcnt(12)\n"
"s_waitcnt lgkmcnt(0)\n"
"s_barrier\n"
"s_cmp_lg_u32 s66, 0\n"
"s_cbranch_scc1 S61_%=\n"
"v_or_b32 v24, 1, v24\n"
"v_add_u32 v24, 1, v24\n"
"buffer_store_dword v24, v26, s[36:39], s22 offen sc1\n"
"S61_%=:\n"
"s_add_u32 m0, s45, 98304\n"
"s_add_u32 s72, s44, 1572864\n"
"buffer_load_dwordx4 v1, s[40:43], s72 offen lds\n"
"s_add_u32 m0, s45, 99328\n"
"s_add_u32 s72, s44, 1671168\n"
"buffer_load_dwordx4 v2, s[40:43], s72 offen lds\n"
"s_add_u32 m0, s45, 100352\n"
"s_add_u32 s72, s44, 1769472\n"
"buffer_load_dwordx4 v1, s[40:43], s72 offen lds\n"
"s_add_u32 m0, s45, 101376\n"
"s_add_u32 s72, s44, 1867776\n"
"buffer_load_dwordx4 v2, s[40:43], s72 offen lds\n"
"s_add_u32 m0, s45, 106496\n"
"s_add_u32 s72, s44, 1572992\n"
"buffer_load_dwordx4 v1, s[40:43], s72 offen lds\n"
"s_add_u32 m0, s45, 107520\n"
"s_add_u32 s72, s44, 1671296\n"
"buffer_load_dwordx4 v2, s[40:43], s72 offen lds\n"
"s_add_u32 m0, s45, 108544\n"
"s_add_u32 s72, s44, 1769600\n"
"buffer_load_dwordx4 v1, s[40:43], s72 offen lds\n"
"s_add_u32 m0, s45, 109568\n"
"s_add_u32 s72, s44, 1867904\n"
"buffer_load_dwordx4 v2, s[40:43], s72 offen lds\n"
"s_add_u32 m0, s45, 114688\n"
"s_add_u32 s72, s44, 1573120\n"
"buffer_load_dwordx4 v1, s[40:43], s72 offen lds\n"
"s_add_u32 m0, s45, 115712\n"
"s_add_u32 s72, s44, 1671424\n"
"buffer_load_dwordx4 v2, s[40:43], s72 offen lds\n"
"s_add_u32 m0, s45, 116736\n"
"s_add_u32 s72, s44, 1769728\n"
"buffer_load_dwordx4 v1, s[40:43], s72 offen lds\n"
"s_add_u32 m0, s45, 117760\n"
"s_add_u32 s72, s44, 1868032\n"
"buffer_load_dwordx4 v2, s[40:43], s72 offen lds\n"
"v_add_u32 v20, 0, v3\n"
"v_add_u32 v21, 0, v4\n"
"v_add_u32 v22, 0, v5\n"
"ds_read_b64_tr_b16 v[168:169], v20 offset:0\n"
"ds_read_b64_tr_b16 v[170:171], v20 offset:256\n"
"ds_read_b64_tr_b16 v[172:173], v20 offset:4096\n"
"ds_read_b64_tr_b16 v[174:175], v20 offset:4352\n"
"ds_read_b64_tr_b16 v[184:185], v21 offset:0\n"
"ds_read_b64_tr_b16 v[186:187], v21 offset:256\n"
"ds_read_b64_tr_b16 v[188:189], v21 offset:4096\n"
"ds_read_b64_tr_b16 v[190:191], v21 offset:4352\n"
"ds_read_b64_tr_b16 v[192:193], v22 offset:0\n"
"ds_read_b64_tr_b16 v[194:195], v22 offset:256\n"
"ds_read_b64_tr_b16 v[196:197], v22 offset:4096\n"
"ds_read_b64_tr_b16 v[198:199], v22 offset:4352\n"
"ds_read_b64_tr_b16 v[176:177], v20 offset:24576\n"
"ds_read_b64_tr_b16 v[178:179], v20 offset:24832\n"
"ds_read_b64_tr_b16 v[180:181], v20 offset:28672\n"
"ds_read_b64_tr_b16 v[182:183], v20 offset:28928\n"
"ds_read_b64_tr_b16 v[72:73], v21 offset:24576\n"
"ds_read_b64_tr_b16 v[74:75], v21 offset:24832\n"
"ds_read_b64_tr_b16 v[76:77], v21 offset:28672\n"
"ds_read_b64_tr_b16 v[78:79], v21 offset:28928\n"
"ds_read_b64_tr_b16 a[192:193], v22 offset:24576\n"
"ds_read_b64_tr_b16 a[194:195], v22 offset:24832\n"
"ds_read_b64_tr_b16 a[196:197], v22 offset:28672\n"
"ds_read_b64_tr_b16 a[198:199], v22 offset:28928\n"
"s_waitcnt vmcnt(12)\n"
"s_waitcnt lgkmcnt(0)\n"
"s_barrier\n"
"s_add_u32 m0, s45, 0\n"
"s_add_u32 s72, s44, 2359296\n"
"buffer_load_dwordx4 v1, s[40:43], s72 offen lds\n"
"s_add_u32 m0, s45, 1024\n"
"s_add_u32 s72, s44, 2457600\n"
"buffer_load_dwordx4 v2, s[40:43], s72 offen lds\n"
"s_add_u32 m0, s45, 2048\n"
"s_add_u32 s72, s44, 2555904\n"
"buffer_load_dwordx4 v1, s[40:43], s72 offen lds\n"
"s_add_u32 m0, s45, 3072\n"
"s_add_u32 s72, s44, 2654208\n"
"buffer_load_dwordx4 v2, s[40:43], s72 offen lds\n"
"s_add_u32 m0, s45, 8192\n"
"s_add_u32 s72, s44, 2359424\n"
"buffer_load_dwordx4 v1, s[40:43], s72 offen lds\n"
"s_add_u32 m0, s45, 9216\n"
"s_add_u32 s72, s44, 2457728\n"
"buffer_load_dwordx4 v2, s[40:43], s72 offen lds\n"
"s_add_u32 m0, s45, 10240\n"
"s_add_u32 s72, s44, 2556032\n"
"buffer_load_dwordx4 v1, s[40:43], s72 offen lds\n"
"s_add_u32 m0, s45, 11264\n"
"s_add_u32 s72, s44, 2654336\n"
"buffer_load_dwordx4 v2, s[40:43], s72 offen lds\n"
"s_add_u32 m0, s45, 16384\n"
"s_add_u32 s72, s44, 2359552\n"
"buffer_load_dwordx4 v1, s[40:43], s72 offen lds\n"
"s_add_u32 m0, s45, 17408\n"
"s_add_u32 s72, s44, 2457856\n"
"buffer_load_dwordx4 v2, s[40:43], s72 offen lds\n"
"s_add_u32 m0, s45, 18432\n"
"s_add_u32 s72, s44, 2556160\n"
"buffer_load_dwordx4 v1, s[40:43], s72 offen lds\n"
"s_add_u32 m0, s45, 19456\n"
"s_add_u32 s72, s44, 2654464\n"
"buffer_load_dwordx4 v2, s[40:43], s72 offen lds\n"
"v_add_u32 v20, 49152, v3\n"
"v_add_u32 v21, 49152, v4\n"
"v_add_u32 v22, 49152, v5\n"
"ds_read_b64_tr_b16 v[200:201], v20 offset:0\n"
"ds_read_b64_tr_b16 v[202:203], v20 offset:256\n"
"ds_read_b64_tr_b16 v[204:205], v20 offset:4096\n"
"ds_read_b64_tr_b16 v[206:207], v20 offset:4352\n"
"ds_read_b64_tr_b16 v[208:209], v21 offset:0\n"
"ds_read_b64_tr_b16 v[210:211], v21 offset:256\n"
"ds_read_b64_tr_b16 v[212:213], v21 offset:4096\n"
"ds_read_b64_tr_b16 v[214:215], v21 offset:4352\n"
"ds_read_b64_tr_b16 a[40:41], v22 offset:0\n"
"ds_read_b64_tr_b16 a[42:43], v22 offset:256\n"
"ds_read_b64_tr_b16 a[44:45], v22 offset:4096\n"
"ds_read_b64_tr_b16 a[46:47], v22 offset:4352\n"
"ds_read_b64_tr_b16 a[80:81], v20 offset:24576\n"
"ds_read_b64_tr_b16 a[82:83], v20 offset:24832\n"
"ds_read_b64_tr_b16 a[84:85], v20 offset:28672\n"
"ds_read_b64_tr_b16 a[86:87], v20 offset:28928\n"
"ds_read_b64_tr_b16 a[120:121], v21 offset:24576\n"
"ds_read_b64_tr_b16 a[122:123], v21 offset:24832\n"
"ds_read_b64_tr_b16 a[124:125], v21 offset:28672\n"
"ds_read_b64_tr_b16 a[126:127], v21 offset:28928\n"
"ds_read_b64_tr_b16 a[200:201], v22 offset:24576\n"
"ds_read_b64_tr_b16 a[202:203], v22 offset:24832\n"
"ds_read_b64_tr_b16 a[204:205], v22 offset:28672\n"
"ds_read_b64_tr_b16 a[206:207], v22 offset:28928\n"
"s_waitcnt vmcnt(12)\n"
"s_waitcnt lgkmcnt(0)\n"
"s_barrier\n"
"s_add_u32 m0, s45, 49152\n"
"s_add_u32 s72, s44, 3145728\n"
"buffer_load_dwordx4 v1, s[40:43], s72 offen lds\n"
"s_add_u32 m0, s45, 50176\n"
"s_add_u32 s72, s44, 3244032\n"
"buffer_load_dwordx4 v2, s[40:43], s72 offen lds\n"
"s_add_u32 m0, s45, 51200\n"
"s_add_u32 s72, s44, 3342336\n"
"buffer_load_dwordx4 v1, s[40:43], s72 offen lds\n"
"s_add_u32 m0, s45, 52224\n"
"s_add_u32 s72, s44, 3440640\n"
"buffer_load_dwordx4 v2, s[40:43], s72 offen lds\n"
"s_add_u32 m0, s45, 57344\n"
"s_add_u32 s72, s44, 3145856\n"
"buffer_load_dwordx4 v1, s[40:43], s72 offen lds\n"
"s_add_u32 m0, s45, 58368\n"
"s_add_u32 s72, s44, 3244160\n"
"buffer_load_dwordx4 v2, s[40:43], s72 offen lds\n"
"s_add_u32 m0, s45, 59392\n"
"s_add_u32 s72, s44, 3342464\n"
"buffer_load_dwordx4 v1, s[40:43], s72 offen lds\n"
"s_add_u32 m0, s45, 60416\n"
"s_add_u32 s72, s44, 3440768\n"
"buffer_load_dwordx4 v2, s[40:43], s72 offen lds\n"
"s_add_u32 m0, s45, 65536\n"
"s_add_u32 s72, s44, 3145984\n"
"buffer_load_dwordx4 v1, s[40:43], s72 offen lds\n"
"s_add_u32 m0, s45, 66560\n"
"s_add_u32 s72, s44, 3244288\n"
"buffer_load_dwordx4 v2, s[40:43], s72 offen lds\n"
"s_add_u32 m0, s45, 67584\n"
"s_add_u32 s72, s44, 3342592\n"
"buffer_load_dwordx4 v1, s[40:43], s72 offen lds\n"
"s_add_u32 m0, s45, 68608\n"
"s_add_u32 s72, s44, 3440896\n"
"buffer_load_dwordx4 v2, s[40:43], s72 offen lds\n"
"v_add_u32 v20, 98304, v3\n"
"v_add_u32 v21, 98304, v4\n"
"v_add_u32 v22, 98304, v5\n"
"ds_read_b64_tr_b16 v[216:217], v20 offset:0\n"
"ds_read_b64_tr_b16 v[218:219], v20 offset:256\n"
"ds_read_b64_tr_b16 v[220:221], v20 offset:4096\n"
"ds_read_b64_tr_b16 v[222:223], v20 offset:4352\n"
"ds_read_b64_tr_b16 v[224:225], v21 offset:0\n"
"ds_read_b64_tr_b16 v[226:227], v21 offset:256\n"
"ds_read_b64_tr_b16 v[228:229], v21 offset:4096\n"
"ds_read_b64_tr_b16 v[230:231], v21 offset:4352\n"
"ds_read_b64_tr_b16 a[48:49], v22 offset:0\n"
"ds_read_b64_tr_b16 a[50:51], v22 offset:256\n"
"ds_read_b64_tr_b16 a[52:53], v22 offset:4096\n"
"ds_read_b64_tr_b16 a[54:55], v22 offset:4352\n"
"ds_read_b64_tr_b16 a[88:89], v20 offset:24576\n"
"ds_read_b64_tr_b16 a[90:91], v20 offset:24832\n"
"ds_read_b64_tr_b16 a[92:93], v20 offset:28672\n"
"ds_read_b64_tr_b16 a[94:95], v20 offset:28928\n"
"ds_read_b64_tr_b16 a[128:129], v21 offset:24576\n"
"ds_read_b64_tr_b16 a[130:131], v21 offset:24832\n"
"ds_read_b64_tr_b16 a[132:133], v21 offset:28672\n"
"ds_read_b64_tr_b16 a[134:135], v21 offset:28928\n"
"ds_read_b64_tr_b16 a[208:209], v22 offset:24576\n"
"ds_read_b64_tr_b16 a[210:211], v22 offset:24832\n"
"ds_read_b64_tr_b16 a[212:213], v22 offset:28672\n"
"ds_read_b64_tr_b16 a[214:215], v22 offset:28928\n"
"s_waitcnt vmcnt(12)\n"
"s_waitcnt lgkmcnt(0)\n"
"s_barrier\n"
"s_add_u32 m0, s45, 98304\n"
"s_add_u32 s72, s44, 3932160\n"
"buffer_load_dwordx4 v1, s[40:43], s72 offen lds\n"
"s_add_u32 m0, s45, 99328\n"
"s_add_u32 s72, s44, 4030464\n"
"buffer_load_dwordx4 v2, s[40:43], s72 offen lds\n"
"s_add_u32 m0, s45, 100352\n"
"s_add_u32 s72, s44, 4128768\n"
"buffer_load_dwordx4 v1, s[40:43], s72 offen lds\n"
"s_add_u32 m0, s45, 101376\n"
"s_add_u32 s72, s44, 4227072\n"
"buffer_load_dwordx4 v2, s[40:43], s72 offen lds\n"
"s_add_u32 m0, s45, 106496\n"
"s_add_u32 s72, s44, 3932288\n"
"buffer_load_dwordx4 v1, s[40:43], s72 offen lds\n"
"s_add_u32 m0, s45, 107520\n"
"s_add_u32 s72, s44, 4030592\n"
"buffer_load_dwordx4 v2, s[40:43], s72 offen lds\n"
"s_add_u32 m0, s45, 108544\n"
"s_add_u32 s72, s44, 4128896\n"
"buffer_load_dwordx4 v1, s[40:43], s72 offen lds\n"
"s_add_u32 m0, s45, 109568\n"
"s_add_u32 s72, s44, 4227200\n"
"buffer_load_dwordx4 v2, s[40:43], s72 offen lds\n"
"s_add_u32 m0, s45, 114688\n"
"s_add_u32 s72, s44, 3932416\n"
"buffer_load_dwordx4 v1, s[40:43], s72 offen lds\n"
"s_add_u32 m0, s45, 115712\n"
"s_add_u32 s72, s44, 4030720\n"
"buffer_load_dwordx4 v2, s[40:43], s72 offen lds\n"
"s_add_u32 m0, s45, 116736\n"
"s_add_u32 s72, s44, 4129024\n"
"buffer_load_dwordx4 v1, s[40:43], s72 offen lds\n"
"s_add_u32 m0, s45, 117760\n"
"s_add_u32 s72, s44, 4227328\n"
"buffer_load_dwordx4 v2, s[40:43], s72 offen lds\n"
"v_add_u32 v20, 0, v3\n"
"v_add_u32 v21, 0, v4\n"
"v_add_u32 v22, 0, v5\n"
"ds_read_b64_tr_b16 v[232:233], v20 offset:0\n"
"ds_read_b64_tr_b16 v[234:235], v20 offset:256\n"
"ds_read_b64_tr_b16 v[236:237], v20 offset:4096\n"
"ds_read_b64_tr_b16 v[238:239], v20 offset:4352\n"
"ds_read_b64_tr_b16 v[240:241], v21 offset:0\n"
"ds_read_b64_tr_b16 v[242:243], v21 offset:256\n"
"ds_read_b64_tr_b16 v[244:245], v21 offset:4096\n"
"ds_read_b64_tr_b16 v[246:247], v21 offset:4352\n"
"ds_read_b64_tr_b16 v[96:97], v22 offset:0\n"
"ds_read_b64_tr_b16 v[98:99], v22 offset:256\n"
"ds_read_b64_tr_b16 v[100:101], v22 offset:4096\n"
"ds_read_b64_tr_b16 v[102:103], v22 offset:4352\n"
"ds_read_b64_tr_b16 v[112:113], v20 offset:24576\n"
"ds_read_b64_tr_b16 v[114:115], v20 offset:24832\n"
"ds_read_b64_tr_b16 v[116:117], v20 offset:28672\n"
"ds_read_b64_tr_b16 v[118:119], v20 offset:28928\n"
"ds_read_b64_tr_b16 a[136:137], v21 offset:24576\n"
"ds_read_b64_tr_b16 a[138:139], v21 offset:24832\n"
"ds_read_b64_tr_b16 a[140:141], v21 offset:28672\n"
"ds_read_b64_tr_b16 a[142:143], v21 offset:28928\n"
"ds_read_b64_tr_b16 a[216:217], v22 offset:24576\n"
"ds_read_b64_tr_b16 a[218:219], v22 offset:24832\n"
"ds_read_b64_tr_b16 a[220:221], v22 offset:28672\n"
"ds_read_b64_tr_b16 a[222:223], v22 offset:28928\n"
"s_waitcnt vmcnt(12)\n"
"s_waitcnt lgkmcnt(0)\n"
"s_barrier\n"
"s_add_u32 m0, s45, 0\n"
"s_add_u32 s72, s44, 4718592\n"
"buffer_load_dwordx4 v1, s[40:43], s72 offen lds\n"
"s_add_u32 m0, s45, 1024\n"
"s_add_u32 s72, s44, 4816896\n"
"buffer_load_dwordx4 v2, s[40:43], s72 offen lds\n"
"s_add_u32 m0, s45, 2048\n"
"s_add_u32 s72, s44, 4915200\n"
"buffer_load_dwordx4 v1, s[40:43], s72 offen lds\n"
"s_add_u32 m0, s45, 3072\n"
"s_add_u32 s72, s44, 5013504\n"
"buffer_load_dwordx4 v2, s[40:43], s72 offen lds\n"
"s_add_u32 m0, s45, 8192\n"
"s_add_u32 s72, s44, 4718720\n"
"buffer_load_dwordx4 v1, s[40:43], s72 offen lds\n"
"s_add_u32 m0, s45, 9216\n"
"s_add_u32 s72, s44, 4817024\n"
"buffer_load_dwordx4 v2, s[40:43], s72 offen lds\n"
"s_add_u32 m0, s45, 10240\n"
"s_add_u32 s72, s44, 4915328\n"
"buffer_load_dwordx4 v1, s[40:43], s72 offen lds\n"
"s_add_u32 m0, s45, 11264\n"
"s_add_u32 s72, s44, 5013632\n"
"buffer_load_dwordx4 v2, s[40:43], s72 offen lds\n"
"s_add_u32 m0, s45, 16384\n"
"s_add_u32 s72, s44, 4718848\n"
"buffer_load_dwordx4 v1, s[40:43], s72 offen lds\n"
"s_add_u32 m0, s45, 17408\n"
"s_add_u32 s72, s44, 4817152\n"
"buffer_load_dwordx4 v2, s[40:43], s72 offen lds\n"
"s_add_u32 m0, s45, 18432\n"
"s_add_u32 s72, s44, 4915456\n"
"buffer_load_dwordx4 v1, s[40:43], s72 offen lds\n"
"s_add_u32 m0, s45, 19456\n"
"s_add_u32 s72, s44, 5013760\n"
"buffer_load_dwordx4 v2, s[40:43], s72 offen lds\n"
"v_add_u32 v20, 49152, v3\n"
"v_add_u32 v21, 49152, v4\n"
"v_add_u32 v22, 49152, v5\n"
"ds_read_b64_tr_b16 v[248:249], v20 offset:0\n"
"ds_read_b64_tr_b16 v[250:251], v20 offset:256\n"
"ds_read_b64_tr_b16 v[252:253], v20 offset:4096\n"
"ds_read_b64_tr_b16 v[254:255], v20 offset:4352\n"
"ds_read_b64_tr_b16 a[0:1], v21 offset:0\n"
"ds_read_b64_tr_b16 a[2:3], v21 offset:256\n"
"ds_read_b64_tr_b16 a[4:5], v21 offset:4096\n"
"ds_read_b64_tr_b16 a[6:7], v21 offset:4352\n"
"ds_read_b64_tr_b16 a[56:57], v22 offset:0\n"
"ds_read_b64_tr_b16 a[58:59], v22 offset:256\n"
"ds_read_b64_tr_b16 a[60:61], v22 offset:4096\n"
"ds_read_b64_tr_b16 a[62:63], v22 offset:4352\n"
"ds_read_b64_tr_b16 a[96:97], v20 offset:24576\n"
"ds_read_b64_tr_b16 a[98:99], v20 offset:24832\n"
"ds_read_b64_tr_b16 a[100:101], v20 offset:28672\n"
"ds_read_b64_tr_b16 a[102:103], v20 offset:28928\n"
"ds_read_b64_tr_b16 a[144:145], v21 offset:24576\n"
"ds_read_b64_tr_b16 a[146:147], v21 offset:24832\n"
"ds_read_b64_tr_b16 a[148:149], v21 offset:28672\n"
"ds_read_b64_tr_b16 a[150:151], v21 offset:28928\n"
"ds_read_b64_tr_b16 a[224:225], v22 offset:24576\n"
"ds_read_b64_tr_b16 a[226:227], v22 offset:24832\n"
"ds_read_b64_tr_b16 a[228:229], v22 offset:28672\n"
"ds_read_b64_tr_b16 a[230:231], v22 offset:28928\n"
"s_waitcnt vmcnt(12)\n"
"s_waitcnt lgkmcnt(0)\n"
"s_barrier\n"
"s_add_u32 m0, s45, 49152\n"
"s_add_u32 s72, s44, 5505024\n"
"buffer_load_dwordx4 v1, s[40:43], s72 offen lds\n"
"s_add_u32 m0, s45, 50176\n"
"s_add_u32 s72, s44, 5603328\n"
"buffer_load_dwordx4 v2, s[40:43], s72 offen lds\n"
"s_add_u32 m0, s45, 51200\n"
"s_add_u32 s72, s44, 5701632\n"
"buffer_load_dwordx4 v1, s[40:43], s72 offen lds\n"
"s_add_u32 m0, s45, 52224\n"
"s_add_u32 s72, s44, 5799936\n"
"buffer_load_dwordx4 v2, s[40:43], s72 offen lds\n"
"s_add_u32 m0, s45, 57344\n"
"s_add_u32 s72, s44, 5505152\n"
"buffer_load_dwordx4 v1, s[40:43], s72 offen lds\n"
"s_add_u32 m0, s45, 58368\n"
"s_add_u32 s72, s44, 5603456\n"
"buffer_load_dwordx4 v2, s[40:43], s72 offen lds\n"
"s_add_u32 m0, s45, 59392\n"
"s_add_u32 s72, s44, 5701760\n"
"buffer_load_dwordx4 v1, s[40:43], s72 offen lds\n"
"s_add_u32 m0, s45, 60416\n"
"s_add_u32 s72, s44, 5800064\n"
"buffer_load_dwordx4 v2, s[40:43], s72 offen lds\n"
"s_add_u32 m0, s45, 65536\n"
"s_add_u32 s72, s44, 5505280\n"
"buffer_load_dwordx4 v1, s[40:43], s72 offen lds\n"
"s_add_u32 m0, s45, 66560\n"
"s_add_u32 s72, s44, 5603584\n"
"buffer_load_dwordx4 v2, s[40:43], s72 offen lds\n"
"s_add_u32 m0, s45, 67584\n"
"s_add_u32 s72, s44, 5701888\n"
"buffer_load_dwordx4 v1, s[40:43], s72 offen lds\n"
"s_add_u32 m0, s45, 68608\n"
"s_add_u32 s72, s44, 5800192\n"
"buffer_load_dwordx4 v2, s[40:43], s72 offen lds\n"
"v_add_u32 v20, 98304, v3\n"
"v_add_u32 v21, 98304, v4\n"
"v_add_u32 v22, 98304, v5\n"
"ds_read_b64_tr_b16 v[80:81], v20 offset:0\n"
"ds_read_b64_tr_b16 v[82:83], v20 offset:256\n"
"ds_read_b64_tr_b16 v[84:85], v20 offset:4096\n"
"ds_read_b64_tr_b16 v[86:87], v20 offset:4352\n"
"ds_read_b64_tr_b16 a[8:9], v21 offset:0\n"
"ds_read_b64_tr_b16 a[10:11], v21 offset:256\n"
"ds_read_b64_tr_b16 a[12:13], v21 offset:4096\n"
"ds_read_b64_tr_b16 a[14:15], v21 offset:4352\n"
"ds_read_b64_tr_b16 a[64:65], v22 offset:0\n"
"ds_read_b64_tr_b16 a[66:67], v22 offset:256\n"
"ds_read_b64_tr_b16 a[68:69], v22 offset:4096\n"
"ds_read_b64_tr_b16 a[70:71], v22 offset:4352\n"
"ds_read_b64_tr_b16 a[104:105], v20 offset:24576\n"
"ds_read_b64_tr_b16 a[106:107], v20 offset:24832\n"
"ds_read_b64_tr_b16 a[108:109], v20 offset:28672\n"
"ds_read_b64_tr_b16 a[110:111], v20 offset:28928\n"
"ds_read_b64_tr_b16 a[152:153], v21 offset:24576\n"
"ds_read_b64_tr_b16 a[154:155], v21 offset:24832\n"
"ds_read_b64_tr_b16 a[156:157], v21 offset:28672\n"
"ds_read_b64_tr_b16 a[158:159], v21 offset:28928\n"
"ds_read_b64_tr_b16 a[232:233], v22 offset:24576\n"
"ds_read_b64_tr_b16 a[234:235], v22 offset:24832\n"
"ds_read_b64_tr_b16 a[236:237], v22 offset:28672\n"
"ds_read_b64_tr_b16 a[238:239], v22 offset:28928\n"
"s_waitcnt vmcnt(12)\n"
"s_waitcnt lgkmcnt(0)\n"
"s_barrier\n"
"s_add_u32 m0, s45, 98304\n"
"s_add_u32 s72, s44, 6291456\n"
"buffer_load_dwordx4 v1, s[40:43], s72 offen lds\n"
"s_add_u32 m0, s45, 99328\n"
"s_add_u32 s72, s44, 6389760\n"
"buffer_load_dwordx4 v2, s[40:43], s72 offen lds\n"
"s_add_u32 m0, s45, 100352\n"
"s_add_u32 s72, s44, 6488064\n"
"buffer_load_dwordx4 v1, s[40:43], s72 offen lds\n"
"s_add_u32 m0, s45, 101376\n"
"s_add_u32 s72, s44, 6586368\n"
"buffer_load_dwordx4 v2, s[40:43], s72 offen lds\n"
"s_add_u32 m0, s45, 106496\n"
"s_add_u32 s72, s44, 6291584\n"
"buffer_load_dwordx4 v1, s[40:43], s72 offen lds\n"
"s_add_u32 m0, s45, 107520\n"
"s_add_u32 s72, s44, 6389888\n"
"buffer_load_dwordx4 v2, s[40:43], s72 offen lds\n"
"s_add_u32 m0, s45, 108544\n"
"s_add_u32 s72, s44, 6488192\n"
"buffer_load_dwordx4 v1, s[40:43], s72 offen lds\n"
"s_add_u32 m0, s45, 109568\n"
"s_add_u32 s72, s44, 6586496\n"
"buffer_load_dwordx4 v2, s[40:43], s72 offen lds\n"
"s_add_u32 m0, s45, 114688\n"
"s_add_u32 s72, s44, 6291712\n"
"buffer_load_dwordx4 v1, s[40:43], s72 offen lds\n"
"s_add_u32 m0, s45, 115712\n"
"s_add_u32 s72, s44, 6390016\n"
"buffer_load_dwordx4 v2, s[40:43], s72 offen lds\n"
"s_add_u32 m0, s45, 116736\n"
"s_add_u32 s72, s44, 6488320\n"
"buffer_load_dwordx4 v1, s[40:43], s72 offen lds\n"
"s_add_u32 m0, s45, 117760\n"
"s_add_u32 s72, s44, 6586624\n"
"buffer_load_dwordx4 v2, s[40:43], s72 offen lds\n"
"v_add_u32 v20, 0, v3\n"
"v_add_u32 v21, 0, v4\n"
"v_add_u32 v22, 0, v5\n"
"ds_read_b64_tr_b16 a[16:17], v20 offset:0\n"
"ds_read_b64_tr_b16 a[18:19], v20 offset:256\n"
"ds_read_b64_tr_b16 a[20:21], v20 offset:4096\n"
"ds_read_b64_tr_b16 a[22:23], v20 offset:4352\n"
"ds_read_b64_tr_b16 a[24:25], v21 offset:0\n"
"ds_read_b64_tr_b16 a[26:27], v21 offset:256\n"
"ds_read_b64_tr_b16 a[28:29], v21 offset:4096\n"
"ds_read_b64_tr_b16 a[30:31], v21 offset:4352\n"
"ds_read_b64_tr_b16 a[72:73], v22 offset:0\n"
"ds_read_b64_tr_b16 a[74:75], v22 offset:256\n"
"ds_read_b64_tr_b16 a[76:77], v22 offset:4096\n"
"ds_read_b64_tr_b16 a[78:79], v22 offset:4352\n"
"ds_read_b64_tr_b16 v[120:121], v20 offset:24576\n"
"ds_read_b64_tr_b16 v[122:123], v20 offset:24832\n"
"ds_read_b64_tr_b16 v[124:125], v20 offset:28672\n"
"ds_read_b64_tr_b16 v[126:127], v20 offset:28928\n"
"ds_read_b64_tr_b16 a[160:161], v21 offset:24576\n"
"ds_read_b64_tr_b16 a[162:163], v21 offset:24832\n"
"ds_read_b64_tr_b16 a[164:165], v21 offset:28672\n"
"ds_read_b64_tr_b16 a[166:167], v21 offset:28928\n"
"ds_read_b64_tr_b16 a[240:241], v22 offset:24576\n"
"ds_read_b64_tr_b16 a[242:243], v22 offset:24832\n"
"ds_read_b64_tr_b16 a[244:245], v22 offset:28672\n"
"ds_read_b64_tr_b16 a[246:247], v22 offset:28928\n"
"s_waitcnt vmcnt(12)\n"
"s_waitcnt lgkmcnt(0)\n"
"s_barrier\n"
"s_add_u32 m0, s45, 0\n"
"s_add_u32 s72, s44, 7077888\n"
"buffer_load_dwordx4 v1, s[40:43], s72 offen lds\n"
"s_add_u32 m0, s45, 1024\n"
"s_add_u32 s72, s44, 7176192\n"
"buffer_load_dwordx4 v2, s[40:43], s72 offen lds\n"
"s_add_u32 m0, s45, 2048\n"
"s_add_u32 s72, s44, 7274496\n"
"buffer_load_dwordx4 v1, s[40:43], s72 offen lds\n"
"s_add_u32 m0, s45, 3072\n"
"s_add_u32 s72, s44, 7372800\n"
"buffer_load_dwordx4 v2, s[40:43], s72 offen lds\n"
"s_add_u32 m0, s45, 8192\n"
"s_add_u32 s72, s44, 7078016\n"
"buffer_load_dwordx4 v1, s[40:43], s72 offen lds\n"
"s_add_u32 m0, s45, 9216\n"
"s_add_u32 s72, s44, 7176320\n"
"buffer_load_dwordx4 v2, s[40:43], s72 offen lds\n"
"s_add_u32 m0, s45, 10240\n"
"s_add_u32 s72, s44, 7274624\n"
"buffer_load_dwordx4 v1, s[40:43], s72 offen lds\n"
"s_add_u32 m0, s45, 11264\n"
"s_add_u32 s72, s44, 7372928\n"
"buffer_load_dwordx4 v2, s[40:43], s72 offen lds\n"
"s_add_u32 m0, s45, 16384\n"
"s_add_u32 s72, s44, 7078144\n"
"buffer_load_dwordx4 v1, s[40:43], s72 offen lds\n"
"s_add_u32 m0, s45, 17408\n"
"s_add_u32 s72, s44, 7176448\n"
"buffer_load_dwordx4 v2, s[40:43], s72 offen lds\n"
"s_add_u32 m0, s45, 18432\n"
"s_add_u32 s72, s44, 7274752\n"
"buffer_load_dwordx4 v1, s[40:43], s72 offen lds\n"
"s_add_u32 m0, s45, 19456\n"
"s_add_u32 s72, s44, 7373056\n"
"buffer_load_dwordx4 v2, s[40:43], s72 offen lds\n"
"v_add_u32 v20, 49152, v3\n"
"v_add_u32 v21, 49152, v4\n"
"v_add_u32 v22, 49152, v5\n"
"ds_read_b64_tr_b16 v[88:89], v20 offset:0\n"
"ds_read_b64_tr_b16 v[90:91], v20 offset:256\n"
"ds_read_b64_tr_b16 v[92:93], v20 offset:4096\n"
"ds_read_b64_tr_b16 v[94:95], v20 offset:4352\n"
"ds_read_b64_tr_b16 a[32:33], v21 offset:0\n"
"ds_read_b64_tr_b16 a[34:35], v21 offset:256\n"
"ds_read_b64_tr_b16 a[36:37], v21 offset:4096\n"
"ds_read_b64_tr_b16 a[38:39], v21 offset:4352\n"
"ds_read_b64_tr_b16 v[104:105], v22 offset:0\n"
"ds_read_b64_tr_b16 v[106:107], v22 offset:256\n"
"ds_read_b64_tr_b16 v[108:109], v22 offset:4096\n"
"ds_read_b64_tr_b16 v[110:111], v22 offset:4352\n"
"ds_read_b64_tr_b16 a[112:113], v20 offset:24576\n"
"ds_read_b64_tr_b16 a[114:115], v20 offset:24832\n"
"ds_read_b64_tr_b16 a[116:117], v20 offset:28672\n"
"ds_read_b64_tr_b16 a[118:119], v20 offset:28928\n"
"ds_read_b64_tr_b16 a[168:169], v21 offset:24576\n"
"ds_read_b64_tr_b16 a[170:171], v21 offset:24832\n"
"ds_read_b64_tr_b16 a[172:173], v21 offset:28672\n"
"ds_read_b64_tr_b16 a[174:175], v21 offset:28928\n"
"ds_read_b64_tr_b16 a[248:249], v22 offset:24576\n"
"ds_read_b64_tr_b16 a[250:251], v22 offset:24832\n"
"ds_read_b64_tr_b16 a[252:253], v22 offset:28672\n"
"ds_read_b64_tr_b16 a[254:255], v22 offset:28928\n"
"s_cmp_lg_u32 s66, 0\n"
"s_cbranch_scc1 S62_%=\n"
"s_mov_b32 s76, 131072\n"
"s_mov_b64 exec, 1\n"
"s_branch S64_%=\n"
"S63_%=:\n"
"s_sleep 4\n"
"S64_%=:\n"
"v_mov_b32 v29, 0\n"
"buffer_atomic_add v29, v26, s[36:39], s23 offen sc0 sc1\n"
"s_sub_u32 s76, s76, 1\n"
"s_cbranch_scc1 SABORT_%=\n"
"s_waitcnt vmcnt(0)\n"
"v_xor_b32 v29, v29, v24\n"
"v_cmp_lt_u32 vcc, 1, v29\n"
"s_nop 0\n"
"s_cbranch_vccnz S63_%=\n"
"v_mov_b32 v28, 153600\n"
"ds_write_b32 v28, v29\n"
"v_mov_b32 v24, 1\n"
"s_add_u32 s22, s23, 16384\n"
"buffer_store_dword v24, v26, s[36:39], s22 offen sc1\n"
"s_mov_b64 exec, -1\n"
"S62_%=:\n"
"s_waitcnt lgkmcnt(0)\n"
"s_barrier\n"
"v_mov_b32 v28, 153600\n"
"ds_read_b32 v28, v28\n"
"s_waitcnt lgkmcnt(0)\n"
"v_readfirstlane_b32 s22, v28\n"
"s_nop 0\n"
"s_lshl_b32 s22, s22, 13\n"
"v_xor_b32 v12, s22, v12\n"
"v_xor_b32 v13, s22, v13\n"
"s_xor_b32 s68, s68, s22\n"
"s_xor_b32 s20, s20, s22\n"
"s_waitcnt vmcnt(12)\n"
"s_waitcnt lgkmcnt(0)\n"
"s_barrier\n"
"v_add_u32 v20, 98304, v3\n"
"v_add_u32 v21, 98304, v4\n"
"v_add_u32 v22, 98304, v5\n"
"ds_read_b64_tr_b16 v[32:33], v20 offset:0\n"
"ds_read_b64_tr_b16 v[34:35], v20 offset:256\n"
"ds_read_b64_tr_b16 v[36:37], v20 offset:4096\n"
"ds_read_b64_tr_b16 v[38:39], v20 offset:4352\n"
"ds_read_b64_tr_b16 v[128:129], v21 offset:0\n"
"ds_read_b64_tr_b16 v[130:131], v21 offset:256\n"
"ds_read_b64_tr_b16 v[132:133], v21 offset:4096\n"
"ds_read_b64_tr_b16 v[134:135], v21 offset:4352\n"
"ds_read_b64_tr_b16 v[136:137], v22 offset:0\n"
"ds_read_b64_tr_b16 v[138:139], v22 offset:256\n"
"ds_read_b64_tr_b16 v[140:141], v22 offset:4096\n"
"ds_read_b64_tr_b16 v[142:143], v22 offset:4352\n"
"ds_read_b64_tr_b16 v[40:41], v20 offset:24576\n"
"ds_read_b64_tr_b16 v[42:43], v20 offset:24832\n"
"ds_read_b64_tr_b16 v[44:45], v20 offset:28672\n"
"ds_read_b64_tr_b16 v[46:47], v20 offset:28928\n"
"ds_read_b64_tr_b16 v[48:49], v21 offset:24576\n"
"ds_read_b64_tr_b16 v[50:51], v21 offset:24832\n"
"ds_read_b64_tr_b16 v[52:53], v21 offset:28672\n"
"ds_read_b64_tr_b16 v[54:55], v21 offset:28928\n"
"ds_read_b64_tr_b16 a[176:177], v22 offset:24576\n"
"ds_read_b64_tr_b16 a[178:179], v22 offset:24832\n"
"ds_read_b64_tr_b16 a[180:181], v22 offset:28672\n"
"ds_read_b64_tr_b16 a[182:183], v22 offset:28928\n"
"s_waitcnt vmcnt(0)\n"
"s_waitcnt lgkmcnt(0)\n"
"s_barrier\n"
"v_add_u32 v20, 0, v3\n"
"v_add_u32 v21, 0, v4\n"
"v_add_u32 v22, 0, v5\n"
"ds_read_b64_tr_b16 v[56:57], v20 offset:0\n"
"ds_read_b64_tr_b16 v[58:59], v20 offset:256\n"
"ds_read_b64_tr_b16 v[60:61], v20 offset:4096\n"
"ds_read_b64_tr_b16 v[62:63], v20 offset:4352\n"
"ds_read_b64_tr_b16 v[152:153], v21 offset:0\n"
"ds_read_b64_tr_b16 v[154:155], v21 offset:256\n"
"ds_read_b64_tr_b16 v[156:157], v21 offset:4096\n"
"ds_read_b64_tr_b16 v[158:159], v21 offset:4352\n"
"ds_read_b64_tr_b16 v[160:161], v22 offset:0\n"
"ds_read_b64_tr_b16 v[162:163], v22 offset:256\n"
"ds_read_b64_tr_b16 v[164:165], v22 offset:4096\n"
"ds_read_b64_tr_b16 v[166:167], v22 offset:4352\n"
"ds_read_b64_tr_b16 v[144:145], v20 offset:24576\n"
"ds_read_b64_tr_b16 v[146:147], v20 offset:24832\n"
"ds_read_b64_tr_b16 v[148:149], v20 offset:28672\n"
"ds_read_b64_tr_b16 v[150:151], v20 offset:28928\n"
"ds_read_b64_tr_b16 v[64:65], v21 offset:24576\n"
"ds_read_b64_tr_b16 v[66:67], v21 offset:24832\n"
"ds_read_b64_tr_b16 v[68:69], v21 offset:28672\n"
"ds_read_b64_tr_b16 v[70:71], v21 offset:28928\n"
"ds_read_b64_tr_b16 a[184:185], v22 offset:24576\n"
"ds_read_b64_tr_b16 a[186:187], v22 offset:24832\n"
"ds_read_b64_tr_b16 a[188:189], v22 offset:28672\n"
"ds_read_b64_tr_b16 a[190:191], v22 offset:28928\n"
"s_waitcnt lgkmcnt(0)\n"
"s_barrier\n"
"buffer_load_dword v28, v12, s[36:39], 0 offen sc1\n"
"ds_write_b128 v15, v[32:35] offset:0\n"
"ds_write_b128 v15, v[36:39] offset:1024\n"
"ds_write_b128 v15, v[40:43] offset:2048\n"
"ds_write_b128 v15, v[44:47] offset:3072\n"
"ds_write_b128 v15, v[48:51] offset:4096\n"
"ds_write_b128 v15, v[52:55] offset:5120\n"
"ds_write_b128 v15, v[56:59] offset:6144\n"
"ds_write_b128 v15, v[60:63] offset:7168\n"
"ds_write_b128 v15, v[64:67] offset:8192\n"
"ds_write_b128 v15, v[68:71] offset:9216\n"
"ds_write_b128 v15, v[72:75] offset:10240\n"
"ds_write_b128 v15, v[76:79] offset:11264\n"
"ds_write_b128 v15, v[80:83] offset:12288\n"
"ds_write_b128 v15, v[84:87] offset:13312\n"
"ds_write_b128 v15, v[88:91] offset:14336\n"
"ds_write_b128 v15, v[92:95] offset:15360\n"
"ds_write_b128 v15, v[96:99] offset:16384\n"
"ds_write_b128 v15, v[100:103] offset:17408\n"
"ds_write_b128 v15, v[104:107] offset:18432\n"
"ds_write_b128 v15, v[108:111] offset:19456\n"
"ds_write_b128 v15, v[112:115] offset:20480\n"
"ds_write_b128 v15, v[116:119] offset:21504\n"
"ds_write_b128 v15, v[120:123] offset:22528\n"
"ds_write_b128 v15, v[124:127] offset:23552\n"
"s_waitcnt lgkmcnt(0)\n"
"ds_read_b128 v[96:99], v15 offset:0\n"
"ds_read_b128 v[100:103], v15 offset:1024\n"
"ds_read_b128 v[72:75], v15 offset:2048\n"
"ds_read_b128 v[76:79], v15 offset:3072\n"
"ds_read_b128 v[80:83], v15 offset:4096\n"
"ds_read_b128 v[84:87], v15 offset:5120\n"
"ds_read_b128 v[88:91], v15 offset:6144\n"
"ds_read_b128 v[92:95], v15 offset:7168\n"
"ds_read_b128 v[104:107], v15 offset:8192\n"
"ds_read_b128 v[108:111], v15 offset:9216\n"
"ds_read_b128 v[112:115], v15 offset:10240\n"
"ds_read_b128 v[116:119], v15 offset:11264\n"
"v_mov_b32 v32, 0\n"
"v_mov_b32 v33, 0\n"
"v_mov_b32 v34, 0\n"
"v_mov_b32 v35, 0\n"
"v_mov_b32 v36, 0\n"
"v_mov_b32 v37, 0\n"
"v_mov_b32 v38, 0\n"
"v_mov_b32 v39, 0\n"
"v_mov_b32 v40, 0\n"
"v_mov_b32 v41, 0\n"
"v_mov_b32 v42, 0\n"
"v_mov_b32 v43, 0\n"
"v_mov_b32 v44, 0\n"
"v_mov_b32 v45, 0\n"
"v_mov_b32 v46, 0\n"
"v_mov_b32 v47, 0\n"
"v_mov_b32 v48, 0\n"
"v_mov_b32 v49, 0\n"
"v_mov_b32 v50, 0\n"
"v_mov_b32 v51, 0\n"
"v_mov_b32 v52, 0\n"
"v_mov_b32 v53, 0\n"
"v_mov_b32 v54, 0\n"
"v_mov_b32 v55, 0\n"
"v_mov_b32 v120, 0\n"
"v_mov_b32 v121, 0\n"
"v_mov_b32 v122, 0\n"
"v_mov_b32 v123, 0\n"
"v_mov_b32 v124, 0\n"
"v_mov_b32 v125, 0\n"
"v_mov_b32 v126, 0\n"
"v_mov_b32 v127, 0\n"
"s_waitcnt vmcnt(0)\n"
"s_nop 3\n"
"S66_%=:\n"
"s_mov_b32 s45, 0\n"
"s_cmp_eq_u32 s73, 63\n"
"s_cselect_b32 s49, 234881024, s68\n"
"s_xor_b32 s49, s49, 16384\n"
"s_cmp_gt_u32 s69, s73\n"
"s_cbranch_scc1 S67_%=\n"
"s_add_u32 s75, s73, 1\n"
"v_cmp_gt_u32 vcc, s75, v28\n"
"s_nop 0\n"
"s_cbranch_vccz S71_%=\n"
"s_mov_b32 s76, 131072\n"
"s_branch S73_%=\n"
"S72_%=:\n"
"s_sleep 2\n"
"S73_%=:\n"
"buffer_load_dword v28, v12, s[36:39], 0 offen sc1\n"
"s_sub_u32 s76, s76, 1\n"
"s_cbranch_scc1 SABORT_%=\n"
"s_waitcnt vmcnt(0)\n"
"v_cmp_gt_u32 vcc, s75, v28\n"
"s_nop 0\n"
"s_cbranch_vccnz S72_%=\n"
"S71_%=:\n"
"s_add_u32 s51, s50, s47\n"
"s_add_u32 m0, s51, 0\n"
"s_add_u32 s72, s46, 0\n"
"buffer_load_dwordx4 v11, s[36:39], s72 offen sc1 lds\n"
"s_add_u32 m0, s51, 1024\n"
"s_add_u32 s72, s46, 2048\n"
"buffer_load_dwordx4 v11, s[36:39], s72 offen sc1 lds\n"
"s_add_u32 m0, s51, 2048\n"
"s_add_u32 s72, s46, 4096\n"
"buffer_load_dwordx4 v11, s[36:39], s72 offen sc1 lds\n"
"s_add_u32 s46, s46, 393216\n"
"s_sub_u32 s50, 53248, s50\n"
"s_add_u32 s69, s69, 1\n"
"s_cmp_lg_u32 s73, 0\n"
"s_cbranch_scc1 S67_%=\n"
"s_mov_b32 s43, 940\n"
"s_memrealtime s[4:5]\n"
"s_nop 1\n"
"s_memrealtime s[6:7]\n"
"s_nop 1\n"
"s_memrealtime s[8:9]\n"
"s_nop 1\n"
"s_memrealtime s[10:11]\n"
"s_nop 1\n"
"s_memrealtime s[12:13]\n"
"s_nop 1\n"
"s_memrealtime s[14:15]\n"
"s_nop 1\n"
"s_memrealtime s[16:17]\n"
"s_nop 1\n"
"s_memrealtime s[18:19]\n"
"s_waitcnt lgkmcnt(0)\n"
"s_add_u32 s64, s4, s6\n"
"s_add_u32 s64, s64, s8\n"
"s_add_u32 s64, s64, s10\n"
"s_add_u32 s64, s64, s12\n"
"s_add_u32 s64, s64, s14\n"
"s_add_u32 s64, s64, s16\n"
"s_add_u32 s64, s64, s18\n"
"s_add_u32 s65, s64, s43\n"
"s_add_u32 s40, s20, 16384\n"
"s_xor_b32 s40, s40, 8192\n"
"v_lshlrev_b32 v18, 2, v0\n"
"s_lshl_b32 s77, s73, 14\n"
"s_add_u32 s77, s77, s20\n"
"s_add_u32 s77, s77, s21\n"
"v_mov_b32 v29, s65\n"
"v_lshrrev_b32 v30, 2, v0\n"
"s_mov_b64 exec, 0xffff\n"
"buffer_store_dword v29, v30, s[36:39], s77 offen sc1\n"
"s_mov_b64 exec, -1\n"
"s_add_u32 s76, s43, 60\n"
"v_mov_b32 v30, s76\n"
"v_mov_b32 v29, 0\n"
"ds_write_b32 v29, v30 offset:16896\n"
"S67_%=:\n"
"s_add_u32 s75, s73, 2\n"
"s_min_u32 s75, s75, 64\n"
"v_cmp_gt_u32 vcc, s75, v28\n"
"s_nop 0\n"
"s_cmp_eq_u64 vcc, 0\n"
"s_cselect_b32 s70, s46, 234881024\n"
"s_cselect_b32 s71, 393216, 0\n"
"s_cselect_b32 s72, 1, 0\n"
"s_cselect_b32 s75, 53248, 0\n"
"s_add_u32 m0, s50, s47\n"
"s_add_u32 s46, s46, s71\n"
"s_add_u32 s69, s69, s72\n"
"s_xor_b32 s50, s50, s75\n"
"s_add_u32 s71, s70, 1024\n"
"s_add_u32 s72, s70, 2048\n"
"s_lshl_b32 s44, s73, 14\n"
"s_add_u32 s44, s44, s20\n"
"s_add_u32 s41, s44, 4096\n"
"s_add_u32 s60, s68, 72704\n"
"s_add_u32 s61, s68, 103424\n"
"s_add_u32 s62, s68, 5120\n"
"s_mov_b32 s76, 100\n"
"S76_%=:\n"
"s_memrealtime s[4:5]\n"
"s_nop 1\n"
"s_memrealtime s[6:7]\n"
"s_nop 1\n"
"s_memrealtime s[8:9]\n"
"s_nop 1\n"
"s_memrealtime s[10:11]\n"
"s_nop 1\n"
"s_memrealtime s[12:13]\n"
"s_nop 1\n"
"s_memrealtime s[14:15]\n"
"s_nop 1\n"
"s_memrealtime s[16:17]\n"
"s_nop 1\n"
"s_memrealtime s[18:19]\n"
"s_waitcnt lgkmcnt(0)\n"
"s_add_u32 s80, s4, s6\n"
"s_add_u32 s80, s80, s8\n"
"s_add_u32 s80, s80, s10\n"
"s_add_u32 s80, s80, s12\n"
"s_add_u32 s80, s80, s14\n"
"s_add_u32 s80, s80, s16\n"
"s_add_u32 s80, s80, s18\n"
"s_sub_u32 s76, s76, 1\n"
"s_cselect_b32 s79, 0, -1\n"
"s_sub_u32 s78, s64, s80\n"
"s_and_b32 s78, s78, s79\n"
"s_cmp_gt_i32 s78, 80\n"
"s_cbranch_scc1 S76_%=\n"
"s_max_i32 s78, s78, 0\n"
"s_add_u32 s80, s80, s78\n"
"s_mul_i32 s78, s78, 3\n"
"S75_%=:\n"
"s_sub_u32 s78, s78, 28\n"
"s_cbranch_scc0 S75_%=\n"
"s_waitcnt lgkmcnt(0)\n"
"s_barrier\n"
"buffer_load_dwordx4 v[56:59], v0, s[36:39], s60 offen offset:0 sc1\n"
"s_nop 12\n"
"buffer_load_dwordx4 v[60:63], v0, s[36:39], s61 offen offset:0 sc1\n"
"s_nop 12\n"
"s_nop 8\n"
"buffer_load_dwordx4 v[64:67], v0, s[36:39], s62 offen offset:0 sc1\n"
"buffer_load_dword v30, v0, s[36:39], s38 offen\n"
"S74_%=:\n"
"s_waitcnt vmcnt(3)\n"
"v_smfmac_f32_16x16x64_bf16 v[32:35], v[56:59], v[96:103], v6\n"
"ds_write_b128 v0, v[56:59] offset:8192\n"
"v_smfmac_f32_16x16x64_bf16 v[44:47], v[56:59], v[72:79], v6\n"
"v_smfmac_f32_16x16x64_bf16 v[36:39], v[56:59], v[128:135], v6\n"
"v_smfmac_f32_16x16x64_bf16 v[48:51], v[56:59], v[80:87], v6\n"
"s_waitcnt vmcnt(2)\n"
"ds_write_b128 v0, v[60:63] offset:9216\n"
"v_smfmac_f32_16x16x64_bf16 v[40:43], v[56:59], v[136:143], v6\n"
"v_max_u32 v29, v59, v63\n"
"v_smfmac_f32_16x16x64_bf16 v[32:35], v[60:63], v[88:95], v6\n"
"v_smfmac_f32_16x16x64_bf16 v[44:47], v[60:63], v[144:151], v6\n"
"v_smfmac_f32_16x16x64_bf16 v[36:39], v[60:63], v[152:159], v6\n"
"s_waitcnt vmcnt(1)\n"
"v_max_u32 v29, v29, v67\n"
"v_smfmac_f32_16x16x64_bf16 v[48:51], v[60:63], v[104:111], v6\n"
"v_cmp_eq_u32 vcc, -1, v29\n"
"v_smfmac_f32_16x16x64_bf16 v[40:43], v[60:63], v[160:167], v6\n"
"s_cbranch_vccnz S68_%=\n"
"v_smfmac_f32_16x16x64_bf16 v[32:35], v[64:67], v[168:175], v6\n"
"s_waitcnt lgkmcnt(0)\n"
"s_barrier\n"
"ds_read_b128 v[68:71], v0 offset:1024\n"
"v_smfmac_f32_16x16x64_bf16 v[44:47], v[64:67], v[176:183], v6\n"
"ds_read_b128 v[72:75], v0 offset:2048\n"
"v_smfmac_f32_16x16x64_bf16 v[36:39], v[64:67], v[184:191], v6\n"
"ds_read_b128 v[76:79], v0 offset:3072\n"
"v_smfmac_f32_16x16x64_bf16 v[48:51], v[64:67], v[112:119], v6\n"
"v_smfmac_f32_16x16x64_bf16 v[40:43], v[64:67], v[192:199], v6\n"
"ds_read_b128 v[96:99], v15 offset:12288\n"
"s_waitcnt lgkmcnt(3)\n"
"v_smfmac_f32_16x16x64_bf16 v[32:35], v[68:71], v[200:207], v6\n"
"ds_read_b128 v[80:83], v0 offset:4096\n"
"v_smfmac_f32_16x16x64_bf16 v[36:39], v[68:71], v[208:215], v6\n"
"ds_read_b128 v[100:103], v15 offset:13312\n"
"s_waitcnt lgkmcnt(4)\n"
"v_smfmac_f32_16x16x64_bf16 v[32:35], v[72:75], v[216:223], v6\n"
"ds_read_b128 v[84:87], v0 offset:5120\n"
"v_smfmac_f32_16x16x64_bf16 v[36:39], v[72:75], v[224:231], v6\n"
"ds_read_b128 v[104:107], v15 offset:14336\n"
"s_waitcnt lgkmcnt(5)\n"
"v_smfmac_f32_16x16x64_bf16 v[32:35], v[76:79], v[232:239], v6\n"
"ds_read_b128 v[88:91], v0 offset:6144\n"
"v_smfmac_f32_16x16x64_bf16 v[36:39], v[76:79], v[240:247], v6\n"
"ds_read_b128 v[108:111], v15 offset:15360\n"
"s_waitcnt lgkmcnt(5)\n"
"v_smfmac_f32_16x16x64_bf16 v[32:35], v[80:83], v[248:255], v6\n"
"ds_read_b128 v[92:95], v0 offset:7168\n"
"v_smfmac_f32_16x16x64_bf16 v[36:39], v[80:83], a[0:7], v6\n"
"ds_read_b128 v[112:115], v15 offset:16384\n"
"s_waitcnt lgkmcnt(5)\n"
"v_smfmac_f32_16x16x64_bf16 v[32:35], v[84:87], v[96:103], v6\n"
"v_smfmac_f32_16x16x64_bf16 v[36:39], v[84:87], a[8:15], v6\n"
"ds_read_b128 v[116:119], v15 offset:17408\n"
"s_waitcnt lgkmcnt(4)\n"
"v_smfmac_f32_16x16x64_bf16 v[32:35], v[88:91], a[16:23], v6\n"
"v_smfmac_f32_16x16x64_bf16 v[36:39], v[88:91], a[24:31], v6\n"
"ds_read_b128 v[96:99], v15 offset:18432\n"
"s_waitcnt lgkmcnt(3)\n"
"v_smfmac_f32_16x16x64_bf16 v[32:35], v[92:95], v[104:111], v6\n"
"ds_read_b128 v[100:103], v15 offset:19456\n"
"v_smfmac_f32_16x16x64_bf16 v[36:39], v[92:95], a[32:39], v6\n"
"v_smfmac_f32_16x16x64_bf16 v[40:43], v[68:71], a[40:47], v6\n"
"ds_read_b128 v[104:107], v15 offset:20480\n"
"v_smfmac_f32_16x16x64_bf16 v[40:43], v[72:75], a[48:55], v6\n"
"ds_read_b128 v[108:111], v15 offset:21504\n"
"s_waitcnt lgkmcnt(4)\n"
"v_smfmac_f32_16x16x64_bf16 v[40:43], v[76:79], v[112:119], v6\n"
"v_add_f32 v32, v32, v34\n"
"v_smfmac_f32_16x16x64_bf16 v[40:43], v[80:83], a[56:63], v6\n"
"ds_read_b128 v[112:115], v15 offset:22528\n"
"v_add_f32 v33, v33, v35\n"
"v_smfmac_f32_16x16x64_bf16 v[40:43], v[84:87], a[64:71], v6\n"
"v_cvt_pk_bf16_f32 v32, v32, v33\n"
"v_smfmac_f32_16x16x64_bf16 v[40:43], v[88:91], a[72:79], v6\n"
"ds_read_b128 v[116:119], v15 offset:23552\n"
"v_add_f32 v36, v36, v38\n"
"s_waitcnt lgkmcnt(4)\n"
"v_smfmac_f32_16x16x64_bf16 v[40:43], v[92:95], v[96:103], v6\n"
"ds_write_b32 v7, v32 offset:640\n"
"v_add_f32 v37, v37, v39\n"
"v_smfmac_f32_16x16x64_bf16 v[44:47], v[68:71], a[80:87], v6\n"
"ds_read_b128 v[96:99], v19 offset:0\n"
"v_cvt_pk_bf16_f32 v36, v36, v37\n"
"v_smfmac_f32_16x16x64_bf16 v[44:47], v[72:75], a[88:95], v6\n"
"ds_read_b128 v[100:103], v19 offset:1024\n"
"s_waitcnt lgkmcnt(5)\n"
"v_smfmac_f32_16x16x64_bf16 v[44:47], v[76:79], v[104:111], v6\n"
"ds_write_b32 v7, v36 offset:1664\n"
"v_smfmac_f32_16x16x64_bf16 v[44:47], v[80:83], a[96:103], v6\n"
"ds_read_b128 v[104:107], v19 offset:6144\n"
"v_add_f32 v40, v40, v42\n"
"v_smfmac_f32_16x16x64_bf16 v[44:47], v[84:87], a[104:111], v6\n"
"ds_read_b128 v[108:111], v19 offset:7168\n"
"v_add_f32 v41, v41, v43\n"
"s_waitcnt lgkmcnt(6)\n"
"v_smfmac_f32_16x16x64_bf16 v[44:47], v[88:91], v[112:119], v6\n"
"v_cvt_pk_bf16_f32 v40, v40, v41\n"
"v_smfmac_f32_16x16x64_bf16 v[44:47], v[92:95], a[112:119], v6\n"
"ds_write_b32 v7, v40 offset:2688\n"
"v_smfmac_f32_16x16x64_bf16 v[48:51], v[68:71], a[120:127], v6\n"
"ds_read_b128 v[112:115], v19 offset:2048\n"
"v_smfmac_f32_16x16x64_bf16 v[48:51], v[72:75], a[128:135], v6\n"
"ds_read_b128 v[116:119], v19 offset:3072\n"
"v_smfmac_f32_16x16x64_bf16 v[48:51], v[76:79], a[136:143], v6\n"
"buffer_load_dwordx4 v11, s[36:39], s70 offen offset:0 sc1 lds\n"
"s_nop 0\n"
"v_add_f32 v44, v44, v46\n"
"v_smfmac_f32_16x16x64_bf16 v[48:51], v[80:83], a[144:151], v6\n"
"s_waitcnt lgkmcnt(0)\n"
"s_barrier\n"
"ds_read_b64_tr_b16 v[32:33], v8 offset:0\n"
"v_add_f32 v45, v45, v47\n"
"v_smfmac_f32_16x16x64_bf16 v[48:51], v[84:87], a[152:159], v6\n"
"ds_read_b64_tr_b16 v[34:35], v8 offset:256\n"
"ds_read_b64_tr_b16 v[36:37], v8 offset:1024\n"
"v_smfmac_f32_16x16x64_bf16 v[48:51], v[88:91], a[160:167], v6\n"
"ds_read_b64_tr_b16 v[38:39], v8 offset:1280\n"
"v_cvt_pk_bf16_f32 v44, v44, v45\n"
"v_smfmac_f32_16x16x64_bf16 v[48:51], v[92:95], a[168:175], v6\n"
"buffer_load_dwordx4 v11, s[36:39], s71 offen offset:1024 sc1 lds\n"
"ds_write_b32 v7, v44 offset:3712\n"
"v_smfmac_f32_16x16x64_bf16 v[52:55], v[56:59], a[176:183], v6\n"
"ds_read_b64_tr_b16 v[40:41], v8 offset:2048\n"
"ds_read_b64_tr_b16 v[42:43], v8 offset:2304\n"
"v_smfmac_f32_16x16x64_bf16 v[52:55], v[60:63], a[184:191], v6\n"
"ds_read_b128 v[56:59], v19 offset:8192\n"
"v_smfmac_f32_16x16x64_bf16 v[52:55], v[64:67], a[192:199], v6\n"
"ds_read_b128 v[60:63], v19 offset:9216\n"
"v_add_f32 v48, v48, v50\n"
"v_add_f32 v49, v49, v51\n"
"v_smfmac_f32_16x16x64_bf16 v[52:55], v[68:71], a[200:207], v6\n"
"buffer_load_dwordx4 v11, s[36:39], s72 offen offset:2048 sc1 lds\n"
"ds_read_b128 v[64:67], v19 offset:4096\n"
"v_cvt_pk_bf16_f32 v48, v48, v49\n"
"v_smfmac_f32_16x16x64_bf16 v[52:55], v[72:75], a[208:215], v6\n"
"ds_write_b32 v7, v48 offset:4736\n"
"v_smfmac_f32_16x16x64_bf16 v[52:55], v[76:79], a[216:223], v6\n"
"ds_read_b128 v[68:71], v19 offset:5120\n"
"v_smfmac_f32_16x16x64_bf16 v[52:55], v[80:83], a[224:231], v6\n"
"ds_read_b128 v[72:75], v19 offset:10240\n"
"v_smfmac_f32_16x16x64_bf16 v[52:55], v[84:87], a[232:239], v6\n"
"ds_read_b128 v[76:79], v19 offset:11264\n"
"v_smfmac_f32_16x16x64_bf16 v[52:55], v[88:91], a[240:247], v6\n"
"s_waitcnt lgkmcnt(3)\n"
"s_barrier\n"
"ds_read_b64_tr_b16 v[80:81], v8 offset:3072\n"
"ds_read_b64_tr_b16 v[82:83], v8 offset:3328\n"
"v_smfmac_f32_16x16x64_bf16 v[52:55], v[92:95], a[248:255], v6\n"
"ds_read_b64_tr_b16 v[84:85], v8 offset:4096\n"
"ds_read_b64_tr_b16 v[86:87], v8 offset:4352\n"
"v_smfmac_f32_16x16x64_bf16 v[120:123], v[32:35], v[96:103], v6\n"
"v_smfmac_f32_16x16x64_bf16 v[124:127], v[32:35], v[104:111], v6\n"
"s_nop 3\n"
"v_add_f32 v52, v52, v54\n"
"v_add_f32 v53, v53, v55\n"
"v_cvt_pk_bf16_f32 v52, v52, v53\n"
"ds_write_b32 v7, v52 offset:5760\n"
"v_smfmac_f32_16x16x64_bf16 v[120:123], v[36:39], v[112:119], v6\n"
"v_smfmac_f32_16x16x64_bf16 v[124:127], v[36:39], v[56:63], v6\n"
"s_waitcnt lgkmcnt(7)\n"
"v_smfmac_f32_16x16x64_bf16 v[120:123], v[40:43], v[64:71], v6\n"
"s_waitcnt lgkmcnt(5)\n"
"v_smfmac_f32_16x16x64_bf16 v[124:127], v[40:43], v[72:79], v6\n"
"s_mov_b64 vcc, s[82:83]\n"
"s_waitcnt lgkmcnt(0)\n"
"s_barrier\n"
"ds_read_b64_tr_b16 v[88:89], v8 offset:5120\n"
"ds_read_b64_tr_b16 v[90:91], v8 offset:5376\n"
"v_mfma_f32_16x16x32_bf16 v[20:23], v[32:35], v[80:83], 0\n"
"v_add_f32 v120, v120, v122\n"
"v_add_f32 v121, v121, v123\n"
"v_mfma_f32_16x16x32_bf16 v[20:23], v[36:39], v[84:87], v[20:23]\n"
"v_add_f32 v124, v124, v126\n"
"v_add_f32 v125, v125, v127\n"
"v_cndmask_b32_dpp v97, v120, v121, vcc quad_perm:[2,3,2,3] row_mask:0xf bank_mask:0xf\n"
"s_nop 0\n"
"v_cndmask_b32_dpp v99, v124, v125, vcc quad_perm:[2,3,2,3] row_mask:0xf bank_mask:0xf\n"
"s_waitcnt lgkmcnt(0)\n"
"v_mfma_f32_16x16x32_bf16 v[20:23], v[40:43], v[88:91], v[20:23]\n"
"s_not_b64 vcc, s[82:83]\n"
"v_cndmask_b32_dpp v96, v121, v120, vcc quad_perm:[0,1,0,1] row_mask:0xf bank_mask:0xf\n"
"v_cndmask_b32_dpp v98, v125, v124, vcc quad_perm:[0,1,0,1] row_mask:0xf bank_mask:0xf\n"
"v_max_f32 v24, v97, v99\n"
"v_max3_f32 v24, v24, v96, v98\n"
"s_nop 1\n"
"v_max_f32_dpp v24, v24, v24 quad_perm:[1,0,3,2] row_mask:0xf bank_mask:0xf\n"
"v_add_f32_dpp v3, v22, v20 quad_perm:[2,3,2,3] row_mask:0xf bank_mask:0xf\n"
"v_add_f32_dpp v4, v21, v23 quad_perm:[0,1,0,1] row_mask:0xf bank_mask:0xf\n"
"v_max_f32_dpp v24, v24, v24 row_ror:4 row_mask:0xf bank_mask:0xf\n"
"v_cndmask_b32 v100, v4, v3, vcc\n"
"s_nop 0\n"
"v_max_f32_dpp v24, v24, v24 row_ror:8 row_mask:0xf bank_mask:0xf\n"
"v_mul_f32 v25, s74, v24\n"
"S70_%=:\n"
"v_fma_f32 v102, v96, s74, -v25\n"
"v_fma_f32 v103, v97, s74, -v25\n"
"v_fma_f32 v104, v98, s74, -v25\n"
"v_fma_f32 v105, v99, s74, -v25\n"
"v_fma_f32 v3, v100, s74, -v25\n"
"v_exp_f32 v102, v102\n"
"v_exp_f32 v103, v103\n"
"v_exp_f32 v104, v104\n"
"v_exp_f32 v105, v105\n"
"v_exp_f32 v5, v3\n"
"v_add_f32 v26, v102, v103\n"
"v_add_f32 v4, v104, v105\n"
"v_add_f32 v26, v26, v5\n"
"v_add_f32 v26, v26, v4\n"
"v_cmp_nge_f32 vcc, s81, v3\n"
"v_mov_b32_dpp v4, v5 quad_perm:[1,1,3,3] row_mask:0xf bank_mask:0xf\n"
"v_add_f32_dpp v26, v26, v26 quad_perm:[1,0,3,2] row_mask:0xf bank_mask:0xf\n"
"s_nop 1\n"
"v_add_f32_dpp v26, v26, v26 row_ror:4 row_mask:0xf bank_mask:0xf\n"
"s_cbranch_vccnz S69_%=\n"
"S69ON_%=:\n"
"s_nop 0\n"
"v_add_f32_dpp v26, v26, v26 row_ror:8 row_mask:0xf bank_mask:0xf\n"
"v_rcp_f32 v26, v26\n"
"s_nop 0\n"
"v_mul_f32 v5, v5, v26\n"
"v_mul_f32 v4, v4, v26\n"
"v_cvt_pk_bf16_f32 v2, v5, v4\n"
"v_min_u32 v2, -2, v2\n"
"buffer_store_dword v2, v10, s[36:39], s49 offen sc1\n"
"s_memrealtime s[4:5]\n"
"s_nop 1\n"
"s_memrealtime s[6:7]\n"
"s_nop 1\n"
"s_memrealtime s[8:9]\n"
"s_nop 1\n"
"s_memrealtime s[10:11]\n"
"s_nop 1\n"
"s_memrealtime s[12:13]\n"
"s_nop 1\n"
"s_memrealtime s[14:15]\n"
"s_nop 1\n"
"s_memrealtime s[16:17]\n"
"s_nop 1\n"
"s_memrealtime s[18:19]\n"
"buffer_load_dword v9, v18, s[36:39], s44 offen sc1\n"
"buffer_load_dword v17, v18, s[36:39], s41 offen sc1\n"
"buffer_load_dword v27, v18, s[36:39], s40 offen sc1\n"
"s_waitcnt lgkmcnt(0)\n"
"s_add_u32 s42, s4, s6\n"
"s_add_u32 s42, s42, s8\n"
"s_add_u32 s42, s42, s10\n"
"s_add_u32 s42, s42, s12\n"
"s_add_u32 s42, s42, s14\n"
"s_add_u32 s42, s42, s16\n"
"s_add_u32 s42, s42, s18\n"
"s_nop 0\n"
"s_add_u32 s75, s73, 3\n"
"s_min_u32 s75, s75, 64\n"
"v_cmp_gt_u32 vcc, s75, v28\n"
"s_nop 0\n"
"s_mov_b64 exec, vcc\n"
"buffer_load_dword v28, v12, s[36:39], 0 offen sc1\n"
"s_mov_b64 exec, -1\n"
"s_add_u32 s48, s48, 98304\n"
"s_add_u32 s73, s73, 1\n"
"s_add_u32 s79, s68, 131072\n"
"s_and_b32 s79, s79, 0x3fffff\n"
"s_xor_b32 s68, s79, s68\n"
"s_lshr_b32 s68, s68, 8\n"
"s_and_b32 s68, s68, 8192\n"
"s_xor_b32 s68, s68, s79\n"
"v_xor_b32 v19, 53248, v19\n"
"s_cmp_lt_u32 s73, 64\n"
"s_cbranch_scc0 S65_%=\n"
"ds_read_b128 v[96:99], v15 offset:0\n"
"ds_read_b128 v[100:103], v15 offset:1024\n"
"ds_read_b128 v[72:75], v15 offset:2048\n"
"ds_read_b128 v[76:79], v15 offset:3072\n"
"ds_read_b128 v[80:83], v15 offset:4096\n"
"ds_read_b128 v[84:87], v15 offset:5120\n"
"ds_read_b128 v[88:91], v15 offset:6144\n"
"ds_read_b128 v[92:95], v15 offset:7168\n"
"ds_read_b128 v[104:107], v15 offset:8192\n"
"ds_read_b128 v[108:111], v15 offset:9216\n"
"ds_read_b128 v[112:115], v15 offset:10240\n"
"ds_read_b128 v[116:119], v15 offset:11264\n"
"v_mov_b32 v32, 0\n"
"v_mov_b32 v33, 0\n"
"v_mov_b32 v34, 0\n"
"v_mov_b32 v35, 0\n"
"v_mov_b32 v36, 0\n"
"v_mov_b32 v37, 0\n"
"v_mov_b32 v38, 0\n"
"v_mov_b32 v39, 0\n"
"v_mov_b32 v40, 0\n"
"v_mov_b32 v41, 0\n"
"v_mov_b32 v42, 0\n"
"v_mov_b32 v43, 0\n"
"v_mov_b32 v44, 0\n"
"v_mov_b32 v45, 0\n"
"v_mov_b32 v46, 0\n"
"v_mov_b32 v47, 0\n"
"v_mov_b32 v48, 0\n"
"v_mov_b32 v49, 0\n"
"v_mov_b32 v50, 0\n"
"v_mov_b32 v51, 0\n"
"v_mov_b32 v52, 0\n"
"v_mov_b32 v53, 0\n"
"v_mov_b32 v54, 0\n"
"v_mov_b32 v55, 0\n"
"v_mov_b32 v120, 0\n"
"v_mov_b32 v121, 0\n"
"v_mov_b32 v122, 0\n"
"v_mov_b32 v123, 0\n"
"v_mov_b32 v124, 0\n"
"v_mov_b32 v125, 0\n"
"v_mov_b32 v126, 0\n"
"v_mov_b32 v127, 0\n"
"s_waitcnt vmcnt(1)\n"
"s_xor_b32 s40, s44, 8192\n"
"v_cmp_eq_u32 vcc, -1, v17\n"
"s_nop 1\n"
"v_cndmask_b32 v9, v17, v9, vcc\n"
"s_bfe_i32 s75, s63, 524312\n"
"s_lshl_b32 s75, s75, 2\n"
"s_add_u32 s42, s42, s75\n"
"v_cmp_eq_u32 vcc, -1, v9\n"
"v_subrev_u32 v9, s42, v9\n"
"s_mov_b64 s[70:71], vcc\n"
"v_cmp_eq_u32 vcc, -1, v27\n"
"v_max_i32_dpp v9, v9, v9 row_ror:1 row_mask:0xf bank_mask:0xf\n"
"v_max_i32_dpp v27, v27, v27 row_ror:1 row_mask:0xf bank_mask:0xf\n"
"s_nop 0\n"
"v_max_i32_dpp v9, v9, v9 row_ror:2 row_mask:0xf bank_mask:0xf\n"
"v_max_i32_dpp v27, v27, v27 row_ror:2 row_mask:0xf bank_mask:0xf\n"
"s_nop 0\n"
"v_max_i32_dpp v9, v9, v9 row_ror:4 row_mask:0xf bank_mask:0xf\n"
"v_max_i32_dpp v27, v27, v27 row_ror:4 row_mask:0xf bank_mask:0xf\n"
"s_nop 0\n"
"v_max_i32_dpp v9, v9, v9 row_ror:8 row_mask:0xf bank_mask:0xf\n"
"v_max_i32_dpp v27, v27, v27 row_ror:8 row_mask:0xf bank_mask:0xf\n"
"v_readlane_b32 s4, v9, 0\n"
"v_readlane_b32 s5, v9, 16\n"
"v_readlane_b32 s6, v9, 32\n"
"v_readlane_b32 s7, v9, 48\n"
"v_readlane_b32 s8, v27, 0\n"
"v_readlane_b32 s9, v27, 16\n"
"v_readlane_b32 s10, v27, 32\n"
"v_readlane_b32 s11, v27, 48\n"
"s_max_i32 s4, s4, s5\n"
"s_max_i32 s6, s6, s7\n"
"s_max_i32 s64, s4, s6\n"
"s_max_i32 s8, s8, s9\n"
"s_max_i32 s10, s10, s11\n"
"s_max_i32 s65, s8, s10\n"
"s_and_b32 vcc_hi, vcc_hi, 255\n"
"s_cmp_eq_u64 vcc, 0\n"
"s_cselect_b32 s65, s65, 430\n"
"s_min_u32 s65, s65, 1600\n"
"s_cmp_eq_u64 s[70:71], 0\n"
"s_cselect_b32 s64, s64, -800\n"
"s_min_i32 s64, s64, 3200\n"
"s_bfe_i32 s75, s63, 524304\n"
"s_lshl_b32 s75, s75, 2\n"
"s_lshl_b32 s75, s75, 2\n"
"s_bfe_i32 s76, s63, 524288\n"
"s_lshl_b32 s76, s76, 2\n"
"s_add_u32 s76, s76, s64\n"
"s_cmp_lt_i32 s76, -56\n"
"s_cselect_b32 s64, s75, s64\n"
"s_add_u32 s64, s64, s65\n"
"s_bfe_i32 s75, s63, 524288\n"
"s_lshl_b32 s75, s75, 2\n"
"s_add_u32 s64, s64, s75\n"
"s_bfe_i32 s75, s63, 524296\n"
"s_lshl_b32 s75, s75, 2\n"
"s_add_u32 s64, s64, s75\n"
"s_add_u32 s64, s64, s42\n"
"s_sub_u32 s65, s42, s80\n"
"s_sub_u32 s75, s65, s43\n"
"s_min_i32 s75, s75, 48\n"
"s_cmp_lt_i32 s75, -64\n"
"s_cselect_b32 s76, 0, 2\n"
"s_ashr_i32 s75, s75, s76\n"
"s_add_u32 s43, s43, s75\n"
"s_add_u32 s65, s43, s64\n"
"s_lshl_b32 s77, s73, 14\n"
"s_add_u32 s77, s77, s20\n"
"s_add_u32 s77, s77, s21\n"
"v_mov_b32 v29, s65\n"
"v_lshrrev_b32 v30, 2, v0\n"
"s_mov_b64 exec, 0xffff\n"
"buffer_store_dword v29, v30, s[36:39], s77 offen sc1\n"
"s_mov_b64 exec, -1\n"
"s_add_u32 s76, s43, 60\n"
"v_mov_b32 v30, s76\n"
"v_mov_b32 v29, 0\n"
"ds_write_b32 v29, v30 offset:16896\n"
"s_waitcnt vmcnt(1)\n"
"s_branch S66_%=\n"
"S68_%=:\n"
"s_waitcnt lgkmcnt(0)\n"
"s_mov_b32 s76, 131072\n"
"S77_%=:\n"
"s_memrealtime s[4:5]\n"
"buffer_load_dwordx4 v[56:59], v0, s[36:39], s60 offen offset:0 sc1\n"
"buffer_load_dwordx4 v[60:63], v0, s[36:39], s61 offen offset:0 sc1\n"
"buffer_load_dwordx4 v[64:67], v0, s[36:39], s62 offen offset:0 sc1\n"
"v_mov_b32 v29, 0\n"
"ds_read_b32 v31, v29 offset:16896\n"
"s_lshl_b32 s77, s73, 14\n"
"s_add_u32 s77, s77, s20\n"
"s_add_u32 s77, s77, s21\n"
"s_add_u32 s77, s77, 4096\n"
"v_mov_b32 v32, 0\n"
"v_mov_b32 v33, 0\n"
"v_mov_b32 v34, 0\n"
"v_mov_b32 v35, 0\n"
"v_mov_b32 v36, 0\n"
"v_mov_b32 v37, 0\n"
"v_mov_b32 v38, 0\n"
"v_mov_b32 v39, 0\n"
"v_mov_b32 v40, 0\n"
"v_mov_b32 v41, 0\n"
"v_mov_b32 v42, 0\n"
"v_mov_b32 v43, 0\n"
"v_mov_b32 v44, 0\n"
"v_mov_b32 v45, 0\n"
"v_mov_b32 v46, 0\n"
"v_mov_b32 v47, 0\n"
"v_mov_b32 v48, 0\n"
"v_mov_b32 v49, 0\n"
"v_mov_b32 v50, 0\n"
"v_mov_b32 v51, 0\n"
"v_mov_b32 v52, 0\n"
"v_mov_b32 v53, 0\n"
"v_mov_b32 v54, 0\n"
"v_mov_b32 v55, 0\n"
"s_sub_u32 s76, s76, 1\n"
"s_cbranch_scc1 SABORT_%=\n"
"s_waitcnt vmcnt(2)\n"
"v_max3_u32 v29, v56, v57, 0\n"
"v_max3_u32 v29, v58, v59, v29\n"
"s_waitcnt vmcnt(1)\n"
"v_max3_u32 v29, v60, v61, v29\n"
"v_max3_u32 v29, v62, v63, v29\n"
"s_waitcnt vmcnt(0)\n"
"v_max3_u32 v29, v64, v65, v29\n"
"v_max3_u32 v29, v66, v67, v29\n"
"v_cmp_eq_u32 vcc, -1, v29\n"
"s_nop 0\n"
"s_cbranch_vccnz S77_%=\n"
"s_waitcnt lgkmcnt(0)\n"
"v_lshl_add_u32 v31, s4, 3, v31\n"
"v_lshrrev_b32 v30, 2, v0\n"
"s_mov_b64 exec, 0xffff\n"
"buffer_store_dword v31, v30, s[36:39], s77 offen sc1\n"
"s_mov_b64 exec, -1\n"
"s_branch S74_%=\n"
"S69_%=:\n"
"s_cmp_lg_u32 s45, 0\n"
"s_cbranch_scc1 S78_%=\n"
"v_mov_b32 v3, 0\n"
"v_and_b32 v4, 0x7fff, v80\n"
"v_max_u32 v3, v3, v4\n"
"v_bfe_u32 v4, v80, 16, 15\n"
"v_max_u32 v3, v3, v4\n"
"v_and_b32 v4, 0x7fff, v81\n"
"v_max_u32 v3, v3, v4\n"
"v_bfe_u32 v4, v81, 16, 15\n"
"v_max_u32 v3, v3, v4\n"
"v_and_b32 v4, 0x7fff, v82\n"
"v_max_u32 v3, v3, v4\n"
"v_bfe_u32 v4, v82, 16, 15\n"
"v_max_u32 v3, v3, v4\n"
"v_and_b32 v4, 0x7fff, v83\n"
"v_max_u32 v3, v3, v4\n"
"v_bfe_u32 v4, v83, 16, 15\n"
"v_max_u32 v3, v3, v4\n"
"v_and_b32 v4, 0x7fff, v84\n"
"v_max_u32 v3, v3, v4\n"
"v_bfe_u32 v4, v84, 16, 15\n"
"v_max_u32 v3, v3, v4\n"
"v_and_b32 v4, 0x7fff, v85\n"
"v_max_u32 v3, v3, v4\n"
"v_bfe_u32 v4, v85, 16, 15\n"
"v_max_u32 v3, v3, v4\n"
"v_and_b32 v4, 0x7fff, v86\n"
"v_max_u32 v3, v3, v4\n"
"v_bfe_u32 v4, v86, 16, 15\n"
"v_max_u32 v3, v3, v4\n"
"v_and_b32 v4, 0x7fff, v87\n"
"v_max_u32 v3, v3, v4\n"
"v_bfe_u32 v4, v87, 16, 15\n"
"v_max_u32 v3, v3, v4\n"
"v_and_b32 v4, 0x7fff, v88\n"
"v_max_u32 v3, v3, v4\n"
"v_bfe_u32 v4, v88, 16, 15\n"
"v_max_u32 v3, v3, v4\n"
"v_and_b32 v4, 0x7fff, v89\n"
"v_max_u32 v3, v3, v4\n"
"v_bfe_u32 v4, v89, 16, 15\n"
"v_max_u32 v3, v3, v4\n"
"v_and_b32 v4, 0x7fff, v90\n"
"v_max_u32 v3, v3, v4\n"
"v_bfe_u32 v4, v90, 16, 15\n"
"v_max_u32 v3, v3, v4\n"
"v_and_b32 v4, 0x7fff, v91\n"
"v_max_u32 v3, v3, v4\n"
"v_bfe_u32 v4, v91, 16, 15\n"
"v_max_u32 v3, v3, v4\n"
"v_cmp_lt_u32 vcc, 0x7f80, v3\n"
"s_nop 0\n"
"s_cbranch_vccz S78_%=\n"
"s_mov_b32 s45, 1\n"
"ds_read_b128 v[96:99], v15 offset:0\n"
"ds_read_b128 v[100:103], v15 offset:1024\n"
"ds_read_b128 v[72:75], v15 offset:2048\n"
"ds_read_b128 v[76:79], v15 offset:3072\n"
"ds_read_b128 v[80:83], v15 offset:4096\n"
"ds_read_b128 v[84:87], v15 offset:5120\n"
"ds_read_b128 v[88:91], v15 offset:6144\n"
"ds_read_b128 v[92:95], v15 offset:7168\n"
"ds_read_b128 v[104:107], v15 offset:8192\n"
"ds_read_b128 v[108:111], v15 offset:9216\n"
"ds_read_b128 v[112:115], v15 offset:10240\n"
"ds_read_b128 v[116:119], v15 offset:11264\n"
"v_mov_b32 v32, 0\n"
"v_mov_b32 v33, 0\n"
"v_mov_b32 v34, 0\n"
"v_mov_b32 v35, 0\n"
"v_mov_b32 v36, 0\n"
"v_mov_b32 v37, 0\n"
"v_mov_b32 v38, 0\n"
"v_mov_b32 v39, 0\n"
"v_mov_b32 v40, 0\n"
"v_mov_b32 v41, 0\n"
"v_mov_b32 v42, 0\n"
"v_mov_b32 v43, 0\n"
"v_mov_b32 v44, 0\n"
"v_mov_b32 v45, 0\n"
"v_mov_b32 v46, 0\n"
"v_mov_b32 v47, 0\n"
"v_mov_b32 v48, 0\n"
"v_mov_b32 v49, 0\n"
"v_mov_b32 v50, 0\n"
"v_mov_b32 v51, 0\n"
"v_mov_b32 v52, 0\n"
"v_mov_b32 v53, 0\n"
"v_mov_b32 v54, 0\n"
"v_mov_b32 v55, 0\n"
"v_mov_b32 v120, 0\n"
"v_mov_b32 v121, 0\n"
"v_mov_b32 v122, 0\n"
"v_mov_b32 v123, 0\n"
"v_mov_b32 v124, 0\n"
"v_mov_b32 v125, 0\n"
"v_mov_b32 v126, 0\n"
"v_mov_b32 v127, 0\n"
"s_waitcnt lgkmcnt(0)\n"
"s_barrier\n"
"s_branch S68_%=\n"
"S78_%=:\n"
"s_cmp_eq_u32 s45, 2\n"
"s_cbranch_scc1 S79_%=\n"
"s_mov_b32 s45, 2\n"
"v_max_f32 v24, v24, v100\n"
"s_nop 1\n"
"v_max_f32_dpp v24, v24, v24 quad_perm:[1,0,3,2] row_mask:0xf bank_mask:0xf\n"
"s_nop 1\n"
"v_max_f32_dpp v24, v24, v24 row_ror:4 row_mask:0xf bank_mask:0xf\n"
"s_nop 1\n"
"v_max_f32_dpp v24, v24, v24 row_ror:8 row_mask:0xf bank_mask:0xf\n"
"v_mul_f32 v25, s74, v24\n"
"s_branch S70_%=\n"
"S79_%=:\n"
"s_branch S69ON_%=\n"
"S65_%=:\n"
"s_waitcnt vmcnt(0)\n"
"s_barrier\n"
"s_branch S5_%=\n"
"S5_%=:\n"
"s_endpgm\n"
"SABORT_%=:\n"
"s_mov_b64 exec, -1\n"
"s_nop 7\n"
"s_nop 7\n"
"v_mov_b32 v0, 46268544\n"
"v_mov_b32 v1, 1\n"
"buffer_store_dword v1, v0, s[36:39], 0 offen sc1\n"
"s_endpgm\n");
}

DI void role_v(const Ctx& c, int h) {
    SETUP
    setsrd(sa, 4, P->Wv, DM * NH * HD * 2); setsrd(sa, 8, P->Wo, NH * HD * DM * 2);
    sa[12] = w * 32 * (NH * HD * 2) + h * HD * 2; sa[13] = O_COMM + (c.par << 13) + (RHO << 14); sa[14] = w * 4096;
    sa[15] = (h * HD + 96 * (w & 1)) * (DM * 2) + (w >> 1) * 128; sb[0] = w * 12288; sb[1] = O_U + h * 256; sb[2] = c.par << 13; sb[11] = P->tune; sb[12] = (32 + h) * 64;
    va[1] = r8 * (NH * HD * 2) + ((c8 ^ ((r8 >> 2) << 1)) * 16); va[2] = va[1] ^ 64;
    const int row0 = 16 * (g >> 1) + 8 * (q >> 1) + 4 * (g & 1) + (q & 1), key = (row0 >> 2) & 3;
#pragma unroll
    for (int t = 0; t < 3; ++t) { const int T = 3 * w + t; va[3 + t] = (T >> 2) * 16384 + row0 * 128 + (((2 * (T & 3) + (p >> 1)) ^ (key << 1)) * 16) + (p & 1) * 8; }
    va[6] = (lane & 2) ? 0xEEEE : 0x4444;
    va[7] = r8 * (DM * 2) + ((c8 ^ (((r8 >> 1) & 3) << 1)) * 16);
#pragma unroll
    for (int o = 0; o < 2; ++o) { const int row = 8 * (g >> 1) + 4 * (g & 1) + 2 * o + (q & 1), k2 = (row >> 1) & 3;
        va[8 + o] = (w >> 1) * 24576 + row * 128 + (((4 * (w & 1) + 2 * (q >> 1) + (p >> 1)) ^ (k2 << 1)) * 16) + (p & 1) * 8; }
    va[10] = L_QK + (g >> 1) * 1568 + (16 * w + n) * 8 + (g & 1) * 4; va[11] = (lane & 8) ? 0xEEEE : 0x4444;
    va[12] = L_QK + (p & 1) * 1568 + (8 * g + q) * 8; va[13] = one(lane, F_U + h * 128);
    va[15] = L_RES + w * 24576 + lane * 16;
    vb[0] = (2 * w + (g >> 1)) * 16384 + n * 16 + (g & 1) * 8;
    RUN("s_nop 4\n"
"s_mov_b32 s68, s49\n"
"s_mov_b32 s73, 0\n"
"s_mov_b32 s69, 0\n"
"s_mov_b32 s93, 0\n"
"s_mov_b32 s21, s64\n"
"s_and_b32 s20, s49, 8192\n"
"s_xor_b32 s20, s20, 0\n"
"s_add_u32 s20, s20, 100663296\n"
"s_cmp_eq_u32 s66, 1\n"
"s_cbranch_scc1 V2_%=\n"
"s_cmp_eq_u32 s66, 2\n"
"s_cbranch_scc1 V3_%=\n"
"s_cmp_eq_u32 s66, 3\n"
"s_cbranch_scc1 V4_%=\n"
"V1_%=:\n"
"s_mov_b32 s22, 46301184\n"
"s_mov_b32 s23, 46317568\n"
"s_cmp_lg_u32 s66, 0\n"
"s_cbranch_scc1 V6_%=\n"
"v_mov_b32 v24, 0\n"
"s_mov_b64 exec, 1\n"
"buffer_atomic_add v24, v26, s[36:39], s23 offen sc0 sc1\n"
"s_mov_b64 exec, -1\n"
"V6_%=:\n"
"s_add_u32 m0, s50, 0\n"
"s_add_u32 s72, s48, 0\n"
"buffer_load_dwordx4 v1, s[40:43], s72 offen lds\n"
"s_add_u32 m0, s50, 1024\n"
"s_add_u32 s72, s48, 98304\n"
"buffer_load_dwordx4 v2, s[40:43], s72 offen lds\n"
"s_add_u32 m0, s50, 2048\n"
"s_add_u32 s72, s48, 196608\n"
"buffer_load_dwordx4 v1, s[40:43], s72 offen lds\n"
"s_add_u32 m0, s50, 3072\n"
"s_add_u32 s72, s48, 294912\n"
"buffer_load_dwordx4 v2, s[40:43], s72 offen lds\n"
"s_add_u32 m0, s50, 16384\n"
"s_add_u32 s72, s48, 128\n"
"buffer_load_dwordx4 v1, s[40:43], s72 offen lds\n"
"s_add_u32 m0, s50, 17408\n"
"s_add_u32 s72, s48, 98432\n"
"buffer_load_dwordx4 v2, s[40:43], s72 offen lds\n"
"s_add_u32 m0, s50, 18432\n"
"s_add_u32 s72, s48, 196736\n"
"buffer_load_dwordx4 v1, s[40:43], s72 offen lds\n"
"s_add_u32 m0, s50, 19456\n"
"s_add_u32 s72, s48, 295040\n"
"buffer_load_dwordx4 v2, s[40:43], s72 offen lds\n"
"s_add_u32 m0, s50, 32768\n"
"s_add_u32 s72, s48, 256\n"
"buffer_load_dwordx4 v1, s[40:43], s72 offen lds\n"
"s_add_u32 m0, s50, 33792\n"
"s_add_u32 s72, s48, 98560\n"
"buffer_load_dwordx4 v2, s[40:43], s72 offen lds\n"
"s_add_u32 m0, s50, 34816\n"
"s_add_u32 s72, s48, 196864\n"
"buffer_load_dwordx4 v1, s[40:43], s72 offen lds\n"
"s_add_u32 m0, s50, 35840\n"
"s_add_u32 s72, s48, 295168\n"
"buffer_load_dwordx4 v2, s[40:43], s72 offen lds\n"
"s_add_u32 m0, s50, 49152\n"
"s_add_u32 s72, s48, 1572864\n"
"buffer_load_dwordx4 v1, s[40:43], s72 offen lds\n"
"s_add_u32 m0, s50, 50176\n"
"s_add_u32 s72, s48, 1671168\n"
"buffer_load_dwordx4 v2, s[40:43], s72 offen lds\n"
"s_add_u32 m0, s50, 51200\n"
"s_add_u32 s72, s48, 1769472\n"
"buffer_load_dwordx4 v1, s[40:43], s72 offen lds\n"
"s_add_u32 m0, s50, 52224\n"
"s_add_u32 s72, s48, 1867776\n"
"buffer_load_dwordx4 v2, s[40:43], s72 offen lds\n"
"s_add_u32 m0, s50, 65536\n"
"s_add_u32 s72, s48, 1572992\n"
"buffer_load_dwordx4 v1, s[40:43], s72 offen lds\n"
"s_add_u32 m0, s50, 66560\n"
"s_add_u32 s72, s48, 1671296\n"
"buffer_load_dwordx4 v2, s[40:43], s72 offen lds\n"
"s_add_u32 m0, s50, 67584\n"
"s_add_u32 s72, s48, 1769600\n"
"buffer_load_dwordx4 v1, s[40:43], s72 offen lds\n"
"s_add_u32 m0, s50, 68608\n"
"s_add_u32 s72, s48, 1867904\n"
"buffer_load_dwordx4 v2, s[40:43], s72 offen lds\n"
"s_add_u32 m0, s50, 81920\n"
"s_add_u32 s72, s48, 1573120\n"
"buffer_load_dwordx4 v1, s[40:43], s72 offen lds\n"
"s_add_u32 m0, s50, 82944\n"
"s_add_u32 s72, s48, 1671424\n"
"buffer_load_dwordx4 v2, s[40:43], s72 offen lds\n"
"s_add_u32 m0, s50, 83968\n"
"s_add_u32 s72, s48, 1769728\n"
"buffer_load_dwordx4 v1, s[40:43], s72 offen lds\n"
"s_add_u32 m0, s50, 84992\n"
"s_add_u32 s72, s48, 1868032\n"
"buffer_load_dwordx4 v2, s[40:43], s72 offen lds\n"
"s_waitcnt vmcnt(12)\n"
"s_waitcnt lgkmcnt(0)\n"
"s_barrier\n"
"s_cmp_lg_u32 s66, 0\n"
"s_cbranch_scc1 V7_%=\n"
"v_or_b32 v24, 1, v24\n"
"v_add_u32 v24, 1, v24\n"
"buffer_store_dword v24, v26, s[36:39], s22 offen sc1\n"
"V7_%=:\n"
"s_add_u32 m0, s50, 98304\n"
"s_add_u32 s72, s48, 3145728\n"
"buffer_load_dwordx4 v1, s[40:43], s72 offen lds\n"
"s_add_u32 m0, s50, 99328\n"
"s_add_u32 s72, s48, 3244032\n"
"buffer_load_dwordx4 v2, s[40:43], s72 offen lds\n"
"s_add_u32 m0, s50, 100352\n"
"s_add_u32 s72, s48, 3342336\n"
"buffer_load_dwordx4 v1, s[40:43], s72 offen lds\n"
"s_add_u32 m0, s50, 101376\n"
"s_add_u32 s72, s48, 3440640\n"
"buffer_load_dwordx4 v2, s[40:43], s72 offen lds\n"
"s_add_u32 m0, s50, 114688\n"
"s_add_u32 s72, s48, 3145856\n"
"buffer_load_dwordx4 v1, s[40:43], s72 offen lds\n"
"s_add_u32 m0, s50, 115712\n"
"s_add_u32 s72, s48, 3244160\n"
"buffer_load_dwordx4 v2, s[40:43], s72 offen lds\n"
"s_add_u32 m0, s50, 116736\n"
"s_add_u32 s72, s48, 3342464\n"
"buffer_load_dwordx4 v1, s[40:43], s72 offen lds\n"
"s_add_u32 m0, s50, 117760\n"
"s_add_u32 s72, s48, 3440768\n"
"buffer_load_dwordx4 v2, s[40:43], s72 offen lds\n"
"s_add_u32 m0, s50, 131072\n"
"s_add_u32 s72, s48, 3145984\n"
"buffer_load_dwordx4 v1, s[40:43], s72 offen lds\n"
"s_add_u32 m0, s50, 132096\n"
"s_add_u32 s72, s48, 3244288\n"
"buffer_load_dwordx4 v2, s[40:43], s72 offen lds\n"
"s_add_u32 m0, s50, 133120\n"
"s_add_u32 s72, s48, 3342592\n"
"buffer_load_dwordx4 v1, s[40:43], s72 offen lds\n"
"s_add_u32 m0, s50, 134144\n"
"s_add_u32 s72, s48, 3440896\n"
"buffer_load_dwordx4 v2, s[40:43], s72 offen lds\n"
"v_add_u32 v20, 0, v3\n"
"v_add_u32 v21, 0, v4\n"
"v_add_u32 v22, 0, v5\n"
"ds_read_b64_tr_b16 v[32:33], v20 offset:0\n"
"ds_read_b64_tr_b16 v[34:35], v20 offset:256\n"
"ds_read_b64_tr_b16 v[36:37], v20 offset:4096\n"
"ds_read_b64_tr_b16 v[38:39], v20 offset:4352\n"
"ds_read_b64_tr_b16 v[40:41], v21 offset:0\n"
"ds_read_b64_tr_b16 v[42:43], v21 offset:256\n"
"ds_read_b64_tr_b16 v[44:45], v21 offset:4096\n"
"ds_read_b64_tr_b16 v[46:47], v21 offset:4352\n"
"ds_read_b64_tr_b16 v[48:49], v22 offset:0\n"
"ds_read_b64_tr_b16 v[50:51], v22 offset:256\n"
"ds_read_b64_tr_b16 v[52:53], v22 offset:4096\n"
"ds_read_b64_tr_b16 v[54:55], v22 offset:4352\n"
"ds_read_b64_tr_b16 v[56:57], v20 offset:8192\n"
"ds_read_b64_tr_b16 v[58:59], v20 offset:8448\n"
"ds_read_b64_tr_b16 v[60:61], v20 offset:12288\n"
"ds_read_b64_tr_b16 v[62:63], v20 offset:12544\n"
"ds_read_b64_tr_b16 v[64:65], v21 offset:8192\n"
"ds_read_b64_tr_b16 v[66:67], v21 offset:8448\n"
"ds_read_b64_tr_b16 v[68:69], v21 offset:12288\n"
"ds_read_b64_tr_b16 v[70:71], v21 offset:12544\n"
"ds_read_b64_tr_b16 v[72:73], v22 offset:8192\n"
"ds_read_b64_tr_b16 v[74:75], v22 offset:8448\n"
"ds_read_b64_tr_b16 v[76:77], v22 offset:12288\n"
"ds_read_b64_tr_b16 v[78:79], v22 offset:12544\n"
"s_waitcnt vmcnt(12)\n"
"s_waitcnt lgkmcnt(0)\n"
"s_barrier\n"
"s_add_u32 m0, s50, 0\n"
"s_add_u32 s72, s48, 4718592\n"
"buffer_load_dwordx4 v1, s[40:43], s72 offen lds\n"
"s_add_u32 m0, s50, 1024\n"
"s_add_u32 s72, s48, 4816896\n"
"buffer_load_dwordx4 v2, s[40:43], s72 offen lds\n"
"s_add_u32 m0, s50, 2048\n"
"s_add_u32 s72, s48, 4915200\n"
"buffer_load_dwordx4 v1, s[40:43], s72 offen lds\n"
"s_add_u32 m0, s50, 3072\n"
"s_add_u32 s72, s48, 5013504\n"
"buffer_load_dwordx4 v2, s[40:43], s72 offen lds\n"
"s_add_u32 m0, s50, 16384\n"
"s_add_u32 s72, s48, 4718720\n"
"buffer_load_dwordx4 v1, s[40:43], s72 offen lds\n"
"s_add_u32 m0, s50, 17408\n"
"s_add_u32 s72, s48, 4817024\n"
"buffer_load_dwordx4 v2, s[40:43], s72 offen lds\n"
"s_add_u32 m0, s50, 18432\n"
"s_add_u32 s72, s48, 4915328\n"
"buffer_load_dwordx4 v1, s[40:43], s72 offen lds\n"
"s_add_u32 m0, s50, 19456\n"
"s_add_u32 s72, s48, 5013632\n"
"buffer_load_dwordx4 v2, s[40:43], s72 offen lds\n"
"s_add_u32 m0, s50, 32768\n"
"s_add_u32 s72, s48, 4718848\n"
"buffer_load_dwordx4 v1, s[40:43], s72 offen lds\n"
"s_add_u32 m0, s50, 33792\n"
"s_add_u32 s72, s48, 4817152\n"
"buffer_load_dwordx4 v2, s[40:43], s72 offen lds\n"
"s_add_u32 m0, s50, 34816\n"
"s_add_u32 s72, s48, 4915456\n"
"buffer_load_dwordx4 v1, s[40:43], s72 offen lds\n"
"s_add_u32 m0, s50, 35840\n"
"s_add_u32 s72, s48, 5013760\n"
"buffer_load_dwordx4 v2, s[40:43], s72 offen lds\n"
"v_add_u32 v20, 49152, v3\n"
"v_add_u32 v21, 49152, v4\n"
"v_add_u32 v22, 49152, v5\n"
"ds_read_b64_tr_b16 v[128:129], v20 offset:0\n"
"ds_read_b64_tr_b16 v[130:131], v20 offset:256\n"
"ds_read_b64_tr_b16 v[132:133], v20 offset:4096\n"
"ds_read_b64_tr_b16 v[134:135], v20 offset:4352\n"
"ds_read_b64_tr_b16 v[136:137], v21 offset:0\n"
"ds_read_b64_tr_b16 v[138:139], v21 offset:256\n"
"ds_read_b64_tr_b16 v[140:141], v21 offset:4096\n"
"ds_read_b64_tr_b16 v[142:143], v21 offset:4352\n"
"ds_read_b64_tr_b16 v[144:145], v22 offset:0\n"
"ds_read_b64_tr_b16 v[146:147], v22 offset:256\n"
"ds_read_b64_tr_b16 v[148:149], v22 offset:4096\n"
"ds_read_b64_tr_b16 v[150:151], v22 offset:4352\n"
"ds_read_b64_tr_b16 v[152:153], v20 offset:8192\n"
"ds_read_b64_tr_b16 v[154:155], v20 offset:8448\n"
"ds_read_b64_tr_b16 v[156:157], v20 offset:12288\n"
"ds_read_b64_tr_b16 v[158:159], v20 offset:12544\n"
"ds_read_b64_tr_b16 v[208:209], v21 offset:8192\n"
"ds_read_b64_tr_b16 v[210:211], v21 offset:8448\n"
"ds_read_b64_tr_b16 v[212:213], v21 offset:12288\n"
"ds_read_b64_tr_b16 v[214:215], v21 offset:12544\n"
"ds_read_b64_tr_b16 a[8:9], v22 offset:8192\n"
"ds_read_b64_tr_b16 a[10:11], v22 offset:8448\n"
"ds_read_b64_tr_b16 a[12:13], v22 offset:12288\n"
"ds_read_b64_tr_b16 a[14:15], v22 offset:12544\n"
"s_waitcnt vmcnt(12)\n"
"s_waitcnt lgkmcnt(0)\n"
"s_barrier\n"
"s_add_u32 m0, s50, 49152\n"
"s_add_u32 s72, s48, 6291456\n"
"buffer_load_dwordx4 v1, s[40:43], s72 offen lds\n"
"s_add_u32 m0, s50, 50176\n"
"s_add_u32 s72, s48, 6389760\n"
"buffer_load_dwordx4 v2, s[40:43], s72 offen lds\n"
"s_add_u32 m0, s50, 51200\n"
"s_add_u32 s72, s48, 6488064\n"
"buffer_load_dwordx4 v1, s[40:43], s72 offen lds\n"
"s_add_u32 m0, s50, 52224\n"
"s_add_u32 s72, s48, 6586368\n"
"buffer_load_dwordx4 v2, s[40:43], s72 offen lds\n"
"s_add_u32 m0, s50, 65536\n"
"s_add_u32 s72, s48, 6291584\n"
"buffer_load_dwordx4 v1, s[40:43], s72 offen lds\n"
"s_add_u32 m0, s50, 66560\n"
"s_add_u32 s72, s48, 6389888\n"
"buffer_load_dwordx4 v2, s[40:43], s72 offen lds\n"
"s_add_u32 m0, s50, 67584\n"
"s_add_u32 s72, s48, 6488192\n"
"buffer_load_dwordx4 v1, s[40:43], s72 offen lds\n"
"s_add_u32 m0, s50, 68608\n"
"s_add_u32 s72, s48, 6586496\n"
"buffer_load_dwordx4 v2, s[40:43], s72 offen lds\n"
"s_add_u32 m0, s50, 81920\n"
"s_add_u32 s72, s48, 6291712\n"
"buffer_load_dwordx4 v1, s[40:43], s72 offen lds\n"
"s_add_u32 m0, s50, 82944\n"
"s_add_u32 s72, s48, 6390016\n"
"buffer_load_dwordx4 v2, s[40:43], s72 offen lds\n"
"s_add_u32 m0, s50, 83968\n"
"s_add_u32 s72, s48, 6488320\n"
"buffer_load_dwordx4 v1, s[40:43], s72 offen lds\n"
"s_add_u32 m0, s50, 84992\n"
"s_add_u32 s72, s48, 6586624\n"
"buffer_load_dwordx4 v2, s[40:43], s72 offen lds\n"
"v_add_u32 v20, 98304, v3\n"
"v_add_u32 v21, 98304, v4\n"
"v_add_u32 v22, 98304, v5\n"
"ds_read_b64_tr_b16 v[160:161], v20 offset:0\n"
"ds_read_b64_tr_b16 v[162:163], v20 offset:256\n"
"ds_read_b64_tr_b16 v[164:165], v20 offset:4096\n"
"ds_read_b64_tr_b16 v[166:167], v20 offset:4352\n"
"ds_read_b64_tr_b16 v[216:217], v21 offset:0\n"
"ds_read_b64_tr_b16 v[218:219], v21 offset:256\n"
"ds_read_b64_tr_b16 v[220:221], v21 offset:4096\n"
"ds_read_b64_tr_b16 v[222:223], v21 offset:4352\n"
"ds_read_b64_tr_b16 v[80:81], v22 offset:0\n"
"ds_read_b64_tr_b16 v[82:83], v22 offset:256\n"
"ds_read_b64_tr_b16 v[84:85], v22 offset:4096\n"
"ds_read_b64_tr_b16 v[86:87], v22 offset:4352\n"
"ds_read_b64_tr_b16 v[168:169], v20 offset:8192\n"
"ds_read_b64_tr_b16 v[170:171], v20 offset:8448\n"
"ds_read_b64_tr_b16 v[172:173], v20 offset:12288\n"
"ds_read_b64_tr_b16 v[174:175], v20 offset:12544\n"
"ds_read_b64_tr_b16 v[224:225], v21 offset:8192\n"
"ds_read_b64_tr_b16 v[226:227], v21 offset:8448\n"
"ds_read_b64_tr_b16 v[228:229], v21 offset:12288\n"
"ds_read_b64_tr_b16 v[230:231], v21 offset:12544\n"
"ds_read_b64_tr_b16 a[16:17], v22 offset:8192\n"
"ds_read_b64_tr_b16 a[18:19], v22 offset:8448\n"
"ds_read_b64_tr_b16 a[20:21], v22 offset:12288\n"
"ds_read_b64_tr_b16 a[22:23], v22 offset:12544\n"
"s_waitcnt vmcnt(12)\n"
"s_waitcnt lgkmcnt(0)\n"
"s_barrier\n"
"s_add_u32 m0, s52, 98304\n"
"s_add_u32 s72, s51, 0\n"
"buffer_load_dwordx4 v7, s[44:47], s72 offen lds\n"
"s_add_u32 m0, s52, 99328\n"
"s_add_u32 s72, s51, 10240\n"
"buffer_load_dwordx4 v7, s[44:47], s72 offen lds\n"
"s_add_u32 m0, s52, 100352\n"
"s_add_u32 s72, s51, 20480\n"
"buffer_load_dwordx4 v7, s[44:47], s72 offen lds\n"
"s_add_u32 m0, s52, 101376\n"
"s_add_u32 s72, s51, 30720\n"
"buffer_load_dwordx4 v7, s[44:47], s72 offen lds\n"
"s_add_u32 m0, s52, 102400\n"
"s_add_u32 s72, s51, 40960\n"
"buffer_load_dwordx4 v7, s[44:47], s72 offen lds\n"
"s_add_u32 m0, s52, 103424\n"
"s_add_u32 s72, s51, 51200\n"
"buffer_load_dwordx4 v7, s[44:47], s72 offen lds\n"
"s_add_u32 m0, s52, 104448\n"
"s_add_u32 s72, s51, 61440\n"
"buffer_load_dwordx4 v7, s[44:47], s72 offen lds\n"
"s_add_u32 m0, s52, 105472\n"
"s_add_u32 s72, s51, 71680\n"
"buffer_load_dwordx4 v7, s[44:47], s72 offen lds\n"
"s_add_u32 m0, s52, 106496\n"
"s_add_u32 s72, s51, 81920\n"
"buffer_load_dwordx4 v7, s[44:47], s72 offen lds\n"
"s_add_u32 m0, s52, 107520\n"
"s_add_u32 s72, s51, 92160\n"
"buffer_load_dwordx4 v7, s[44:47], s72 offen lds\n"
"s_add_u32 m0, s52, 108544\n"
"s_add_u32 s72, s51, 102400\n"
"buffer_load_dwordx4 v7, s[44:47], s72 offen lds\n"
"s_add_u32 m0, s52, 109568\n"
"s_add_u32 s72, s51, 112640\n"
"buffer_load_dwordx4 v7, s[44:47], s72 offen lds\n"
"v_add_u32 v20, 0, v3\n"
"v_add_u32 v21, 0, v4\n"
"v_add_u32 v22, 0, v5\n"
"ds_read_b64_tr_b16 v[176:177], v20 offset:0\n"
"ds_read_b64_tr_b16 v[178:179], v20 offset:256\n"
"ds_read_b64_tr_b16 v[180:181], v20 offset:4096\n"
"ds_read_b64_tr_b16 v[182:183], v20 offset:4352\n"
"ds_read_b64_tr_b16 v[232:233], v21 offset:0\n"
"ds_read_b64_tr_b16 v[234:235], v21 offset:256\n"
"ds_read_b64_tr_b16 v[236:237], v21 offset:4096\n"
"ds_read_b64_tr_b16 v[238:239], v21 offset:4352\n"
"ds_read_b64_tr_b16 a[24:25], v22 offset:0\n"
"ds_read_b64_tr_b16 a[26:27], v22 offset:256\n"
"ds_read_b64_tr_b16 a[28:29], v22 offset:4096\n"
"ds_read_b64_tr_b16 a[30:31], v22 offset:4352\n"
"ds_read_b64_tr_b16 v[184:185], v20 offset:8192\n"
"ds_read_b64_tr_b16 v[186:187], v20 offset:8448\n"
"ds_read_b64_tr_b16 v[188:189], v20 offset:12288\n"
"ds_read_b64_tr_b16 v[190:191], v20 offset:12544\n"
"ds_read_b64_tr_b16 v[240:241], v21 offset:8192\n"
"ds_read_b64_tr_b16 v[242:243], v21 offset:8448\n"
"ds_read_b64_tr_b16 v[244:245], v21 offset:12288\n"
"ds_read_b64_tr_b16 v[246:247], v21 offset:12544\n"
"ds_read_b64_tr_b16 a[32:33], v22 offset:8192\n"
"ds_read_b64_tr_b16 a[34:35], v22 offset:8448\n"
"ds_read_b64_tr_b16 a[36:37], v22 offset:12288\n"
"ds_read_b64_tr_b16 a[38:39], v22 offset:12544\n"
"s_waitcnt vmcnt(12)\n"
"s_waitcnt lgkmcnt(0)\n"
"s_barrier\n"
"s_add_u32 m0, s52, 0\n"
"s_add_u32 s72, s51, 256\n"
"buffer_load_dwordx4 v7, s[44:47], s72 offen lds\n"
"s_add_u32 m0, s52, 1024\n"
"s_add_u32 s72, s51, 10496\n"
"buffer_load_dwordx4 v7, s[44:47], s72 offen lds\n"
"s_add_u32 m0, s52, 2048\n"
"s_add_u32 s72, s51, 20736\n"
"buffer_load_dwordx4 v7, s[44:47], s72 offen lds\n"
"s_add_u32 m0, s52, 3072\n"
"s_add_u32 s72, s51, 30976\n"
"buffer_load_dwordx4 v7, s[44:47], s72 offen lds\n"
"s_add_u32 m0, s52, 4096\n"
"s_add_u32 s72, s51, 41216\n"
"buffer_load_dwordx4 v7, s[44:47], s72 offen lds\n"
"s_add_u32 m0, s52, 5120\n"
"s_add_u32 s72, s51, 51456\n"
"buffer_load_dwordx4 v7, s[44:47], s72 offen lds\n"
"s_add_u32 m0, s52, 6144\n"
"s_add_u32 s72, s51, 61696\n"
"buffer_load_dwordx4 v7, s[44:47], s72 offen lds\n"
"s_add_u32 m0, s52, 7168\n"
"s_add_u32 s72, s51, 71936\n"
"buffer_load_dwordx4 v7, s[44:47], s72 offen lds\n"
"s_add_u32 m0, s52, 8192\n"
"s_add_u32 s72, s51, 82176\n"
"buffer_load_dwordx4 v7, s[44:47], s72 offen lds\n"
"s_add_u32 m0, s52, 9216\n"
"s_add_u32 s72, s51, 92416\n"
"buffer_load_dwordx4 v7, s[44:47], s72 offen lds\n"
"s_add_u32 m0, s52, 10240\n"
"s_add_u32 s72, s51, 102656\n"
"buffer_load_dwordx4 v7, s[44:47], s72 offen lds\n"
"s_add_u32 m0, s52, 11264\n"
"s_add_u32 s72, s51, 112896\n"
"buffer_load_dwordx4 v7, s[44:47], s72 offen lds\n"
"v_add_u32 v20, 49152, v3\n"
"v_add_u32 v21, 49152, v4\n"
"v_add_u32 v22, 49152, v5\n"
"ds_read_b64_tr_b16 v[192:193], v20 offset:0\n"
"ds_read_b64_tr_b16 v[194:195], v20 offset:256\n"
"ds_read_b64_tr_b16 v[196:197], v20 offset:4096\n"
"ds_read_b64_tr_b16 v[198:199], v20 offset:4352\n"
"ds_read_b64_tr_b16 v[248:249], v21 offset:0\n"
"ds_read_b64_tr_b16 v[250:251], v21 offset:256\n"
"ds_read_b64_tr_b16 v[252:253], v21 offset:4096\n"
"ds_read_b64_tr_b16 v[254:255], v21 offset:4352\n"
"ds_read_b64_tr_b16 a[40:41], v22 offset:0\n"
"ds_read_b64_tr_b16 a[42:43], v22 offset:256\n"
"ds_read_b64_tr_b16 a[44:45], v22 offset:4096\n"
"ds_read_b64_tr_b16 a[46:47], v22 offset:4352\n"
"ds_read_b64_tr_b16 v[200:201], v20 offset:8192\n"
"ds_read_b64_tr_b16 v[202:203], v20 offset:8448\n"
"ds_read_b64_tr_b16 v[204:205], v20 offset:12288\n"
"ds_read_b64_tr_b16 v[206:207], v20 offset:12544\n"
"ds_read_b64_tr_b16 a[0:1], v21 offset:8192\n"
"ds_read_b64_tr_b16 a[2:3], v21 offset:8448\n"
"ds_read_b64_tr_b16 a[4:5], v21 offset:12288\n"
"ds_read_b64_tr_b16 a[6:7], v21 offset:12544\n"
"ds_read_b64_tr_b16 a[48:49], v22 offset:8192\n"
"ds_read_b64_tr_b16 a[50:51], v22 offset:8448\n"
"ds_read_b64_tr_b16 a[52:53], v22 offset:12288\n"
"ds_read_b64_tr_b16 a[54:55], v22 offset:12544\n"
"s_waitcnt vmcnt(12)\n"
"s_waitcnt lgkmcnt(0)\n"
"s_barrier\n"
"s_add_u32 m0, s52, 49152\n"
"s_add_u32 s72, s51, 512\n"
"buffer_load_dwordx4 v7, s[44:47], s72 offen lds\n"
"s_add_u32 m0, s52, 50176\n"
"s_add_u32 s72, s51, 10752\n"
"buffer_load_dwordx4 v7, s[44:47], s72 offen lds\n"
"s_add_u32 m0, s52, 51200\n"
"s_add_u32 s72, s51, 20992\n"
"buffer_load_dwordx4 v7, s[44:47], s72 offen lds\n"
"s_add_u32 m0, s52, 52224\n"
"s_add_u32 s72, s51, 31232\n"
"buffer_load_dwordx4 v7, s[44:47], s72 offen lds\n"
"s_add_u32 m0, s52, 53248\n"
"s_add_u32 s72, s51, 41472\n"
"buffer_load_dwordx4 v7, s[44:47], s72 offen lds\n"
"s_add_u32 m0, s52, 54272\n"
"s_add_u32 s72, s51, 51712\n"
"buffer_load_dwordx4 v7, s[44:47], s72 offen lds\n"
"s_add_u32 m0, s52, 55296\n"
"s_add_u32 s72, s51, 61952\n"
"buffer_load_dwordx4 v7, s[44:47], s72 offen lds\n"
"s_add_u32 m0, s52, 56320\n"
"s_add_u32 s72, s51, 72192\n"
"buffer_load_dwordx4 v7, s[44:47], s72 offen lds\n"
"s_add_u32 m0, s52, 57344\n"
"s_add_u32 s72, s51, 82432\n"
"buffer_load_dwordx4 v7, s[44:47], s72 offen lds\n"
"s_add_u32 m0, s52, 58368\n"
"s_add_u32 s72, s51, 92672\n"
"buffer_load_dwordx4 v7, s[44:47], s72 offen lds\n"
"s_add_u32 m0, s52, 59392\n"
"s_add_u32 s72, s51, 102912\n"
"buffer_load_dwordx4 v7, s[44:47], s72 offen lds\n"
"s_add_u32 m0, s52, 60416\n"
"s_add_u32 s72, s51, 113152\n"
"buffer_load_dwordx4 v7, s[44:47], s72 offen lds\n"
"v_add_u32 v20, 98304, v8\n"
"v_add_u32 v21, 98304, v9\n"
"ds_read_b64_tr_b16 v[88:89], v20 offset:0\n"
"ds_read_b64_tr_b16 v[90:91], v21 offset:0\n"
"ds_read_b64_tr_b16 v[92:93], v20 offset:6144\n"
"ds_read_b64_tr_b16 v[94:95], v21 offset:6144\n"
"ds_read_b64_tr_b16 a[80:81], v20 offset:12288\n"
"ds_read_b64_tr_b16 a[82:83], v21 offset:12288\n"
"ds_read_b64_tr_b16 a[84:85], v20 offset:18432\n"
"ds_read_b64_tr_b16 a[86:87], v21 offset:18432\n"
"ds_read_b64_tr_b16 a[104:105], v20 offset:2048\n"
"ds_read_b64_tr_b16 a[106:107], v21 offset:2048\n"
"ds_read_b64_tr_b16 a[108:109], v20 offset:8192\n"
"ds_read_b64_tr_b16 a[110:111], v21 offset:8192\n"
"ds_read_b64_tr_b16 a[128:129], v20 offset:14336\n"
"ds_read_b64_tr_b16 a[130:131], v21 offset:14336\n"
"ds_read_b64_tr_b16 a[132:133], v20 offset:20480\n"
"ds_read_b64_tr_b16 a[134:135], v21 offset:20480\n"
"ds_read_b64_tr_b16 a[152:153], v20 offset:4096\n"
"ds_read_b64_tr_b16 a[154:155], v21 offset:4096\n"
"ds_read_b64_tr_b16 a[156:157], v20 offset:10240\n"
"ds_read_b64_tr_b16 a[158:159], v21 offset:10240\n"
"ds_read_b64_tr_b16 a[160:161], v20 offset:16384\n"
"ds_read_b64_tr_b16 a[162:163], v21 offset:16384\n"
"ds_read_b64_tr_b16 a[164:165], v20 offset:22528\n"
"ds_read_b64_tr_b16 a[166:167], v21 offset:22528\n"
"s_waitcnt vmcnt(12)\n"
"s_waitcnt lgkmcnt(0)\n"
"s_barrier\n"
"s_add_u32 m0, s52, 98304\n"
"s_add_u32 s72, s51, 768\n"
"buffer_load_dwordx4 v7, s[44:47], s72 offen lds\n"
"s_add_u32 m0, s52, 99328\n"
"s_add_u32 s72, s51, 11008\n"
"buffer_load_dwordx4 v7, s[44:47], s72 offen lds\n"
"s_add_u32 m0, s52, 100352\n"
"s_add_u32 s72, s51, 21248\n"
"buffer_load_dwordx4 v7, s[44:47], s72 offen lds\n"
"s_add_u32 m0, s52, 101376\n"
"s_add_u32 s72, s51, 31488\n"
"buffer_load_dwordx4 v7, s[44:47], s72 offen lds\n"
"s_add_u32 m0, s52, 102400\n"
"s_add_u32 s72, s51, 41728\n"
"buffer_load_dwordx4 v7, s[44:47], s72 offen lds\n"
"s_add_u32 m0, s52, 103424\n"
"s_add_u32 s72, s51, 51968\n"
"buffer_load_dwordx4 v7, s[44:47], s72 offen lds\n"
"s_add_u32 m0, s52, 104448\n"
"s_add_u32 s72, s51, 62208\n"
"buffer_load_dwordx4 v7, s[44:47], s72 offen lds\n"
"s_add_u32 m0, s52, 105472\n"
"s_add_u32 s72, s51, 72448\n"
"buffer_load_dwordx4 v7, s[44:47], s72 offen lds\n"
"s_add_u32 m0, s52, 106496\n"
"s_add_u32 s72, s51, 82688\n"
"buffer_load_dwordx4 v7, s[44:47], s72 offen lds\n"
"s_add_u32 m0, s52, 107520\n"
"s_add_u32 s72, s51, 92928\n"
"buffer_load_dwordx4 v7, s[44:47], s72 offen lds\n"
"s_add_u32 m0, s52, 108544\n"
"s_add_u32 s72, s51, 103168\n"
"buffer_load_dwordx4 v7, s[44:47], s72 offen lds\n"
"s_add_u32 m0, s52, 109568\n"
"s_add_u32 s72, s51, 113408\n"
"buffer_load_dwordx4 v7, s[44:47], s72 offen lds\n"
"v_add_u32 v20, 0, v8\n"
"v_add_u32 v21, 0, v9\n"
"ds_read_b64_tr_b16 a[56:57], v20 offset:0\n"
"ds_read_b64_tr_b16 a[58:59], v21 offset:0\n"
"ds_read_b64_tr_b16 a[60:61], v20 offset:6144\n"
"ds_read_b64_tr_b16 a[62:63], v21 offset:6144\n"
"ds_read_b64_tr_b16 a[88:89], v20 offset:12288\n"
"ds_read_b64_tr_b16 a[90:91], v21 offset:12288\n"
"ds_read_b64_tr_b16 a[92:93], v20 offset:18432\n"
"ds_read_b64_tr_b16 a[94:95], v21 offset:18432\n"
"ds_read_b64_tr_b16 a[112:113], v20 offset:2048\n"
"ds_read_b64_tr_b16 a[114:115], v21 offset:2048\n"
"ds_read_b64_tr_b16 a[116:117], v20 offset:8192\n"
"ds_read_b64_tr_b16 a[118:119], v21 offset:8192\n"
"ds_read_b64_tr_b16 a[136:137], v20 offset:14336\n"
"ds_read_b64_tr_b16 a[138:139], v21 offset:14336\n"
"ds_read_b64_tr_b16 a[140:141], v20 offset:20480\n"
"ds_read_b64_tr_b16 a[142:143], v21 offset:20480\n"
"ds_read_b64_tr_b16 v[104:105], v20 offset:4096\n"
"ds_read_b64_tr_b16 v[106:107], v21 offset:4096\n"
"ds_read_b64_tr_b16 v[108:109], v20 offset:10240\n"
"ds_read_b64_tr_b16 v[110:111], v21 offset:10240\n"
"ds_read_b64_tr_b16 a[168:169], v20 offset:16384\n"
"ds_read_b64_tr_b16 a[170:171], v21 offset:16384\n"
"ds_read_b64_tr_b16 a[172:173], v20 offset:22528\n"
"ds_read_b64_tr_b16 a[174:175], v21 offset:22528\n"
"s_waitcnt vmcnt(12)\n"
"s_waitcnt lgkmcnt(0)\n"
"s_barrier\n"
"s_add_u32 m0, s52, 0\n"
"s_add_u32 s72, s51, 1024\n"
"buffer_load_dwordx4 v7, s[44:47], s72 offen lds\n"
"s_add_u32 m0, s52, 1024\n"
"s_add_u32 s72, s51, 11264\n"
"buffer_load_dwordx4 v7, s[44:47], s72 offen lds\n"
"s_add_u32 m0, s52, 2048\n"
"s_add_u32 s72, s51, 21504\n"
"buffer_load_dwordx4 v7, s[44:47], s72 offen lds\n"
"s_add_u32 m0, s52, 3072\n"
"s_add_u32 s72, s51, 31744\n"
"buffer_load_dwordx4 v7, s[44:47], s72 offen lds\n"
"s_add_u32 m0, s52, 4096\n"
"s_add_u32 s72, s51, 41984\n"
"buffer_load_dwordx4 v7, s[44:47], s72 offen lds\n"
"s_add_u32 m0, s52, 5120\n"
"s_add_u32 s72, s51, 52224\n"
"buffer_load_dwordx4 v7, s[44:47], s72 offen lds\n"
"s_add_u32 m0, s52, 6144\n"
"s_add_u32 s72, s51, 62464\n"
"buffer_load_dwordx4 v7, s[44:47], s72 offen lds\n"
"s_add_u32 m0, s52, 7168\n"
"s_add_u32 s72, s51, 72704\n"
"buffer_load_dwordx4 v7, s[44:47], s72 offen lds\n"
"s_add_u32 m0, s52, 8192\n"
"s_add_u32 s72, s51, 82944\n"
"buffer_load_dwordx4 v7, s[44:47], s72 offen lds\n"
"s_add_u32 m0, s52, 9216\n"
"s_add_u32 s72, s51, 93184\n"
"buffer_load_dwordx4 v7, s[44:47], s72 offen lds\n"
"s_add_u32 m0, s52, 10240\n"
"s_add_u32 s72, s51, 103424\n"
"buffer_load_dwordx4 v7, s[44:47], s72 offen lds\n"
"s_add_u32 m0, s52, 11264\n"
"s_add_u32 s72, s51, 113664\n"
"buffer_load_dwordx4 v7, s[44:47], s72 offen lds\n"
"v_add_u32 v20, 49152, v8\n"
"v_add_u32 v21, 49152, v9\n"
"ds_read_b64_tr_b16 a[64:65], v20 offset:0\n"
"ds_read_b64_tr_b16 a[66:67], v21 offset:0\n"
"ds_read_b64_tr_b16 a[68:69], v20 offset:6144\n"
"ds_read_b64_tr_b16 a[70:71], v21 offset:6144\n"
"ds_read_b64_tr_b16 v[96:97], v20 offset:12288\n"
"ds_read_b64_tr_b16 v[98:99], v21 offset:12288\n"
"ds_read_b64_tr_b16 v[100:101], v20 offset:18432\n"
"ds_read_b64_tr_b16 v[102:103], v21 offset:18432\n"
"ds_read_b64_tr_b16 a[120:121], v20 offset:2048\n"
"ds_read_b64_tr_b16 a[122:123], v21 offset:2048\n"
"ds_read_b64_tr_b16 a[124:125], v20 offset:8192\n"
"ds_read_b64_tr_b16 a[126:127], v21 offset:8192\n"
"ds_read_b64_tr_b16 a[144:145], v20 offset:14336\n"
"ds_read_b64_tr_b16 a[146:147], v21 offset:14336\n"
"ds_read_b64_tr_b16 a[148:149], v20 offset:20480\n"
"ds_read_b64_tr_b16 a[150:151], v21 offset:20480\n"
"ds_read_b64_tr_b16 a[176:177], v20 offset:4096\n"
"ds_read_b64_tr_b16 a[178:179], v21 offset:4096\n"
"ds_read_b64_tr_b16 a[180:181], v20 offset:10240\n"
"ds_read_b64_tr_b16 a[182:183], v21 offset:10240\n"
"ds_read_b64_tr_b16 a[184:185], v20 offset:16384\n"
"ds_read_b64_tr_b16 a[186:187], v21 offset:16384\n"
"ds_read_b64_tr_b16 a[188:189], v20 offset:22528\n"
"ds_read_b64_tr_b16 a[190:191], v21 offset:22528\n"
"s_cmp_lg_u32 s66, 0\n"
"s_cbranch_scc1 V8_%=\n"
"s_mov_b32 s76, 131072\n"
"s_mov_b64 exec, 1\n"
"s_branch V10_%=\n"
"V9_%=:\n"
"s_sleep 4\n"
"V10_%=:\n"
"v_mov_b32 v29, 0\n"
"buffer_atomic_add v29, v26, s[36:39], s23 offen sc0 sc1\n"
"s_sub_u32 s76, s76, 1\n"
"s_cbranch_scc1 VABORT_%=\n"
"s_waitcnt vmcnt(0)\n"
"v_xor_b32 v29, v29, v24\n"
"v_cmp_lt_u32 vcc, 1, v29\n"
"s_nop 0\n"
"s_cbranch_vccnz V9_%=\n"
"v_mov_b32 v28, 153600\n"
"ds_write_b32 v28, v29\n"
"v_mov_b32 v24, 1\n"
"s_add_u32 s22, s23, 16384\n"
"buffer_store_dword v24, v26, s[36:39], s22 offen sc1\n"
"s_mov_b64 exec, -1\n"
"V8_%=:\n"
"s_waitcnt lgkmcnt(0)\n"
"s_barrier\n"
"v_mov_b32 v28, 153600\n"
"ds_read_b32 v28, v28\n"
"s_waitcnt lgkmcnt(0)\n"
"v_readfirstlane_b32 s22, v28\n"
"s_nop 0\n"
"s_lshl_b32 s22, s22, 13\n"
"v_xor_b32 v13, s22, v13\n"
"s_xor_b32 s68, s68, s22\n"
"s_xor_b32 s20, s20, s22\n"
"s_xor_b32 s54, s54, s22\n"
"s_waitcnt vmcnt(12)\n"
"s_waitcnt lgkmcnt(0)\n"
"s_barrier\n"
"v_add_u32 v20, 98304, v8\n"
"v_add_u32 v21, 98304, v9\n"
"ds_read_b64_tr_b16 a[72:73], v20 offset:0\n"
"ds_read_b64_tr_b16 a[74:75], v21 offset:0\n"
"ds_read_b64_tr_b16 a[76:77], v20 offset:6144\n"
"ds_read_b64_tr_b16 a[78:79], v21 offset:6144\n"
"ds_read_b64_tr_b16 a[96:97], v20 offset:12288\n"
"ds_read_b64_tr_b16 a[98:99], v21 offset:12288\n"
"ds_read_b64_tr_b16 a[100:101], v20 offset:18432\n"
"ds_read_b64_tr_b16 a[102:103], v21 offset:18432\n"
"ds_read_b64_tr_b16 a[192:193], v20 offset:2048\n"
"ds_read_b64_tr_b16 a[194:195], v21 offset:2048\n"
"ds_read_b64_tr_b16 a[196:197], v20 offset:8192\n"
"ds_read_b64_tr_b16 a[198:199], v21 offset:8192\n"
"ds_read_b64_tr_b16 a[200:201], v20 offset:14336\n"
"ds_read_b64_tr_b16 a[202:203], v21 offset:14336\n"
"ds_read_b64_tr_b16 a[204:205], v20 offset:20480\n"
"ds_read_b64_tr_b16 a[206:207], v21 offset:20480\n"
"ds_read_b64_tr_b16 v[112:113], v20 offset:4096\n"
"ds_read_b64_tr_b16 v[114:115], v21 offset:4096\n"
"ds_read_b64_tr_b16 v[116:117], v20 offset:10240\n"
"ds_read_b64_tr_b16 v[118:119], v21 offset:10240\n"
"ds_read_b64_tr_b16 a[208:209], v20 offset:16384\n"
"ds_read_b64_tr_b16 a[210:211], v21 offset:16384\n"
"ds_read_b64_tr_b16 a[212:213], v20 offset:22528\n"
"ds_read_b64_tr_b16 a[214:215], v21 offset:22528\n"
"s_waitcnt vmcnt(0)\n"
"s_waitcnt lgkmcnt(0)\n"
"s_barrier\n"
"v_add_u32 v20, 0, v8\n"
"v_add_u32 v21, 0, v9\n"
"ds_read_b64_tr_b16 a[216:217], v20 offset:0\n"
"ds_read_b64_tr_b16 a[218:219], v21 offset:0\n"
"ds_read_b64_tr_b16 a[220:221], v20 offset:6144\n"
"ds_read_b64_tr_b16 a[222:223], v21 offset:6144\n"
"ds_read_b64_tr_b16 a[224:225], v20 offset:12288\n"
"ds_read_b64_tr_b16 a[226:227], v21 offset:12288\n"
"ds_read_b64_tr_b16 a[228:229], v20 offset:18432\n"
"ds_read_b64_tr_b16 a[230:231], v21 offset:18432\n"
"ds_read_b64_tr_b16 a[232:233], v20 offset:2048\n"
"ds_read_b64_tr_b16 a[234:235], v21 offset:2048\n"
"ds_read_b64_tr_b16 a[236:237], v20 offset:8192\n"
"ds_read_b64_tr_b16 a[238:239], v21 offset:8192\n"
"ds_read_b64_tr_b16 v[120:121], v20 offset:14336\n"
"ds_read_b64_tr_b16 v[122:123], v21 offset:14336\n"
"ds_read_b64_tr_b16 v[124:125], v20 offset:20480\n"
"ds_read_b64_tr_b16 v[126:127], v21 offset:20480\n"
"ds_read_b64_tr_b16 a[240:241], v20 offset:4096\n"
"ds_read_b64_tr_b16 a[242:243], v21 offset:4096\n"
"ds_read_b64_tr_b16 a[244:245], v20 offset:10240\n"
"ds_read_b64_tr_b16 a[246:247], v21 offset:10240\n"
"ds_read_b64_tr_b16 a[248:249], v20 offset:16384\n"
"ds_read_b64_tr_b16 a[250:251], v21 offset:16384\n"
"ds_read_b64_tr_b16 a[252:253], v20 offset:22528\n"
"ds_read_b64_tr_b16 a[254:255], v21 offset:22528\n"
"s_waitcnt lgkmcnt(0)\n"
"s_barrier\n"
"ds_write_b128 v15, v[32:35] offset:0\n"
"ds_write_b128 v15, v[36:39] offset:1024\n"
"ds_write_b128 v15, v[40:43] offset:2048\n"
"ds_write_b128 v15, v[44:47] offset:3072\n"
"ds_write_b128 v15, v[48:51] offset:4096\n"
"ds_write_b128 v15, v[52:55] offset:5120\n"
"ds_write_b128 v15, v[56:59] offset:6144\n"
"ds_write_b128 v15, v[60:63] offset:7168\n"
"ds_write_b128 v15, v[64:67] offset:8192\n"
"ds_write_b128 v15, v[68:71] offset:9216\n"
"ds_write_b128 v15, v[72:75] offset:10240\n"
"ds_write_b128 v15, v[76:79] offset:11264\n"
"ds_write_b128 v15, v[80:83] offset:12288\n"
"ds_write_b128 v15, v[84:87] offset:13312\n"
"ds_write_b128 v15, v[88:91] offset:14336\n"
"ds_write_b128 v15, v[92:95] offset:15360\n"
"ds_write_b128 v15, v[96:99] offset:16384\n"
"ds_write_b128 v15, v[100:103] offset:17408\n"
"ds_write_b128 v15, v[104:107] offset:18432\n"
"ds_write_b128 v15, v[108:111] offset:19456\n"
"ds_write_b128 v15, v[112:115] offset:20480\n"
"ds_write_b128 v15, v[116:119] offset:21504\n"
"ds_write_b128 v15, v[120:123] offset:22528\n"
"ds_write_b128 v15, v[124:127] offset:23552\n"
"s_waitcnt lgkmcnt(0)\n"
"ds_read_b128 v[96:99], v15 offset:0\n"
"ds_read_b128 v[100:103], v15 offset:1024\n"
"ds_read_b128 v[72:75], v15 offset:2048\n"
"ds_read_b128 v[76:79], v15 offset:3072\n"
"ds_read_b128 v[104:107], v15 offset:4096\n"
"ds_read_b128 v[108:111], v15 offset:5120\n"
"ds_read_b128 v[80:83], v15 offset:6144\n"
"ds_read_b128 v[84:87], v15 offset:7168\n"
"ds_read_b128 v[112:115], v15 offset:8192\n"
"ds_read_b128 v[116:119], v15 offset:9216\n"
"ds_read_b128 v[88:91], v15 offset:10240\n"
"ds_read_b128 v[92:95], v15 offset:11264\n"
"v_mov_b32 v32, 0\n"
"v_mov_b32 v33, 0\n"
"v_mov_b32 v34, 0\n"
"v_mov_b32 v35, 0\n"
"v_mov_b32 v36, 0\n"
"v_mov_b32 v37, 0\n"
"v_mov_b32 v38, 0\n"
"v_mov_b32 v39, 0\n"
"v_mov_b32 v40, 0\n"
"v_mov_b32 v41, 0\n"
"v_mov_b32 v42, 0\n"
"v_mov_b32 v43, 0\n"
"v_mov_b32 v44, 0\n"
"v_mov_b32 v45, 0\n"
"v_mov_b32 v46, 0\n"
"v_mov_b32 v47, 0\n"
"v_mov_b32 v48, 0\n"
"v_mov_b32 v49, 0\n"
"v_mov_b32 v50, 0\n"
"v_mov_b32 v51, 0\n"
"v_mov_b32 v52, 0\n"
"v_mov_b32 v53, 0\n"
"v_mov_b32 v54, 0\n"
"v_mov_b32 v55, 0\n"
"v_mov_b32 v120, 0\n"
"v_mov_b32 v121, 0\n"
"v_mov_b32 v122, 0\n"
"v_mov_b32 v123, 0\n"
"v_mov_b32 v124, 0\n"
"v_mov_b32 v125, 0\n"
"v_mov_b32 v126, 0\n"
"v_mov_b32 v127, 0\n"
"s_add_u32 s70, s69, 0\n"
"s_lshr_b32 s71, s70, 8\n"
"s_xor_b32 s71, s71, s54\n"
"s_and_b32 s71, s71, 0x2000\n"
"s_add_u32 s70, s70, s71\n"
"s_add_u32 s55, s70, s53\n"
"s_add_u32 s70, s69, 131072\n"
"s_lshr_b32 s71, s70, 8\n"
"s_xor_b32 s71, s71, s54\n"
"s_and_b32 s71, s71, 0x2000\n"
"s_add_u32 s70, s70, s71\n"
"s_add_u32 s56, s70, s53\n"
"s_add_u32 s70, s69, 262144\n"
"s_lshr_b32 s71, s70, 8\n"
"s_xor_b32 s71, s71, s54\n"
"s_and_b32 s71, s71, 0x2000\n"
"s_add_u32 s70, s70, s71\n"
"s_add_u32 s57, s70, s53\n"
"s_add_u32 s70, s69, 393216\n"
"s_lshr_b32 s71, s70, 8\n"
"s_xor_b32 s71, s71, s54\n"
"s_and_b32 s71, s71, 0x2000\n"
"s_add_u32 s70, s70, s71\n"
"s_add_u32 s58, s70, s53\n"
"s_add_u32 s70, s69, 524288\n"
"s_lshr_b32 s71, s70, 8\n"
"s_xor_b32 s71, s71, s54\n"
"s_and_b32 s71, s71, 0x2000\n"
"s_add_u32 s70, s70, s71\n"
"s_add_u32 s59, s70, s53\n"
"s_nop 0\n"
"V12_%=:\n"
"s_add_u32 s60, s68, 5120\n"
"s_add_u32 s61, s68, 6144\n"
"s_add_u32 s62, s68, 7168\n"
"s_waitcnt lgkmcnt(0)\n"
"s_barrier\n"
"buffer_load_dwordx4 v[56:59], v0, s[36:39], s60 offen offset:0 sc1\n"
"s_nop 12\n"
"buffer_load_dwordx4 v[60:63], v0, s[36:39], s61 offen offset:0 sc1\n"
"s_nop 12\n"
"buffer_load_dwordx4 v[64:67], v0, s[36:39], s62 offen offset:0 sc1\n"
"buffer_load_dword v30, v0, s[36:39], s38 offen\n"
"V13_%=:\n"
"s_waitcnt vmcnt(3)\n"
"v_smfmac_f32_16x16x64_bf16 v[32:35], v[56:59], v[96:103], v6\n"
"ds_write_b128 v0, v[56:59] offset:0\n"
"v_max3_u32 v29, v56, v57, 0\n"
"v_smfmac_f32_16x16x64_bf16 v[36:39], v[56:59], v[72:79], v6\n"
"v_max3_u32 v29, v58, v59, v29\n"
"v_smfmac_f32_16x16x64_bf16 v[40:43], v[56:59], v[104:111], v6\n"
"s_waitcnt vmcnt(2)\n"
"ds_write_b128 v0, v[60:63] offset:1024\n"
"v_max3_u32 v29, v60, v61, v29\n"
"v_smfmac_f32_16x16x64_bf16 v[32:35], v[60:63], v[80:87], v6\n"
"v_max3_u32 v29, v62, v63, v29\n"
"v_smfmac_f32_16x16x64_bf16 v[36:39], v[60:63], v[112:119], v6\n"
"s_waitcnt vmcnt(1)\n"
"ds_write_b128 v0, v[64:67] offset:2048\n"
"v_max3_u32 v29, v64, v65, v29\n"
"v_max3_u32 v29, v66, v67, v29\n"
"v_smfmac_f32_16x16x64_bf16 v[40:43], v[60:63], v[88:95], v6\n"
"v_cmp_eq_u32 vcc, -1, v29\n"
"v_smfmac_f32_16x16x64_bf16 v[32:35], v[64:67], v[128:135], v6\n"
"v_smfmac_f32_16x16x64_bf16 v[36:39], v[64:67], v[136:143], v6\n"
"s_cbranch_vccnz V14_%=\n"
"s_waitcnt lgkmcnt(0)\n"
"s_barrier\n"
"ds_read_b128 v[68:71], v0 offset:3072\n"
"ds_read_b128 v[72:75], v0 offset:4096\n"
"v_smfmac_f32_16x16x64_bf16 v[40:43], v[64:67], v[144:151], v6\n"
"ds_read_b128 v[76:79], v0 offset:5120\n"
"ds_read_b128 v[80:83], v0 offset:6144\n"
"ds_read_b128 v[84:87], v0 offset:7168\n"
"ds_read_b128 v[88:91], v0 offset:8192\n"
"ds_read_b128 v[92:95], v0 offset:9216\n"
"s_waitcnt lgkmcnt(6)\n"
"v_smfmac_f32_16x16x64_bf16 v[32:35], v[68:71], v[152:159], v6\n"
"s_waitcnt lgkmcnt(5)\n"
"v_smfmac_f32_16x16x64_bf16 v[32:35], v[72:75], v[160:167], v6\n"
"s_waitcnt lgkmcnt(4)\n"
"v_smfmac_f32_16x16x64_bf16 v[32:35], v[76:79], v[168:175], v6\n"
"s_waitcnt lgkmcnt(3)\n"
"v_smfmac_f32_16x16x64_bf16 v[32:35], v[80:83], v[176:183], v6\n"
"s_waitcnt lgkmcnt(2)\n"
"v_smfmac_f32_16x16x64_bf16 v[32:35], v[84:87], v[184:191], v6\n"
"ds_read_b128 v[96:99], v15 offset:12288\n"
"s_waitcnt lgkmcnt(2)\n"
"v_smfmac_f32_16x16x64_bf16 v[32:35], v[88:91], v[192:199], v6\n"
"ds_read_b128 v[100:103], v15 offset:13312\n"
"s_waitcnt lgkmcnt(2)\n"
"v_smfmac_f32_16x16x64_bf16 v[32:35], v[92:95], v[200:207], v6\n"
"ds_read_b128 v[104:107], v15 offset:14336\n"
"s_nop 0\n"
"v_smfmac_f32_16x16x64_bf16 v[36:39], v[68:71], v[208:215], v6\n"
"ds_read_b128 v[108:111], v15 offset:15360\n"
"s_nop 0\n"
"v_smfmac_f32_16x16x64_bf16 v[36:39], v[72:75], v[216:223], v6\n"
"ds_read_b128 v[112:115], v15 offset:16384\n"
"v_smfmac_f32_16x16x64_bf16 v[36:39], v[76:79], v[224:231], v6\n"
"ds_read_b128 v[116:119], v15 offset:17408\n"
"v_add_f32 v32, v32, v34\n"
"v_smfmac_f32_16x16x64_bf16 v[36:39], v[80:83], v[232:239], v6\n"
"v_add_f32 v33, v33, v35\n"
"v_cvt_pk_bf16_f32 v32, v32, v33\n"
"v_smfmac_f32_16x16x64_bf16 v[36:39], v[84:87], v[240:247], v6\n"
"ds_write_b32 v10, v32 offset:0\n"
"v_smfmac_f32_16x16x64_bf16 v[36:39], v[88:91], v[248:255], v6\n"
"v_smfmac_f32_16x16x64_bf16 v[36:39], v[92:95], a[0:7], v6\n"
"s_nop 0\n"
"v_smfmac_f32_16x16x64_bf16 v[40:43], v[68:71], a[8:15], v6\n"
"s_nop 0\n"
"s_waitcnt lgkmcnt(5)\n"
"v_smfmac_f32_16x16x64_bf16 v[40:43], v[72:75], v[96:103], v6\n"
"v_smfmac_f32_16x16x64_bf16 v[40:43], v[76:79], a[16:23], v6\n"
"s_waitcnt lgkmcnt(0)\n"
"s_barrier\n"
"ds_read_b64_tr_b16 v[56:57], v12 offset:0\n"
"ds_read_b64_tr_b16 v[58:59], v12 offset:32\n"
"v_add_f32 v36, v36, v38\n"
"v_smfmac_f32_16x16x64_bf16 v[40:43], v[80:83], a[24:31], v6\n"
"ds_read_b64_tr_b16 v[60:61], v12 offset:256\n"
"ds_read_b64_tr_b16 v[62:63], v12 offset:288\n"
"v_add_f32 v37, v37, v39\n"
"v_cvt_pk_bf16_f32 v36, v36, v37\n"
"v_smfmac_f32_16x16x64_bf16 v[40:43], v[84:87], a[32:39], v6\n"
"ds_write_b32 v10, v36 offset:512\n"
"v_smfmac_f32_16x16x64_bf16 v[40:43], v[88:91], a[40:47], v6\n"
"v_smfmac_f32_16x16x64_bf16 v[40:43], v[92:95], a[48:55], v6\n"
"ds_read_b128 v[96:99], v15 offset:18432\n"
"s_nop 0\n"
"s_waitcnt lgkmcnt(4)\n"
"v_smfmac_f32_16x16x64_bf16 v[44:47], v[56:59], v[104:111], v11\n"
"ds_read_b128 v[100:103], v15 offset:19456\n"
"s_nop 0\n"
"v_smfmac_f32_16x16x64_bf16 v[48:51], v[56:59], a[56:63], v11\n"
"v_smfmac_f32_16x16x64_bf16 v[52:55], v[56:59], a[64:71], v11\n"
"s_waitcnt lgkmcnt(2)\n"
"s_barrier\n"
"ds_read_b64_tr_b16 v[64:65], v12 offset:512\n"
"ds_read_b64_tr_b16 v[66:67], v12 offset:544\n"
"v_add_f32 v40, v40, v42\n"
"v_smfmac_f32_16x16x64_bf16 v[120:123], v[56:59], a[72:79], v11\n"
"ds_read_b64_tr_b16 v[68:69], v12 offset:768\n"
"ds_read_b64_tr_b16 v[70:71], v12 offset:800\n"
"v_add_f32 v41, v41, v43\n"
"v_cvt_pk_bf16_f32 v40, v40, v41\n"
"v_smfmac_f32_16x16x64_bf16 v[44:47], v[60:63], a[80:87], v11\n"
"ds_write_b32 v10, v40 offset:1024\n"
"v_smfmac_f32_16x16x64_bf16 v[48:51], v[60:63], a[88:95], v11\n"
"v_smfmac_f32_16x16x64_bf16 v[52:55], v[60:63], v[112:119], v11\n"
"ds_read_b128 v[104:107], v15 offset:20480\n"
"v_smfmac_f32_16x16x64_bf16 v[120:123], v[60:63], a[96:103], v11\n"
"ds_read_b128 v[108:111], v15 offset:21504\n"
"s_waitcnt lgkmcnt(5)\n"
"v_smfmac_f32_16x16x64_bf16 v[44:47], v[64:67], a[104:111], v11\n"
"v_smfmac_f32_16x16x64_bf16 v[48:51], v[64:67], a[112:119], v11\n"
"s_waitcnt lgkmcnt(2)\n"
"s_barrier\n"
"ds_read_b64_tr_b16 v[72:73], v12 offset:1024\n"
"ds_read_b64_tr_b16 v[74:75], v12 offset:1056\n"
"v_smfmac_f32_16x16x64_bf16 v[52:55], v[64:67], a[120:127], v11\n"
"ds_read_b64_tr_b16 v[76:77], v12 offset:1280\n"
"ds_read_b64_tr_b16 v[78:79], v12 offset:1312\n"
"v_smfmac_f32_16x16x64_bf16 v[44:47], v[68:71], a[128:135], v11\n"
"v_smfmac_f32_16x16x64_bf16 v[48:51], v[68:71], a[136:143], v11\n"
"v_smfmac_f32_16x16x64_bf16 v[52:55], v[68:71], a[144:151], v11\n"
"s_waitcnt lgkmcnt(2)\n"
"v_smfmac_f32_16x16x64_bf16 v[44:47], v[72:75], a[152:159], v11\n"
"ds_read_b128 v[112:115], v15 offset:22528\n"
"s_waitcnt lgkmcnt(1)\n"
"v_smfmac_f32_16x16x64_bf16 v[44:47], v[76:79], a[160:167], v11\n"
"ds_read_b128 v[116:119], v15 offset:23552\n"
"v_smfmac_f32_16x16x64_bf16 v[48:51], v[72:75], v[96:103], v11\n"
"s_nop 1\n"
"v_smfmac_f32_16x16x64_bf16 v[48:51], v[76:79], a[168:175], v11\n"
"s_nop 1\n"
"v_smfmac_f32_16x16x64_bf16 v[52:55], v[72:75], a[176:183], v11\n"
"v_cvt_pk_bf16_f32 v80, v44, v45\n"
"v_smfmac_f32_16x16x64_bf16 v[52:55], v[76:79], a[184:191], v11\n"
"v_cvt_pk_bf16_f32 v81, v46, v47\n"
"v_min_u32 v80, -2, v80\n"
"v_smfmac_f32_16x16x64_bf16 v[120:123], v[64:67], a[192:199], v11\n"
"v_min_u32 v81, -2, v81\n"
"v_smfmac_f32_16x16x64_bf16 v[120:123], v[68:71], a[200:207], v11\n"
"buffer_store_dwordx2 v[80:81], v16, s[36:39], s55 offen sc1\n"
"v_cvt_pk_bf16_f32 v82, v48, v49\n"
"v_smfmac_f32_16x16x64_bf16 v[120:123], v[72:75], v[104:111], v11\n"
"v_cvt_pk_bf16_f32 v83, v50, v51\n"
"v_min_u32 v82, -2, v82\n"
"v_smfmac_f32_16x16x64_bf16 v[120:123], v[76:79], a[208:215], v11\n"
"v_min_u32 v83, -2, v83\n"
"v_smfmac_f32_16x16x64_bf16 v[124:127], v[56:59], a[216:223], v11\n"
"buffer_store_dwordx2 v[82:83], v16, s[36:39], s56 offen sc1\n"
"v_cvt_pk_bf16_f32 v84, v52, v53\n"
"v_smfmac_f32_16x16x64_bf16 v[124:127], v[60:63], a[224:231], v11\n"
"v_cvt_pk_bf16_f32 v85, v54, v55\n"
"v_min_u32 v84, -2, v84\n"
"v_smfmac_f32_16x16x64_bf16 v[124:127], v[64:67], a[232:239], v11\n"
"v_min_u32 v85, -2, v85\n"
"s_waitcnt lgkmcnt(0)\n"
"v_smfmac_f32_16x16x64_bf16 v[124:127], v[68:71], v[112:119], v11\n"
"buffer_store_dwordx2 v[84:85], v16, s[36:39], s57 offen sc1\n"
"v_cvt_pk_bf16_f32 v86, v120, v121\n"
"v_smfmac_f32_16x16x64_bf16 v[124:127], v[72:75], a[240:247], v11\n"
"v_cvt_pk_bf16_f32 v87, v122, v123\n"
"v_min_u32 v86, -2, v86\n"
"v_smfmac_f32_16x16x64_bf16 v[124:127], v[76:79], a[248:255], v11\n"
"v_min_u32 v87, -2, v87\n"
"buffer_store_dwordx2 v[86:87], v16, s[36:39], s58 offen sc1\n"
"s_nop 5\n"
"v_cvt_pk_bf16_f32 v88, v124, v125\n"
"v_cvt_pk_bf16_f32 v89, v126, v127\n"
"v_min_u32 v88, -2, v88\n"
"v_min_u32 v89, -2, v89\n"
"buffer_store_dwordx2 v[88:89], v16, s[36:39], s59 offen sc1\n"
"s_add_u32 s73, s73, 1\n"
"s_add_u32 s69, s69, 655360\n"
"s_add_u32 s79, s68, 131072\n"
"s_and_b32 s79, s79, 0x3fffff\n"
"s_xor_b32 s68, s79, s68\n"
"s_lshr_b32 s68, s68, 8\n"
"s_and_b32 s68, s68, 8192\n"
"s_xor_b32 s68, s68, s79\n"
"s_cmp_lt_u32 s73, 64\n"
"s_cbranch_scc0 V11_%=\n"
"ds_read_b128 v[96:99], v15 offset:0\n"
"ds_read_b128 v[100:103], v15 offset:1024\n"
"ds_read_b128 v[72:75], v15 offset:2048\n"
"ds_read_b128 v[76:79], v15 offset:3072\n"
"ds_read_b128 v[104:107], v15 offset:4096\n"
"ds_read_b128 v[108:111], v15 offset:5120\n"
"ds_read_b128 v[80:83], v15 offset:6144\n"
"ds_read_b128 v[84:87], v15 offset:7168\n"
"ds_read_b128 v[112:115], v15 offset:8192\n"
"ds_read_b128 v[116:119], v15 offset:9216\n"
"ds_read_b128 v[88:91], v15 offset:10240\n"
"ds_read_b128 v[92:95], v15 offset:11264\n"
"v_mov_b32 v32, 0\n"
"v_mov_b32 v33, 0\n"
"v_mov_b32 v34, 0\n"
"v_mov_b32 v35, 0\n"
"v_mov_b32 v36, 0\n"
"v_mov_b32 v37, 0\n"
"v_mov_b32 v38, 0\n"
"v_mov_b32 v39, 0\n"
"v_mov_b32 v40, 0\n"
"v_mov_b32 v41, 0\n"
"v_mov_b32 v42, 0\n"
"v_mov_b32 v43, 0\n"
"v_mov_b32 v44, 0\n"
"v_mov_b32 v45, 0\n"
"v_mov_b32 v46, 0\n"
"v_mov_b32 v47, 0\n"
"v_mov_b32 v48, 0\n"
"v_mov_b32 v49, 0\n"
"v_mov_b32 v50, 0\n"
"v_mov_b32 v51, 0\n"
"v_mov_b32 v52, 0\n"
"v_mov_b32 v53, 0\n"
"v_mov_b32 v54, 0\n"
"v_mov_b32 v55, 0\n"
"v_mov_b32 v120, 0\n"
"v_mov_b32 v121, 0\n"
"v_mov_b32 v122, 0\n"
"v_mov_b32 v123, 0\n"
"v_mov_b32 v124, 0\n"
"v_mov_b32 v125, 0\n"
"v_mov_b32 v126, 0\n"
"v_mov_b32 v127, 0\n"
"s_add_u32 s70, s69, 0\n"
"s_lshr_b32 s71, s70, 8\n"
"s_xor_b32 s71, s71, s54\n"
"s_and_b32 s71, s71, 0x2000\n"
"s_add_u32 s70, s70, s71\n"
"s_add_u32 s55, s70, s53\n"
"s_add_u32 s70, s69, 131072\n"
"s_lshr_b32 s71, s70, 8\n"
"s_xor_b32 s71, s71, s54\n"
"s_and_b32 s71, s71, 0x2000\n"
"s_add_u32 s70, s70, s71\n"
"s_add_u32 s56, s70, s53\n"
"s_add_u32 s70, s69, 262144\n"
"s_lshr_b32 s71, s70, 8\n"
"s_xor_b32 s71, s71, s54\n"
"s_and_b32 s71, s71, 0x2000\n"
"s_add_u32 s70, s70, s71\n"
"s_add_u32 s57, s70, s53\n"
"s_add_u32 s70, s69, 393216\n"
"s_lshr_b32 s71, s70, 8\n"
"s_xor_b32 s71, s71, s54\n"
"s_and_b32 s71, s71, 0x2000\n"
"s_add_u32 s70, s70, s71\n"
"s_add_u32 s58, s70, s53\n"
"s_add_u32 s70, s69, 524288\n"
"s_lshr_b32 s71, s70, 8\n"
"s_xor_b32 s71, s71, s54\n"
"s_and_b32 s71, s71, 0x2000\n"
"s_add_u32 s70, s70, s71\n"
"s_add_u32 s59, s70, s53\n"
"s_branch V12_%=\n"
"V14_%=:\n"
"s_waitcnt lgkmcnt(0)\n"
"s_mov_b32 s76, 131072\n"
"V15_%=:\n"
"s_memrealtime s[4:5]\n"
"buffer_load_dwordx4 v[56:59], v0, s[36:39], s60 offen offset:0 sc1\n"
"buffer_load_dwordx4 v[60:63], v0, s[36:39], s61 offen offset:0 sc1\n"
"buffer_load_dwordx4 v[64:67], v0, s[36:39], s62 offen offset:0 sc1\n"
"v_mov_b32 v29, 0\n"
"ds_read_b32 v31, v29 offset:16896\n"
"s_lshl_b32 s77, s73, 14\n"
"s_add_u32 s77, s77, s20\n"
"s_add_u32 s77, s77, s21\n"
"s_add_u32 s77, s77, 4096\n"
"v_mov_b32 v32, 0\n"
"v_mov_b32 v33, 0\n"
"v_mov_b32 v34, 0\n"
"v_mov_b32 v35, 0\n"
"v_mov_b32 v36, 0\n"
"v_mov_b32 v37, 0\n"
"v_mov_b32 v38, 0\n"
"v_mov_b32 v39, 0\n"
"v_mov_b32 v40, 0\n"
"v_mov_b32 v41, 0\n"
"v_mov_b32 v42, 0\n"
"v_mov_b32 v43, 0\n"
"s_sub_u32 s76, s76, 1\n"
"s_cbranch_scc1 VABORT_%=\n"
"s_waitcnt vmcnt(2)\n"
"v_max3_u32 v29, v56, v57, 0\n"
"v_max3_u32 v29, v58, v59, v29\n"
"s_waitcnt vmcnt(1)\n"
"v_max3_u32 v29, v60, v61, v29\n"
"v_max3_u32 v29, v62, v63, v29\n"
"s_waitcnt vmcnt(0)\n"
"v_max3_u32 v29, v64, v65, v29\n"
"v_max3_u32 v29, v66, v67, v29\n"
"v_cmp_eq_u32 vcc, -1, v29\n"
"s_nop 0\n"
"s_cbranch_vccnz V15_%=\n"
"s_waitcnt lgkmcnt(0)\n"
"v_lshl_add_u32 v31, s4, 3, v31\n"
"v_lshrrev_b32 v30, 2, v0\n"
"s_mov_b64 exec, 0xffff\n"
"buffer_store_dword v31, v30, s[36:39], s77 offen sc1\n"
"s_mov_b64 exec, -1\n"
"s_branch V13_%=\n"
"V11_%=:\n"
"s_waitcnt vmcnt(0)\n"
"s_barrier\n"
"s_branch V5_%=\n"
"V2_%=:\n"
"s_mov_b32 s22, 46301184\n"
"s_mov_b32 s23, 46317568\n"
"s_cmp_lg_u32 s66, 0\n"
"s_cbranch_scc1 V16_%=\n"
"v_mov_b32 v24, 0\n"
"s_mov_b64 exec, 1\n"
"buffer_atomic_add v24, v26, s[36:39], s23 offen sc0 sc1\n"
"s_mov_b64 exec, -1\n"
"V16_%=:\n"
"s_add_u32 m0, s50, 0\n"
"s_add_u32 s72, s48, 0\n"
"buffer_load_dwordx4 v1, s[40:43], s72 offen lds\n"
"s_add_u32 m0, s50, 1024\n"
"s_add_u32 s72, s48, 98304\n"
"buffer_load_dwordx4 v2, s[40:43], s72 offen lds\n"
"s_add_u32 m0, s50, 2048\n"
"s_add_u32 s72, s48, 196608\n"
"buffer_load_dwordx4 v1, s[40:43], s72 offen lds\n"
"s_add_u32 m0, s50, 3072\n"
"s_add_u32 s72, s48, 294912\n"
"buffer_load_dwordx4 v2, s[40:43], s72 offen lds\n"
"s_add_u32 m0, s50, 16384\n"
"s_add_u32 s72, s48, 128\n"
"buffer_load_dwordx4 v1, s[40:43], s72 offen lds\n"
"s_add_u32 m0, s50, 17408\n"
"s_add_u32 s72, s48, 98432\n"
"buffer_load_dwordx4 v2, s[40:43], s72 offen lds\n"
"s_add_u32 m0, s50, 18432\n"
"s_add_u32 s72, s48, 196736\n"
"buffer_load_dwordx4 v1, s[40:43], s72 offen lds\n"
"s_add_u32 m0, s50, 19456\n"
"s_add_u32 s72, s48, 295040\n"
"buffer_load_dwordx4 v2, s[40:43], s72 offen lds\n"
"s_add_u32 m0, s50, 32768\n"
"s_add_u32 s72, s48, 256\n"
"buffer_load_dwordx4 v1, s[40:43], s72 offen lds\n"
"s_add_u32 m0, s50, 33792\n"
"s_add_u32 s72, s48, 98560\n"
"buffer_load_dwordx4 v2, s[40:43], s72 offen lds\n"
"s_add_u32 m0, s50, 34816\n"
"s_add_u32 s72, s48, 196864\n"
"buffer_load_dwordx4 v1, s[40:43], s72 offen lds\n"
"s_add_u32 m0, s50, 35840\n"
"s_add_u32 s72, s48, 295168\n"
"buffer_load_dwordx4 v2, s[40:43], s72 offen lds\n"
"s_add_u32 m0, s50, 49152\n"
"s_add_u32 s72, s48, 1572864\n"
"buffer_load_dwordx4 v1, s[40:43], s72 offen lds\n"
"s_add_u32 m0, s50, 50176\n"
"s_add_u32 s72, s48, 1671168\n"
"buffer_load_dwordx4 v2, s[40:43], s72 offen lds\n"
"s_add_u32 m0, s50, 51200\n"
"s_add_u32 s72, s48, 1769472\n"
"buffer_load_dwordx4 v1, s[40:43], s72 offen lds\n"
"s_add_u32 m0, s50, 52224\n"
"s_add_u32 s72, s48, 1867776\n"
"buffer_load_dwordx4 v2, s[40:43], s72 offen lds\n"
"s_add_u32 m0, s50, 65536\n"
"s_add_u32 s72, s48, 1572992\n"
"buffer_load_dwordx4 v1, s[40:43], s72 offen lds\n"
"s_add_u32 m0, s50, 66560\n"
"s_add_u32 s72, s48, 1671296\n"
"buffer_load_dwordx4 v2, s[40:43], s72 offen lds\n"
"s_add_u32 m0, s50, 67584\n"
"s_add_u32 s72, s48, 1769600\n"
"buffer_load_dwordx4 v1, s[40:43], s72 offen lds\n"
"s_add_u32 m0, s50, 68608\n"
"s_add_u32 s72, s48, 1867904\n"
"buffer_load_dwordx4 v2, s[40:43], s72 offen lds\n"
"s_add_u32 m0, s50, 81920\n"
"s_add_u32 s72, s48, 1573120\n"
"buffer_load_dwordx4 v1, s[40:43], s72 offen lds\n"
"s_add_u32 m0, s50, 82944\n"
"s_add_u32 s72, s48, 1671424\n"
"buffer_load_dwordx4 v2, s[40:43], s72 offen lds\n"
"s_add_u32 m0, s50, 83968\n"
"s_add_u32 s72, s48, 1769728\n"
"buffer_load_dwordx4 v1, s[40:43], s72 offen lds\n"
"s_add_u32 m0, s50, 84992\n"
"s_add_u32 s72, s48, 1868032\n"
"buffer_load_dwordx4 v2, s[40:43], s72 offen lds\n"
"s_waitcnt vmcnt(12)\n"
"s_waitcnt lgkmcnt(0)\n"
"s_barrier\n"
"s_cmp_lg_u32 s66, 0\n"
"s_cbranch_scc1 V17_%=\n"
"v_or_b32 v24, 1, v24\n"
"v_add_u32 v24, 1, v24\n"
"buffer_store_dword v24, v26, s[36:39], s22 offen sc1\n"
"V17_%=:\n"
"s_add_u32 m0, s50, 98304\n"
"s_add_u32 s72, s48, 3145728\n"
"buffer_load_dwordx4 v1, s[40:43], s72 offen lds\n"
"s_add_u32 m0, s50, 99328\n"
"s_add_u32 s72, s48, 3244032\n"
"buffer_load_dwordx4 v2, s[40:43], s72 offen lds\n"
"s_add_u32 m0, s50, 100352\n"
"s_add_u32 s72, s48, 3342336\n"
"buffer_load_dwordx4 v1, s[40:43], s72 offen lds\n"
"s_add_u32 m0, s50, 101376\n"
"s_add_u32 s72, s48, 3440640\n"
"buffer_load_dwordx4 v2, s[40:43], s72 offen lds\n"
"s_add_u32 m0, s50, 114688\n"
"s_add_u32 s72, s48, 3145856\n"
"buffer_load_dwordx4 v1, s[40:43], s72 offen lds\n"
"s_add_u32 m0, s50, 115712\n"
"s_add_u32 s72, s48, 3244160\n"
"buffer_load_dwordx4 v2, s[40:43], s72 offen lds\n"
"s_add_u32 m0, s50, 116736\n"
"s_add_u32 s72, s48, 3342464\n"
"buffer_load_dwordx4 v1, s[40:43], s72 offen lds\n"
"s_add_u32 m0, s50, 117760\n"
"s_add_u32 s72, s48, 3440768\n"
"buffer_load_dwordx4 v2, s[40:43], s72 offen lds\n"
"s_add_u32 m0, s50, 131072\n"
"s_add_u32 s72, s48, 3145984\n"
"buffer_load_dwordx4 v1, s[40:43], s72 offen lds\n"
"s_add_u32 m0, s50, 132096\n"
"s_add_u32 s72, s48, 3244288\n"
"buffer_load_dwordx4 v2, s[40:43], s72 offen lds\n"
"s_add_u32 m0, s50, 133120\n"
"s_add_u32 s72, s48, 3342592\n"
"buffer_load_dwordx4 v1, s[40:43], s72 offen lds\n"
"s_add_u32 m0, s50, 134144\n"
"s_add_u32 s72, s48, 3440896\n"
"buffer_load_dwordx4 v2, s[40:43], s72 offen lds\n"
"v_add_u32 v20, 0, v3\n"
"v_add_u32 v21, 0, v4\n"
"v_add_u32 v22, 0, v5\n"
"ds_read_b64_tr_b16 v[184:185], v20 offset:0\n"
"ds_read_b64_tr_b16 v[186:187], v20 offset:256\n"
"ds_read_b64_tr_b16 v[188:189], v20 offset:4096\n"
"ds_read_b64_tr_b16 v[190:191], v20 offset:4352\n"
"ds_read_b64_tr_b16 v[240:241], v21 offset:0\n"
"ds_read_b64_tr_b16 v[242:243], v21 offset:256\n"
"ds_read_b64_tr_b16 v[244:245], v21 offset:4096\n"
"ds_read_b64_tr_b16 v[246:247], v21 offset:4352\n"
"ds_read_b64_tr_b16 a[32:33], v22 offset:0\n"
"ds_read_b64_tr_b16 a[34:35], v22 offset:256\n"
"ds_read_b64_tr_b16 a[36:37], v22 offset:4096\n"
"ds_read_b64_tr_b16 a[38:39], v22 offset:4352\n"
"ds_read_b64_tr_b16 v[192:193], v20 offset:8192\n"
"ds_read_b64_tr_b16 v[194:195], v20 offset:8448\n"
"ds_read_b64_tr_b16 v[196:197], v20 offset:12288\n"
"ds_read_b64_tr_b16 v[198:199], v20 offset:12544\n"
"ds_read_b64_tr_b16 v[248:249], v21 offset:8192\n"
"ds_read_b64_tr_b16 v[250:251], v21 offset:8448\n"
"ds_read_b64_tr_b16 v[252:253], v21 offset:12288\n"
"ds_read_b64_tr_b16 v[254:255], v21 offset:12544\n"
"ds_read_b64_tr_b16 a[40:41], v22 offset:8192\n"
"ds_read_b64_tr_b16 a[42:43], v22 offset:8448\n"
"ds_read_b64_tr_b16 a[44:45], v22 offset:12288\n"
"ds_read_b64_tr_b16 a[46:47], v22 offset:12544\n"
"s_waitcnt vmcnt(12)\n"
"s_waitcnt lgkmcnt(0)\n"
"s_barrier\n"
"s_add_u32 m0, s50, 0\n"
"s_add_u32 s72, s48, 4718592\n"
"buffer_load_dwordx4 v1, s[40:43], s72 offen lds\n"
"s_add_u32 m0, s50, 1024\n"
"s_add_u32 s72, s48, 4816896\n"
"buffer_load_dwordx4 v2, s[40:43], s72 offen lds\n"
"s_add_u32 m0, s50, 2048\n"
"s_add_u32 s72, s48, 4915200\n"
"buffer_load_dwordx4 v1, s[40:43], s72 offen lds\n"
"s_add_u32 m0, s50, 3072\n"
"s_add_u32 s72, s48, 5013504\n"
"buffer_load_dwordx4 v2, s[40:43], s72 offen lds\n"
"s_add_u32 m0, s50, 16384\n"
"s_add_u32 s72, s48, 4718720\n"
"buffer_load_dwordx4 v1, s[40:43], s72 offen lds\n"
"s_add_u32 m0, s50, 17408\n"
"s_add_u32 s72, s48, 4817024\n"
"buffer_load_dwordx4 v2, s[40:43], s72 offen lds\n"
"s_add_u32 m0, s50, 18432\n"
"s_add_u32 s72, s48, 4915328\n"
"buffer_load_dwordx4 v1, s[40:43], s72 offen lds\n"
"s_add_u32 m0, s50, 19456\n"
"s_add_u32 s72, s48, 5013632\n"
"buffer_load_dwordx4 v2, s[40:43], s72 offen lds\n"
"s_add_u32 m0, s50, 32768\n"
"s_add_u32 s72, s48, 4718848\n"
"buffer_load_dwordx4 v1, s[40:43], s72 offen lds\n"
"s_add_u32 m0, s50, 33792\n"
"s_add_u32 s72, s48, 4817152\n"
"buffer_load_dwordx4 v2, s[40:43], s72 offen lds\n"
"s_add_u32 m0, s50, 34816\n"
"s_add_u32 s72, s48, 4915456\n"
"buffer_load_dwordx4 v1, s[40:43], s72 offen lds\n"
"s_add_u32 m0, s50, 35840\n"
"s_add_u32 s72, s48, 5013760\n"
"buffer_load_dwordx4 v2, s[40:43], s72 offen lds\n"
"v_add_u32 v20, 49152, v3\n"
"v_add_u32 v21, 49152, v4\n"
"v_add_u32 v22, 49152, v5\n"
"ds_read_b64_tr_b16 v[200:201], v20 offset:0\n"
"ds_read_b64_tr_b16 v[202:203], v20 offset:256\n"
"ds_read_b64_tr_b16 v[204:205], v20 offset:4096\n"
"ds_read_b64_tr_b16 v[206:207], v20 offset:4352\n"
"ds_read_b64_tr_b16 a[0:1], v21 offset:0\n"
"ds_read_b64_tr_b16 a[2:3], v21 offset:256\n"
"ds_read_b64_tr_b16 a[4:5], v21 offset:4096\n"
"ds_read_b64_tr_b16 a[6:7], v21 offset:4352\n"
"ds_read_b64_tr_b16 a[48:49], v22 offset:0\n"
"ds_read_b64_tr_b16 a[50:51], v22 offset:256\n"
"ds_read_b64_tr_b16 a[52:53], v22 offset:4096\n"
"ds_read_b64_tr_b16 a[54:55], v22 offset:4352\n"
"ds_read_b64_tr_b16 v[32:33], v20 offset:8192\n"
"ds_read_b64_tr_b16 v[34:35], v20 offset:8448\n"
"ds_read_b64_tr_b16 v[36:37], v20 offset:12288\n"
"ds_read_b64_tr_b16 v[38:39], v20 offset:12544\n"
"ds_read_b64_tr_b16 v[40:41], v21 offset:8192\n"
"ds_read_b64_tr_b16 v[42:43], v21 offset:8448\n"
"ds_read_b64_tr_b16 v[44:45], v21 offset:12288\n"
"ds_read_b64_tr_b16 v[46:47], v21 offset:12544\n"
"ds_read_b64_tr_b16 v[48:49], v22 offset:8192\n"
"ds_read_b64_tr_b16 v[50:51], v22 offset:8448\n"
"ds_read_b64_tr_b16 v[52:53], v22 offset:12288\n"
"ds_read_b64_tr_b16 v[54:55], v22 offset:12544\n"
"s_waitcnt vmcnt(12)\n"
"s_waitcnt lgkmcnt(0)\n"
"s_barrier\n"
"s_add_u32 m0, s50, 49152\n"
"s_add_u32 s72, s48, 6291456\n"
"buffer_load_dwordx4 v1, s[40:43], s72 offen lds\n"
"s_add_u32 m0, s50, 50176\n"
"s_add_u32 s72, s48, 6389760\n"
"buffer_load_dwordx4 v2, s[40:43], s72 offen lds\n"
"s_add_u32 m0, s50, 51200\n"
"s_add_u32 s72, s48, 6488064\n"
"buffer_load_dwordx4 v1, s[40:43], s72 offen lds\n"
"s_add_u32 m0, s50, 52224\n"
"s_add_u32 s72, s48, 6586368\n"
"buffer_load_dwordx4 v2, s[40:43], s72 offen lds\n"
"s_add_u32 m0, s50, 65536\n"
"s_add_u32 s72, s48, 6291584\n"
"buffer_load_dwordx4 v1, s[40:43], s72 offen lds\n"
"s_add_u32 m0, s50, 66560\n"
"s_add_u32 s72, s48, 6389888\n"
"buffer_load_dwordx4 v2, s[40:43], s72 offen lds\n"
"s_add_u32 m0, s50, 67584\n"
"s_add_u32 s72, s48, 6488192\n"
"buffer_load_dwordx4 v1, s[40:43], s72 offen lds\n"
"s_add_u32 m0, s50, 68608\n"
"s_add_u32 s72, s48, 6586496\n"
"buffer_load_dwordx4 v2, s[40:43], s72 offen lds\n"
"s_add_u32 m0, s50, 81920\n"
"s_add_u32 s72, s48, 6291712\n"
"buffer_load_dwordx4 v1, s[40:43], s72 offen lds\n"
"s_add_u32 m0, s50, 82944\n"
"s_add_u32 s72, s48, 6390016\n"
"buffer_load_dwordx4 v2, s[40:43], s72 offen lds\n"
"s_add_u32 m0, s50, 83968\n"
"s_add_u32 s72, s48, 6488320\n"
"buffer_load_dwordx4 v1, s[40:43], s72 offen lds\n"
"s_add_u32 m0, s50, 84992\n"
"s_add_u32 s72, s48, 6586624\n"
"buffer_load_dwordx4 v2, s[40:43], s72 offen lds\n"
"v_add_u32 v20, 98304, v3\n"
"v_add_u32 v21, 98304, v4\n"
"v_add_u32 v22, 98304, v5\n"
"ds_read_b64_tr_b16 v[56:57], v20 offset:0\n"
"ds_read_b64_tr_b16 v[58:59], v20 offset:256\n"
"ds_read_b64_tr_b16 v[60:61], v20 offset:4096\n"
"ds_read_b64_tr_b16 v[62:63], v20 offset:4352\n"
"ds_read_b64_tr_b16 v[64:65], v21 offset:0\n"
"ds_read_b64_tr_b16 v[66:67], v21 offset:256\n"
"ds_read_b64_tr_b16 v[68:69], v21 offset:4096\n"
"ds_read_b64_tr_b16 v[70:71], v21 offset:4352\n"
"ds_read_b64_tr_b16 v[72:73], v22 offset:0\n"
"ds_read_b64_tr_b16 v[74:75], v22 offset:256\n"
"ds_read_b64_tr_b16 v[76:77], v22 offset:4096\n"
"ds_read_b64_tr_b16 v[78:79], v22 offset:4352\n"
"ds_read_b64_tr_b16 v[128:129], v20 offset:8192\n"
"ds_read_b64_tr_b16 v[130:131], v20 offset:8448\n"
"ds_read_b64_tr_b16 v[132:133], v20 offset:12288\n"
"ds_read_b64_tr_b16 v[134:135], v20 offset:12544\n"
"ds_read_b64_tr_b16 v[136:137], v21 offset:8192\n"
"ds_read_b64_tr_b16 v[138:139], v21 offset:8448\n"
"ds_read_b64_tr_b16 v[140:141], v21 offset:12288\n"
"ds_read_b64_tr_b16 v[142:143], v21 offset:12544\n"
"ds_read_b64_tr_b16 v[144:145], v22 offset:8192\n"
"ds_read_b64_tr_b16 v[146:147], v22 offset:8448\n"
"ds_read_b64_tr_b16 v[148:149], v22 offset:12288\n"
"ds_read_b64_tr_b16 v[150:151], v22 offset:12544\n"
"s_waitcnt vmcnt(12)\n"
"s_waitcnt lgkmcnt(0)\n"
"s_barrier\n"
"s_add_u32 m0, s52, 98304\n"
"s_add_u32 s72, s51, 0\n"
"buffer_load_dwordx4 v7, s[44:47], s72 offen lds\n"
"s_add_u32 m0, s52, 99328\n"
"s_add_u32 s72, s51, 10240\n"
"buffer_load_dwordx4 v7, s[44:47], s72 offen lds\n"
"s_add_u32 m0, s52, 100352\n"
"s_add_u32 s72, s51, 20480\n"
"buffer_load_dwordx4 v7, s[44:47], s72 offen lds\n"
"s_add_u32 m0, s52, 101376\n"
"s_add_u32 s72, s51, 30720\n"
"buffer_load_dwordx4 v7, s[44:47], s72 offen lds\n"
"s_add_u32 m0, s52, 102400\n"
"s_add_u32 s72, s51, 40960\n"
"buffer_load_dwordx4 v7, s[44:47], s72 offen lds\n"
"s_add_u32 m0, s52, 103424\n"
"s_add_u32 s72, s51, 51200\n"
"buffer_load_dwordx4 v7, s[44:47], s72 offen lds\n"
"s_add_u32 m0, s52, 104448\n"
"s_add_u32 s72, s51, 61440\n"
"buffer_load_dwordx4 v7, s[44:47], s72 offen lds\n"
"s_add_u32 m0, s52, 105472\n"
"s_add_u32 s72, s51, 71680\n"
"buffer_load_dwordx4 v7, s[44:47], s72 offen lds\n"
"s_add_u32 m0, s52, 106496\n"
"s_add_u32 s72, s51, 81920\n"
"buffer_load_dwordx4 v7, s[44:47], s72 offen lds\n"
"s_add_u32 m0, s52, 107520\n"
"s_add_u32 s72, s51, 92160\n"
"buffer_load_dwordx4 v7, s[44:47], s72 offen lds\n"
"s_add_u32 m0, s52, 108544\n"
"s_add_u32 s72, s51, 102400\n"
"buffer_load_dwordx4 v7, s[44:47], s72 offen lds\n"
"s_add_u32 m0, s52, 109568\n"
"s_add_u32 s72, s51, 112640\n"
"buffer_load_dwordx4 v7, s[44:47], s72 offen lds\n"
"v_add_u32 v20, 0, v3\n"
"v_add_u32 v21, 0, v4\n"
"v_add_u32 v22, 0, v5\n"
"ds_read_b64_tr_b16 v[152:153], v20 offset:0\n"
"ds_read_b64_tr_b16 v[154:155], v20 offset:256\n"
"ds_read_b64_tr_b16 v[156:157], v20 offset:4096\n"
"ds_read_b64_tr_b16 v[158:159], v20 offset:4352\n"
"ds_read_b64_tr_b16 v[208:209], v21 offset:0\n"
"ds_read_b64_tr_b16 v[210:211], v21 offset:256\n"
"ds_read_b64_tr_b16 v[212:213], v21 offset:4096\n"
"ds_read_b64_tr_b16 v[214:215], v21 offset:4352\n"
"ds_read_b64_tr_b16 a[8:9], v22 offset:0\n"
"ds_read_b64_tr_b16 a[10:11], v22 offset:256\n"
"ds_read_b64_tr_b16 a[12:13], v22 offset:4096\n"
"ds_read_b64_tr_b16 a[14:15], v22 offset:4352\n"
"ds_read_b64_tr_b16 v[160:161], v20 offset:8192\n"
"ds_read_b64_tr_b16 v[162:163], v20 offset:8448\n"
"ds_read_b64_tr_b16 v[164:165], v20 offset:12288\n"
"ds_read_b64_tr_b16 v[166:167], v20 offset:12544\n"
"ds_read_b64_tr_b16 v[216:217], v21 offset:8192\n"
"ds_read_b64_tr_b16 v[218:219], v21 offset:8448\n"
"ds_read_b64_tr_b16 v[220:221], v21 offset:12288\n"
"ds_read_b64_tr_b16 v[222:223], v21 offset:12544\n"
"ds_read_b64_tr_b16 v[80:81], v22 offset:8192\n"
"ds_read_b64_tr_b16 v[82:83], v22 offset:8448\n"
"ds_read_b64_tr_b16 v[84:85], v22 offset:12288\n"
"ds_read_b64_tr_b16 v[86:87], v22 offset:12544\n"
"s_waitcnt vmcnt(12)\n"
"s_waitcnt lgkmcnt(0)\n"
"s_barrier\n"
"s_add_u32 m0, s52, 0\n"
"s_add_u32 s72, s51, 256\n"
"buffer_load_dwordx4 v7, s[44:47], s72 offen lds\n"
"s_add_u32 m0, s52, 1024\n"
"s_add_u32 s72, s51, 10496\n"
"buffer_load_dwordx4 v7, s[44:47], s72 offen lds\n"
"s_add_u32 m0, s52, 2048\n"
"s_add_u32 s72, s51, 20736\n"
"buffer_load_dwordx4 v7, s[44:47], s72 offen lds\n"
"s_add_u32 m0, s52, 3072\n"
"s_add_u32 s72, s51, 30976\n"
"buffer_load_dwordx4 v7, s[44:47], s72 offen lds\n"
"s_add_u32 m0, s52, 4096\n"
"s_add_u32 s72, s51, 41216\n"
"buffer_load_dwordx4 v7, s[44:47], s72 offen lds\n"
"s_add_u32 m0, s52, 5120\n"
"s_add_u32 s72, s51, 51456\n"
"buffer_load_dwordx4 v7, s[44:47], s72 offen lds\n"
"s_add_u32 m0, s52, 6144\n"
"s_add_u32 s72, s51, 61696\n"
"buffer_load_dwordx4 v7, s[44:47], s72 offen lds\n"
"s_add_u32 m0, s52, 7168\n"
"s_add_u32 s72, s51, 71936\n"
"buffer_load_dwordx4 v7, s[44:47], s72 offen lds\n"
"s_add_u32 m0, s52, 8192\n"
"s_add_u32 s72, s51, 82176\n"
"buffer_load_dwordx4 v7, s[44:47], s72 offen lds\n"
"s_add_u32 m0, s52, 9216\n"
"s_add_u32 s72, s51, 92416\n"
"buffer_load_dwordx4 v7, s[44:47], s72 offen lds\n"
"s_add_u32 m0, s52, 10240\n"
"s_add_u32 s72, s51, 102656\n"
"buffer_load_dwordx4 v7, s[44:47], s72 offen lds\n"
"s_add_u32 m0, s52, 11264\n"
"s_add_u32 s72, s51, 112896\n"
"buffer_load_dwordx4 v7, s[44:47], s72 offen lds\n"
"v_add_u32 v20, 49152, v3\n"
"v_add_u32 v21, 49152, v4\n"
"v_add_u32 v22, 49152, v5\n"
"ds_read_b64_tr_b16 v[168:169], v20 offset:0\n"
"ds_read_b64_tr_b16 v[170:171], v20 offset:256\n"
"ds_read_b64_tr_b16 v[172:173], v20 offset:4096\n"
"ds_read_b64_tr_b16 v[174:175], v20 offset:4352\n"
"ds_read_b64_tr_b16 v[224:225], v21 offset:0\n"
"ds_read_b64_tr_b16 v[226:227], v21 offset:256\n"
"ds_read_b64_tr_b16 v[228:229], v21 offset:4096\n"
"ds_read_b64_tr_b16 v[230:231], v21 offset:4352\n"
"ds_read_b64_tr_b16 a[16:17], v22 offset:0\n"
"ds_read_b64_tr_b16 a[18:19], v22 offset:256\n"
"ds_read_b64_tr_b16 a[20:21], v22 offset:4096\n"
"ds_read_b64_tr_b16 a[22:23], v22 offset:4352\n"
"ds_read_b64_tr_b16 v[176:177], v20 offset:8192\n"
"ds_read_b64_tr_b16 v[178:179], v20 offset:8448\n"
"ds_read_b64_tr_b16 v[180:181], v20 offset:12288\n"
"ds_read_b64_tr_b16 v[182:183], v20 offset:12544\n"
"ds_read_b64_tr_b16 v[232:233], v21 offset:8192\n"
"ds_read_b64_tr_b16 v[234:235], v21 offset:8448\n"
"ds_read_b64_tr_b16 v[236:237], v21 offset:12288\n"
"ds_read_b64_tr_b16 v[238:239], v21 offset:12544\n"
"ds_read_b64_tr_b16 a[24:25], v22 offset:8192\n"
"ds_read_b64_tr_b16 a[26:27], v22 offset:8448\n"
"ds_read_b64_tr_b16 a[28:29], v22 offset:12288\n"
"ds_read_b64_tr_b16 a[30:31], v22 offset:12544\n"
"s_waitcnt vmcnt(12)\n"
"s_waitcnt lgkmcnt(0)\n"
"s_barrier\n"
"s_add_u32 m0, s52, 49152\n"
"s_add_u32 s72, s51, 512\n"
"buffer_load_dwordx4 v7, s[44:47], s72 offen lds\n"
"s_add_u32 m0, s52, 50176\n"
"s_add_u32 s72, s51, 10752\n"
"buffer_load_dwordx4 v7, s[44:47], s72 offen lds\n"
"s_add_u32 m0, s52, 51200\n"
"s_add_u32 s72, s51, 20992\n"
"buffer_load_dwordx4 v7, s[44:47], s72 offen lds\n"
"s_add_u32 m0, s52, 52224\n"
"s_add_u32 s72, s51, 31232\n"
"buffer_load_dwordx4 v7, s[44:47], s72 offen lds\n"
"s_add_u32 m0, s52, 53248\n"
"s_add_u32 s72, s51, 41472\n"
"buffer_load_dwordx4 v7, s[44:47], s72 offen lds\n"
"s_add_u32 m0, s52, 54272\n"
"s_add_u32 s72, s51, 51712\n"
"buffer_load_dwordx4 v7, s[44:47], s72 offen lds\n"
"s_add_u32 m0, s52, 55296\n"
"s_add_u32 s72, s51, 61952\n"
"buffer_load_dwordx4 v7, s[44:47], s72 offen lds\n"
"s_add_u32 m0, s52, 56320\n"
"s_add_u32 s72, s51, 72192\n"
"buffer_load_dwordx4 v7, s[44:47], s72 offen lds\n"
"s_add_u32 m0, s52, 57344\n"
"s_add_u32 s72, s51, 82432\n"
"buffer_load_dwordx4 v7, s[44:47], s72 offen lds\n"
"s_add_u32 m0, s52, 58368\n"
"s_add_u32 s72, s51, 92672\n"
"buffer_load_dwordx4 v7, s[44:47], s72 offen lds\n"
"s_add_u32 m0, s52, 59392\n"
"s_add_u32 s72, s51, 102912\n"
"buffer_load_dwordx4 v7, s[44:47], s72 offen lds\n"
"s_add_u32 m0, s52, 60416\n"
"s_add_u32 s72, s51, 113152\n"
"buffer_load_dwordx4 v7, s[44:47], s72 offen lds\n"
"v_add_u32 v20, 98304, v8\n"
"v_add_u32 v21, 98304, v9\n"
"ds_read_b64_tr_b16 v[88:89], v20 offset:0\n"
"ds_read_b64_tr_b16 v[90:91], v21 offset:0\n"
"ds_read_b64_tr_b16 v[92:93], v20 offset:6144\n"
"ds_read_b64_tr_b16 v[94:95], v21 offset:6144\n"
"ds_read_b64_tr_b16 a[80:81], v20 offset:12288\n"
"ds_read_b64_tr_b16 a[82:83], v21 offset:12288\n"
"ds_read_b64_tr_b16 a[84:85], v20 offset:18432\n"
"ds_read_b64_tr_b16 a[86:87], v21 offset:18432\n"
"ds_read_b64_tr_b16 a[104:105], v20 offset:2048\n"
"ds_read_b64_tr_b16 a[106:107], v21 offset:2048\n"
"ds_read_b64_tr_b16 a[108:109], v20 offset:8192\n"
"ds_read_b64_tr_b16 a[110:111], v21 offset:8192\n"
"ds_read_b64_tr_b16 a[128:129], v20 offset:14336\n"
"ds_read_b64_tr_b16 a[130:131], v21 offset:14336\n"
"ds_read_b64_tr_b16 a[132:133], v20 offset:20480\n"
"ds_read_b64_tr_b16 a[134:135], v21 offset:20480\n"
"ds_read_b64_tr_b16 a[152:153], v20 offset:4096\n"
"ds_read_b64_tr_b16 a[154:155], v21 offset:4096\n"
"ds_read_b64_tr_b16 a[156:157], v20 offset:10240\n"
"ds_read_b64_tr_b16 a[158:159], v21 offset:10240\n"
"ds_read_b64_tr_b16 a[160:161], v20 offset:16384\n"
"ds_read_b64_tr_b16 a[162:163], v21 offset:16384\n"
"ds_read_b64_tr_b16 a[164:165], v20 offset:22528\n"
"ds_read_b64_tr_b16 a[166:167], v21 offset:22528\n"
"s_waitcnt vmcnt(12)\n"
"s_waitcnt lgkmcnt(0)\n"
"s_barrier\n"
"s_add_u32 m0, s52, 98304\n"
"s_add_u32 s72, s51, 768\n"
"buffer_load_dwordx4 v7, s[44:47], s72 offen lds\n"
"s_add_u32 m0, s52, 99328\n"
"s_add_u32 s72, s51, 11008\n"
"buffer_load_dwordx4 v7, s[44:47], s72 offen lds\n"
"s_add_u32 m0, s52, 100352\n"
"s_add_u32 s72, s51, 21248\n"
"buffer_load_dwordx4 v7, s[44:47], s72 offen lds\n"
"s_add_u32 m0, s52, 101376\n"
"s_add_u32 s72, s51, 31488\n"
"buffer_load_dwordx4 v7, s[44:47], s72 offen lds\n"
"s_add_u32 m0, s52, 102400\n"
"s_add_u32 s72, s51, 41728\n"
"buffer_load_dwordx4 v7, s[44:47], s72 offen lds\n"
"s_add_u32 m0, s52, 103424\n"
"s_add_u32 s72, s51, 51968\n"
"buffer_load_dwordx4 v7, s[44:47], s72 offen lds\n"
"s_add_u32 m0, s52, 104448\n"
"s_add_u32 s72, s51, 62208\n"
"buffer_load_dwordx4 v7, s[44:47], s72 offen lds\n"
"s_add_u32 m0, s52, 105472\n"
"s_add_u32 s72, s51, 72448\n"
"buffer_load_dwordx4 v7, s[44:47], s72 offen lds\n"
"s_add_u32 m0, s52, 106496\n"
"s_add_u32 s72, s51, 82688\n"
"buffer_load_dwordx4 v7, s[44:47], s72 offen lds\n"
"s_add_u32 m0, s52, 107520\n"
"s_add_u32 s72, s51, 92928\n"
"buffer_load_dwordx4 v7, s[44:47], s72 offen lds\n"
"s_add_u32 m0, s52, 108544\n"
"s_add_u32 s72, s51, 103168\n"
"buffer_load_dwordx4 v7, s[44:47], s72 offen lds\n"
"s_add_u32 m0, s52, 109568\n"
"s_add_u32 s72, s51, 113408\n"
"buffer_load_dwordx4 v7, s[44:47], s72 offen lds\n"
"v_add_u32 v20, 0, v8\n"
"v_add_u32 v21, 0, v9\n"
"ds_read_b64_tr_b16 a[56:57], v20 offset:0\n"
"ds_read_b64_tr_b16 a[58:59], v21 offset:0\n"
"ds_read_b64_tr_b16 a[60:61], v20 offset:6144\n"
"ds_read_b64_tr_b16 a[62:63], v21 offset:6144\n"
"ds_read_b64_tr_b16 a[88:89], v20 offset:12288\n"
"ds_read_b64_tr_b16 a[90:91], v21 offset:12288\n"
"ds_read_b64_tr_b16 a[92:93], v20 offset:18432\n"
"ds_read_b64_tr_b16 a[94:95], v21 offset:18432\n"
"ds_read_b64_tr_b16 a[112:113], v20 offset:2048\n"
"ds_read_b64_tr_b16 a[114:115], v21 offset:2048\n"
"ds_read_b64_tr_b16 a[116:117], v20 offset:8192\n"
"ds_read_b64_tr_b16 a[118:119], v21 offset:8192\n"
"ds_read_b64_tr_b16 a[136:137], v20 offset:14336\n"
"ds_read_b64_tr_b16 a[138:139], v21 offset:14336\n"
"ds_read_b64_tr_b16 a[140:141], v20 offset:20480\n"
"ds_read_b64_tr_b16 a[142:143], v21 offset:20480\n"
"ds_read_b64_tr_b16 v[104:105], v20 offset:4096\n"
"ds_read_b64_tr_b16 v[106:107], v21 offset:4096\n"
"ds_read_b64_tr_b16 v[108:109], v20 offset:10240\n"
"ds_read_b64_tr_b16 v[110:111], v21 offset:10240\n"
"ds_read_b64_tr_b16 a[168:169], v20 offset:16384\n"
"ds_read_b64_tr_b16 a[170:171], v21 offset:16384\n"
"ds_read_b64_tr_b16 a[172:173], v20 offset:22528\n"
"ds_read_b64_tr_b16 a[174:175], v21 offset:22528\n"
"s_waitcnt vmcnt(12)\n"
"s_waitcnt lgkmcnt(0)\n"
"s_barrier\n"
"s_add_u32 m0, s52, 0\n"
"s_add_u32 s72, s51, 1024\n"
"buffer_load_dwordx4 v7, s[44:47], s72 offen lds\n"
"s_add_u32 m0, s52, 1024\n"
"s_add_u32 s72, s51, 11264\n"
"buffer_load_dwordx4 v7, s[44:47], s72 offen lds\n"
"s_add_u32 m0, s52, 2048\n"
"s_add_u32 s72, s51, 21504\n"
"buffer_load_dwordx4 v7, s[44:47], s72 offen lds\n"
"s_add_u32 m0, s52, 3072\n"
"s_add_u32 s72, s51, 31744\n"
"buffer_load_dwordx4 v7, s[44:47], s72 offen lds\n"
"s_add_u32 m0, s52, 4096\n"
"s_add_u32 s72, s51, 41984\n"
"buffer_load_dwordx4 v7, s[44:47], s72 offen lds\n"
"s_add_u32 m0, s52, 5120\n"
"s_add_u32 s72, s51, 52224\n"
"buffer_load_dwordx4 v7, s[44:47], s72 offen lds\n"
"s_add_u32 m0, s52, 6144\n"
"s_add_u32 s72, s51, 62464\n"
"buffer_load_dwordx4 v7, s[44:47], s72 offen lds\n"
"s_add_u32 m0, s52, 7168\n"
"s_add_u32 s72, s51, 72704\n"
"buffer_load_dwordx4 v7, s[44:47], s72 offen lds\n"
"s_add_u32 m0, s52, 8192\n"
"s_add_u32 s72, s51, 82944\n"
"buffer_load_dwordx4 v7, s[44:47], s72 offen lds\n"
"s_add_u32 m0, s52, 9216\n"
"s_add_u32 s72, s51, 93184\n"
"buffer_load_dwordx4 v7, s[44:47], s72 offen lds\n"
"s_add_u32 m0, s52, 10240\n"
"s_add_u32 s72, s51, 103424\n"
"buffer_load_dwordx4 v7, s[44:47], s72 offen lds\n"
"s_add_u32 m0, s52, 11264\n"
"s_add_u32 s72, s51, 113664\n"
"buffer_load_dwordx4 v7, s[44:47], s72 offen lds\n"
"v_add_u32 v20, 49152, v8\n"
"v_add_u32 v21, 49152, v9\n"
"ds_read_b64_tr_b16 a[64:65], v20 offset:0\n"
"ds_read_b64_tr_b16 a[66:67], v21 offset:0\n"
"ds_read_b64_tr_b16 a[68:69], v20 offset:6144\n"
"ds_read_b64_tr_b16 a[70:71], v21 offset:6144\n"
"ds_read_b64_tr_b16 v[96:97], v20 offset:12288\n"
"ds_read_b64_tr_b16 v[98:99], v21 offset:12288\n"
"ds_read_b64_tr_b16 v[100:101], v20 offset:18432\n"
"ds_read_b64_tr_b16 v[102:103], v21 offset:18432\n"
"ds_read_b64_tr_b16 a[120:121], v20 offset:2048\n"
"ds_read_b64_tr_b16 a[122:123], v21 offset:2048\n"
"ds_read_b64_tr_b16 a[124:125], v20 offset:8192\n"
"ds_read_b64_tr_b16 a[126:127], v21 offset:8192\n"
"ds_read_b64_tr_b16 a[144:145], v20 offset:14336\n"
"ds_read_b64_tr_b16 a[146:147], v21 offset:14336\n"
"ds_read_b64_tr_b16 a[148:149], v20 offset:20480\n"
"ds_read_b64_tr_b16 a[150:151], v21 offset:20480\n"
"ds_read_b64_tr_b16 a[176:177], v20 offset:4096\n"
"ds_read_b64_tr_b16 a[178:179], v21 offset:4096\n"
"ds_read_b64_tr_b16 a[180:181], v20 offset:10240\n"
"ds_read_b64_tr_b16 a[182:183], v21 offset:10240\n"
"ds_read_b64_tr_b16 a[184:185], v20 offset:16384\n"
"ds_read_b64_tr_b16 a[186:187], v21 offset:16384\n"
"ds_read_b64_tr_b16 a[188:189], v20 offset:22528\n"
"ds_read_b64_tr_b16 a[190:191], v21 offset:22528\n"
"s_cmp_lg_u32 s66, 0\n"
"s_cbranch_scc1 V18_%=\n"
"s_mov_b32 s76, 131072\n"
"s_mov_b64 exec, 1\n"
"s_branch V20_%=\n"
"V19_%=:\n"
"s_sleep 4\n"
"V20_%=:\n"
"v_mov_b32 v29, 0\n"
"buffer_atomic_add v29, v26, s[36:39], s23 offen sc0 sc1\n"
"s_sub_u32 s76, s76, 1\n"
"s_cbranch_scc1 VABORT_%=\n"
"s_waitcnt vmcnt(0)\n"
"v_xor_b32 v29, v29, v24\n"
"v_cmp_lt_u32 vcc, 1, v29\n"
"s_nop 0\n"
"s_cbranch_vccnz V19_%=\n"
"v_mov_b32 v28, 153600\n"
"ds_write_b32 v28, v29\n"
"v_mov_b32 v24, 1\n"
"s_add_u32 s22, s23, 16384\n"
"buffer_store_dword v24, v26, s[36:39], s22 offen sc1\n"
"s_mov_b64 exec, -1\n"
"V18_%=:\n"
"s_waitcnt lgkmcnt(0)\n"
"s_barrier\n"
"v_mov_b32 v28, 153600\n"
"ds_read_b32 v28, v28\n"
"s_waitcnt lgkmcnt(0)\n"
"v_readfirstlane_b32 s22, v28\n"
"s_nop 0\n"
"s_lshl_b32 s22, s22, 13\n"
"v_xor_b32 v13, s22, v13\n"
"s_xor_b32 s68, s68, s22\n"
"s_xor_b32 s20, s20, s22\n"
"s_xor_b32 s54, s54, s22\n"
"s_waitcnt vmcnt(12)\n"
"s_waitcnt lgkmcnt(0)\n"
"s_barrier\n"
"v_add_u32 v20, 98304, v8\n"
"v_add_u32 v21, 98304, v9\n"
"ds_read_b64_tr_b16 a[72:73], v20 offset:0\n"
"ds_read_b64_tr_b16 a[74:75], v21 offset:0\n"
"ds_read_b64_tr_b16 a[76:77], v20 offset:6144\n"
"ds_read_b64_tr_b16 a[78:79], v21 offset:6144\n"
"ds_read_b64_tr_b16 a[96:97], v20 offset:12288\n"
"ds_read_b64_tr_b16 a[98:99], v21 offset:12288\n"
"ds_read_b64_tr_b16 a[100:101], v20 offset:18432\n"
"ds_read_b64_tr_b16 a[102:103], v21 offset:18432\n"
"ds_read_b64_tr_b16 a[192:193], v20 offset:2048\n"
"ds_read_b64_tr_b16 a[194:195], v21 offset:2048\n"
"ds_read_b64_tr_b16 a[196:197], v20 offset:8192\n"
"ds_read_b64_tr_b16 a[198:199], v21 offset:8192\n"
"ds_read_b64_tr_b16 a[200:201], v20 offset:14336\n"
"ds_read_b64_tr_b16 a[202:203], v21 offset:14336\n"
"ds_read_b64_tr_b16 a[204:205], v20 offset:20480\n"
"ds_read_b64_tr_b16 a[206:207], v21 offset:20480\n"
"ds_read_b64_tr_b16 v[112:113], v20 offset:4096\n"
"ds_read_b64_tr_b16 v[114:115], v21 offset:4096\n"
"ds_read_b64_tr_b16 v[116:117], v20 offset:10240\n"
"ds_read_b64_tr_b16 v[118:119], v21 offset:10240\n"
"ds_read_b64_tr_b16 a[208:209], v20 offset:16384\n"
"ds_read_b64_tr_b16 a[210:211], v21 offset:16384\n"
"ds_read_b64_tr_b16 a[212:213], v20 offset:22528\n"
"ds_read_b64_tr_b16 a[214:215], v21 offset:22528\n"
"s_waitcnt vmcnt(0)\n"
"s_waitcnt lgkmcnt(0)\n"
"s_barrier\n"
"v_add_u32 v20, 0, v8\n"
"v_add_u32 v21, 0, v9\n"
"ds_read_b64_tr_b16 a[216:217], v20 offset:0\n"
"ds_read_b64_tr_b16 a[218:219], v21 offset:0\n"
"ds_read_b64_tr_b16 a[220:221], v20 offset:6144\n"
"ds_read_b64_tr_b16 a[222:223], v21 offset:6144\n"
"ds_read_b64_tr_b16 a[224:225], v20 offset:12288\n"
"ds_read_b64_tr_b16 a[226:227], v21 offset:12288\n"
"ds_read_b64_tr_b16 a[228:229], v20 offset:18432\n"
"ds_read_b64_tr_b16 a[230:231], v21 offset:18432\n"
"ds_read_b64_tr_b16 a[232:233], v20 offset:2048\n"
"ds_read_b64_tr_b16 a[234:235], v21 offset:2048\n"
"ds_read_b64_tr_b16 a[236:237], v20 offset:8192\n"
"ds_read_b64_tr_b16 a[238:239], v21 offset:8192\n"
"ds_read_b64_tr_b16 v[120:121], v20 offset:14336\n"
"ds_read_b64_tr_b16 v[122:123], v21 offset:14336\n"
"ds_read_b64_tr_b16 v[124:125], v20 offset:20480\n"
"ds_read_b64_tr_b16 v[126:127], v21 offset:20480\n"
"ds_read_b64_tr_b16 a[240:241], v20 offset:4096\n"
"ds_read_b64_tr_b16 a[242:243], v21 offset:4096\n"
"ds_read_b64_tr_b16 a[244:245], v20 offset:10240\n"
"ds_read_b64_tr_b16 a[246:247], v21 offset:10240\n"
"ds_read_b64_tr_b16 a[248:249], v20 offset:16384\n"
"ds_read_b64_tr_b16 a[250:251], v21 offset:16384\n"
"ds_read_b64_tr_b16 a[252:253], v20 offset:22528\n"
"ds_read_b64_tr_b16 a[254:255], v21 offset:22528\n"
"s_waitcnt lgkmcnt(0)\n"
"s_barrier\n"
"ds_write_b128 v15, v[32:35] offset:0\n"
"ds_write_b128 v15, v[36:39] offset:1024\n"
"ds_write_b128 v15, v[40:43] offset:2048\n"
"ds_write_b128 v15, v[44:47] offset:3072\n"
"ds_write_b128 v15, v[48:51] offset:4096\n"
"ds_write_b128 v15, v[52:55] offset:5120\n"
"ds_write_b128 v15, v[56:59] offset:6144\n"
"ds_write_b128 v15, v[60:63] offset:7168\n"
"ds_write_b128 v15, v[64:67] offset:8192\n"
"ds_write_b128 v15, v[68:71] offset:9216\n"
"ds_write_b128 v15, v[72:75] offset:10240\n"
"ds_write_b128 v15, v[76:79] offset:11264\n"
"ds_write_b128 v15, v[80:83] offset:12288\n"
"ds_write_b128 v15, v[84:87] offset:13312\n"
"ds_write_b128 v15, v[88:91] offset:14336\n"
"ds_write_b128 v15, v[92:95] offset:15360\n"
"ds_write_b128 v15, v[96:99] offset:16384\n"
"ds_write_b128 v15, v[100:103] offset:17408\n"
"ds_write_b128 v15, v[104:107] offset:18432\n"
"ds_write_b128 v15, v[108:111] offset:19456\n"
"ds_write_b128 v15, v[112:115] offset:20480\n"
"ds_write_b128 v15, v[116:119] offset:21504\n"
"ds_write_b128 v15, v[120:123] offset:22528\n"
"ds_write_b128 v15, v[124:127] offset:23552\n"
"s_waitcnt lgkmcnt(0)\n"
"ds_read_b128 v[96:99], v15 offset:0\n"
"ds_read_b128 v[100:103], v15 offset:1024\n"
"ds_read_b128 v[72:75], v15 offset:2048\n"
"ds_read_b128 v[76:79], v15 offset:3072\n"
"ds_read_b128 v[104:107], v15 offset:4096\n"
"ds_read_b128 v[108:111], v15 offset:5120\n"
"ds_read_b128 v[80:83], v15 offset:6144\n"
"ds_read_b128 v[84:87], v15 offset:7168\n"
"ds_read_b128 v[112:115], v15 offset:8192\n"
"ds_read_b128 v[116:119], v15 offset:9216\n"
"ds_read_b128 v[88:91], v15 offset:10240\n"
"ds_read_b128 v[92:95], v15 offset:11264\n"
"v_mov_b32 v32, 0\n"
"v_mov_b32 v33, 0\n"
"v_mov_b32 v34, 0\n"
"v_mov_b32 v35, 0\n"
"v_mov_b32 v36, 0\n"
"v_mov_b32 v37, 0\n"
"v_mov_b32 v38, 0\n"
"v_mov_b32 v39, 0\n"
"v_mov_b32 v40, 0\n"
"v_mov_b32 v41, 0\n"
"v_mov_b32 v42, 0\n"
"v_mov_b32 v43, 0\n"
"v_mov_b32 v44, 0\n"
"v_mov_b32 v45, 0\n"
"v_mov_b32 v46, 0\n"
"v_mov_b32 v47, 0\n"
"v_mov_b32 v48, 0\n"
"v_mov_b32 v49, 0\n"
"v_mov_b32 v50, 0\n"
"v_mov_b32 v51, 0\n"
"v_mov_b32 v52, 0\n"
"v_mov_b32 v53, 0\n"
"v_mov_b32 v54, 0\n"
"v_mov_b32 v55, 0\n"
"v_mov_b32 v120, 0\n"
"v_mov_b32 v121, 0\n"
"v_mov_b32 v122, 0\n"
"v_mov_b32 v123, 0\n"
"v_mov_b32 v124, 0\n"
"v_mov_b32 v125, 0\n"
"v_mov_b32 v126, 0\n"
"v_mov_b32 v127, 0\n"
"s_add_u32 s70, s69, 0\n"
"s_lshr_b32 s71, s70, 8\n"
"s_xor_b32 s71, s71, s54\n"
"s_and_b32 s71, s71, 0x2000\n"
"s_add_u32 s70, s70, s71\n"
"s_add_u32 s55, s70, s53\n"
"s_add_u32 s70, s69, 131072\n"
"s_lshr_b32 s71, s70, 8\n"
"s_xor_b32 s71, s71, s54\n"
"s_and_b32 s71, s71, 0x2000\n"
"s_add_u32 s70, s70, s71\n"
"s_add_u32 s56, s70, s53\n"
"s_add_u32 s70, s69, 262144\n"
"s_lshr_b32 s71, s70, 8\n"
"s_xor_b32 s71, s71, s54\n"
"s_and_b32 s71, s71, 0x2000\n"
"s_add_u32 s70, s70, s71\n"
"s_add_u32 s57, s70, s53\n"
"s_add_u32 s70, s69, 393216\n"
"s_lshr_b32 s71, s70, 8\n"
"s_xor_b32 s71, s71, s54\n"
"s_and_b32 s71, s71, 0x2000\n"
"s_add_u32 s70, s70, s71\n"
"s_add_u32 s58, s70, s53\n"
"s_add_u32 s70, s69, 524288\n"
"s_lshr_b32 s71, s70, 8\n"
"s_xor_b32 s71, s71, s54\n"
"s_and_b32 s71, s71, 0x2000\n"
"s_add_u32 s70, s70, s71\n"
"s_add_u32 s59, s70, s53\n"
"s_nop 0\n"
"V22_%=:\n"
"s_add_u32 s60, s68, 37888\n"
"s_add_u32 s61, s68, 38912\n"
"s_add_u32 s62, s68, 39936\n"
"s_waitcnt lgkmcnt(0)\n"
"s_barrier\n"
"buffer_load_dwordx4 v[56:59], v0, s[36:39], s60 offen offset:0 sc1\n"
"s_nop 12\n"
"buffer_load_dwordx4 v[60:63], v0, s[36:39], s61 offen offset:0 sc1\n"
"s_nop 12\n"
"buffer_load_dwordx4 v[64:67], v0, s[36:39], s62 offen offset:0 sc1\n"
"buffer_load_dword v30, v0, s[36:39], s38 offen\n"
"V23_%=:\n"
"s_waitcnt vmcnt(3)\n"
"v_smfmac_f32_16x16x64_bf16 v[32:35], v[56:59], v[96:103], v6\n"
"ds_write_b128 v0, v[56:59] offset:3072\n"
"v_max3_u32 v29, v56, v57, 0\n"
"v_smfmac_f32_16x16x64_bf16 v[36:39], v[56:59], v[72:79], v6\n"
"v_max3_u32 v29, v58, v59, v29\n"
"v_smfmac_f32_16x16x64_bf16 v[40:43], v[56:59], v[104:111], v6\n"
"s_waitcnt vmcnt(2)\n"
"ds_write_b128 v0, v[60:63] offset:4096\n"
"v_max3_u32 v29, v60, v61, v29\n"
"v_smfmac_f32_16x16x64_bf16 v[32:35], v[60:63], v[80:87], v6\n"
"v_max3_u32 v29, v62, v63, v29\n"
"v_smfmac_f32_16x16x64_bf16 v[36:39], v[60:63], v[112:119], v6\n"
"s_waitcnt vmcnt(1)\n"
"ds_write_b128 v0, v[64:67] offset:5120\n"
"v_max3_u32 v29, v64, v65, v29\n"
"v_max3_u32 v29, v66, v67, v29\n"
"v_smfmac_f32_16x16x64_bf16 v[40:43], v[60:63], v[88:95], v6\n"
"v_cmp_eq_u32 vcc, -1, v29\n"
"v_smfmac_f32_16x16x64_bf16 v[32:35], v[64:67], v[128:135], v6\n"
"v_smfmac_f32_16x16x64_bf16 v[36:39], v[64:67], v[136:143], v6\n"
"s_cbranch_vccnz V24_%=\n"
"s_waitcnt lgkmcnt(0)\n"
"s_barrier\n"
"ds_read_b128 v[68:71], v0 offset:6144\n"
"ds_read_b128 v[72:75], v0 offset:7168\n"
"v_smfmac_f32_16x16x64_bf16 v[40:43], v[64:67], v[144:151], v6\n"
"ds_read_b128 v[76:79], v0 offset:8192\n"
"ds_read_b128 v[80:83], v0 offset:9216\n"
"ds_read_b128 v[84:87], v0 offset:0\n"
"ds_read_b128 v[88:91], v0 offset:1024\n"
"ds_read_b128 v[92:95], v0 offset:2048\n"
"s_waitcnt lgkmcnt(6)\n"
"v_smfmac_f32_16x16x64_bf16 v[32:35], v[68:71], v[152:159], v6\n"
"s_waitcnt lgkmcnt(5)\n"
"v_smfmac_f32_16x16x64_bf16 v[32:35], v[72:75], v[160:167], v6\n"
"s_waitcnt lgkmcnt(4)\n"
"v_smfmac_f32_16x16x64_bf16 v[32:35], v[76:79], v[168:175], v6\n"
"s_waitcnt lgkmcnt(3)\n"
"v_smfmac_f32_16x16x64_bf16 v[32:35], v[80:83], v[176:183], v6\n"
"s_waitcnt lgkmcnt(2)\n"
"v_smfmac_f32_16x16x64_bf16 v[32:35], v[84:87], v[184:191], v6\n"
"ds_read_b128 v[96:99], v15 offset:12288\n"
"s_waitcnt lgkmcnt(2)\n"
"v_smfmac_f32_16x16x64_bf16 v[32:35], v[88:91], v[192:199], v6\n"
"ds_read_b128 v[100:103], v15 offset:13312\n"
"s_waitcnt lgkmcnt(2)\n"
"v_smfmac_f32_16x16x64_bf16 v[32:35], v[92:95], v[200:207], v6\n"
"ds_read_b128 v[104:107], v15 offset:14336\n"
"s_nop 0\n"
"v_smfmac_f32_16x16x64_bf16 v[36:39], v[68:71], v[208:215], v6\n"
"ds_read_b128 v[108:111], v15 offset:15360\n"
"s_nop 0\n"
"v_smfmac_f32_16x16x64_bf16 v[36:39], v[72:75], v[216:223], v6\n"
"ds_read_b128 v[112:115], v15 offset:16384\n"
"v_smfmac_f32_16x16x64_bf16 v[36:39], v[76:79], v[224:231], v6\n"
"ds_read_b128 v[116:119], v15 offset:17408\n"
"v_add_f32 v32, v32, v34\n"
"v_smfmac_f32_16x16x64_bf16 v[36:39], v[80:83], v[232:239], v6\n"
"v_add_f32 v33, v33, v35\n"
"v_cvt_pk_bf16_f32 v32, v32, v33\n"
"v_smfmac_f32_16x16x64_bf16 v[36:39], v[84:87], v[240:247], v6\n"
"ds_write_b32 v10, v32 offset:0\n"
"v_smfmac_f32_16x16x64_bf16 v[36:39], v[88:91], v[248:255], v6\n"
"v_smfmac_f32_16x16x64_bf16 v[36:39], v[92:95], a[0:7], v6\n"
"s_nop 0\n"
"v_smfmac_f32_16x16x64_bf16 v[40:43], v[68:71], a[8:15], v6\n"
"s_nop 0\n"
"s_waitcnt lgkmcnt(5)\n"
"v_smfmac_f32_16x16x64_bf16 v[40:43], v[72:75], v[96:103], v6\n"
"v_smfmac_f32_16x16x64_bf16 v[40:43], v[76:79], a[16:23], v6\n"
"s_waitcnt lgkmcnt(0)\n"
"s_barrier\n"
"ds_read_b64_tr_b16 v[56:57], v12 offset:0\n"
"ds_read_b64_tr_b16 v[58:59], v12 offset:32\n"
"v_add_f32 v36, v36, v38\n"
"v_smfmac_f32_16x16x64_bf16 v[40:43], v[80:83], a[24:31], v6\n"
"ds_read_b64_tr_b16 v[60:61], v12 offset:256\n"
"ds_read_b64_tr_b16 v[62:63], v12 offset:288\n"
"v_add_f32 v37, v37, v39\n"
"v_cvt_pk_bf16_f32 v36, v36, v37\n"
"v_smfmac_f32_16x16x64_bf16 v[40:43], v[84:87], a[32:39], v6\n"
"ds_write_b32 v10, v36 offset:512\n"
"v_smfmac_f32_16x16x64_bf16 v[40:43], v[88:91], a[40:47], v6\n"
"v_smfmac_f32_16x16x64_bf16 v[40:43], v[92:95], a[48:55], v6\n"
"ds_read_b128 v[96:99], v15 offset:18432\n"
"s_nop 0\n"
"s_waitcnt lgkmcnt(4)\n"
"v_smfmac_f32_16x16x64_bf16 v[44:47], v[56:59], v[104:111], v11\n"
"ds_read_b128 v[100:103], v15 offset:19456\n"
"s_nop 0\n"
"v_smfmac_f32_16x16x64_bf16 v[48:51], v[56:59], a[56:63], v11\n"
"v_smfmac_f32_16x16x64_bf16 v[52:55], v[56:59], a[64:71], v11\n"
"s_waitcnt lgkmcnt(2)\n"
"s_barrier\n"
"ds_read_b64_tr_b16 v[64:65], v12 offset:512\n"
"ds_read_b64_tr_b16 v[66:67], v12 offset:544\n"
"v_add_f32 v40, v40, v42\n"
"v_smfmac_f32_16x16x64_bf16 v[120:123], v[56:59], a[72:79], v11\n"
"ds_read_b64_tr_b16 v[68:69], v12 offset:768\n"
"ds_read_b64_tr_b16 v[70:71], v12 offset:800\n"
"v_add_f32 v41, v41, v43\n"
"v_cvt_pk_bf16_f32 v40, v40, v41\n"
"v_smfmac_f32_16x16x64_bf16 v[44:47], v[60:63], a[80:87], v11\n"
"ds_write_b32 v10, v40 offset:1024\n"
"v_smfmac_f32_16x16x64_bf16 v[48:51], v[60:63], a[88:95], v11\n"
"v_smfmac_f32_16x16x64_bf16 v[52:55], v[60:63], v[112:119], v11\n"
"ds_read_b128 v[104:107], v15 offset:20480\n"
"v_smfmac_f32_16x16x64_bf16 v[120:123], v[60:63], a[96:103], v11\n"
"ds_read_b128 v[108:111], v15 offset:21504\n"
"s_waitcnt lgkmcnt(5)\n"
"v_smfmac_f32_16x16x64_bf16 v[44:47], v[64:67], a[104:111], v11\n"
"v_smfmac_f32_16x16x64_bf16 v[48:51], v[64:67], a[112:119], v11\n"
"s_waitcnt lgkmcnt(2)\n"
"s_barrier\n"
"ds_read_b64_tr_b16 v[72:73], v12 offset:1024\n"
"ds_read_b64_tr_b16 v[74:75], v12 offset:1056\n"
"v_smfmac_f32_16x16x64_bf16 v[52:55], v[64:67], a[120:127], v11\n"
"ds_read_b64_tr_b16 v[76:77], v12 offset:1280\n"
"ds_read_b64_tr_b16 v[78:79], v12 offset:1312\n"
"v_smfmac_f32_16x16x64_bf16 v[44:47], v[68:71], a[128:135], v11\n"
"v_smfmac_f32_16x16x64_bf16 v[48:51], v[68:71], a[136:143], v11\n"
"v_smfmac_f32_16x16x64_bf16 v[52:55], v[68:71], a[144:151], v11\n"
"s_waitcnt lgkmcnt(2)\n"
"v_smfmac_f32_16x16x64_bf16 v[44:47], v[72:75], a[152:159], v11\n"
"ds_read_b128 v[112:115], v15 offset:22528\n"
"s_waitcnt lgkmcnt(1)\n"
"v_smfmac_f32_16x16x64_bf16 v[44:47], v[76:79], a[160:167], v11\n"
"ds_read_b128 v[116:119], v15 offset:23552\n"
"v_smfmac_f32_16x16x64_bf16 v[48:51], v[72:75], v[96:103], v11\n"
"s_nop 1\n"
"v_smfmac_f32_16x16x64_bf16 v[48:51], v[76:79], a[168:175], v11\n"
"s_nop 1\n"
"v_smfmac_f32_16x16x64_bf16 v[52:55], v[72:75], a[176:183], v11\n"
"v_cvt_pk_bf16_f32 v80, v44, v45\n"
"v_smfmac_f32_16x16x64_bf16 v[52:55], v[76:79], a[184:191], v11\n"
"v_cvt_pk_bf16_f32 v81, v46, v47\n"
"v_min_u32 v80, -2, v80\n"
"v_smfmac_f32_16x16x64_bf16 v[120:123], v[64:67], a[192:199], v11\n"
"v_min_u32 v81, -2, v81\n"
"v_smfmac_f32_16x16x64_bf16 v[120:123], v[68:71], a[200:207], v11\n"
"buffer_store_dwordx2 v[80:81], v16, s[36:39], s55 offen sc1\n"
"v_cvt_pk_bf16_f32 v82, v48, v49\n"
"v_smfmac_f32_16x16x64_bf16 v[120:123], v[72:75], v[104:111], v11\n"
"v_cvt_pk_bf16_f32 v83, v50, v51\n"
"v_min_u32 v82, -2, v82\n"
"v_smfmac_f32_16x16x64_bf16 v[120:123], v[76:79], a[208:215], v11\n"
"v_min_u32 v83, -2, v83\n"
"v_smfmac_f32_16x16x64_bf16 v[124:127], v[56:59], a[216:223], v11\n"
"buffer_store_dwordx2 v[82:83], v16, s[36:39], s56 offen sc1\n"
"v_cvt_pk_bf16_f32 v84, v52, v53\n"
"v_smfmac_f32_16x16x64_bf16 v[124:127], v[60:63], a[224:231], v11\n"
"v_cvt_pk_bf16_f32 v85, v54, v55\n"
"v_min_u32 v84, -2, v84\n"
"v_smfmac_f32_16x16x64_bf16 v[124:127], v[64:67], a[232:239], v11\n"
"v_min_u32 v85, -2, v85\n"
"s_waitcnt lgkmcnt(0)\n"
"v_smfmac_f32_16x16x64_bf16 v[124:127], v[68:71], v[112:119], v11\n"
"buffer_store_dwordx2 v[84:85], v16, s[36:39], s57 offen sc1\n"
"v_cvt_pk_bf16_f32 v86, v120, v121\n"
"v_smfmac_f32_16x16x64_bf16 v[124:127], v[72:75], a[240:247], v11\n"
"v_cvt_pk_bf16_f32 v87, v122, v123\n"
"v_min_u32 v86, -2, v86\n"
"v_smfmac_f32_16x16x64_bf16 v[124:127], v[76:79], a[248:255], v11\n"
"v_min_u32 v87, -2, v87\n"
"buffer_store_dwordx2 v[86:87], v16, s[36:39], s58 offen sc1\n"
"s_nop 5\n"
"v_cvt_pk_bf16_f32 v88, v124, v125\n"
"v_cvt_pk_bf16_f32 v89, v126, v127\n"
"v_min_u32 v88, -2, v88\n"
"v_min_u32 v89, -2, v89\n"
"buffer_store_dwordx2 v[88:89], v16, s[36:39], s59 offen sc1\n"
"s_add_u32 s73, s73, 1\n"
"s_add_u32 s69, s69, 655360\n"
"s_add_u32 s79, s68, 131072\n"
"s_and_b32 s79, s79, 0x3fffff\n"
"s_xor_b32 s68, s79, s68\n"
"s_lshr_b32 s68, s68, 8\n"
"s_and_b32 s68, s68, 8192\n"
"s_xor_b32 s68, s68, s79\n"
"s_cmp_lt_u32 s73, 64\n"
"s_cbranch_scc0 V21_%=\n"
"ds_read_b128 v[96:99], v15 offset:0\n"
"ds_read_b128 v[100:103], v15 offset:1024\n"
"ds_read_b128 v[72:75], v15 offset:2048\n"
"ds_read_b128 v[76:79], v15 offset:3072\n"
"ds_read_b128 v[104:107], v15 offset:4096\n"
"ds_read_b128 v[108:111], v15 offset:5120\n"
"ds_read_b128 v[80:83], v15 offset:6144\n"
"ds_read_b128 v[84:87], v15 offset:7168\n"
"ds_read_b128 v[112:115], v15 offset:8192\n"
"ds_read_b128 v[116:119], v15 offset:9216\n"
"ds_read_b128 v[88:91], v15 offset:10240\n"
"ds_read_b128 v[92:95], v15 offset:11264\n"
"v_mov_b32 v32, 0\n"
"v_mov_b32 v33, 0\n"
"v_mov_b32 v34, 0\n"
"v_mov_b32 v35, 0\n"
"v_mov_b32 v36, 0\n"
"v_mov_b32 v37, 0\n"
"v_mov_b32 v38, 0\n"
"v_mov_b32 v39, 0\n"
"v_mov_b32 v40, 0\n"
"v_mov_b32 v41, 0\n"
"v_mov_b32 v42, 0\n"
"v_mov_b32 v43, 0\n"
"v_mov_b32 v44, 0\n"
"v_mov_b32 v45, 0\n"
"v_mov_b32 v46, 0\n"
"v_mov_b32 v47, 0\n"
"v_mov_b32 v48, 0\n"
"v_mov_b32 v49, 0\n"
"v_mov_b32 v50, 0\n"
"v_mov_b32 v51, 0\n"
"v_mov_b32 v52, 0\n"
"v_mov_b32 v53, 0\n"
"v_mov_b32 v54, 0\n"
"v_mov_b32 v55, 0\n"
"v_mov_b32 v120, 0\n"
"v_mov_b32 v121, 0\n"
"v_mov_b32 v122, 0\n"
"v_mov_b32 v123, 0\n"
"v_mov_b32 v124, 0\n"
"v_mov_b32 v125, 0\n"
"v_mov_b32 v126, 0\n"
"v_mov_b32 v127, 0\n"
"s_add_u32 s70, s69, 0\n"
"s_lshr_b32 s71, s70, 8\n"
"s_xor_b32 s71, s71, s54\n"
"s_and_b32 s71, s71, 0x2000\n"
"s_add_u32 s70, s70, s71\n"
"s_add_u32 s55, s70, s53\n"
"s_add_u32 s70, s69, 131072\n"
"s_lshr_b32 s71, s70, 8\n"
"s_xor_b32 s71, s71, s54\n"
"s_and_b32 s71, s71, 0x2000\n"
"s_add_u32 s70, s70, s71\n"
"s_add_u32 s56, s70, s53\n"
"s_add_u32 s70, s69, 262144\n"
"s_lshr_b32 s71, s70, 8\n"
"s_xor_b32 s71, s71, s54\n"
"s_and_b32 s71, s71, 0x2000\n"
"s_add_u32 s70, s70, s71\n"
"s_add_u32 s57, s70, s53\n"
"s_add_u32 s70, s69, 393216\n"
"s_lshr_b32 s71, s70, 8\n"
"s_xor_b32 s71, s71, s54\n"
"s_and_b32 s71, s71, 0x2000\n"
"s_add_u32 s70, s70, s71\n"
"s_add_u32 s58, s70, s53\n"
"s_add_u32 s70, s69, 524288\n"
"s_lshr_b32 s71, s70, 8\n"
"s_xor_b32 s71, s71, s54\n"
"s_and_b32 s71, s71, 0x2000\n"
"s_add_u32 s70, s70, s71\n"
"s_add_u32 s59, s70, s53\n"
"s_branch V22_%=\n"
"V24_%=:\n"
"s_waitcnt lgkmcnt(0)\n"
"s_mov_b32 s76, 131072\n"
"V25_%=:\n"
"s_memrealtime s[4:5]\n"
"buffer_load_dwordx4 v[56:59], v0, s[36:39], s60 offen offset:0 sc1\n"
"buffer_load_dwordx4 v[60:63], v0, s[36:39], s61 offen offset:0 sc1\n"
"buffer_load_dwordx4 v[64:67], v0, s[36:39], s62 offen offset:0 sc1\n"
"v_mov_b32 v29, 0\n"
"ds_read_b32 v31, v29 offset:16896\n"
"s_lshl_b32 s77, s73, 14\n"
"s_add_u32 s77, s77, s20\n"
"s_add_u32 s77, s77, s21\n"
"s_add_u32 s77, s77, 4096\n"
"v_mov_b32 v32, 0\n"
"v_mov_b32 v33, 0\n"
"v_mov_b32 v34, 0\n"
"v_mov_b32 v35, 0\n"
"v_mov_b32 v36, 0\n"
"v_mov_b32 v37, 0\n"
"v_mov_b32 v38, 0\n"
"v_mov_b32 v39, 0\n"
"v_mov_b32 v40, 0\n"
"v_mov_b32 v41, 0\n"
"v_mov_b32 v42, 0\n"
"v_mov_b32 v43, 0\n"
"s_sub_u32 s76, s76, 1\n"
"s_cbranch_scc1 VABORT_%=\n"
"s_waitcnt vmcnt(2)\n"
"v_max3_u32 v29, v56, v57, 0\n"
"v_max3_u32 v29, v58, v59, v29\n"
"s_waitcnt vmcnt(1)\n"
"v_max3_u32 v29, v60, v61, v29\n"
"v_max3_u32 v29, v62, v63, v29\n"
"s_waitcnt vmcnt(0)\n"
"v_max3_u32 v29, v64, v65, v29\n"
"v_max3_u32 v29, v66, v67, v29\n"
"v_cmp_eq_u32 vcc, -1, v29\n"
"s_nop 0\n"
"s_cbranch_vccnz V25_%=\n"
"s_waitcnt lgkmcnt(0)\n"
"v_lshl_add_u32 v31, s4, 3, v31\n"
"v_lshrrev_b32 v30, 2, v0\n"
"s_mov_b64 exec, 0xffff\n"
"buffer_store_dword v31, v30, s[36:39], s77 offen sc1\n"
"s_mov_b64 exec, -1\n"
"s_branch V23_%=\n"
"V21_%=:\n"
"s_waitcnt vmcnt(0)\n"
"s_barrier\n"
"s_branch V5_%=\n"
"V3_%=:\n"
"s_mov_b32 s22, 46301184\n"
"s_mov_b32 s23, 46317568\n"
"s_cmp_lg_u32 s66, 0\n"
"s_cbranch_scc1 V26_%=\n"
"v_mov_b32 v24, 0\n"
"s_mov_b64 exec, 1\n"
"buffer_atomic_add v24, v26, s[36:39], s23 offen sc0 sc1\n"
"s_mov_b64 exec, -1\n"
"V26_%=:\n"
"s_add_u32 m0, s50, 0\n"
"s_add_u32 s72, s48, 0\n"
"buffer_load_dwordx4 v1, s[40:43], s72 offen lds\n"
"s_add_u32 m0, s50, 1024\n"
"s_add_u32 s72, s48, 98304\n"
"buffer_load_dwordx4 v2, s[40:43], s72 offen lds\n"
"s_add_u32 m0, s50, 2048\n"
"s_add_u32 s72, s48, 196608\n"
"buffer_load_dwordx4 v1, s[40:43], s72 offen lds\n"
"s_add_u32 m0, s50, 3072\n"
"s_add_u32 s72, s48, 294912\n"
"buffer_load_dwordx4 v2, s[40:43], s72 offen lds\n"
"s_add_u32 m0, s50, 16384\n"
"s_add_u32 s72, s48, 128\n"
"buffer_load_dwordx4 v1, s[40:43], s72 offen lds\n"
"s_add_u32 m0, s50, 17408\n"
"s_add_u32 s72, s48, 98432\n"
"buffer_load_dwordx4 v2, s[40:43], s72 offen lds\n"
"s_add_u32 m0, s50, 18432\n"
"s_add_u32 s72, s48, 196736\n"
"buffer_load_dwordx4 v1, s[40:43], s72 offen lds\n"
"s_add_u32 m0, s50, 19456\n"
"s_add_u32 s72, s48, 295040\n"
"buffer_load_dwordx4 v2, s[40:43], s72 offen lds\n"
"s_add_u32 m0, s50, 32768\n"
"s_add_u32 s72, s48, 256\n"
"buffer_load_dwordx4 v1, s[40:43], s72 offen lds\n"
"s_add_u32 m0, s50, 33792\n"
"s_add_u32 s72, s48, 98560\n"
"buffer_load_dwordx4 v2, s[40:43], s72 offen lds\n"
"s_add_u32 m0, s50, 34816\n"
"s_add_u32 s72, s48, 196864\n"
"buffer_load_dwordx4 v1, s[40:43], s72 offen lds\n"
"s_add_u32 m0, s50, 35840\n"
"s_add_u32 s72, s48, 295168\n"
"buffer_load_dwordx4 v2, s[40:43], s72 offen lds\n"
"s_add_u32 m0, s50, 49152\n"
"s_add_u32 s72, s48, 1572864\n"
"buffer_load_dwordx4 v1, s[40:43], s72 offen lds\n"
"s_add_u32 m0, s50, 50176\n"
"s_add_u32 s72, s48, 1671168\n"
"buffer_load_dwordx4 v2, s[40:43], s72 offen lds\n"
"s_add_u32 m0, s50, 51200\n"
"s_add_u32 s72, s48, 1769472\n"
"buffer_load_dwordx4 v1, s[40:43], s72 offen lds\n"
"s_add_u32 m0, s50, 52224\n"
"s_add_u32 s72, s48, 1867776\n"
"buffer_load_dwordx4 v2, s[40:43], s72 offen lds\n"
"s_add_u32 m0, s50, 65536\n"
"s_add_u32 s72, s48, 1572992\n"
"buffer_load_dwordx4 v1, s[40:43], s72 offen lds\n"
"s_add_u32 m0, s50, 66560\n"
"s_add_u32 s72, s48, 1671296\n"
"buffer_load_dwordx4 v2, s[40:43], s72 offen lds\n"
"s_add_u32 m0, s50, 67584\n"
"s_add_u32 s72, s48, 1769600\n"
"buffer_load_dwordx4 v1, s[40:43], s72 offen lds\n"
"s_add_u32 m0, s50, 68608\n"
"s_add_u32 s72, s48, 1867904\n"
"buffer_load_dwordx4 v2, s[40:43], s72 offen lds\n"
"s_add_u32 m0, s50, 81920\n"
"s_add_u32 s72, s48, 1573120\n"
"buffer_load_dwordx4 v1, s[40:43], s72 offen lds\n"
"s_add_u32 m0, s50, 82944\n"
"s_add_u32 s72, s48, 1671424\n"
"buffer_load_dwordx4 v2, s[40:43], s72 offen lds\n"
"s_add_u32 m0, s50, 83968\n"
"s_add_u32 s72, s48, 1769728\n"
"buffer_load_dwordx4 v1, s[40:43], s72 offen lds\n"
"s_add_u32 m0, s50, 84992\n"
"s_add_u32 s72, s48, 1868032\n"
"buffer_load_dwordx4 v2, s[40:43], s72 offen lds\n"
"s_waitcnt vmcnt(12)\n"
"s_waitcnt lgkmcnt(0)\n"
"s_barrier\n"
"s_cmp_lg_u32 s66, 0\n"
"s_cbranch_scc1 V27_%=\n"
"v_or_b32 v24, 1, v24\n"
"v_add_u32 v24, 1, v24\n"
"buffer_store_dword v24, v26, s[36:39], s22 offen sc1\n"
"V27_%=:\n"
"s_add_u32 m0, s50, 98304\n"
"s_add_u32 s72, s48, 3145728\n"
"buffer_load_dwordx4 v1, s[40:43], s72 offen lds\n"
"s_add_u32 m0, s50, 99328\n"
"s_add_u32 s72, s48, 3244032\n"
"buffer_load_dwordx4 v2, s[40:43], s72 offen lds\n"
"s_add_u32 m0, s50, 100352\n"
"s_add_u32 s72, s48, 3342336\n"
"buffer_load_dwordx4 v1, s[40:43], s72 offen lds\n"
"s_add_u32 m0, s50, 101376\n"
"s_add_u32 s72, s48, 3440640\n"
"buffer_load_dwordx4 v2, s[40:43], s72 offen lds\n"
"s_add_u32 m0, s50, 114688\n"
"s_add_u32 s72, s48, 3145856\n"
"buffer_load_dwordx4 v1, s[40:43], s72 offen lds\n"
"s_add_u32 m0, s50, 115712\n"
"s_add_u32 s72, s48, 3244160\n"
"buffer_load_dwordx4 v2, s[40:43], s72 offen lds\n"
"s_add_u32 m0, s50, 116736\n"
"s_add_u32 s72, s48, 3342464\n"
"buffer_load_dwordx4 v1, s[40:43], s72 offen lds\n"
"s_add_u32 m0, s50, 117760\n"
"s_add_u32 s72, s48, 3440768\n"
"buffer_load_dwordx4 v2, s[40:43], s72 offen lds\n"
"s_add_u32 m0, s50, 131072\n"
"s_add_u32 s72, s48, 3145984\n"
"buffer_load_dwordx4 v1, s[40:43], s72 offen lds\n"
"s_add_u32 m0, s50, 132096\n"
"s_add_u32 s72, s48, 3244288\n"
"buffer_load_dwordx4 v2, s[40:43], s72 offen lds\n"
"s_add_u32 m0, s50, 133120\n"
"s_add_u32 s72, s48, 3342592\n"
"buffer_load_dwordx4 v1, s[40:43], s72 offen lds\n"
"s_add_u32 m0, s50, 134144\n"
"s_add_u32 s72, s48, 3440896\n"
"buffer_load_dwordx4 v2, s[40:43], s72 offen lds\n"
"v_add_u32 v20, 0, v3\n"
"v_add_u32 v21, 0, v4\n"
"v_add_u32 v22, 0, v5\n"
"ds_read_b64_tr_b16 v[168:169], v20 offset:0\n"
"ds_read_b64_tr_b16 v[170:171], v20 offset:256\n"
"ds_read_b64_tr_b16 v[172:173], v20 offset:4096\n"
"ds_read_b64_tr_b16 v[174:175], v20 offset:4352\n"
"ds_read_b64_tr_b16 v[224:225], v21 offset:0\n"
"ds_read_b64_tr_b16 v[226:227], v21 offset:256\n"
"ds_read_b64_tr_b16 v[228:229], v21 offset:4096\n"
"ds_read_b64_tr_b16 v[230:231], v21 offset:4352\n"
"ds_read_b64_tr_b16 a[16:17], v22 offset:0\n"
"ds_read_b64_tr_b16 a[18:19], v22 offset:256\n"
"ds_read_b64_tr_b16 a[20:21], v22 offset:4096\n"
"ds_read_b64_tr_b16 a[22:23], v22 offset:4352\n"
"ds_read_b64_tr_b16 v[176:177], v20 offset:8192\n"
"ds_read_b64_tr_b16 v[178:179], v20 offset:8448\n"
"ds_read_b64_tr_b16 v[180:181], v20 offset:12288\n"
"ds_read_b64_tr_b16 v[182:183], v20 offset:12544\n"
"ds_read_b64_tr_b16 v[232:233], v21 offset:8192\n"
"ds_read_b64_tr_b16 v[234:235], v21 offset:8448\n"
"ds_read_b64_tr_b16 v[236:237], v21 offset:12288\n"
"ds_read_b64_tr_b16 v[238:239], v21 offset:12544\n"
"ds_read_b64_tr_b16 a[24:25], v22 offset:8192\n"
"ds_read_b64_tr_b16 a[26:27], v22 offset:8448\n"
"ds_read_b64_tr_b16 a[28:29], v22 offset:12288\n"
"ds_read_b64_tr_b16 a[30:31], v22 offset:12544\n"
"s_waitcnt vmcnt(12)\n"
"s_waitcnt lgkmcnt(0)\n"
"s_barrier\n"
"s_add_u32 m0, s50, 0\n"
"s_add_u32 s72, s48, 4718592\n"
"buffer_load_dwordx4 v1, s[40:43], s72 offen lds\n"
"s_add_u32 m0, s50, 1024\n"
"s_add_u32 s72, s48, 4816896\n"
"buffer_load_dwordx4 v2, s[40:43], s72 offen lds\n"
"s_add_u32 m0, s50, 2048\n"
"s_add_u32 s72, s48, 4915200\n"
"buffer_load_dwordx4 v1, s[40:43], s72 offen lds\n"
"s_add_u32 m0, s50, 3072\n"
"s_add_u32 s72, s48, 5013504\n"
"buffer_load_dwordx4 v2, s[40:43], s72 offen lds\n"
"s_add_u32 m0, s50, 16384\n"
"s_add_u32 s72, s48, 4718720\n"
"buffer_load_dwordx4 v1, s[40:43], s72 offen lds\n"
"s_add_u32 m0, s50, 17408\n"
"s_add_u32 s72, s48, 4817024\n"
"buffer_load_dwordx4 v2, s[40:43], s72 offen lds\n"
"s_add_u32 m0, s50, 18432\n"
"s_add_u32 s72, s48, 4915328\n"
"buffer_load_dwordx4 v1, s[40:43], s72 offen lds\n"
"s_add_u32 m0, s50, 19456\n"
"s_add_u32 s72, s48, 5013632\n"
"buffer_load_dwordx4 v2, s[40:43], s72 offen lds\n"
"s_add_u32 m0, s50, 32768\n"
"s_add_u32 s72, s48, 4718848\n"
"buffer_load_dwordx4 v1, s[40:43], s72 offen lds\n"
"s_add_u32 m0, s50, 33792\n"
"s_add_u32 s72, s48, 4817152\n"
"buffer_load_dwordx4 v2, s[40:43], s72 offen lds\n"
"s_add_u32 m0, s50, 34816\n"
"s_add_u32 s72, s48, 4915456\n"
"buffer_load_dwordx4 v1, s[40:43], s72 offen lds\n"
"s_add_u32 m0, s50, 35840\n"
"s_add_u32 s72, s48, 5013760\n"
"buffer_load_dwordx4 v2, s[40:43], s72 offen lds\n"
"v_add_u32 v20, 49152, v3\n"
"v_add_u32 v21, 49152, v4\n"
"v_add_u32 v22, 49152, v5\n"
"ds_read_b64_tr_b16 v[184:185], v20 offset:0\n"
"ds_read_b64_tr_b16 v[186:187], v20 offset:256\n"
"ds_read_b64_tr_b16 v[188:189], v20 offset:4096\n"
"ds_read_b64_tr_b16 v[190:191], v20 offset:4352\n"
"ds_read_b64_tr_b16 v[240:241], v21 offset:0\n"
"ds_read_b64_tr_b16 v[242:243], v21 offset:256\n"
"ds_read_b64_tr_b16 v[244:245], v21 offset:4096\n"
"ds_read_b64_tr_b16 v[246:247], v21 offset:4352\n"
"ds_read_b64_tr_b16 a[32:33], v22 offset:0\n"
"ds_read_b64_tr_b16 a[34:35], v22 offset:256\n"
"ds_read_b64_tr_b16 a[36:37], v22 offset:4096\n"
"ds_read_b64_tr_b16 a[38:39], v22 offset:4352\n"
"ds_read_b64_tr_b16 v[192:193], v20 offset:8192\n"
"ds_read_b64_tr_b16 v[194:195], v20 offset:8448\n"
"ds_read_b64_tr_b16 v[196:197], v20 offset:12288\n"
"ds_read_b64_tr_b16 v[198:199], v20 offset:12544\n"
"ds_read_b64_tr_b16 v[248:249], v21 offset:8192\n"
"ds_read_b64_tr_b16 v[250:251], v21 offset:8448\n"
"ds_read_b64_tr_b16 v[252:253], v21 offset:12288\n"
"ds_read_b64_tr_b16 v[254:255], v21 offset:12544\n"
"ds_read_b64_tr_b16 a[40:41], v22 offset:8192\n"
"ds_read_b64_tr_b16 a[42:43], v22 offset:8448\n"
"ds_read_b64_tr_b16 a[44:45], v22 offset:12288\n"
"ds_read_b64_tr_b16 a[46:47], v22 offset:12544\n"
"s_waitcnt vmcnt(12)\n"
"s_waitcnt lgkmcnt(0)\n"
"s_barrier\n"
"s_add_u32 m0, s50, 49152\n"
"s_add_u32 s72, s48, 6291456\n"
"buffer_load_dwordx4 v1, s[40:43], s72 offen lds\n"
"s_add_u32 m0, s50, 50176\n"
"s_add_u32 s72, s48, 6389760\n"
"buffer_load_dwordx4 v2, s[40:43], s72 offen lds\n"
"s_add_u32 m0, s50, 51200\n"
"s_add_u32 s72, s48, 6488064\n"
"buffer_load_dwordx4 v1, s[40:43], s72 offen lds\n"
"s_add_u32 m0, s50, 52224\n"
"s_add_u32 s72, s48, 6586368\n"
"buffer_load_dwordx4 v2, s[40:43], s72 offen lds\n"
"s_add_u32 m0, s50, 65536\n"
"s_add_u32 s72, s48, 6291584\n"
"buffer_load_dwordx4 v1, s[40:43], s72 offen lds\n"
"s_add_u32 m0, s50, 66560\n"
"s_add_u32 s72, s48, 6389888\n"
"buffer_load_dwordx4 v2, s[40:43], s72 offen lds\n"
"s_add_u32 m0, s50, 67584\n"
"s_add_u32 s72, s48, 6488192\n"
"buffer_load_dwordx4 v1, s[40:43], s72 offen lds\n"
"s_add_u32 m0, s50, 68608\n"
"s_add_u32 s72, s48, 6586496\n"
"buffer_load_dwordx4 v2, s[40:43], s72 offen lds\n"
"s_add_u32 m0, s50, 81920\n"
"s_add_u32 s72, s48, 6291712\n"
"buffer_load_dwordx4 v1, s[40:43], s72 offen lds\n"
"s_add_u32 m0, s50, 82944\n"
"s_add_u32 s72, s48, 6390016\n"
"buffer_load_dwordx4 v2, s[40:43], s72 offen lds\n"
"s_add_u32 m0, s50, 83968\n"
"s_add_u32 s72, s48, 6488320\n"
"buffer_load_dwordx4 v1, s[40:43], s72 offen lds\n"
"s_add_u32 m0, s50, 84992\n"
"s_add_u32 s72, s48, 6586624\n"
"buffer_load_dwordx4 v2, s[40:43], s72 offen lds\n"
"v_add_u32 v20, 98304, v3\n"
"v_add_u32 v21, 98304, v4\n"
"v_add_u32 v22, 98304, v5\n"
"ds_read_b64_tr_b16 v[200:201], v20 offset:0\n"
"ds_read_b64_tr_b16 v[202:203], v20 offset:256\n"
"ds_read_b64_tr_b16 v[204:205], v20 offset:4096\n"
"ds_read_b64_tr_b16 v[206:207], v20 offset:4352\n"
"ds_read_b64_tr_b16 a[0:1], v21 offset:0\n"
"ds_read_b64_tr_b16 a[2:3], v21 offset:256\n"
"ds_read_b64_tr_b16 a[4:5], v21 offset:4096\n"
"ds_read_b64_tr_b16 a[6:7], v21 offset:4352\n"
"ds_read_b64_tr_b16 a[48:49], v22 offset:0\n"
"ds_read_b64_tr_b16 a[50:51], v22 offset:256\n"
"ds_read_b64_tr_b16 a[52:53], v22 offset:4096\n"
"ds_read_b64_tr_b16 a[54:55], v22 offset:4352\n"
"ds_read_b64_tr_b16 v[128:129], v20 offset:8192\n"
"ds_read_b64_tr_b16 v[130:131], v20 offset:8448\n"
"ds_read_b64_tr_b16 v[132:133], v20 offset:12288\n"
"ds_read_b64_tr_b16 v[134:135], v20 offset:12544\n"
"ds_read_b64_tr_b16 v[136:137], v21 offset:8192\n"
"ds_read_b64_tr_b16 v[138:139], v21 offset:8448\n"
"ds_read_b64_tr_b16 v[140:141], v21 offset:12288\n"
"ds_read_b64_tr_b16 v[142:143], v21 offset:12544\n"
"ds_read_b64_tr_b16 v[144:145], v22 offset:8192\n"
"ds_read_b64_tr_b16 v[146:147], v22 offset:8448\n"
"ds_read_b64_tr_b16 v[148:149], v22 offset:12288\n"
"ds_read_b64_tr_b16 v[150:151], v22 offset:12544\n"
"s_waitcnt vmcnt(12)\n"
"s_waitcnt lgkmcnt(0)\n"
"s_barrier\n"
"s_add_u32 m0, s52, 98304\n"
"s_add_u32 s72, s51, 0\n"
"buffer_load_dwordx4 v7, s[44:47], s72 offen lds\n"
"s_add_u32 m0, s52, 99328\n"
"s_add_u32 s72, s51, 10240\n"
"buffer_load_dwordx4 v7, s[44:47], s72 offen lds\n"
"s_add_u32 m0, s52, 100352\n"
"s_add_u32 s72, s51, 20480\n"
"buffer_load_dwordx4 v7, s[44:47], s72 offen lds\n"
"s_add_u32 m0, s52, 101376\n"
"s_add_u32 s72, s51, 30720\n"
"buffer_load_dwordx4 v7, s[44:47], s72 offen lds\n"
"s_add_u32 m0, s52, 102400\n"
"s_add_u32 s72, s51, 40960\n"
"buffer_load_dwordx4 v7, s[44:47], s72 offen lds\n"
"s_add_u32 m0, s52, 103424\n"
"s_add_u32 s72, s51, 51200\n"
"buffer_load_dwordx4 v7, s[44:47], s72 offen lds\n"
"s_add_u32 m0, s52, 104448\n"
"s_add_u32 s72, s51, 61440\n"
"buffer_load_dwordx4 v7, s[44:47], s72 offen lds\n"
"s_add_u32 m0, s52, 105472\n"
"s_add_u32 s72, s51, 71680\n"
"buffer_load_dwordx4 v7, s[44:47], s72 offen lds\n"
"s_add_u32 m0, s52, 106496\n"
"s_add_u32 s72, s51, 81920\n"
"buffer_load_dwordx4 v7, s[44:47], s72 offen lds\n"
"s_add_u32 m0, s52, 107520\n"
"s_add_u32 s72, s51, 92160\n"
"buffer_load_dwordx4 v7, s[44:47], s72 offen lds\n"
"s_add_u32 m0, s52, 108544\n"
"s_add_u32 s72, s51, 102400\n"
"buffer_load_dwordx4 v7, s[44:47], s72 offen lds\n"
"s_add_u32 m0, s52, 109568\n"
"s_add_u32 s72, s51, 112640\n"
"buffer_load_dwordx4 v7, s[44:47], s72 offen lds\n"
"v_add_u32 v20, 0, v3\n"
"v_add_u32 v21, 0, v4\n"
"v_add_u32 v22, 0, v5\n"
"ds_read_b64_tr_b16 v[32:33], v20 offset:0\n"
"ds_read_b64_tr_b16 v[34:35], v20 offset:256\n"
"ds_read_b64_tr_b16 v[36:37], v20 offset:4096\n"
"ds_read_b64_tr_b16 v[38:39], v20 offset:4352\n"
"ds_read_b64_tr_b16 v[40:41], v21 offset:0\n"
"ds_read_b64_tr_b16 v[42:43], v21 offset:256\n"
"ds_read_b64_tr_b16 v[44:45], v21 offset:4096\n"
"ds_read_b64_tr_b16 v[46:47], v21 offset:4352\n"
"ds_read_b64_tr_b16 v[48:49], v22 offset:0\n"
"ds_read_b64_tr_b16 v[50:51], v22 offset:256\n"
"ds_read_b64_tr_b16 v[52:53], v22 offset:4096\n"
"ds_read_b64_tr_b16 v[54:55], v22 offset:4352\n"
"ds_read_b64_tr_b16 v[56:57], v20 offset:8192\n"
"ds_read_b64_tr_b16 v[58:59], v20 offset:8448\n"
"ds_read_b64_tr_b16 v[60:61], v20 offset:12288\n"
"ds_read_b64_tr_b16 v[62:63], v20 offset:12544\n"
"ds_read_b64_tr_b16 v[64:65], v21 offset:8192\n"
"ds_read_b64_tr_b16 v[66:67], v21 offset:8448\n"
"ds_read_b64_tr_b16 v[68:69], v21 offset:12288\n"
"ds_read_b64_tr_b16 v[70:71], v21 offset:12544\n"
"ds_read_b64_tr_b16 v[72:73], v22 offset:8192\n"
"ds_read_b64_tr_b16 v[74:75], v22 offset:8448\n"
"ds_read_b64_tr_b16 v[76:77], v22 offset:12288\n"
"ds_read_b64_tr_b16 v[78:79], v22 offset:12544\n"
"s_waitcnt vmcnt(12)\n"
"s_waitcnt lgkmcnt(0)\n"
"s_barrier\n"
"s_add_u32 m0, s52, 0\n"
"s_add_u32 s72, s51, 256\n"
"buffer_load_dwordx4 v7, s[44:47], s72 offen lds\n"
"s_add_u32 m0, s52, 1024\n"
"s_add_u32 s72, s51, 10496\n"
"buffer_load_dwordx4 v7, s[44:47], s72 offen lds\n"
"s_add_u32 m0, s52, 2048\n"
"s_add_u32 s72, s51, 20736\n"
"buffer_load_dwordx4 v7, s[44:47], s72 offen lds\n"
"s_add_u32 m0, s52, 3072\n"
"s_add_u32 s72, s51, 30976\n"
"buffer_load_dwordx4 v7, s[44:47], s72 offen lds\n"
"s_add_u32 m0, s52, 4096\n"
"s_add_u32 s72, s51, 41216\n"
"buffer_load_dwordx4 v7, s[44:47], s72 offen lds\n"
"s_add_u32 m0, s52, 5120\n"
"s_add_u32 s72, s51, 51456\n"
"buffer_load_dwordx4 v7, s[44:47], s72 offen lds\n"
"s_add_u32 m0, s52, 6144\n"
"s_add_u32 s72, s51, 61696\n"
"buffer_load_dwordx4 v7, s[44:47], s72 offen lds\n"
"s_add_u32 m0, s52, 7168\n"
"s_add_u32 s72, s51, 71936\n"
"buffer_load_dwordx4 v7, s[44:47], s72 offen lds\n"
"s_add_u32 m0, s52, 8192\n"
"s_add_u32 s72, s51, 82176\n"
"buffer_load_dwordx4 v7, s[44:47], s72 offen lds\n"
"s_add_u32 m0, s52, 9216\n"
"s_add_u32 s72, s51, 92416\n"
"buffer_load_dwordx4 v7, s[44:47], s72 offen lds\n"
"s_add_u32 m0, s52, 10240\n"
"s_add_u32 s72, s51, 102656\n"
"buffer_load_dwordx4 v7, s[44:47], s72 offen lds\n"
"s_add_u32 m0, s52, 11264\n"
"s_add_u32 s72, s51, 112896\n"
"buffer_load_dwordx4 v7, s[44:47], s72 offen lds\n"
"v_add_u32 v20, 49152, v3\n"
"v_add_u32 v21, 49152, v4\n"
"v_add_u32 v22, 49152, v5\n"
"ds_read_b64_tr_b16 v[152:153], v20 offset:0\n"
"ds_read_b64_tr_b16 v[154:155], v20 offset:256\n"
"ds_read_b64_tr_b16 v[156:157], v20 offset:4096\n"
"ds_read_b64_tr_b16 v[158:159], v20 offset:4352\n"
"ds_read_b64_tr_b16 v[208:209], v21 offset:0\n"
"ds_read_b64_tr_b16 v[210:211], v21 offset:256\n"
"ds_read_b64_tr_b16 v[212:213], v21 offset:4096\n"
"ds_read_b64_tr_b16 v[214:215], v21 offset:4352\n"
"ds_read_b64_tr_b16 a[8:9], v22 offset:0\n"
"ds_read_b64_tr_b16 a[10:11], v22 offset:256\n"
"ds_read_b64_tr_b16 a[12:13], v22 offset:4096\n"
"ds_read_b64_tr_b16 a[14:15], v22 offset:4352\n"
"ds_read_b64_tr_b16 v[160:161], v20 offset:8192\n"
"ds_read_b64_tr_b16 v[162:163], v20 offset:8448\n"
"ds_read_b64_tr_b16 v[164:165], v20 offset:12288\n"
"ds_read_b64_tr_b16 v[166:167], v20 offset:12544\n"
"ds_read_b64_tr_b16 v[216:217], v21 offset:8192\n"
"ds_read_b64_tr_b16 v[218:219], v21 offset:8448\n"
"ds_read_b64_tr_b16 v[220:221], v21 offset:12288\n"
"ds_read_b64_tr_b16 v[222:223], v21 offset:12544\n"
"ds_read_b64_tr_b16 v[80:81], v22 offset:8192\n"
"ds_read_b64_tr_b16 v[82:83], v22 offset:8448\n"
"ds_read_b64_tr_b16 v[84:85], v22 offset:12288\n"
"ds_read_b64_tr_b16 v[86:87], v22 offset:12544\n"
"s_waitcnt vmcnt(12)\n"
"s_waitcnt lgkmcnt(0)\n"
"s_barrier\n"
"s_add_u32 m0, s52, 49152\n"
"s_add_u32 s72, s51, 512\n"
"buffer_load_dwordx4 v7, s[44:47], s72 offen lds\n"
"s_add_u32 m0, s52, 50176\n"
"s_add_u32 s72, s51, 10752\n"
"buffer_load_dwordx4 v7, s[44:47], s72 offen lds\n"
"s_add_u32 m0, s52, 51200\n"
"s_add_u32 s72, s51, 20992\n"
"buffer_load_dwordx4 v7, s[44:47], s72 offen lds\n"
"s_add_u32 m0, s52, 52224\n"
"s_add_u32 s72, s51, 31232\n"
"buffer_load_dwordx4 v7, s[44:47], s72 offen lds\n"
"s_add_u32 m0, s52, 53248\n"
"s_add_u32 s72, s51, 41472\n"
"buffer_load_dwordx4 v7, s[44:47], s72 offen lds\n"
"s_add_u32 m0, s52, 54272\n"
"s_add_u32 s72, s51, 51712\n"
"buffer_load_dwordx4 v7, s[44:47], s72 offen lds\n"
"s_add_u32 m0, s52, 55296\n"
"s_add_u32 s72, s51, 61952\n"
"buffer_load_dwordx4 v7, s[44:47], s72 offen lds\n"
"s_add_u32 m0, s52, 56320\n"
"s_add_u32 s72, s51, 72192\n"
"buffer_load_dwordx4 v7, s[44:47], s72 offen lds\n"
"s_add_u32 m0, s52, 57344\n"
"s_add_u32 s72, s51, 82432\n"
"buffer_load_dwordx4 v7, s[44:47], s72 offen lds\n"
"s_add_u32 m0, s52, 58368\n"
"s_add_u32 s72, s51, 92672\n"
"buffer_load_dwordx4 v7, s[44:47], s72 offen lds\n"
"s_add_u32 m0, s52, 59392\n"
"s_add_u32 s72, s51, 102912\n"
"buffer_load_dwordx4 v7, s[44:47], s72 offen lds\n"
"s_add_u32 m0, s52, 60416\n"
"s_add_u32 s72, s51, 113152\n"
"buffer_load_dwordx4 v7, s[44:47], s72 offen lds\n"
"v_add_u32 v20, 98304, v8\n"
"v_add_u32 v21, 98304, v9\n"
"ds_read_b64_tr_b16 v[88:89], v20 offset:0\n"
"ds_read_b64_tr_b16 v[90:91], v21 offset:0\n"
"ds_read_b64_tr_b16 v[92:93], v20 offset:6144\n"
"ds_read_b64_tr_b16 v[94:95], v21 offset:6144\n"
"ds_read_b64_tr_b16 a[80:81], v20 offset:12288\n"
"ds_read_b64_tr_b16 a[82:83], v21 offset:12288\n"
"ds_read_b64_tr_b16 a[84:85], v20 offset:18432\n"
"ds_read_b64_tr_b16 a[86:87], v21 offset:18432\n"
"ds_read_b64_tr_b16 a[104:105], v20 offset:2048\n"
"ds_read_b64_tr_b16 a[106:107], v21 offset:2048\n"
"ds_read_b64_tr_b16 a[108:109], v20 offset:8192\n"
"ds_read_b64_tr_b16 a[110:111], v21 offset:8192\n"
"ds_read_b64_tr_b16 a[128:129], v20 offset:14336\n"
"ds_read_b64_tr_b16 a[130:131], v21 offset:14336\n"
"ds_read_b64_tr_b16 a[132:133], v20 offset:20480\n"
"ds_read_b64_tr_b16 a[134:135], v21 offset:20480\n"
"ds_read_b64_tr_b16 a[152:153], v20 offset:4096\n"
"ds_read_b64_tr_b16 a[154:155], v21 offset:4096\n"
"ds_read_b64_tr_b16 a[156:157], v20 offset:10240\n"
"ds_read_b64_tr_b16 a[158:159], v21 offset:10240\n"
"ds_read_b64_tr_b16 a[160:161], v20 offset:16384\n"
"ds_read_b64_tr_b16 a[162:163], v21 offset:16384\n"
"ds_read_b64_tr_b16 a[164:165], v20 offset:22528\n"
"ds_read_b64_tr_b16 a[166:167], v21 offset:22528\n"
"s_waitcnt vmcnt(12)\n"
"s_waitcnt lgkmcnt(0)\n"
"s_barrier\n"
"s_add_u32 m0, s52, 98304\n"
"s_add_u32 s72, s51, 768\n"
"buffer_load_dwordx4 v7, s[44:47], s72 offen lds\n"
"s_add_u32 m0, s52, 99328\n"
"s_add_u32 s72, s51, 11008\n"
"buffer_load_dwordx4 v7, s[44:47], s72 offen lds\n"
"s_add_u32 m0, s52, 100352\n"
"s_add_u32 s72, s51, 21248\n"
"buffer_load_dwordx4 v7, s[44:47], s72 offen lds\n"
"s_add_u32 m0, s52, 101376\n"
"s_add_u32 s72, s51, 31488\n"
"buffer_load_dwordx4 v7, s[44:47], s72 offen lds\n"
"s_add_u32 m0, s52, 102400\n"
"s_add_u32 s72, s51, 41728\n"
"buffer_load_dwordx4 v7, s[44:47], s72 offen lds\n"
"s_add_u32 m0, s52, 103424\n"
"s_add_u32 s72, s51, 51968\n"
"buffer_load_dwordx4 v7, s[44:47], s72 offen lds\n"
"s_add_u32 m0, s52, 104448\n"
"s_add_u32 s72, s51, 62208\n"
"buffer_load_dwordx4 v7, s[44:47], s72 offen lds\n"
"s_add_u32 m0, s52, 105472\n"
"s_add_u32 s72, s51, 72448\n"
"buffer_load_dwordx4 v7, s[44:47], s72 offen lds\n"
"s_add_u32 m0, s52, 106496\n"
"s_add_u32 s72, s51, 82688\n"
"buffer_load_dwordx4 v7, s[44:47], s72 offen lds\n"
"s_add_u32 m0, s52, 107520\n"
"s_add_u32 s72, s51, 92928\n"
"buffer_load_dwordx4 v7, s[44:47], s72 offen lds\n"
"s_add_u32 m0, s52, 108544\n"
"s_add_u32 s72, s51, 103168\n"
"buffer_load_dwordx4 v7, s[44:47], s72 offen lds\n"
"s_add_u32 m0, s52, 109568\n"
"s_add_u32 s72, s51, 113408\n"
"buffer_load_dwordx4 v7, s[44:47], s72 offen lds\n"
"v_add_u32 v20, 0, v8\n"
"v_add_u32 v21, 0, v9\n"
"ds_read_b64_tr_b16 a[56:57], v20 offset:0\n"
"ds_read_b64_tr_b16 a[58:59], v21 offset:0\n"
"ds_read_b64_tr_b16 a[60:61], v20 offset:6144\n"
"ds_read_b64_tr_b16 a[62:63], v21 offset:6144\n"
"ds_read_b64_tr_b16 a[88:89], v20 offset:12288\n"
"ds_read_b64_tr_b16 a[90:91], v21 offset:12288\n"
"ds_read_b64_tr_b16 a[92:93], v20 offset:18432\n"
"ds_read_b64_tr_b16 a[94:95], v21 offset:18432\n"
"ds_read_b64_tr_b16 a[112:113], v20 offset:2048\n"
"ds_read_b64_tr_b16 a[114:115], v21 offset:2048\n"
"ds_read_b64_tr_b16 a[116:117], v20 offset:8192\n"
"ds_read_b64_tr_b16 a[118:119], v21 offset:8192\n"
"ds_read_b64_tr_b16 a[136:137], v20 offset:14336\n"
"ds_read_b64_tr_b16 a[138:139], v21 offset:14336\n"
"ds_read_b64_tr_b16 a[140:141], v20 offset:20480\n"
"ds_read_b64_tr_b16 a[142:143], v21 offset:20480\n"
"ds_read_b64_tr_b16 v[104:105], v20 offset:4096\n"
"ds_read_b64_tr_b16 v[106:107], v21 offset:4096\n"
"ds_read_b64_tr_b16 v[108:109], v20 offset:10240\n"
"ds_read_b64_tr_b16 v[110:111], v21 offset:10240\n"
"ds_read_b64_tr_b16 a[168:169], v20 offset:16384\n"
"ds_read_b64_tr_b16 a[170:171], v21 offset:16384\n"
"ds_read_b64_tr_b16 a[172:173], v20 offset:22528\n"
"ds_read_b64_tr_b16 a[174:175], v21 offset:22528\n"
"s_waitcnt vmcnt(12)\n"
"s_waitcnt lgkmcnt(0)\n"
"s_barrier\n"
"s_add_u32 m0, s52, 0\n"
"s_add_u32 s72, s51, 1024\n"
"buffer_load_dwordx4 v7, s[44:47], s72 offen lds\n"
"s_add_u32 m0, s52, 1024\n"
"s_add_u32 s72, s51, 11264\n"
"buffer_load_dwordx4 v7, s[44:47], s72 offen lds\n"
"s_add_u32 m0, s52, 2048\n"
"s_add_u32 s72, s51, 21504\n"
"buffer_load_dwordx4 v7, s[44:47], s72 offen lds\n"
"s_add_u32 m0, s52, 3072\n"
"s_add_u32 s72, s51, 31744\n"
"buffer_load_dwordx4 v7, s[44:47], s72 offen lds\n"
"s_add_u32 m0, s52, 4096\n"
"s_add_u32 s72, s51, 41984\n"
"buffer_load_dwordx4 v7, s[44:47], s72 offen lds\n"
"s_add_u32 m0, s52, 5120\n"
"s_add_u32 s72, s51, 52224\n"
"buffer_load_dwordx4 v7, s[44:47], s72 offen lds\n"
"s_add_u32 m0, s52, 6144\n"
"s_add_u32 s72, s51, 62464\n"
"buffer_load_dwordx4 v7, s[44:47], s72 offen lds\n"
"s_add_u32 m0, s52, 7168\n"
"s_add_u32 s72, s51, 72704\n"
"buffer_load_dwordx4 v7, s[44:47], s72 offen lds\n"
"s_add_u32 m0, s52, 8192\n"
"s_add_u32 s72, s51, 82944\n"
"buffer_load_dwordx4 v7, s[44:47], s72 offen lds\n"
"s_add_u32 m0, s52, 9216\n"
"s_add_u32 s72, s51, 93184\n"
"buffer_load_dwordx4 v7, s[44:47], s72 offen lds\n"
"s_add_u32 m0, s52, 10240\n"
"s_add_u32 s72, s51, 103424\n"
"buffer_load_dwordx4 v7, s[44:47], s72 offen lds\n"
"s_add_u32 m0, s52, 11264\n"
"s_add_u32 s72, s51, 113664\n"
"buffer_load_dwordx4 v7, s[44:47], s72 offen lds\n"
"v_add_u32 v20, 49152, v8\n"
"v_add_u32 v21, 49152, v9\n"
"ds_read_b64_tr_b16 a[64:65], v20 offset:0\n"
"ds_read_b64_tr_b16 a[66:67], v21 offset:0\n"
"ds_read_b64_tr_b16 a[68:69], v20 offset:6144\n"
"ds_read_b64_tr_b16 a[70:71], v21 offset:6144\n"
"ds_read_b64_tr_b16 v[96:97], v20 offset:12288\n"
"ds_read_b64_tr_b16 v[98:99], v21 offset:12288\n"
"ds_read_b64_tr_b16 v[100:101], v20 offset:18432\n"
"ds_read_b64_tr_b16 v[102:103], v21 offset:18432\n"
"ds_read_b64_tr_b16 a[120:121], v20 offset:2048\n"
"ds_read_b64_tr_b16 a[122:123], v21 offset:2048\n"
"ds_read_b64_tr_b16 a[124:125], v20 offset:8192\n"
"ds_read_b64_tr_b16 a[126:127], v21 offset:8192\n"
"ds_read_b64_tr_b16 a[144:145], v20 offset:14336\n"
"ds_read_b64_tr_b16 a[146:147], v21 offset:14336\n"
"ds_read_b64_tr_b16 a[148:149], v20 offset:20480\n"
"ds_read_b64_tr_b16 a[150:151], v21 offset:20480\n"
"ds_read_b64_tr_b16 a[176:177], v20 offset:4096\n"
"ds_read_b64_tr_b16 a[178:179], v21 offset:4096\n"
"ds_read_b64_tr_b16 a[180:181], v20 offset:10240\n"
"ds_read_b64_tr_b16 a[182:183], v21 offset:10240\n"
"ds_read_b64_tr_b16 a[184:185], v20 offset:16384\n"
"ds_read_b64_tr_b16 a[186:187], v21 offset:16384\n"
"ds_read_b64_tr_b16 a[188:189], v20 offset:22528\n"
"ds_read_b64_tr_b16 a[190:191], v21 offset:22528\n"
"s_cmp_lg_u32 s66, 0\n"
"s_cbranch_scc1 V28_%=\n"
"s_mov_b32 s76, 131072\n"
"s_mov_b64 exec, 1\n"
"s_branch V30_%=\n"
"V29_%=:\n"
"s_sleep 4\n"
"V30_%=:\n"
"v_mov_b32 v29, 0\n"
"buffer_atomic_add v29, v26, s[36:39], s23 offen sc0 sc1\n"
"s_sub_u32 s76, s76, 1\n"
"s_cbranch_scc1 VABORT_%=\n"
"s_waitcnt vmcnt(0)\n"
"v_xor_b32 v29, v29, v24\n"
"v_cmp_lt_u32 vcc, 1, v29\n"
"s_nop 0\n"
"s_cbranch_vccnz V29_%=\n"
"v_mov_b32 v28, 153600\n"
"ds_write_b32 v28, v29\n"
"v_mov_b32 v24, 1\n"
"s_add_u32 s22, s23, 16384\n"
"buffer_store_dword v24, v26, s[36:39], s22 offen sc1\n"
"s_mov_b64 exec, -1\n"
"V28_%=:\n"
"s_waitcnt lgkmcnt(0)\n"
"s_barrier\n"
"v_mov_b32 v28, 153600\n"
"ds_read_b32 v28, v28\n"
"s_waitcnt lgkmcnt(0)\n"
"v_readfirstlane_b32 s22, v28\n"
"s_nop 0\n"
"s_lshl_b32 s22, s22, 13\n"
"v_xor_b32 v13, s22, v13\n"
"s_xor_b32 s68, s68, s22\n"
"s_xor_b32 s20, s20, s22\n"
"s_xor_b32 s54, s54, s22\n"
"s_waitcnt vmcnt(12)\n"
"s_waitcnt lgkmcnt(0)\n"
"s_barrier\n"
"v_add_u32 v20, 98304, v8\n"
"v_add_u32 v21, 98304, v9\n"
"ds_read_b64_tr_b16 a[72:73], v20 offset:0\n"
"ds_read_b64_tr_b16 a[74:75], v21 offset:0\n"
"ds_read_b64_tr_b16 a[76:77], v20 offset:6144\n"
"ds_read_b64_tr_b16 a[78:79], v21 offset:6144\n"
"ds_read_b64_tr_b16 a[96:97], v20 offset:12288\n"
"ds_read_b64_tr_b16 a[98:99], v21 offset:12288\n"
"ds_read_b64_tr_b16 a[100:101], v20 offset:18432\n"
"ds_read_b64_tr_b16 a[102:103], v21 offset:18432\n"
"ds_read_b64_tr_b16 a[192:193], v20 offset:2048\n"
"ds_read_b64_tr_b16 a[194:195], v21 offset:2048\n"
"ds_read_b64_tr_b16 a[196:197], v20 offset:8192\n"
"ds_read_b64_tr_b16 a[198:199], v21 offset:8192\n"
"ds_read_b64_tr_b16 a[200:201], v20 offset:14336\n"
"ds_read_b64_tr_b16 a[202:203], v21 offset:14336\n"
"ds_read_b64_tr_b16 a[204:205], v20 offset:20480\n"
"ds_read_b64_tr_b16 a[206:207], v21 offset:20480\n"
"ds_read_b64_tr_b16 v[112:113], v20 offset:4096\n"
"ds_read_b64_tr_b16 v[114:115], v21 offset:4096\n"
"ds_read_b64_tr_b16 v[116:117], v20 offset:10240\n"
"ds_read_b64_tr_b16 v[118:119], v21 offset:10240\n"
"ds_read_b64_tr_b16 a[208:209], v20 offset:16384\n"
"ds_read_b64_tr_b16 a[210:211], v21 offset:16384\n"
"ds_read_b64_tr_b16 a[212:213], v20 offset:22528\n"
"ds_read_b64_tr_b16 a[214:215], v21 offset:22528\n"
"s_waitcnt vmcnt(0)\n"
"s_waitcnt lgkmcnt(0)\n"
"s_barrier\n"
"v_add_u32 v20, 0, v8\n"
"v_add_u32 v21, 0, v9\n"
"ds_read_b64_tr_b16 a[216:217], v20 offset:0\n"
"ds_read_b64_tr_b16 a[218:219], v21 offset:0\n"
"ds_read_b64_tr_b16 a[220:221], v20 offset:6144\n"
"ds_read_b64_tr_b16 a[222:223], v21 offset:6144\n"
"ds_read_b64_tr_b16 a[224:225], v20 offset:12288\n"
"ds_read_b64_tr_b16 a[226:227], v21 offset:12288\n"
"ds_read_b64_tr_b16 a[228:229], v20 offset:18432\n"
"ds_read_b64_tr_b16 a[230:231], v21 offset:18432\n"
"ds_read_b64_tr_b16 a[232:233], v20 offset:2048\n"
"ds_read_b64_tr_b16 a[234:235], v21 offset:2048\n"
"ds_read_b64_tr_b16 a[236:237], v20 offset:8192\n"
"ds_read_b64_tr_b16 a[238:239], v21 offset:8192\n"
"ds_read_b64_tr_b16 v[120:121], v20 offset:14336\n"
"ds_read_b64_tr_b16 v[122:123], v21 offset:14336\n"
"ds_read_b64_tr_b16 v[124:125], v20 offset:20480\n"
"ds_read_b64_tr_b16 v[126:127], v21 offset:20480\n"
"ds_read_b64_tr_b16 a[240:241], v20 offset:4096\n"
"ds_read_b64_tr_b16 a[242:243], v21 offset:4096\n"
"ds_read_b64_tr_b16 a[244:245], v20 offset:10240\n"
"ds_read_b64_tr_b16 a[246:247], v21 offset:10240\n"
"ds_read_b64_tr_b16 a[248:249], v20 offset:16384\n"
"ds_read_b64_tr_b16 a[250:251], v21 offset:16384\n"
"ds_read_b64_tr_b16 a[252:253], v20 offset:22528\n"
"ds_read_b64_tr_b16 a[254:255], v21 offset:22528\n"
"s_waitcnt lgkmcnt(0)\n"
"s_barrier\n"
"ds_write_b128 v15, v[32:35] offset:0\n"
"ds_write_b128 v15, v[36:39] offset:1024\n"
"ds_write_b128 v15, v[40:43] offset:2048\n"
"ds_write_b128 v15, v[44:47] offset:3072\n"
"ds_write_b128 v15, v[48:51] offset:4096\n"
"ds_write_b128 v15, v[52:55] offset:5120\n"
"ds_write_b128 v15, v[56:59] offset:6144\n"
"ds_write_b128 v15, v[60:63] offset:7168\n"
"ds_write_b128 v15, v[64:67] offset:8192\n"
"ds_write_b128 v15, v[68:71] offset:9216\n"
"ds_write_b128 v15, v[72:75] offset:10240\n"
"ds_write_b128 v15, v[76:79] offset:11264\n"
"ds_write_b128 v15, v[80:83] offset:12288\n"
"ds_write_b128 v15, v[84:87] offset:13312\n"
"ds_write_b128 v15, v[88:91] offset:14336\n"
"ds_write_b128 v15, v[92:95] offset:15360\n"
"ds_write_b128 v15, v[96:99] offset:16384\n"
"ds_write_b128 v15, v[100:103] offset:17408\n"
"ds_write_b128 v15, v[104:107] offset:18432\n"
"ds_write_b128 v15, v[108:111] offset:19456\n"
"ds_write_b128 v15, v[112:115] offset:20480\n"
"ds_write_b128 v15, v[116:119] offset:21504\n"
"ds_write_b128 v15, v[120:123] offset:22528\n"
"ds_write_b128 v15, v[124:127] offset:23552\n"
"s_waitcnt lgkmcnt(0)\n"
"ds_read_b128 v[96:99], v15 offset:0\n"
"ds_read_b128 v[100:103], v15 offset:1024\n"
"ds_read_b128 v[72:75], v15 offset:2048\n"
"ds_read_b128 v[76:79], v15 offset:3072\n"
"ds_read_b128 v[104:107], v15 offset:4096\n"
"ds_read_b128 v[108:111], v15 offset:5120\n"
"ds_read_b128 v[80:83], v15 offset:6144\n"
"ds_read_b128 v[84:87], v15 offset:7168\n"
"ds_read_b128 v[112:115], v15 offset:8192\n"
"ds_read_b128 v[116:119], v15 offset:9216\n"
"ds_read_b128 v[88:91], v15 offset:10240\n"
"ds_read_b128 v[92:95], v15 offset:11264\n"
"v_mov_b32 v32, 0\n"
"v_mov_b32 v33, 0\n"
"v_mov_b32 v34, 0\n"
"v_mov_b32 v35, 0\n"
"v_mov_b32 v36, 0\n"
"v_mov_b32 v37, 0\n"
"v_mov_b32 v38, 0\n"
"v_mov_b32 v39, 0\n"
"v_mov_b32 v40, 0\n"
"v_mov_b32 v41, 0\n"
"v_mov_b32 v42, 0\n"
"v_mov_b32 v43, 0\n"
"v_mov_b32 v44, 0\n"
"v_mov_b32 v45, 0\n"
"v_mov_b32 v46, 0\n"
"v_mov_b32 v47, 0\n"
"v_mov_b32 v48, 0\n"
"v_mov_b32 v49, 0\n"
"v_mov_b32 v50, 0\n"
"v_mov_b32 v51, 0\n"
"v_mov_b32 v52, 0\n"
"v_mov_b32 v53, 0\n"
"v_mov_b32 v54, 0\n"
"v_mov_b32 v55, 0\n"
"v_mov_b32 v120, 0\n"
"v_mov_b32 v121, 0\n"
"v_mov_b32 v122, 0\n"
"v_mov_b32 v123, 0\n"
"v_mov_b32 v124, 0\n"
"v_mov_b32 v125, 0\n"
"v_mov_b32 v126, 0\n"
"v_mov_b32 v127, 0\n"
"s_add_u32 s70, s69, 0\n"
"s_lshr_b32 s71, s70, 8\n"
"s_xor_b32 s71, s71, s54\n"
"s_and_b32 s71, s71, 0x2000\n"
"s_add_u32 s70, s70, s71\n"
"s_add_u32 s55, s70, s53\n"
"s_add_u32 s70, s69, 131072\n"
"s_lshr_b32 s71, s70, 8\n"
"s_xor_b32 s71, s71, s54\n"
"s_and_b32 s71, s71, 0x2000\n"
"s_add_u32 s70, s70, s71\n"
"s_add_u32 s56, s70, s53\n"
"s_add_u32 s70, s69, 262144\n"
"s_lshr_b32 s71, s70, 8\n"
"s_xor_b32 s71, s71, s54\n"
"s_and_b32 s71, s71, 0x2000\n"
"s_add_u32 s70, s70, s71\n"
"s_add_u32 s57, s70, s53\n"
"s_add_u32 s70, s69, 393216\n"
"s_lshr_b32 s71, s70, 8\n"
"s_xor_b32 s71, s71, s54\n"
"s_and_b32 s71, s71, 0x2000\n"
"s_add_u32 s70, s70, s71\n"
"s_add_u32 s58, s70, s53\n"
"s_add_u32 s70, s69, 524288\n"
"s_lshr_b32 s71, s70, 8\n"
"s_xor_b32 s71, s71, s54\n"
"s_and_b32 s71, s71, 0x2000\n"
"s_add_u32 s70, s70, s71\n"
"s_add_u32 s59, s70, s53\n"
"s_nop 0\n"
"V32_%=:\n"
"v_mov_b32 v25, s73\n"
"s_cmp_gt_u32 s73, 61\n"
"s_cselect_b32 s52, 0, 234881024\n"
"s_add_u32 s60, s68, 70656\n"
"s_add_u32 s61, s68, 71680\n"
"s_add_u32 s62, s68, 39936\n"
"s_waitcnt lgkmcnt(0)\n"
"s_barrier\n"
"buffer_load_dwordx4 v[56:59], v0, s[36:39], s60 offen offset:0 sc1\n"
"s_nop 12\n"
"buffer_load_dwordx4 v[60:63], v0, s[36:39], s61 offen offset:0 sc1\n"
"s_nop 12\n"
"s_nop 8\n"
"buffer_load_dwordx4 v[64:67], v0, s[36:39], s62 offen offset:0 sc1\n"
"buffer_load_dword v30, v0, s[36:39], s38 offen\n"
"V33_%=:\n"
"s_waitcnt vmcnt(3)\n"
"v_smfmac_f32_16x16x64_bf16 v[32:35], v[56:59], v[96:103], v6\n"
"ds_write_b128 v0, v[56:59] offset:6144\n"
"v_max3_u32 v29, v56, v57, 0\n"
"v_smfmac_f32_16x16x64_bf16 v[36:39], v[56:59], v[72:79], v6\n"
"v_max3_u32 v29, v58, v59, v29\n"
"v_smfmac_f32_16x16x64_bf16 v[40:43], v[56:59], v[104:111], v6\n"
"s_waitcnt vmcnt(2)\n"
"ds_write_b128 v0, v[60:63] offset:7168\n"
"v_max3_u32 v29, v60, v61, v29\n"
"v_smfmac_f32_16x16x64_bf16 v[32:35], v[60:63], v[80:87], v6\n"
"v_max3_u32 v29, v62, v63, v29\n"
"v_smfmac_f32_16x16x64_bf16 v[36:39], v[60:63], v[112:119], v6\n"
"s_waitcnt vmcnt(1)\n"
"v_max3_u32 v29, v64, v65, v29\n"
"v_max3_u32 v29, v66, v67, v29\n"
"v_smfmac_f32_16x16x64_bf16 v[40:43], v[60:63], v[88:95], v6\n"
"v_cmp_eq_u32 vcc, -1, v29\n"
"v_smfmac_f32_16x16x64_bf16 v[32:35], v[64:67], v[128:135], v6\n"
"v_smfmac_f32_16x16x64_bf16 v[36:39], v[64:67], v[136:143], v6\n"
"s_cbranch_vccnz V34_%=\n"
"s_waitcnt lgkmcnt(0)\n"
"s_barrier\n"
"ds_read_b128 v[68:71], v0 offset:8192\n"
"ds_read_b128 v[72:75], v0 offset:9216\n"
"v_smfmac_f32_16x16x64_bf16 v[40:43], v[64:67], v[144:151], v6\n"
"ds_read_b128 v[76:79], v0 offset:0\n"
"ds_read_b128 v[80:83], v0 offset:1024\n"
"ds_read_b128 v[84:87], v0 offset:2048\n"
"ds_read_b128 v[88:91], v0 offset:3072\n"
"ds_read_b128 v[92:95], v0 offset:4096\n"
"s_waitcnt lgkmcnt(6)\n"
"v_smfmac_f32_16x16x64_bf16 v[32:35], v[68:71], v[152:159], v6\n"
"s_waitcnt lgkmcnt(5)\n"
"v_smfmac_f32_16x16x64_bf16 v[32:35], v[72:75], v[160:167], v6\n"
"s_waitcnt lgkmcnt(4)\n"
"v_smfmac_f32_16x16x64_bf16 v[32:35], v[76:79], v[168:175], v6\n"
"s_waitcnt lgkmcnt(3)\n"
"v_smfmac_f32_16x16x64_bf16 v[32:35], v[80:83], v[176:183], v6\n"
"buffer_store_dword v25, v13, s[36:39], s52 offen sc1\n"
"s_waitcnt lgkmcnt(2)\n"
"v_smfmac_f32_16x16x64_bf16 v[32:35], v[84:87], v[184:191], v6\n"
"ds_read_b128 v[96:99], v15 offset:12288\n"
"s_waitcnt lgkmcnt(2)\n"
"v_smfmac_f32_16x16x64_bf16 v[32:35], v[88:91], v[192:199], v6\n"
"ds_read_b128 v[100:103], v15 offset:13312\n"
"s_waitcnt lgkmcnt(2)\n"
"v_smfmac_f32_16x16x64_bf16 v[32:35], v[92:95], v[200:207], v6\n"
"ds_read_b128 v[104:107], v15 offset:14336\n"
"s_nop 0\n"
"v_smfmac_f32_16x16x64_bf16 v[36:39], v[68:71], v[208:215], v6\n"
"ds_read_b128 v[108:111], v15 offset:15360\n"
"s_nop 0\n"
"v_smfmac_f32_16x16x64_bf16 v[36:39], v[72:75], v[216:223], v6\n"
"ds_read_b128 v[112:115], v15 offset:16384\n"
"v_smfmac_f32_16x16x64_bf16 v[36:39], v[76:79], v[224:231], v6\n"
"ds_read_b128 v[116:119], v15 offset:17408\n"
"v_add_f32 v32, v32, v34\n"
"v_smfmac_f32_16x16x64_bf16 v[36:39], v[80:83], v[232:239], v6\n"
"v_add_f32 v33, v33, v35\n"
"v_cvt_pk_bf16_f32 v32, v32, v33\n"
"v_smfmac_f32_16x16x64_bf16 v[36:39], v[84:87], v[240:247], v6\n"
"ds_write_b32 v10, v32 offset:0\n"
"v_smfmac_f32_16x16x64_bf16 v[36:39], v[88:91], v[248:255], v6\n"
"v_smfmac_f32_16x16x64_bf16 v[36:39], v[92:95], a[0:7], v6\n"
"s_nop 0\n"
"v_smfmac_f32_16x16x64_bf16 v[40:43], v[68:71], a[8:15], v6\n"
"s_nop 0\n"
"s_waitcnt lgkmcnt(5)\n"
"v_smfmac_f32_16x16x64_bf16 v[40:43], v[72:75], v[96:103], v6\n"
"v_smfmac_f32_16x16x64_bf16 v[40:43], v[76:79], a[16:23], v6\n"
"s_waitcnt lgkmcnt(0)\n"
"s_barrier\n"
"ds_read_b64_tr_b16 v[56:57], v12 offset:0\n"
"ds_read_b64_tr_b16 v[58:59], v12 offset:32\n"
"v_add_f32 v36, v36, v38\n"
"v_smfmac_f32_16x16x64_bf16 v[40:43], v[80:83], a[24:31], v6\n"
"ds_read_b64_tr_b16 v[60:61], v12 offset:256\n"
"ds_read_b64_tr_b16 v[62:63], v12 offset:288\n"
"v_add_f32 v37, v37, v39\n"
"v_cvt_pk_bf16_f32 v36, v36, v37\n"
"v_smfmac_f32_16x16x64_bf16 v[40:43], v[84:87], a[32:39], v6\n"
"ds_write_b32 v10, v36 offset:512\n"
"v_smfmac_f32_16x16x64_bf16 v[40:43], v[88:91], a[40:47], v6\n"
"v_smfmac_f32_16x16x64_bf16 v[40:43], v[92:95], a[48:55], v6\n"
"ds_read_b128 v[96:99], v15 offset:18432\n"
"s_nop 0\n"
"s_waitcnt lgkmcnt(4)\n"
"v_smfmac_f32_16x16x64_bf16 v[44:47], v[56:59], v[104:111], v11\n"
"ds_read_b128 v[100:103], v15 offset:19456\n"
"s_nop 0\n"
"v_smfmac_f32_16x16x64_bf16 v[48:51], v[56:59], a[56:63], v11\n"
"v_smfmac_f32_16x16x64_bf16 v[52:55], v[56:59], a[64:71], v11\n"
"s_waitcnt lgkmcnt(2)\n"
"s_barrier\n"
"ds_read_b64_tr_b16 v[64:65], v12 offset:512\n"
"ds_read_b64_tr_b16 v[66:67], v12 offset:544\n"
"v_add_f32 v40, v40, v42\n"
"v_smfmac_f32_16x16x64_bf16 v[120:123], v[56:59], a[72:79], v11\n"
"ds_read_b64_tr_b16 v[68:69], v12 offset:768\n"
"ds_read_b64_tr_b16 v[70:71], v12 offset:800\n"
"v_add_f32 v41, v41, v43\n"
"v_cvt_pk_bf16_f32 v40, v40, v41\n"
"v_smfmac_f32_16x16x64_bf16 v[44:47], v[60:63], a[80:87], v11\n"
"ds_write_b32 v10, v40 offset:1024\n"
"v_smfmac_f32_16x16x64_bf16 v[48:51], v[60:63], a[88:95], v11\n"
"v_smfmac_f32_16x16x64_bf16 v[52:55], v[60:63], v[112:119], v11\n"
"ds_read_b128 v[104:107], v15 offset:20480\n"
"v_smfmac_f32_16x16x64_bf16 v[120:123], v[60:63], a[96:103], v11\n"
"ds_read_b128 v[108:111], v15 offset:21504\n"
"s_waitcnt lgkmcnt(5)\n"
"v_smfmac_f32_16x16x64_bf16 v[44:47], v[64:67], a[104:111], v11\n"
"v_smfmac_f32_16x16x64_bf16 v[48:51], v[64:67], a[112:119], v11\n"
"s_waitcnt lgkmcnt(2)\n"
"s_barrier\n"
"ds_read_b64_tr_b16 v[72:73], v12 offset:1024\n"
"ds_read_b64_tr_b16 v[74:75], v12 offset:1056\n"
"v_smfmac_f32_16x16x64_bf16 v[52:55], v[64:67], a[120:127], v11\n"
"ds_read_b64_tr_b16 v[76:77], v12 offset:1280\n"
"ds_read_b64_tr_b16 v[78:79], v12 offset:1312\n"
"v_smfmac_f32_16x16x64_bf16 v[44:47], v[68:71], a[128:135], v11\n"
"v_smfmac_f32_16x16x64_bf16 v[48:51], v[68:71], a[136:143], v11\n"
"v_smfmac_f32_16x16x64_bf16 v[52:55], v[68:71], a[144:151], v11\n"
"s_waitcnt lgkmcnt(2)\n"
"v_smfmac_f32_16x16x64_bf16 v[44:47], v[72:75], a[152:159], v11\n"
"ds_read_b128 v[112:115], v15 offset:22528\n"
"s_waitcnt lgkmcnt(1)\n"
"v_smfmac_f32_16x16x64_bf16 v[44:47], v[76:79], a[160:167], v11\n"
"ds_read_b128 v[116:119], v15 offset:23552\n"
"v_smfmac_f32_16x16x64_bf16 v[48:51], v[72:75], v[96:103], v11\n"
"s_nop 1\n"
"v_smfmac_f32_16x16x64_bf16 v[48:51], v[76:79], a[168:175], v11\n"
"s_nop 1\n"
"v_smfmac_f32_16x16x64_bf16 v[52:55], v[72:75], a[176:183], v11\n"
"v_cvt_pk_bf16_f32 v80, v44, v45\n"
"v_smfmac_f32_16x16x64_bf16 v[52:55], v[76:79], a[184:191], v11\n"
"v_cvt_pk_bf16_f32 v81, v46, v47\n"
"v_min_u32 v80, -2, v80\n"
"v_smfmac_f32_16x16x64_bf16 v[120:123], v[64:67], a[192:199], v11\n"
"v_min_u32 v81, -2, v81\n"
"v_smfmac_f32_16x16x64_bf16 v[120:123], v[68:71], a[200:207], v11\n"
"buffer_store_dwordx2 v[80:81], v16, s[36:39], s55 offen sc1\n"
"v_cvt_pk_bf16_f32 v82, v48, v49\n"
"v_smfmac_f32_16x16x64_bf16 v[120:123], v[72:75], v[104:111], v11\n"
"v_cvt_pk_bf16_f32 v83, v50, v51\n"
"v_min_u32 v82, -2, v82\n"
"v_smfmac_f32_16x16x64_bf16 v[120:123], v[76:79], a[208:215], v11\n"
"v_min_u32 v83, -2, v83\n"
"v_smfmac_f32_16x16x64_bf16 v[124:127], v[56:59], a[216:223], v11\n"
"buffer_store_dwordx2 v[82:83], v16, s[36:39], s56 offen sc1\n"
"v_cvt_pk_bf16_f32 v84, v52, v53\n"
"v_smfmac_f32_16x16x64_bf16 v[124:127], v[60:63], a[224:231], v11\n"
"v_cvt_pk_bf16_f32 v85, v54, v55\n"
"v_min_u32 v84, -2, v84\n"
"v_smfmac_f32_16x16x64_bf16 v[124:127], v[64:67], a[232:239], v11\n"
"v_min_u32 v85, -2, v85\n"
"s_waitcnt lgkmcnt(0)\n"
"v_smfmac_f32_16x16x64_bf16 v[124:127], v[68:71], v[112:119], v11\n"
"buffer_store_dwordx2 v[84:85], v16, s[36:39], s57 offen sc1\n"
"v_cvt_pk_bf16_f32 v86, v120, v121\n"
"v_smfmac_f32_16x16x64_bf16 v[124:127], v[72:75], a[240:247], v11\n"
"v_cvt_pk_bf16_f32 v87, v122, v123\n"
"v_min_u32 v86, -2, v86\n"
"v_smfmac_f32_16x16x64_bf16 v[124:127], v[76:79], a[248:255], v11\n"
"v_min_u32 v87, -2, v87\n"
"buffer_store_dwordx2 v[86:87], v16, s[36:39], s58 offen sc1\n"
"s_nop 5\n"
"v_cvt_pk_bf16_f32 v88, v124, v125\n"
"v_cvt_pk_bf16_f32 v89, v126, v127\n"
"v_min_u32 v88, -2, v88\n"
"v_min_u32 v89, -2, v89\n"
"buffer_store_dwordx2 v[88:89], v16, s[36:39], s59 offen sc1\n"
"s_add_u32 s73, s73, 1\n"
"s_add_u32 s69, s69, 655360\n"
"s_add_u32 s79, s68, 131072\n"
"s_and_b32 s79, s79, 0x3fffff\n"
"s_xor_b32 s68, s79, s68\n"
"s_lshr_b32 s68, s68, 8\n"
"s_and_b32 s68, s68, 8192\n"
"s_xor_b32 s68, s68, s79\n"
"s_cmp_lt_u32 s73, 64\n"
"s_cbranch_scc0 V31_%=\n"
"ds_read_b128 v[96:99], v15 offset:0\n"
"ds_read_b128 v[100:103], v15 offset:1024\n"
"ds_read_b128 v[72:75], v15 offset:2048\n"
"ds_read_b128 v[76:79], v15 offset:3072\n"
"ds_read_b128 v[104:107], v15 offset:4096\n"
"ds_read_b128 v[108:111], v15 offset:5120\n"
"ds_read_b128 v[80:83], v15 offset:6144\n"
"ds_read_b128 v[84:87], v15 offset:7168\n"
"ds_read_b128 v[112:115], v15 offset:8192\n"
"ds_read_b128 v[116:119], v15 offset:9216\n"
"ds_read_b128 v[88:91], v15 offset:10240\n"
"ds_read_b128 v[92:95], v15 offset:11264\n"
"v_mov_b32 v32, 0\n"
"v_mov_b32 v33, 0\n"
"v_mov_b32 v34, 0\n"
"v_mov_b32 v35, 0\n"
"v_mov_b32 v36, 0\n"
"v_mov_b32 v37, 0\n"
"v_mov_b32 v38, 0\n"
"v_mov_b32 v39, 0\n"
"v_mov_b32 v40, 0\n"
"v_mov_b32 v41, 0\n"
"v_mov_b32 v42, 0\n"
"v_mov_b32 v43, 0\n"
"v_mov_b32 v44, 0\n"
"v_mov_b32 v45, 0\n"
"v_mov_b32 v46, 0\n"
"v_mov_b32 v47, 0\n"
"v_mov_b32 v48, 0\n"
"v_mov_b32 v49, 0\n"
"v_mov_b32 v50, 0\n"
"v_mov_b32 v51, 0\n"
"v_mov_b32 v52, 0\n"
"v_mov_b32 v53, 0\n"
"v_mov_b32 v54, 0\n"
"v_mov_b32 v55, 0\n"
"v_mov_b32 v120, 0\n"
"v_mov_b32 v121, 0\n"
"v_mov_b32 v122, 0\n"
"v_mov_b32 v123, 0\n"
"v_mov_b32 v124, 0\n"
"v_mov_b32 v125, 0\n"
"v_mov_b32 v126, 0\n"
"v_mov_b32 v127, 0\n"
"s_add_u32 s70, s69, 0\n"
"s_lshr_b32 s71, s70, 8\n"
"s_xor_b32 s71, s71, s54\n"
"s_and_b32 s71, s71, 0x2000\n"
"s_add_u32 s70, s70, s71\n"
"s_add_u32 s55, s70, s53\n"
"s_add_u32 s70, s69, 131072\n"
"s_lshr_b32 s71, s70, 8\n"
"s_xor_b32 s71, s71, s54\n"
"s_and_b32 s71, s71, 0x2000\n"
"s_add_u32 s70, s70, s71\n"
"s_add_u32 s56, s70, s53\n"
"s_add_u32 s70, s69, 262144\n"
"s_lshr_b32 s71, s70, 8\n"
"s_xor_b32 s71, s71, s54\n"
"s_and_b32 s71, s71, 0x2000\n"
"s_add_u32 s70, s70, s71\n"
"s_add_u32 s57, s70, s53\n"
"s_add_u32 s70, s69, 393216\n"
"s_lshr_b32 s71, s70, 8\n"
"s_xor_b32 s71, s71, s54\n"
"s_and_b32 s71, s71, 0x2000\n"
"s_add_u32 s70, s70, s71\n"
"s_add_u32 s58, s70, s53\n"
"s_add_u32 s70, s69, 524288\n"
"s_lshr_b32 s71, s70, 8\n"
"s_xor_b32 s71, s71, s54\n"
"s_and_b32 s71, s71, 0x2000\n"
"s_add_u32 s70, s70, s71\n"
"s_add_u32 s59, s70, s53\n"
"s_branch V32_%=\n"
"V34_%=:\n"
"s_waitcnt lgkmcnt(0)\n"
"s_mov_b32 s76, 131072\n"
"V35_%=:\n"
"s_memrealtime s[4:5]\n"
"buffer_load_dwordx4 v[56:59], v0, s[36:39], s60 offen offset:0 sc1\n"
"buffer_load_dwordx4 v[60:63], v0, s[36:39], s61 offen offset:0 sc1\n"
"buffer_load_dwordx4 v[64:67], v0, s[36:39], s62 offen offset:0 sc1\n"
"v_mov_b32 v29, 0\n"
"ds_read_b32 v31, v29 offset:16896\n"
"s_lshl_b32 s77, s73, 14\n"
"s_add_u32 s77, s77, s20\n"
"s_add_u32 s77, s77, s21\n"
"s_add_u32 s77, s77, 4096\n"
"v_mov_b32 v32, 0\n"
"v_mov_b32 v33, 0\n"
"v_mov_b32 v34, 0\n"
"v_mov_b32 v35, 0\n"
"v_mov_b32 v36, 0\n"
"v_mov_b32 v37, 0\n"
"v_mov_b32 v38, 0\n"
"v_mov_b32 v39, 0\n"
"v_mov_b32 v40, 0\n"
"v_mov_b32 v41, 0\n"
"v_mov_b32 v42, 0\n"
"v_mov_b32 v43, 0\n"
"s_sub_u32 s76, s76, 1\n"
"s_cbranch_scc1 VABORT_%=\n"
"s_waitcnt vmcnt(2)\n"
"v_max3_u32 v29, v56, v57, 0\n"
"v_max3_u32 v29, v58, v59, v29\n"
"s_waitcnt vmcnt(1)\n"
"v_max3_u32 v29, v60, v61, v29\n"
"v_max3_u32 v29, v62, v63, v29\n"
"s_waitcnt vmcnt(0)\n"
"v_max3_u32 v29, v64, v65, v29\n"
"v_max3_u32 v29, v66, v67, v29\n"
"v_cmp_eq_u32 vcc, -1, v29\n"
"s_nop 0\n"
"s_cbranch_vccnz V35_%=\n"
"s_waitcnt lgkmcnt(0)\n"
"v_lshl_add_u32 v31, s4, 3, v31\n"
"v_lshrrev_b32 v30, 2, v0\n"
"s_mov_b64 exec, 0xffff\n"
"buffer_store_dword v31, v30, s[36:39], s77 offen sc1\n"
"s_mov_b64 exec, -1\n"
"s_branch V33_%=\n"
"V31_%=:\n"
"s_waitcnt vmcnt(0)\n"
"s_barrier\n"
"v_mov_b32 v29, s73\n"
"buffer_store_dword v29, v13, s[36:39], 0 offen sc1\n"
"s_branch V5_%=\n"
"V4_%=:\n"
"s_mov_b32 s22, 46301184\n"
"s_mov_b32 s23, 46317568\n"
"s_cmp_lg_u32 s66, 0\n"
"s_cbranch_scc1 V36_%=\n"
"v_mov_b32 v24, 0\n"
"s_mov_b64 exec, 1\n"
"buffer_atomic_add v24, v26, s[36:39], s23 offen sc0 sc1\n"
"s_mov_b64 exec, -1\n"
"V36_%=:\n"
"s_add_u32 m0, s50, 0\n"
"s_add_u32 s72, s48, 0\n"
"buffer_load_dwordx4 v1, s[40:43], s72 offen lds\n"
"s_add_u32 m0, s50, 1024\n"
"s_add_u32 s72, s48, 98304\n"
"buffer_load_dwordx4 v2, s[40:43], s72 offen lds\n"
"s_add_u32 m0, s50, 2048\n"
"s_add_u32 s72, s48, 196608\n"
"buffer_load_dwordx4 v1, s[40:43], s72 offen lds\n"
"s_add_u32 m0, s50, 3072\n"
"s_add_u32 s72, s48, 294912\n"
"buffer_load_dwordx4 v2, s[40:43], s72 offen lds\n"
"s_add_u32 m0, s50, 16384\n"
"s_add_u32 s72, s48, 128\n"
"buffer_load_dwordx4 v1, s[40:43], s72 offen lds\n"
"s_add_u32 m0, s50, 17408\n"
"s_add_u32 s72, s48, 98432\n"
"buffer_load_dwordx4 v2, s[40:43], s72 offen lds\n"
"s_add_u32 m0, s50, 18432\n"
"s_add_u32 s72, s48, 196736\n"
"buffer_load_dwordx4 v1, s[40:43], s72 offen lds\n"
"s_add_u32 m0, s50, 19456\n"
"s_add_u32 s72, s48, 295040\n"
"buffer_load_dwordx4 v2, s[40:43], s72 offen lds\n"
"s_add_u32 m0, s50, 32768\n"
"s_add_u32 s72, s48, 256\n"
"buffer_load_dwordx4 v1, s[40:43], s72 offen lds\n"
"s_add_u32 m0, s50, 33792\n"
"s_add_u32 s72, s48, 98560\n"
"buffer_load_dwordx4 v2, s[40:43], s72 offen lds\n"
"s_add_u32 m0, s50, 34816\n"
"s_add_u32 s72, s48, 196864\n"
"buffer_load_dwordx4 v1, s[40:43], s72 offen lds\n"
"s_add_u32 m0, s50, 35840\n"
"s_add_u32 s72, s48, 295168\n"
"buffer_load_dwordx4 v2, s[40:43], s72 offen lds\n"
"s_add_u32 m0, s50, 49152\n"
"s_add_u32 s72, s48, 1572864\n"
"buffer_load_dwordx4 v1, s[40:43], s72 offen lds\n"
"s_add_u32 m0, s50, 50176\n"
"s_add_u32 s72, s48, 1671168\n"
"buffer_load_dwordx4 v2, s[40:43], s72 offen lds\n"
"s_add_u32 m0, s50, 51200\n"
"s_add_u32 s72, s48, 1769472\n"
"buffer_load_dwordx4 v1, s[40:43], s72 offen lds\n"
"s_add_u32 m0, s50, 52224\n"
"s_add_u32 s72, s48, 1867776\n"
"buffer_load_dwordx4 v2, s[40:43], s72 offen lds\n"
"s_add_u32 m0, s50, 65536\n"
"s_add_u32 s72, s48, 1572992\n"
"buffer_load_dwordx4 v1, s[40:43], s72 offen lds\n"
"s_add_u32 m0, s50, 66560\n"
"s_add_u32 s72, s48, 1671296\n"
"buffer_load_dwordx4 v2, s[40:43], s72 offen lds\n"
"s_add_u32 m0, s50, 67584\n"
"s_add_u32 s72, s48, 1769600\n"
"buffer_load_dwordx4 v1, s[40:43], s72 offen lds\n"
"s_add_u32 m0, s50, 68608\n"
"s_add_u32 s72, s48, 1867904\n"
"buffer_load_dwordx4 v2, s[40:43], s72 offen lds\n"
"s_add_u32 m0, s50, 81920\n"
"s_add_u32 s72, s48, 1573120\n"
"buffer_load_dwordx4 v1, s[40:43], s72 offen lds\n"
"s_add_u32 m0, s50, 82944\n"
"s_add_u32 s72, s48, 1671424\n"
"buffer_load_dwordx4 v2, s[40:43], s72 offen lds\n"
"s_add_u32 m0, s50, 83968\n"
"s_add_u32 s72, s48, 1769728\n"
"buffer_load_dwordx4 v1, s[40:43], s72 offen lds\n"
"s_add_u32 m0, s50, 84992\n"
"s_add_u32 s72, s48, 1868032\n"
"buffer_load_dwordx4 v2, s[40:43], s72 offen lds\n"
"s_waitcnt vmcnt(12)\n"
"s_waitcnt lgkmcnt(0)\n"
"s_barrier\n"
"s_cmp_lg_u32 s66, 0\n"
"s_cbranch_scc1 V37_%=\n"
"v_or_b32 v24, 1, v24\n"
"v_add_u32 v24, 1, v24\n"
"buffer_store_dword v24, v26, s[36:39], s22 offen sc1\n"
"V37_%=:\n"
"s_add_u32 m0, s50, 98304\n"
"s_add_u32 s72, s48, 3145728\n"
"buffer_load_dwordx4 v1, s[40:43], s72 offen lds\n"
"s_add_u32 m0, s50, 99328\n"
"s_add_u32 s72, s48, 3244032\n"
"buffer_load_dwordx4 v2, s[40:43], s72 offen lds\n"
"s_add_u32 m0, s50, 100352\n"
"s_add_u32 s72, s48, 3342336\n"
"buffer_load_dwordx4 v1, s[40:43], s72 offen lds\n"
"s_add_u32 m0, s50, 101376\n"
"s_add_u32 s72, s48, 3440640\n"
"buffer_load_dwordx4 v2, s[40:43], s72 offen lds\n"
"s_add_u32 m0, s50, 114688\n"
"s_add_u32 s72, s48, 3145856\n"
"buffer_load_dwordx4 v1, s[40:43], s72 offen lds\n"
"s_add_u32 m0, s50, 115712\n"
"s_add_u32 s72, s48, 3244160\n"
"buffer_load_dwordx4 v2, s[40:43], s72 offen lds\n"
"s_add_u32 m0, s50, 116736\n"
"s_add_u32 s72, s48, 3342464\n"
"buffer_load_dwordx4 v1, s[40:43], s72 offen lds\n"
"s_add_u32 m0, s50, 117760\n"
"s_add_u32 s72, s48, 3440768\n"
"buffer_load_dwordx4 v2, s[40:43], s72 offen lds\n"
"s_add_u32 m0, s50, 131072\n"
"s_add_u32 s72, s48, 3145984\n"
"buffer_load_dwordx4 v1, s[40:43], s72 offen lds\n"
"s_add_u32 m0, s50, 132096\n"
"s_add_u32 s72, s48, 3244288\n"
"buffer_load_dwordx4 v2, s[40:43], s72 offen lds\n"
"s_add_u32 m0, s50, 133120\n"
"s_add_u32 s72, s48, 3342592\n"
"buffer_load_dwordx4 v1, s[40:43], s72 offen lds\n"
"s_add_u32 m0, s50, 134144\n"
"s_add_u32 s72, s48, 3440896\n"
"buffer_load_dwordx4 v2, s[40:43], s72 offen lds\n"
"v_add_u32 v20, 0, v3\n"
"v_add_u32 v21, 0, v4\n"
"v_add_u32 v22, 0, v5\n"
"ds_read_b64_tr_b16 v[128:129], v20 offset:0\n"
"ds_read_b64_tr_b16 v[130:131], v20 offset:256\n"
"ds_read_b64_tr_b16 v[132:133], v20 offset:4096\n"
"ds_read_b64_tr_b16 v[134:135], v20 offset:4352\n"
"ds_read_b64_tr_b16 v[136:137], v21 offset:0\n"
"ds_read_b64_tr_b16 v[138:139], v21 offset:256\n"
"ds_read_b64_tr_b16 v[140:141], v21 offset:4096\n"
"ds_read_b64_tr_b16 v[142:143], v21 offset:4352\n"
"ds_read_b64_tr_b16 v[144:145], v22 offset:0\n"
"ds_read_b64_tr_b16 v[146:147], v22 offset:256\n"
"ds_read_b64_tr_b16 v[148:149], v22 offset:4096\n"
"ds_read_b64_tr_b16 v[150:151], v22 offset:4352\n"
"ds_read_b64_tr_b16 v[152:153], v20 offset:8192\n"
"ds_read_b64_tr_b16 v[154:155], v20 offset:8448\n"
"ds_read_b64_tr_b16 v[156:157], v20 offset:12288\n"
"ds_read_b64_tr_b16 v[158:159], v20 offset:12544\n"
"ds_read_b64_tr_b16 v[208:209], v21 offset:8192\n"
"ds_read_b64_tr_b16 v[210:211], v21 offset:8448\n"
"ds_read_b64_tr_b16 v[212:213], v21 offset:12288\n"
"ds_read_b64_tr_b16 v[214:215], v21 offset:12544\n"
"ds_read_b64_tr_b16 a[8:9], v22 offset:8192\n"
"ds_read_b64_tr_b16 a[10:11], v22 offset:8448\n"
"ds_read_b64_tr_b16 a[12:13], v22 offset:12288\n"
"ds_read_b64_tr_b16 a[14:15], v22 offset:12544\n"
"s_waitcnt vmcnt(12)\n"
"s_waitcnt lgkmcnt(0)\n"
"s_barrier\n"
"s_add_u32 m0, s50, 0\n"
"s_add_u32 s72, s48, 4718592\n"
"buffer_load_dwordx4 v1, s[40:43], s72 offen lds\n"
"s_add_u32 m0, s50, 1024\n"
"s_add_u32 s72, s48, 4816896\n"
"buffer_load_dwordx4 v2, s[40:43], s72 offen lds\n"
"s_add_u32 m0, s50, 2048\n"
"s_add_u32 s72, s48, 4915200\n"
"buffer_load_dwordx4 v1, s[40:43], s72 offen lds\n"
"s_add_u32 m0, s50, 3072\n"
"s_add_u32 s72, s48, 5013504\n"
"buffer_load_dwordx4 v2, s[40:43], s72 offen lds\n"
"s_add_u32 m0, s50, 16384\n"
"s_add_u32 s72, s48, 4718720\n"
"buffer_load_dwordx4 v1, s[40:43], s72 offen lds\n"
"s_add_u32 m0, s50, 17408\n"
"s_add_u32 s72, s48, 4817024\n"
"buffer_load_dwordx4 v2, s[40:43], s72 offen lds\n"
"s_add_u32 m0, s50, 18432\n"
"s_add_u32 s72, s48, 4915328\n"
"buffer_load_dwordx4 v1, s[40:43], s72 offen lds\n"
"s_add_u32 m0, s50, 19456\n"
"s_add_u32 s72, s48, 5013632\n"
"buffer_load_dwordx4 v2, s[40:43], s72 offen lds\n"
"s_add_u32 m0, s50, 32768\n"
"s_add_u32 s72, s48, 4718848\n"
"buffer_load_dwordx4 v1, s[40:43], s72 offen lds\n"
"s_add_u32 m0, s50, 33792\n"
"s_add_u32 s72, s48, 4817152\n"
"buffer_load_dwordx4 v2, s[40:43], s72 offen lds\n"
"s_add_u32 m0, s50, 34816\n"
"s_add_u32 s72, s48, 4915456\n"
"buffer_load_dwordx4 v1, s[40:43], s72 offen lds\n"
"s_add_u32 m0, s50, 35840\n"
"s_add_u32 s72, s48, 5013760\n"
"buffer_load_dwordx4 v2, s[40:43], s72 offen lds\n"
"v_add_u32 v20, 49152, v3\n"
"v_add_u32 v21, 49152, v4\n"
"v_add_u32 v22, 49152, v5\n"
"ds_read_b64_tr_b16 v[160:161], v20 offset:0\n"
"ds_read_b64_tr_b16 v[162:163], v20 offset:256\n"
"ds_read_b64_tr_b16 v[164:165], v20 offset:4096\n"
"ds_read_b64_tr_b16 v[166:167], v20 offset:4352\n"
"ds_read_b64_tr_b16 v[216:217], v21 offset:0\n"
"ds_read_b64_tr_b16 v[218:219], v21 offset:256\n"
"ds_read_b64_tr_b16 v[220:221], v21 offset:4096\n"
"ds_read_b64_tr_b16 v[222:223], v21 offset:4352\n"
"ds_read_b64_tr_b16 v[80:81], v22 offset:0\n"
"ds_read_b64_tr_b16 v[82:83], v22 offset:256\n"
"ds_read_b64_tr_b16 v[84:85], v22 offset:4096\n"
"ds_read_b64_tr_b16 v[86:87], v22 offset:4352\n"
"ds_read_b64_tr_b16 v[168:169], v20 offset:8192\n"
"ds_read_b64_tr_b16 v[170:171], v20 offset:8448\n"
"ds_read_b64_tr_b16 v[172:173], v20 offset:12288\n"
"ds_read_b64_tr_b16 v[174:175], v20 offset:12544\n"
"ds_read_b64_tr_b16 v[224:225], v21 offset:8192\n"
"ds_read_b64_tr_b16 v[226:227], v21 offset:8448\n"
"ds_read_b64_tr_b16 v[228:229], v21 offset:12288\n"
"ds_read_b64_tr_b16 v[230:231], v21 offset:12544\n"
"ds_read_b64_tr_b16 a[16:17], v22 offset:8192\n"
"ds_read_b64_tr_b16 a[18:19], v22 offset:8448\n"
"ds_read_b64_tr_b16 a[20:21], v22 offset:12288\n"
"ds_read_b64_tr_b16 a[22:23], v22 offset:12544\n"
"s_waitcnt vmcnt(12)\n"
"s_waitcnt lgkmcnt(0)\n"
"s_barrier\n"
"s_add_u32 m0, s50, 49152\n"
"s_add_u32 s72, s48, 6291456\n"
"buffer_load_dwordx4 v1, s[40:43], s72 offen lds\n"
"s_add_u32 m0, s50, 50176\n"
"s_add_u32 s72, s48, 6389760\n"
"buffer_load_dwordx4 v2, s[40:43], s72 offen lds\n"
"s_add_u32 m0, s50, 51200\n"
"s_add_u32 s72, s48, 6488064\n"
"buffer_load_dwordx4 v1, s[40:43], s72 offen lds\n"
"s_add_u32 m0, s50, 52224\n"
"s_add_u32 s72, s48, 6586368\n"
"buffer_load_dwordx4 v2, s[40:43], s72 offen lds\n"
"s_add_u32 m0, s50, 65536\n"
"s_add_u32 s72, s48, 6291584\n"
"buffer_load_dwordx4 v1, s[40:43], s72 offen lds\n"
"s_add_u32 m0, s50, 66560\n"
"s_add_u32 s72, s48, 6389888\n"
"buffer_load_dwordx4 v2, s[40:43], s72 offen lds\n"
"s_add_u32 m0, s50, 67584\n"
"s_add_u32 s72, s48, 6488192\n"
"buffer_load_dwordx4 v1, s[40:43], s72 offen lds\n"
"s_add_u32 m0, s50, 68608\n"
"s_add_u32 s72, s48, 6586496\n"
"buffer_load_dwordx4 v2, s[40:43], s72 offen lds\n"
"s_add_u32 m0, s50, 81920\n"
"s_add_u32 s72, s48, 6291712\n"
"buffer_load_dwordx4 v1, s[40:43], s72 offen lds\n"
"s_add_u32 m0, s50, 82944\n"
"s_add_u32 s72, s48, 6390016\n"
"buffer_load_dwordx4 v2, s[40:43], s72 offen lds\n"
"s_add_u32 m0, s50, 83968\n"
"s_add_u32 s72, s48, 6488320\n"
"buffer_load_dwordx4 v1, s[40:43], s72 offen lds\n"
"s_add_u32 m0, s50, 84992\n"
"s_add_u32 s72, s48, 6586624\n"
"buffer_load_dwordx4 v2, s[40:43], s72 offen lds\n"
"v_add_u32 v20, 98304, v3\n"
"v_add_u32 v21, 98304, v4\n"
"v_add_u32 v22, 98304, v5\n"
"ds_read_b64_tr_b16 v[176:177], v20 offset:0\n"
"ds_read_b64_tr_b16 v[178:179], v20 offset:256\n"
"ds_read_b64_tr_b16 v[180:181], v20 offset:4096\n"
"ds_read_b64_tr_b16 v[182:183], v20 offset:4352\n"
"ds_read_b64_tr_b16 v[232:233], v21 offset:0\n"
"ds_read_b64_tr_b16 v[234:235], v21 offset:256\n"
"ds_read_b64_tr_b16 v[236:237], v21 offset:4096\n"
"ds_read_b64_tr_b16 v[238:239], v21 offset:4352\n"
"ds_read_b64_tr_b16 a[24:25], v22 offset:0\n"
"ds_read_b64_tr_b16 a[26:27], v22 offset:256\n"
"ds_read_b64_tr_b16 a[28:29], v22 offset:4096\n"
"ds_read_b64_tr_b16 a[30:31], v22 offset:4352\n"
"ds_read_b64_tr_b16 v[184:185], v20 offset:8192\n"
"ds_read_b64_tr_b16 v[186:187], v20 offset:8448\n"
"ds_read_b64_tr_b16 v[188:189], v20 offset:12288\n"
"ds_read_b64_tr_b16 v[190:191], v20 offset:12544\n"
"ds_read_b64_tr_b16 v[240:241], v21 offset:8192\n"
"ds_read_b64_tr_b16 v[242:243], v21 offset:8448\n"
"ds_read_b64_tr_b16 v[244:245], v21 offset:12288\n"
"ds_read_b64_tr_b16 v[246:247], v21 offset:12544\n"
"ds_read_b64_tr_b16 a[32:33], v22 offset:8192\n"
"ds_read_b64_tr_b16 a[34:35], v22 offset:8448\n"
"ds_read_b64_tr_b16 a[36:37], v22 offset:12288\n"
"ds_read_b64_tr_b16 a[38:39], v22 offset:12544\n"
"s_waitcnt vmcnt(12)\n"
"s_waitcnt lgkmcnt(0)\n"
"s_barrier\n"
"s_add_u32 m0, s52, 98304\n"
"s_add_u32 s72, s51, 0\n"
"buffer_load_dwordx4 v7, s[44:47], s72 offen lds\n"
"s_add_u32 m0, s52, 99328\n"
"s_add_u32 s72, s51, 10240\n"
"buffer_load_dwordx4 v7, s[44:47], s72 offen lds\n"
"s_add_u32 m0, s52, 100352\n"
"s_add_u32 s72, s51, 20480\n"
"buffer_load_dwordx4 v7, s[44:47], s72 offen lds\n"
"s_add_u32 m0, s52, 101376\n"
"s_add_u32 s72, s51, 30720\n"
"buffer_load_dwordx4 v7, s[44:47], s72 offen lds\n"
"s_add_u32 m0, s52, 102400\n"
"s_add_u32 s72, s51, 40960\n"
"buffer_load_dwordx4 v7, s[44:47], s72 offen lds\n"
"s_add_u32 m0, s52, 103424\n"
"s_add_u32 s72, s51, 51200\n"
"buffer_load_dwordx4 v7, s[44:47], s72 offen lds\n"
"s_add_u32 m0, s52, 104448\n"
"s_add_u32 s72, s51, 61440\n"
"buffer_load_dwordx4 v7, s[44:47], s72 offen lds\n"
"s_add_u32 m0, s52, 105472\n"
"s_add_u32 s72, s51, 71680\n"
"buffer_load_dwordx4 v7, s[44:47], s72 offen lds\n"
"s_add_u32 m0, s52, 106496\n"
"s_add_u32 s72, s51, 81920\n"
"buffer_load_dwordx4 v7, s[44:47], s72 offen lds\n"
"s_add_u32 m0, s52, 107520\n"
"s_add_u32 s72, s51, 92160\n"
"buffer_load_dwordx4 v7, s[44:47], s72 offen lds\n"
"s_add_u32 m0, s52, 108544\n"
"s_add_u32 s72, s51, 102400\n"
"buffer_load_dwordx4 v7, s[44:47], s72 offen lds\n"
"s_add_u32 m0, s52, 109568\n"
"s_add_u32 s72, s51, 112640\n"
"buffer_load_dwordx4 v7, s[44:47], s72 offen lds\n"
"v_add_u32 v20, 0, v3\n"
"v_add_u32 v21, 0, v4\n"
"v_add_u32 v22, 0, v5\n"
"ds_read_b64_tr_b16 v[192:193], v20 offset:0\n"
"ds_read_b64_tr_b16 v[194:195], v20 offset:256\n"
"ds_read_b64_tr_b16 v[196:197], v20 offset:4096\n"
"ds_read_b64_tr_b16 v[198:199], v20 offset:4352\n"
"ds_read_b64_tr_b16 v[248:249], v21 offset:0\n"
"ds_read_b64_tr_b16 v[250:251], v21 offset:256\n"
"ds_read_b64_tr_b16 v[252:253], v21 offset:4096\n"
"ds_read_b64_tr_b16 v[254:255], v21 offset:4352\n"
"ds_read_b64_tr_b16 a[40:41], v22 offset:0\n"
"ds_read_b64_tr_b16 a[42:43], v22 offset:256\n"
"ds_read_b64_tr_b16 a[44:45], v22 offset:4096\n"
"ds_read_b64_tr_b16 a[46:47], v22 offset:4352\n"
"ds_read_b64_tr_b16 v[200:201], v20 offset:8192\n"
"ds_read_b64_tr_b16 v[202:203], v20 offset:8448\n"
"ds_read_b64_tr_b16 v[204:205], v20 offset:12288\n"
"ds_read_b64_tr_b16 v[206:207], v20 offset:12544\n"
"ds_read_b64_tr_b16 a[0:1], v21 offset:8192\n"
"ds_read_b64_tr_b16 a[2:3], v21 offset:8448\n"
"ds_read_b64_tr_b16 a[4:5], v21 offset:12288\n"
"ds_read_b64_tr_b16 a[6:7], v21 offset:12544\n"
"ds_read_b64_tr_b16 a[48:49], v22 offset:8192\n"
"ds_read_b64_tr_b16 a[50:51], v22 offset:8448\n"
"ds_read_b64_tr_b16 a[52:53], v22 offset:12288\n"
"ds_read_b64_tr_b16 a[54:55], v22 offset:12544\n"
"s_waitcnt vmcnt(12)\n"
"s_waitcnt lgkmcnt(0)\n"
"s_barrier\n"
"s_add_u32 m0, s52, 0\n"
"s_add_u32 s72, s51, 256\n"
"buffer_load_dwordx4 v7, s[44:47], s72 offen lds\n"
"s_add_u32 m0, s52, 1024\n"
"s_add_u32 s72, s51, 10496\n"
"buffer_load_dwordx4 v7, s[44:47], s72 offen lds\n"
"s_add_u32 m0, s52, 2048\n"
"s_add_u32 s72, s51, 20736\n"
"buffer_load_dwordx4 v7, s[44:47], s72 offen lds\n"
"s_add_u32 m0, s52, 3072\n"
"s_add_u32 s72, s51, 30976\n"
"buffer_load_dwordx4 v7, s[44:47], s72 offen lds\n"
"s_add_u32 m0, s52, 4096\n"
"s_add_u32 s72, s51, 41216\n"
"buffer_load_dwordx4 v7, s[44:47], s72 offen lds\n"
"s_add_u32 m0, s52, 5120\n"
"s_add_u32 s72, s51, 51456\n"
"buffer_load_dwordx4 v7, s[44:47], s72 offen lds\n"
"s_add_u32 m0, s52, 6144\n"
"s_add_u32 s72, s51, 61696\n"
"buffer_load_dwordx4 v7, s[44:47], s72 offen lds\n"
"s_add_u32 m0, s52, 7168\n"
"s_add_u32 s72, s51, 71936\n"
"buffer_load_dwordx4 v7, s[44:47], s72 offen lds\n"
"s_add_u32 m0, s52, 8192\n"
"s_add_u32 s72, s51, 82176\n"
"buffer_load_dwordx4 v7, s[44:47], s72 offen lds\n"
"s_add_u32 m0, s52, 9216\n"
"s_add_u32 s72, s51, 92416\n"
"buffer_load_dwordx4 v7, s[44:47], s72 offen lds\n"
"s_add_u32 m0, s52, 10240\n"
"s_add_u32 s72, s51, 102656\n"
"buffer_load_dwordx4 v7, s[44:47], s72 offen lds\n"
"s_add_u32 m0, s52, 11264\n"
"s_add_u32 s72, s51, 112896\n"
"buffer_load_dwordx4 v7, s[44:47], s72 offen lds\n"
"v_add_u32 v20, 49152, v3\n"
"v_add_u32 v21, 49152, v4\n"
"v_add_u32 v22, 49152, v5\n"
"ds_read_b64_tr_b16 v[32:33], v20 offset:0\n"
"ds_read_b64_tr_b16 v[34:35], v20 offset:256\n"
"ds_read_b64_tr_b16 v[36:37], v20 offset:4096\n"
"ds_read_b64_tr_b16 v[38:39], v20 offset:4352\n"
"ds_read_b64_tr_b16 v[40:41], v21 offset:0\n"
"ds_read_b64_tr_b16 v[42:43], v21 offset:256\n"
"ds_read_b64_tr_b16 v[44:45], v21 offset:4096\n"
"ds_read_b64_tr_b16 v[46:47], v21 offset:4352\n"
"ds_read_b64_tr_b16 v[48:49], v22 offset:0\n"
"ds_read_b64_tr_b16 v[50:51], v22 offset:256\n"
"ds_read_b64_tr_b16 v[52:53], v22 offset:4096\n"
"ds_read_b64_tr_b16 v[54:55], v22 offset:4352\n"
"ds_read_b64_tr_b16 v[56:57], v20 offset:8192\n"
"ds_read_b64_tr_b16 v[58:59], v20 offset:8448\n"
"ds_read_b64_tr_b16 v[60:61], v20 offset:12288\n"
"ds_read_b64_tr_b16 v[62:63], v20 offset:12544\n"
"ds_read_b64_tr_b16 v[64:65], v21 offset:8192\n"
"ds_read_b64_tr_b16 v[66:67], v21 offset:8448\n"
"ds_read_b64_tr_b16 v[68:69], v21 offset:12288\n"
"ds_read_b64_tr_b16 v[70:71], v21 offset:12544\n"
"ds_read_b64_tr_b16 v[72:73], v22 offset:8192\n"
"ds_read_b64_tr_b16 v[74:75], v22 offset:8448\n"
"ds_read_b64_tr_b16 v[76:77], v22 offset:12288\n"
"ds_read_b64_tr_b16 v[78:79], v22 offset:12544\n"
"s_waitcnt vmcnt(12)\n"
"s_waitcnt lgkmcnt(0)\n"
"s_barrier\n"
"s_add_u32 m0, s52, 49152\n"
"s_add_u32 s72, s51, 512\n"
"buffer_load_dwordx4 v7, s[44:47], s72 offen lds\n"
"s_add_u32 m0, s52, 50176\n"
"s_add_u32 s72, s51, 10752\n"
"buffer_load_dwordx4 v7, s[44:47], s72 offen lds\n"
"s_add_u32 m0, s52, 51200\n"
"s_add_u32 s72, s51, 20992\n"
"buffer_load_dwordx4 v7, s[44:47], s72 offen lds\n"
"s_add_u32 m0, s52, 52224\n"
"s_add_u32 s72, s51, 31232\n"
"buffer_load_dwordx4 v7, s[44:47], s72 offen lds\n"
"s_add_u32 m0, s52, 53248\n"
"s_add_u32 s72, s51, 41472\n"
"buffer_load_dwordx4 v7, s[44:47], s72 offen lds\n"
"s_add_u32 m0, s52, 54272\n"
"s_add_u32 s72, s51, 51712\n"
"buffer_load_dwordx4 v7, s[44:47], s72 offen lds\n"
"s_add_u32 m0, s52, 55296\n"
"s_add_u32 s72, s51, 61952\n"
"buffer_load_dwordx4 v7, s[44:47], s72 offen lds\n"
"s_add_u32 m0, s52, 56320\n"
"s_add_u32 s72, s51, 72192\n"
"buffer_load_dwordx4 v7, s[44:47], s72 offen lds\n"
"s_add_u32 m0, s52, 57344\n"
"s_add_u32 s72, s51, 82432\n"
"buffer_load_dwordx4 v7, s[44:47], s72 offen lds\n"
"s_add_u32 m0, s52, 58368\n"
"s_add_u32 s72, s51, 92672\n"
"buffer_load_dwordx4 v7, s[44:47], s72 offen lds\n"
"s_add_u32 m0, s52, 59392\n"
"s_add_u32 s72, s51, 102912\n"
"buffer_load_dwordx4 v7, s[44:47], s72 offen lds\n"
"s_add_u32 m0, s52, 60416\n"
"s_add_u32 s72, s51, 113152\n"
"buffer_load_dwordx4 v7, s[44:47], s72 offen lds\n"
"v_add_u32 v20, 98304, v8\n"
"v_add_u32 v21, 98304, v9\n"
"ds_read_b64_tr_b16 v[88:89], v20 offset:0\n"
"ds_read_b64_tr_b16 v[90:91], v21 offset:0\n"
"ds_read_b64_tr_b16 v[92:93], v20 offset:6144\n"
"ds_read_b64_tr_b16 v[94:95], v21 offset:6144\n"
"ds_read_b64_tr_b16 a[80:81], v20 offset:12288\n"
"ds_read_b64_tr_b16 a[82:83], v21 offset:12288\n"
"ds_read_b64_tr_b16 a[84:85], v20 offset:18432\n"
"ds_read_b64_tr_b16 a[86:87], v21 offset:18432\n"
"ds_read_b64_tr_b16 a[104:105], v20 offset:2048\n"
"ds_read_b64_tr_b16 a[106:107], v21 offset:2048\n"
"ds_read_b64_tr_b16 a[108:109], v20 offset:8192\n"
"ds_read_b64_tr_b16 a[110:111], v21 offset:8192\n"
"ds_read_b64_tr_b16 a[128:129], v20 offset:14336\n"
"ds_read_b64_tr_b16 a[130:131], v21 offset:14336\n"
"ds_read_b64_tr_b16 a[132:133], v20 offset:20480\n"
"ds_read_b64_tr_b16 a[134:135], v21 offset:20480\n"
"ds_read_b64_tr_b16 a[152:153], v20 offset:4096\n"
"ds_read_b64_tr_b16 a[154:155], v21 offset:4096\n"
"ds_read_b64_tr_b16 a[156:157], v20 offset:10240\n"
"ds_read_b64_tr_b16 a[158:159], v21 offset:10240\n"
"ds_read_b64_tr_b16 a[160:161], v20 offset:16384\n"
"ds_read_b64_tr_b16 a[162:163], v21 offset:16384\n"
"ds_read_b64_tr_b16 a[164:165], v20 offset:22528\n"
"ds_read_b64_tr_b16 a[166:167], v21 offset:22528\n"
"s_waitcnt vmcnt(12)\n"
"s_waitcnt lgkmcnt(0)\n"
"s_barrier\n"
"s_add_u32 m0, s52, 98304\n"
"s_add_u32 s72, s51, 768\n"
"buffer_load_dwordx4 v7, s[44:47], s72 offen lds\n"
"s_add_u32 m0, s52, 99328\n"
"s_add_u32 s72, s51, 11008\n"
"buffer_load_dwordx4 v7, s[44:47], s72 offen lds\n"
"s_add_u32 m0, s52, 100352\n"
"s_add_u32 s72, s51, 21248\n"
"buffer_load_dwordx4 v7, s[44:47], s72 offen lds\n"
"s_add_u32 m0, s52, 101376\n"
"s_add_u32 s72, s51, 31488\n"
"buffer_load_dwordx4 v7, s[44:47], s72 offen lds\n"
"s_add_u32 m0, s52, 102400\n"
"s_add_u32 s72, s51, 41728\n"
"buffer_load_dwordx4 v7, s[44:47], s72 offen lds\n"
"s_add_u32 m0, s52, 103424\n"
"s_add_u32 s72, s51, 51968\n"
"buffer_load_dwordx4 v7, s[44:47], s72 offen lds\n"
"s_add_u32 m0, s52, 104448\n"
"s_add_u32 s72, s51, 62208\n"
"buffer_load_dwordx4 v7, s[44:47], s72 offen lds\n"
"s_add_u32 m0, s52, 105472\n"
"s_add_u32 s72, s51, 72448\n"
"buffer_load_dwordx4 v7, s[44:47], s72 offen lds\n"
"s_add_u32 m0, s52, 106496\n"
"s_add_u32 s72, s51, 82688\n"
"buffer_load_dwordx4 v7, s[44:47], s72 offen lds\n"
"s_add_u32 m0, s52, 107520\n"
"s_add_u32 s72, s51, 92928\n"
"buffer_load_dwordx4 v7, s[44:47], s72 offen lds\n"
"s_add_u32 m0, s52, 108544\n"
"s_add_u32 s72, s51, 103168\n"
"buffer_load_dwordx4 v7, s[44:47], s72 offen lds\n"
"s_add_u32 m0, s52, 109568\n"
"s_add_u32 s72, s51, 113408\n"
"buffer_load_dwordx4 v7, s[44:47], s72 offen lds\n"
"v_add_u32 v20, 0, v8\n"
"v_add_u32 v21, 0, v9\n"
"ds_read_b64_tr_b16 a[56:57], v20 offset:0\n"
"ds_read_b64_tr_b16 a[58:59], v21 offset:0\n"
"ds_read_b64_tr_b16 a[60:61], v20 offset:6144\n"
"ds_read_b64_tr_b16 a[62:63], v21 offset:6144\n"
"ds_read_b64_tr_b16 a[88:89], v20 offset:12288\n"
"ds_read_b64_tr_b16 a[90:91], v21 offset:12288\n"
"ds_read_b64_tr_b16 a[92:93], v20 offset:18432\n"
"ds_read_b64_tr_b16 a[94:95], v21 offset:18432\n"
"ds_read_b64_tr_b16 a[112:113], v20 offset:2048\n"
"ds_read_b64_tr_b16 a[114:115], v21 offset:2048\n"
"ds_read_b64_tr_b16 a[116:117], v20 offset:8192\n"
"ds_read_b64_tr_b16 a[118:119], v21 offset:8192\n"
"ds_read_b64_tr_b16 a[136:137], v20 offset:14336\n"
"ds_read_b64_tr_b16 a[138:139], v21 offset:14336\n"
"ds_read_b64_tr_b16 a[140:141], v20 offset:20480\n"
"ds_read_b64_tr_b16 a[142:143], v21 offset:20480\n"
"ds_read_b64_tr_b16 v[104:105], v20 offset:4096\n"
"ds_read_b64_tr_b16 v[106:107], v21 offset:4096\n"
"ds_read_b64_tr_b16 v[108:109], v20 offset:10240\n"
"ds_read_b64_tr_b16 v[110:111], v21 offset:10240\n"
"ds_read_b64_tr_b16 a[168:169], v20 offset:16384\n"
"ds_read_b64_tr_b16 a[170:171], v21 offset:16384\n"
"ds_read_b64_tr_b16 a[172:173], v20 offset:22528\n"
"ds_read_b64_tr_b16 a[174:175], v21 offset:22528\n"
"s_waitcnt vmcnt(12)\n"
"s_waitcnt lgkmcnt(0)\n"
"s_barrier\n"
"s_add_u32 m0, s52, 0\n"
"s_add_u32 s72, s51, 1024\n"
"buffer_load_dwordx4 v7, s[44:47], s72 offen lds\n"
"s_add_u32 m0, s52, 1024\n"
"s_add_u32 s72, s51, 11264\n"
"buffer_load_dwordx4 v7, s[44:47], s72 offen lds\n"
"s_add_u32 m0, s52, 2048\n"
"s_add_u32 s72, s51, 21504\n"
"buffer_load_dwordx4 v7, s[44:47], s72 offen lds\n"
"s_add_u32 m0, s52, 3072\n"
"s_add_u32 s72, s51, 31744\n"
"buffer_load_dwordx4 v7, s[44:47], s72 offen lds\n"
"s_add_u32 m0, s52, 4096\n"
"s_add_u32 s72, s51, 41984\n"
"buffer_load_dwordx4 v7, s[44:47], s72 offen lds\n"
"s_add_u32 m0, s52, 5120\n"
"s_add_u32 s72, s51, 52224\n"
"buffer_load_dwordx4 v7, s[44:47], s72 offen lds\n"
"s_add_u32 m0, s52, 6144\n"
"s_add_u32 s72, s51, 62464\n"
"buffer_load_dwordx4 v7, s[44:47], s72 offen lds\n"
"s_add_u32 m0, s52, 7168\n"
"s_add_u32 s72, s51, 72704\n"
"buffer_load_dwordx4 v7, s[44:47], s72 offen lds\n"
"s_add_u32 m0, s52, 8192\n"
"s_add_u32 s72, s51, 82944\n"
"buffer_load_dwordx4 v7, s[44:47], s72 offen lds\n"
"s_add_u32 m0, s52, 9216\n"
"s_add_u32 s72, s51, 93184\n"
"buffer_load_dwordx4 v7, s[44:47], s72 offen lds\n"
"s_add_u32 m0, s52, 10240\n"
"s_add_u32 s72, s51, 103424\n"
"buffer_load_dwordx4 v7, s[44:47], s72 offen lds\n"
"s_add_u32 m0, s52, 11264\n"
"s_add_u32 s72, s51, 113664\n"
"buffer_load_dwordx4 v7, s[44:47], s72 offen lds\n"
"v_add_u32 v20, 49152, v8\n"
"v_add_u32 v21, 49152, v9\n"
"ds_read_b64_tr_b16 a[64:65], v20 offset:0\n"
"ds_read_b64_tr_b16 a[66:67], v21 offset:0\n"
"ds_read_b64_tr_b16 a[68:69], v20 offset:6144\n"
"ds_read_b64_tr_b16 a[70:71], v21 offset:6144\n"
"ds_read_b64_tr_b16 v[96:97], v20 offset:12288\n"
"ds_read_b64_tr_b16 v[98:99], v21 offset:12288\n"
"ds_read_b64_tr_b16 v[100:101], v20 offset:18432\n"
"ds_read_b64_tr_b16 v[102:103], v21 offset:18432\n"
"ds_read_b64_tr_b16 a[120:121], v20 offset:2048\n"
"ds_read_b64_tr_b16 a[122:123], v21 offset:2048\n"
"ds_read_b64_tr_b16 a[124:125], v20 offset:8192\n"
"ds_read_b64_tr_b16 a[126:127], v21 offset:8192\n"
"ds_read_b64_tr_b16 a[144:145], v20 offset:14336\n"
"ds_read_b64_tr_b16 a[146:147], v21 offset:14336\n"
"ds_read_b64_tr_b16 a[148:149], v20 offset:20480\n"
"ds_read_b64_tr_b16 a[150:151], v21 offset:20480\n"
"ds_read_b64_tr_b16 a[176:177], v20 offset:4096\n"
"ds_read_b64_tr_b16 a[178:179], v21 offset:4096\n"
"ds_read_b64_tr_b16 a[180:181], v20 offset:10240\n"
"ds_read_b64_tr_b16 a[182:183], v21 offset:10240\n"
"ds_read_b64_tr_b16 a[184:185], v20 offset:16384\n"
"ds_read_b64_tr_b16 a[186:187], v21 offset:16384\n"
"ds_read_b64_tr_b16 a[188:189], v20 offset:22528\n"
"ds_read_b64_tr_b16 a[190:191], v21 offset:22528\n"
"s_cmp_lg_u32 s66, 0\n"
"s_cbranch_scc1 V38_%=\n"
"s_mov_b32 s76, 131072\n"
"s_mov_b64 exec, 1\n"
"s_branch V40_%=\n"
"V39_%=:\n"
"s_sleep 4\n"
"V40_%=:\n"
"v_mov_b32 v29, 0\n"
"buffer_atomic_add v29, v26, s[36:39], s23 offen sc0 sc1\n"
"s_sub_u32 s76, s76, 1\n"
"s_cbranch_scc1 VABORT_%=\n"
"s_waitcnt vmcnt(0)\n"
"v_xor_b32 v29, v29, v24\n"
"v_cmp_lt_u32 vcc, 1, v29\n"
"s_nop 0\n"
"s_cbranch_vccnz V39_%=\n"
"v_mov_b32 v28, 153600\n"
"ds_write_b32 v28, v29\n"
"v_mov_b32 v24, 1\n"
"s_add_u32 s22, s23, 16384\n"
"buffer_store_dword v24, v26, s[36:39], s22 offen sc1\n"
"s_mov_b64 exec, -1\n"
"V38_%=:\n"
"s_waitcnt lgkmcnt(0)\n"
"s_barrier\n"
"v_mov_b32 v28, 153600\n"
"ds_read_b32 v28, v28\n"
"s_waitcnt lgkmcnt(0)\n"
"v_readfirstlane_b32 s22, v28\n"
"s_nop 0\n"
"s_lshl_b32 s22, s22, 13\n"
"v_xor_b32 v13, s22, v13\n"
"s_xor_b32 s68, s68, s22\n"
"s_xor_b32 s20, s20, s22\n"
"s_xor_b32 s54, s54, s22\n"
"s_waitcnt vmcnt(12)\n"
"s_waitcnt lgkmcnt(0)\n"
"s_barrier\n"
"v_add_u32 v20, 98304, v8\n"
"v_add_u32 v21, 98304, v9\n"
"ds_read_b64_tr_b16 a[72:73], v20 offset:0\n"
"ds_read_b64_tr_b16 a[74:75], v21 offset:0\n"
"ds_read_b64_tr_b16 a[76:77], v20 offset:6144\n"
"ds_read_b64_tr_b16 a[78:79], v21 offset:6144\n"
"ds_read_b64_tr_b16 a[96:97], v20 offset:12288\n"
"ds_read_b64_tr_b16 a[98:99], v21 offset:12288\n"
"ds_read_b64_tr_b16 a[100:101], v20 offset:18432\n"
"ds_read_b64_tr_b16 a[102:103], v21 offset:18432\n"
"ds_read_b64_tr_b16 a[192:193], v20 offset:2048\n"
"ds_read_b64_tr_b16 a[194:195], v21 offset:2048\n"
"ds_read_b64_tr_b16 a[196:197], v20 offset:8192\n"
"ds_read_b64_tr_b16 a[198:199], v21 offset:8192\n"
"ds_read_b64_tr_b16 a[200:201], v20 offset:14336\n"
"ds_read_b64_tr_b16 a[202:203], v21 offset:14336\n"
"ds_read_b64_tr_b16 a[204:205], v20 offset:20480\n"
"ds_read_b64_tr_b16 a[206:207], v21 offset:20480\n"
"ds_read_b64_tr_b16 v[112:113], v20 offset:4096\n"
"ds_read_b64_tr_b16 v[114:115], v21 offset:4096\n"
"ds_read_b64_tr_b16 v[116:117], v20 offset:10240\n"
"ds_read_b64_tr_b16 v[118:119], v21 offset:10240\n"
"ds_read_b64_tr_b16 a[208:209], v20 offset:16384\n"
"ds_read_b64_tr_b16 a[210:211], v21 offset:16384\n"
"ds_read_b64_tr_b16 a[212:213], v20 offset:22528\n"
"ds_read_b64_tr_b16 a[214:215], v21 offset:22528\n"
"s_waitcnt vmcnt(0)\n"
"s_waitcnt lgkmcnt(0)\n"
"s_barrier\n"
"v_add_u32 v20, 0, v8\n"
"v_add_u32 v21, 0, v9\n"
"ds_read_b64_tr_b16 a[216:217], v20 offset:0\n"
"ds_read_b64_tr_b16 a[218:219], v21 offset:0\n"
"ds_read_b64_tr_b16 a[220:221], v20 offset:6144\n"
"ds_read_b64_tr_b16 a[222:223], v21 offset:6144\n"
"ds_read_b64_tr_b16 a[224:225], v20 offset:12288\n"
"ds_read_b64_tr_b16 a[226:227], v21 offset:12288\n"
"ds_read_b64_tr_b16 a[228:229], v20 offset:18432\n"
"ds_read_b64_tr_b16 a[230:231], v21 offset:18432\n"
"ds_read_b64_tr_b16 a[232:233], v20 offset:2048\n"
"ds_read_b64_tr_b16 a[234:235], v21 offset:2048\n"
"ds_read_b64_tr_b16 a[236:237], v20 offset:8192\n"
"ds_read_b64_tr_b16 a[238:239], v21 offset:8192\n"
"ds_read_b64_tr_b16 v[120:121], v20 offset:14336\n"
"ds_read_b64_tr_b16 v[122:123], v21 offset:14336\n"
"ds_read_b64_tr_b16 v[124:125], v20 offset:20480\n"
"ds_read_b64_tr_b16 v[126:127], v21 offset:20480\n"
"ds_read_b64_tr_b16 a[240:241], v20 offset:4096\n"
"ds_read_b64_tr_b16 a[242:243], v21 offset:4096\n"
"ds_read_b64_tr_b16 a[244:245], v20 offset:10240\n"
"ds_read_b64_tr_b16 a[246:247], v21 offset:10240\n"
"ds_read_b64_tr_b16 a[248:249], v20 offset:16384\n"
"ds_read_b64_tr_b16 a[250:251], v21 offset:16384\n"
"ds_read_b64_tr_b16 a[252:253], v20 offset:22528\n"
"ds_read_b64_tr_b16 a[254:255], v21 offset:22528\n"
"s_waitcnt lgkmcnt(0)\n"
"s_barrier\n"
"ds_write_b128 v15, v[32:35] offset:0\n"
"ds_write_b128 v15, v[36:39] offset:1024\n"
"ds_write_b128 v15, v[40:43] offset:2048\n"
"ds_write_b128 v15, v[44:47] offset:3072\n"
"ds_write_b128 v15, v[48:51] offset:4096\n"
"ds_write_b128 v15, v[52:55] offset:5120\n"
"ds_write_b128 v15, v[56:59] offset:6144\n"
"ds_write_b128 v15, v[60:63] offset:7168\n"
"ds_write_b128 v15, v[64:67] offset:8192\n"
"ds_write_b128 v15, v[68:71] offset:9216\n"
"ds_write_b128 v15, v[72:75] offset:10240\n"
"ds_write_b128 v15, v[76:79] offset:11264\n"
"ds_write_b128 v15, v[80:83] offset:12288\n"
"ds_write_b128 v15, v[84:87] offset:13312\n"
"ds_write_b128 v15, v[88:91] offset:14336\n"
"ds_write_b128 v15, v[92:95] offset:15360\n"
"ds_write_b128 v15, v[96:99] offset:16384\n"
"ds_write_b128 v15, v[100:103] offset:17408\n"
"ds_write_b128 v15, v[104:107] offset:18432\n"
"ds_write_b128 v15, v[108:111] offset:19456\n"
"ds_write_b128 v15, v[112:115] offset:20480\n"
"ds_write_b128 v15, v[116:119] offset:21504\n"
"ds_write_b128 v15, v[120:123] offset:22528\n"
"ds_write_b128 v15, v[124:127] offset:23552\n"
"s_waitcnt lgkmcnt(0)\n"
"s_mov_b32 s43, 940\n"
"s_memrealtime s[4:5]\n"
"s_nop 1\n"
"s_memrealtime s[6:7]\n"
"s_nop 1\n"
"s_memrealtime s[8:9]\n"
"s_nop 1\n"
"s_memrealtime s[10:11]\n"
"s_nop 1\n"
"s_memrealtime s[12:13]\n"
"s_nop 1\n"
"s_memrealtime s[14:15]\n"
"s_nop 1\n"
"s_memrealtime s[16:17]\n"
"s_nop 1\n"
"s_memrealtime s[18:19]\n"
"s_waitcnt lgkmcnt(0)\n"
"s_add_u32 s64, s4, s6\n"
"s_add_u32 s64, s64, s8\n"
"s_add_u32 s64, s64, s10\n"
"s_add_u32 s64, s64, s12\n"
"s_add_u32 s64, s64, s14\n"
"s_add_u32 s64, s64, s16\n"
"s_add_u32 s64, s64, s18\n"
"s_add_u32 s65, s64, s43\n"
"s_add_u32 s40, s20, 16384\n"
"s_xor_b32 s40, s40, 8192\n"
"v_lshlrev_b32 v18, 2, v0\n"
"s_lshl_b32 s77, s73, 14\n"
"s_add_u32 s77, s77, s20\n"
"s_add_u32 s77, s77, s21\n"
"v_mov_b32 v29, s65\n"
"v_lshrrev_b32 v30, 2, v0\n"
"s_mov_b64 exec, 0xffff\n"
"buffer_store_dword v29, v30, s[36:39], s77 offen sc1\n"
"s_mov_b64 exec, -1\n"
"s_add_u32 s76, s43, 60\n"
"v_mov_b32 v30, s76\n"
"v_mov_b32 v29, 0\n"
"ds_write_b32 v29, v30 offset:16896\n"
"ds_read_b128 v[96:99], v15 offset:0\n"
"ds_read_b128 v[100:103], v15 offset:1024\n"
"ds_read_b128 v[72:75], v15 offset:2048\n"
"ds_read_b128 v[76:79], v15 offset:3072\n"
"ds_read_b128 v[104:107], v15 offset:4096\n"
"ds_read_b128 v[108:111], v15 offset:5120\n"
"ds_read_b128 v[80:83], v15 offset:6144\n"
"ds_read_b128 v[84:87], v15 offset:7168\n"
"ds_read_b128 v[112:115], v15 offset:8192\n"
"ds_read_b128 v[116:119], v15 offset:9216\n"
"ds_read_b128 v[88:91], v15 offset:10240\n"
"ds_read_b128 v[92:95], v15 offset:11264\n"
"v_mov_b32 v32, 0\n"
"v_mov_b32 v33, 0\n"
"v_mov_b32 v34, 0\n"
"v_mov_b32 v35, 0\n"
"v_mov_b32 v36, 0\n"
"v_mov_b32 v37, 0\n"
"v_mov_b32 v38, 0\n"
"v_mov_b32 v39, 0\n"
"v_mov_b32 v40, 0\n"
"v_mov_b32 v41, 0\n"
"v_mov_b32 v42, 0\n"
"v_mov_b32 v43, 0\n"
"v_mov_b32 v44, 0\n"
"v_mov_b32 v45, 0\n"
"v_mov_b32 v46, 0\n"
"v_mov_b32 v47, 0\n"
"v_mov_b32 v48, 0\n"
"v_mov_b32 v49, 0\n"
"v_mov_b32 v50, 0\n"
"v_mov_b32 v51, 0\n"
"v_mov_b32 v52, 0\n"
"v_mov_b32 v53, 0\n"
"v_mov_b32 v54, 0\n"
"v_mov_b32 v55, 0\n"
"v_mov_b32 v120, 0\n"
"v_mov_b32 v121, 0\n"
"v_mov_b32 v122, 0\n"
"v_mov_b32 v123, 0\n"
"v_mov_b32 v124, 0\n"
"v_mov_b32 v125, 0\n"
"v_mov_b32 v126, 0\n"
"v_mov_b32 v127, 0\n"
"s_add_u32 s70, s69, 0\n"
"s_lshr_b32 s71, s70, 8\n"
"s_xor_b32 s71, s71, s54\n"
"s_and_b32 s71, s71, 0x2000\n"
"s_add_u32 s70, s70, s71\n"
"s_add_u32 s55, s70, s53\n"
"s_add_u32 s70, s69, 131072\n"
"s_lshr_b32 s71, s70, 8\n"
"s_xor_b32 s71, s71, s54\n"
"s_and_b32 s71, s71, 0x2000\n"
"s_add_u32 s70, s70, s71\n"
"s_add_u32 s56, s70, s53\n"
"s_add_u32 s70, s69, 262144\n"
"s_lshr_b32 s71, s70, 8\n"
"s_xor_b32 s71, s71, s54\n"
"s_and_b32 s71, s71, 0x2000\n"
"s_add_u32 s70, s70, s71\n"
"s_add_u32 s57, s70, s53\n"
"s_add_u32 s70, s69, 393216\n"
"s_lshr_b32 s71, s70, 8\n"
"s_xor_b32 s71, s71, s54\n"
"s_and_b32 s71, s71, 0x2000\n"
"s_add_u32 s70, s70, s71\n"
"s_add_u32 s58, s70, s53\n"
"s_add_u32 s70, s69, 524288\n"
"s_lshr_b32 s71, s70, 8\n"
"s_xor_b32 s71, s71, s54\n"
"s_and_b32 s71, s71, 0x2000\n"
"s_add_u32 s70, s70, s71\n"
"s_add_u32 s59, s70, s53\n"
"s_nop 0\n"
"V42_%=:\n"
"s_lshl_b32 s44, s73, 14\n"
"s_add_u32 s44, s44, s20\n"
"s_add_u32 s41, s44, 4096\n"
"s_add_u32 s60, s68, 72704\n"
"s_add_u32 s61, s68, 103424\n"
"s_add_u32 s62, s68, 5120\n"
"s_mov_b32 s76, 100\n"
"V46_%=:\n"
"s_memrealtime s[4:5]\n"
"s_nop 1\n"
"s_memrealtime s[6:7]\n"
"s_nop 1\n"
"s_memrealtime s[8:9]\n"
"s_nop 1\n"
"s_memrealtime s[10:11]\n"
"s_nop 1\n"
"s_memrealtime s[12:13]\n"
"s_nop 1\n"
"s_memrealtime s[14:15]\n"
"s_nop 1\n"
"s_memrealtime s[16:17]\n"
"s_nop 1\n"
"s_memrealtime s[18:19]\n"
"s_waitcnt lgkmcnt(0)\n"
"s_add_u32 s80, s4, s6\n"
"s_add_u32 s80, s80, s8\n"
"s_add_u32 s80, s80, s10\n"
"s_add_u32 s80, s80, s12\n"
"s_add_u32 s80, s80, s14\n"
"s_add_u32 s80, s80, s16\n"
"s_add_u32 s80, s80, s18\n"
"s_sub_u32 s76, s76, 1\n"
"s_cselect_b32 s79, 0, -1\n"
"s_sub_u32 s78, s64, s80\n"
"s_and_b32 s78, s78, s79\n"
"s_cmp_gt_i32 s78, 80\n"
"s_cbranch_scc1 V46_%=\n"
"s_max_i32 s78, s78, 0\n"
"s_add_u32 s80, s80, s78\n"
"s_mul_i32 s78, s78, 3\n"
"V45_%=:\n"
"s_sub_u32 s78, s78, 28\n"
"s_cbranch_scc0 V45_%=\n"
"s_waitcnt lgkmcnt(0)\n"
"s_barrier\n"
"buffer_load_dwordx4 v[56:59], v0, s[36:39], s60 offen offset:0 sc1\n"
"s_nop 12\n"
"buffer_load_dwordx4 v[60:63], v0, s[36:39], s61 offen offset:0 sc1\n"
"s_nop 12\n"
"s_nop 8\n"
"buffer_load_dwordx4 v[64:67], v0, s[36:39], s62 offen offset:0 sc1\n"
"buffer_load_dword v30, v0, s[36:39], s38 offen\n"
"V43_%=:\n"
"s_waitcnt vmcnt(3)\n"
"v_smfmac_f32_16x16x64_bf16 v[32:35], v[56:59], v[96:103], v6\n"
"ds_write_b128 v0, v[56:59] offset:8192\n"
"v_max3_u32 v29, v56, v57, 0\n"
"v_smfmac_f32_16x16x64_bf16 v[36:39], v[56:59], v[72:79], v6\n"
"v_max3_u32 v29, v58, v59, v29\n"
"v_smfmac_f32_16x16x64_bf16 v[40:43], v[56:59], v[104:111], v6\n"
"s_waitcnt vmcnt(2)\n"
"ds_write_b128 v0, v[60:63] offset:9216\n"
"v_max3_u32 v29, v60, v61, v29\n"
"v_smfmac_f32_16x16x64_bf16 v[32:35], v[60:63], v[80:87], v6\n"
"v_max3_u32 v29, v62, v63, v29\n"
"v_smfmac_f32_16x16x64_bf16 v[36:39], v[60:63], v[112:119], v6\n"
"s_waitcnt vmcnt(1)\n"
"v_max3_u32 v29, v64, v65, v29\n"
"v_max3_u32 v29, v66, v67, v29\n"
"v_smfmac_f32_16x16x64_bf16 v[40:43], v[60:63], v[88:95], v6\n"
"v_cmp_eq_u32 vcc, -1, v29\n"
"v_smfmac_f32_16x16x64_bf16 v[32:35], v[64:67], v[128:135], v6\n"
"v_smfmac_f32_16x16x64_bf16 v[36:39], v[64:67], v[136:143], v6\n"
"s_cbranch_vccnz V44_%=\n"
"s_waitcnt lgkmcnt(0)\n"
"s_barrier\n"
"ds_read_b128 v[68:71], v0 offset:1024\n"
"ds_read_b128 v[72:75], v0 offset:2048\n"
"v_smfmac_f32_16x16x64_bf16 v[40:43], v[64:67], v[144:151], v6\n"
"ds_read_b128 v[76:79], v0 offset:3072\n"
"ds_read_b128 v[80:83], v0 offset:4096\n"
"ds_read_b128 v[84:87], v0 offset:5120\n"
"ds_read_b128 v[88:91], v0 offset:6144\n"
"ds_read_b128 v[92:95], v0 offset:7168\n"
"s_waitcnt lgkmcnt(6)\n"
"v_smfmac_f32_16x16x64_bf16 v[32:35], v[68:71], v[152:159], v6\n"
"s_waitcnt lgkmcnt(5)\n"
"v_smfmac_f32_16x16x64_bf16 v[32:35], v[72:75], v[160:167], v6\n"
"s_waitcnt lgkmcnt(4)\n"
"v_smfmac_f32_16x16x64_bf16 v[32:35], v[76:79], v[168:175], v6\n"
"s_waitcnt lgkmcnt(3)\n"
"v_smfmac_f32_16x16x64_bf16 v[32:35], v[80:83], v[176:183], v6\n"
"s_waitcnt lgkmcnt(2)\n"
"v_smfmac_f32_16x16x64_bf16 v[32:35], v[84:87], v[184:191], v6\n"
"ds_read_b128 v[96:99], v15 offset:12288\n"
"s_waitcnt lgkmcnt(2)\n"
"v_smfmac_f32_16x16x64_bf16 v[32:35], v[88:91], v[192:199], v6\n"
"ds_read_b128 v[100:103], v15 offset:13312\n"
"s_waitcnt lgkmcnt(2)\n"
"v_smfmac_f32_16x16x64_bf16 v[32:35], v[92:95], v[200:207], v6\n"
"ds_read_b128 v[104:107], v15 offset:14336\n"
"s_nop 0\n"
"v_smfmac_f32_16x16x64_bf16 v[36:39], v[68:71], v[208:215], v6\n"
"ds_read_b128 v[108:111], v15 offset:15360\n"
"s_nop 0\n"
"v_smfmac_f32_16x16x64_bf16 v[36:39], v[72:75], v[216:223], v6\n"
"ds_read_b128 v[112:115], v15 offset:16384\n"
"v_smfmac_f32_16x16x64_bf16 v[36:39], v[76:79], v[224:231], v6\n"
"ds_read_b128 v[116:119], v15 offset:17408\n"
"v_add_f32 v32, v32, v34\n"
"v_smfmac_f32_16x16x64_bf16 v[36:39], v[80:83], v[232:239], v6\n"
"v_add_f32 v33, v33, v35\n"
"v_cvt_pk_bf16_f32 v32, v32, v33\n"
"v_smfmac_f32_16x16x64_bf16 v[36:39], v[84:87], v[240:247], v6\n"
"ds_write_b32 v10, v32 offset:0\n"
"v_smfmac_f32_16x16x64_bf16 v[36:39], v[88:91], v[248:255], v6\n"
"v_smfmac_f32_16x16x64_bf16 v[36:39], v[92:95], a[0:7], v6\n"
"s_nop 0\n"
"v_smfmac_f32_16x16x64_bf16 v[40:43], v[68:71], a[8:15], v6\n"
"s_nop 0\n"
"s_waitcnt lgkmcnt(5)\n"
"v_smfmac_f32_16x16x64_bf16 v[40:43], v[72:75], v[96:103], v6\n"
"v_smfmac_f32_16x16x64_bf16 v[40:43], v[76:79], a[16:23], v6\n"
"s_waitcnt lgkmcnt(0)\n"
"s_barrier\n"
"ds_read_b64_tr_b16 v[56:57], v12 offset:0\n"
"ds_read_b64_tr_b16 v[58:59], v12 offset:32\n"
"v_add_f32 v36, v36, v38\n"
"v_smfmac_f32_16x16x64_bf16 v[40:43], v[80:83], a[24:31], v6\n"
"ds_read_b64_tr_b16 v[60:61], v12 offset:256\n"
"ds_read_b64_tr_b16 v[62:63], v12 offset:288\n"
"v_add_f32 v37, v37, v39\n"
"v_cvt_pk_bf16_f32 v36, v36, v37\n"
"v_smfmac_f32_16x16x64_bf16 v[40:43], v[84:87], a[32:39], v6\n"
"ds_write_b32 v10, v36 offset:512\n"
"v_smfmac_f32_16x16x64_bf16 v[40:43], v[88:91], a[40:47], v6\n"
"v_smfmac_f32_16x16x64_bf16 v[40:43], v[92:95], a[48:55], v6\n"
"ds_read_b128 v[96:99], v15 offset:18432\n"
"s_nop 0\n"
"s_waitcnt lgkmcnt(4)\n"
"v_smfmac_f32_16x16x64_bf16 v[44:47], v[56:59], v[104:111], v11\n"
"ds_read_b128 v[100:103], v15 offset:19456\n"
"s_nop 0\n"
"v_smfmac_f32_16x16x64_bf16 v[48:51], v[56:59], a[56:63], v11\n"
"v_smfmac_f32_16x16x64_bf16 v[52:55], v[56:59], a[64:71], v11\n"
"s_waitcnt lgkmcnt(2)\n"
"s_barrier\n"
"ds_read_b64_tr_b16 v[64:65], v12 offset:512\n"
"ds_read_b64_tr_b16 v[66:67], v12 offset:544\n"
"v_add_f32 v40, v40, v42\n"
"v_smfmac_f32_16x16x64_bf16 v[120:123], v[56:59], a[72:79], v11\n"
"ds_read_b64_tr_b16 v[68:69], v12 offset:768\n"
"ds_read_b64_tr_b16 v[70:71], v12 offset:800\n"
"v_add_f32 v41, v41, v43\n"
"v_cvt_pk_bf16_f32 v40, v40, v41\n"
"v_smfmac_f32_16x16x64_bf16 v[44:47], v[60:63], a[80:87], v11\n"
"ds_write_b32 v10, v40 offset:1024\n"
"v_smfmac_f32_16x16x64_bf16 v[48:51], v[60:63], a[88:95], v11\n"
"v_smfmac_f32_16x16x64_bf16 v[52:55], v[60:63], v[112:119], v11\n"
"ds_read_b128 v[104:107], v15 offset:20480\n"
"v_smfmac_f32_16x16x64_bf16 v[120:123], v[60:63], a[96:103], v11\n"
"ds_read_b128 v[108:111], v15 offset:21504\n"
"s_waitcnt lgkmcnt(5)\n"
"v_smfmac_f32_16x16x64_bf16 v[44:47], v[64:67], a[104:111], v11\n"
"v_smfmac_f32_16x16x64_bf16 v[48:51], v[64:67], a[112:119], v11\n"
"s_waitcnt lgkmcnt(2)\n"
"s_barrier\n"
"ds_read_b64_tr_b16 v[72:73], v12 offset:1024\n"
"ds_read_b64_tr_b16 v[74:75], v12 offset:1056\n"
"v_smfmac_f32_16x16x64_bf16 v[52:55], v[64:67], a[120:127], v11\n"
"ds_read_b64_tr_b16 v[76:77], v12 offset:1280\n"
"ds_read_b64_tr_b16 v[78:79], v12 offset:1312\n"
"v_smfmac_f32_16x16x64_bf16 v[44:47], v[68:71], a[128:135], v11\n"
"v_smfmac_f32_16x16x64_bf16 v[48:51], v[68:71], a[136:143], v11\n"
"v_smfmac_f32_16x16x64_bf16 v[52:55], v[68:71], a[144:151], v11\n"
"s_waitcnt lgkmcnt(2)\n"
"v_smfmac_f32_16x16x64_bf16 v[44:47], v[72:75], a[152:159], v11\n"
"ds_read_b128 v[112:115], v15 offset:22528\n"
"s_waitcnt lgkmcnt(1)\n"
"v_smfmac_f32_16x16x64_bf16 v[44:47], v[76:79], a[160:167], v11\n"
"ds_read_b128 v[116:119], v15 offset:23552\n"
"v_smfmac_f32_16x16x64_bf16 v[48:51], v[72:75], v[96:103], v11\n"
"s_nop 1\n"
"v_smfmac_f32_16x16x64_bf16 v[48:51], v[76:79], a[168:175], v11\n"
"s_nop 1\n"
"v_smfmac_f32_16x16x64_bf16 v[52:55], v[72:75], a[176:183], v11\n"
"v_cvt_pk_bf16_f32 v80, v44, v45\n"
"v_smfmac_f32_16x16x64_bf16 v[52:55], v[76:79], a[184:191], v11\n"
"v_cvt_pk_bf16_f32 v81, v46, v47\n"
"v_min_u32 v80, -2, v80\n"
"v_smfmac_f32_16x16x64_bf16 v[120:123], v[64:67], a[192:199], v11\n"
"v_min_u32 v81, -2, v81\n"
"v_smfmac_f32_16x16x64_bf16 v[120:123], v[68:71], a[200:207], v11\n"
"buffer_store_dwordx2 v[80:81], v16, s[36:39], s55 offen sc1\n"
"v_cvt_pk_bf16_f32 v82, v48, v49\n"
"v_smfmac_f32_16x16x64_bf16 v[120:123], v[72:75], v[104:111], v11\n"
"v_cvt_pk_bf16_f32 v83, v50, v51\n"
"v_min_u32 v82, -2, v82\n"
"v_smfmac_f32_16x16x64_bf16 v[120:123], v[76:79], a[208:215], v11\n"
"v_min_u32 v83, -2, v83\n"
"v_smfmac_f32_16x16x64_bf16 v[124:127], v[56:59], a[216:223], v11\n"
"buffer_store_dwordx2 v[82:83], v16, s[36:39], s56 offen sc1\n"
"v_cvt_pk_bf16_f32 v84, v52, v53\n"
"v_smfmac_f32_16x16x64_bf16 v[124:127], v[60:63], a[224:231], v11\n"
"v_cvt_pk_bf16_f32 v85, v54, v55\n"
"v_min_u32 v84, -2, v84\n"
"v_smfmac_f32_16x16x64_bf16 v[124:127], v[64:67], a[232:239], v11\n"
"v_min_u32 v85, -2, v85\n"
"s_waitcnt lgkmcnt(0)\n"
"v_smfmac_f32_16x16x64_bf16 v[124:127], v[68:71], v[112:119], v11\n"
"buffer_store_dwordx2 v[84:85], v16, s[36:39], s57 offen sc1\n"
"v_cvt_pk_bf16_f32 v86, v120, v121\n"
"v_smfmac_f32_16x16x64_bf16 v[124:127], v[72:75], a[240:247], v11\n"
"v_cvt_pk_bf16_f32 v87, v122, v123\n"
"v_min_u32 v86, -2, v86\n"
"v_smfmac_f32_16x16x64_bf16 v[124:127], v[76:79], a[248:255], v11\n"
"v_min_u32 v87, -2, v87\n"
"buffer_store_dwordx2 v[86:87], v16, s[36:39], s58 offen sc1\n"
"s_nop 5\n"
"v_cvt_pk_bf16_f32 v88, v124, v125\n"
"v_cvt_pk_bf16_f32 v89, v126, v127\n"
"v_min_u32 v88, -2, v88\n"
"v_min_u32 v89, -2, v89\n"
"buffer_store_dwordx2 v[88:89], v16, s[36:39], s59 offen sc1\n"
"s_memrealtime s[4:5]\n"
"s_nop 1\n"
"s_memrealtime s[6:7]\n"
"s_nop 1\n"
"s_memrealtime s[8:9]\n"
"s_nop 1\n"
"s_memrealtime s[10:11]\n"
"s_nop 1\n"
"s_memrealtime s[12:13]\n"
"s_nop 1\n"
"s_memrealtime s[14:15]\n"
"s_nop 1\n"
"s_memrealtime s[16:17]\n"
"s_nop 1\n"
"s_memrealtime s[18:19]\n"
"buffer_load_dword v26, v18, s[36:39], s44 offen sc1\n"
"buffer_load_dword v17, v18, s[36:39], s41 offen sc1\n"
"buffer_load_dword v27, v18, s[36:39], s40 offen sc1\n"
"s_waitcnt lgkmcnt(0)\n"
"s_add_u32 s42, s4, s6\n"
"s_add_u32 s42, s42, s8\n"
"s_add_u32 s42, s42, s10\n"
"s_add_u32 s42, s42, s12\n"
"s_add_u32 s42, s42, s14\n"
"s_add_u32 s42, s42, s16\n"
"s_add_u32 s42, s42, s18\n"
"s_add_u32 s73, s73, 1\n"
"s_add_u32 s69, s69, 655360\n"
"s_add_u32 s79, s68, 131072\n"
"s_and_b32 s79, s79, 0x3fffff\n"
"s_xor_b32 s68, s79, s68\n"
"s_lshr_b32 s68, s68, 8\n"
"s_and_b32 s68, s68, 8192\n"
"s_xor_b32 s68, s68, s79\n"
"s_cmp_lt_u32 s73, 64\n"
"s_cbranch_scc0 V41_%=\n"
"ds_read_b128 v[96:99], v15 offset:0\n"
"ds_read_b128 v[100:103], v15 offset:1024\n"
"ds_read_b128 v[72:75], v15 offset:2048\n"
"ds_read_b128 v[76:79], v15 offset:3072\n"
"ds_read_b128 v[104:107], v15 offset:4096\n"
"ds_read_b128 v[108:111], v15 offset:5120\n"
"ds_read_b128 v[80:83], v15 offset:6144\n"
"ds_read_b128 v[84:87], v15 offset:7168\n"
"ds_read_b128 v[112:115], v15 offset:8192\n"
"ds_read_b128 v[116:119], v15 offset:9216\n"
"ds_read_b128 v[88:91], v15 offset:10240\n"
"ds_read_b128 v[92:95], v15 offset:11264\n"
"v_mov_b32 v32, 0\n"
"v_mov_b32 v33, 0\n"
"v_mov_b32 v34, 0\n"
"v_mov_b32 v35, 0\n"
"v_mov_b32 v36, 0\n"
"v_mov_b32 v37, 0\n"
"v_mov_b32 v38, 0\n"
"v_mov_b32 v39, 0\n"
"v_mov_b32 v40, 0\n"
"v_mov_b32 v41, 0\n"
"v_mov_b32 v42, 0\n"
"v_mov_b32 v43, 0\n"
"v_mov_b32 v44, 0\n"
"v_mov_b32 v45, 0\n"
"v_mov_b32 v46, 0\n"
"v_mov_b32 v47, 0\n"
"v_mov_b32 v48, 0\n"
"v_mov_b32 v49, 0\n"
"v_mov_b32 v50, 0\n"
"v_mov_b32 v51, 0\n"
"v_mov_b32 v52, 0\n"
"v_mov_b32 v53, 0\n"
"v_mov_b32 v54, 0\n"
"v_mov_b32 v55, 0\n"
"v_mov_b32 v120, 0\n"
"v_mov_b32 v121, 0\n"
"v_mov_b32 v122, 0\n"
"v_mov_b32 v123, 0\n"
"v_mov_b32 v124, 0\n"
"v_mov_b32 v125, 0\n"
"v_mov_b32 v126, 0\n"
"v_mov_b32 v127, 0\n"
"s_add_u32 s70, s69, 0\n"
"s_lshr_b32 s71, s70, 8\n"
"s_xor_b32 s71, s71, s54\n"
"s_and_b32 s71, s71, 0x2000\n"
"s_add_u32 s70, s70, s71\n"
"s_add_u32 s55, s70, s53\n"
"s_add_u32 s70, s69, 131072\n"
"s_lshr_b32 s71, s70, 8\n"
"s_xor_b32 s71, s71, s54\n"
"s_and_b32 s71, s71, 0x2000\n"
"s_add_u32 s70, s70, s71\n"
"s_add_u32 s56, s70, s53\n"
"s_add_u32 s70, s69, 262144\n"
"s_lshr_b32 s71, s70, 8\n"
"s_xor_b32 s71, s71, s54\n"
"s_and_b32 s71, s71, 0x2000\n"
"s_add_u32 s70, s70, s71\n"
"s_add_u32 s57, s70, s53\n"
"s_add_u32 s70, s69, 393216\n"
"s_lshr_b32 s71, s70, 8\n"
"s_xor_b32 s71, s71, s54\n"
"s_and_b32 s71, s71, 0x2000\n"
"s_add_u32 s70, s70, s71\n"
"s_add_u32 s58, s70, s53\n"
"s_add_u32 s70, s69, 524288\n"
"s_lshr_b32 s71, s70, 8\n"
"s_xor_b32 s71, s71, s54\n"
"s_and_b32 s71, s71, 0x2000\n"
"s_add_u32 s70, s70, s71\n"
"s_add_u32 s59, s70, s53\n"
"s_waitcnt vmcnt(0)\n"
"s_xor_b32 s40, s44, 8192\n"
"v_cmp_eq_u32 vcc, -1, v17\n"
"s_nop 1\n"
"v_cndmask_b32 v26, v17, v26, vcc\n"
"s_bfe_i32 s75, s63, 524312\n"
"s_lshl_b32 s75, s75, 2\n"
"s_add_u32 s42, s42, s75\n"
"v_cmp_eq_u32 vcc, -1, v26\n"
"v_subrev_u32 v26, s42, v26\n"
"s_mov_b64 s[70:71], vcc\n"
"v_cmp_eq_u32 vcc, -1, v27\n"
"v_max_i32_dpp v26, v26, v26 row_ror:1 row_mask:0xf bank_mask:0xf\n"
"v_max_i32_dpp v27, v27, v27 row_ror:1 row_mask:0xf bank_mask:0xf\n"
"s_nop 0\n"
"v_max_i32_dpp v26, v26, v26 row_ror:2 row_mask:0xf bank_mask:0xf\n"
"v_max_i32_dpp v27, v27, v27 row_ror:2 row_mask:0xf bank_mask:0xf\n"
"s_nop 0\n"
"v_max_i32_dpp v26, v26, v26 row_ror:4 row_mask:0xf bank_mask:0xf\n"
"v_max_i32_dpp v27, v27, v27 row_ror:4 row_mask:0xf bank_mask:0xf\n"
"s_nop 0\n"
"v_max_i32_dpp v26, v26, v26 row_ror:8 row_mask:0xf bank_mask:0xf\n"
"v_max_i32_dpp v27, v27, v27 row_ror:8 row_mask:0xf bank_mask:0xf\n"
"v_readlane_b32 s4, v26, 0\n"
"v_readlane_b32 s5, v26, 16\n"
"v_readlane_b32 s6, v26, 32\n"
"v_readlane_b32 s7, v26, 48\n"
"v_readlane_b32 s8, v27, 0\n"
"v_readlane_b32 s9, v27, 16\n"
"v_readlane_b32 s10, v27, 32\n"
"v_readlane_b32 s11, v27, 48\n"
"s_max_i32 s4, s4, s5\n"
"s_max_i32 s6, s6, s7\n"
"s_max_i32 s64, s4, s6\n"
"s_max_i32 s8, s8, s9\n"
"s_max_i32 s10, s10, s11\n"
"s_max_i32 s65, s8, s10\n"
"s_and_b32 vcc_hi, vcc_hi, 255\n"
"s_cmp_eq_u64 vcc, 0\n"
"s_cselect_b32 s65, s65, 430\n"
"s_min_u32 s65, s65, 1600\n"
"s_cmp_eq_u64 s[70:71], 0\n"
"s_cselect_b32 s64, s64, -800\n"
"s_min_i32 s64, s64, 3200\n"
"s_bfe_i32 s75, s63, 524304\n"
"s_lshl_b32 s75, s75, 2\n"
"s_lshl_b32 s75, s75, 2\n"
"s_bfe_i32 s76, s63, 524288\n"
"s_lshl_b32 s76, s76, 2\n"
"s_add_u32 s76, s76, s64\n"
"s_cmp_lt_i32 s76, -56\n"
"s_cselect_b32 s64, s75, s64\n"
"s_add_u32 s64, s64, s65\n"
"s_bfe_i32 s75, s63, 524288\n"
"s_lshl_b32 s75, s75, 2\n"
"s_add_u32 s64, s64, s75\n"
"s_bfe_i32 s75, s63, 524296\n"
"s_lshl_b32 s75, s75, 2\n"
"s_add_u32 s64, s64, s75\n"
"s_add_u32 s64, s64, s42\n"
"s_sub_u32 s65, s42, s80\n"
"s_sub_u32 s75, s65, s43\n"
"s_min_i32 s75, s75, 48\n"
"s_cmp_lt_i32 s75, -64\n"
"s_cselect_b32 s76, 0, 2\n"
"s_ashr_i32 s75, s75, s76\n"
"s_add_u32 s43, s43, s75\n"
"s_add_u32 s65, s43, s64\n"
"s_lshl_b32 s77, s73, 14\n"
"s_add_u32 s77, s77, s20\n"
"s_add_u32 s77, s77, s21\n"
"v_mov_b32 v29, s65\n"
"v_lshrrev_b32 v30, 2, v0\n"
"s_mov_b64 exec, 0xffff\n"
"buffer_store_dword v29, v30, s[36:39], s77 offen sc1\n"
"s_mov_b64 exec, -1\n"
"s_add_u32 s76, s43, 60\n"
"v_mov_b32 v30, s76\n"
"v_mov_b32 v29, 0\n"
"ds_write_b32 v29, v30 offset:16896\n"
"s_branch V42_%=\n"
"V44_%=:\n"
"s_waitcnt lgkmcnt(0)\n"
"s_mov_b32 s76, 131072\n"
"V47_%=:\n"
"s_memrealtime s[4:5]\n"
"buffer_load_dwordx4 v[56:59], v0, s[36:39], s60 offen offset:0 sc1\n"
"buffer_load_dwordx4 v[60:63], v0, s[36:39], s61 offen offset:0 sc1\n"
"buffer_load_dwordx4 v[64:67], v0, s[36:39], s62 offen offset:0 sc1\n"
"v_mov_b32 v29, 0\n"
"ds_read_b32 v31, v29 offset:16896\n"
"s_lshl_b32 s77, s73, 14\n"
"s_add_u32 s77, s77, s20\n"
"s_add_u32 s77, s77, s21\n"
"s_add_u32 s77, s77, 4096\n"
"v_mov_b32 v32, 0\n"
"v_mov_b32 v33, 0\n"
"v_mov_b32 v34, 0\n"
"v_mov_b32 v35, 0\n"
"v_mov_b32 v36, 0\n"
"v_mov_b32 v37, 0\n"
"v_mov_b32 v38, 0\n"
"v_mov_b32 v39, 0\n"
"v_mov_b32 v40, 0\n"
"v_mov_b32 v41, 0\n"
"v_mov_b32 v42, 0\n"
"v_mov_b32 v43, 0\n"
"s_sub_u32 s76, s76, 1\n"
"s_cbranch_scc1 VABORT_%=\n"
"s_waitcnt vmcnt(2)\n"
"v_max3_u32 v29, v56, v57, 0\n"
"v_max3_u32 v29, v58, v59, v29\n"
"s_waitcnt vmcnt(1)\n"
"v_max3_u32 v29, v60, v61, v29\n"
"v_max3_u32 v29, v62, v63, v29\n"
"s_waitcnt vmcnt(0)\n"
"v_max3_u32 v29, v64, v65, v29\n"
"v_max3_u32 v29, v66, v67, v29\n"
"v_cmp_eq_u32 vcc, -1, v29\n"
"s_nop 0\n"
"s_cbranch_vccnz V47_%=\n"
"s_waitcnt lgkmcnt(0)\n"
"v_lshl_add_u32 v31, s4, 3, v31\n"
"v_lshrrev_b32 v30, 2, v0\n"
"s_mov_b64 exec, 0xffff\n"
"buffer_store_dword v31, v30, s[36:39], s77 offen sc1\n"
"s_mov_b64 exec, -1\n"
"s_branch V43_%=\n"
"V41_%=:\n"
"s_waitcnt vmcnt(0)\n"
"s_barrier\n"
"s_branch V5_%=\n"
"V5_%=:\n"
"s_endpgm\n"
"VABORT_%=:\n"
"s_mov_b64 exec, -1\n"
"s_nop 7\n"
"s_nop 7\n"
"v_mov_b32 v0, 46268544\n"
"v_mov_b32 v1, 1\n"
"buffer_store_dword v1, v0, s[36:39], 0 offen sc1\n"
"s_endpgm\n");
}

DI void role_c(const Ctx& c, int j) {
    SETUP
    const int kh = r8 & 1;
    sa[8] = O_VW + (j >> 1) * 65536 + (j & 1) * 256; sa[9] = j; sa[10] = c.par; sa[13] = O_COMM + (c.par << 13) + (RHO << 14);
    sb[11] = P->tune;
    va[1] = g * 512 + n * 16; va[2] = (lane & (w ? 8 : 2)) ? 0xEEEE : 0x4444;
    va[3] = (g >> 1) * 640 + (lane & 31) * 16; va[4] = r8 * 640 + 512 + c8 * 16;
    va[5] = F_W + fw(4 * (lane & 31) + ((j >> 1) & 3)); va[6] = lane < 32 ? 0u : ~0u;
    va[7] = mem_item(16 * j + 4 * g + 2 * kh + (n & 1)) + (c8 >> 1) * 4;
    va[8] = (lane + (w & 1) * ((g & 1) - (g >> 1)) * 16) * 8; va[9] = (n & 1) ? 0x03020706u : 0x05040100u;
    RUN("s_nop 4\n"
"s_mov_b32 s68, s49\n"
"s_mov_b32 s73, 0\n"
"s_mov_b32 s93, 0\n"
"s_mov_b32 s21, s64\n"
"s_and_b32 s20, s49, 8192\n"
"s_xor_b32 s20, s20, 0\n"
"s_add_u32 s20, s20, 100663296\n"
"s_cmp_eq_u32 s66, 1\n"
"s_cbranch_scc1 C2_%=\n"
"s_cmp_eq_u32 s66, 2\n"
"s_cbranch_scc1 C3_%=\n"
"s_cmp_eq_u32 s66, 3\n"
"s_cbranch_scc1 C4_%=\n"
"C1_%=:\n"
"v_add_u32 v10, 0x8000, v0\n"
"s_nop 4\n"
"C6_%=:\n"
"s_add_u32 s52, s68, 0\n"
"s_add_u32 s53, s68, 32768\n"
"s_add_u32 s54, s68, 65536\n"
"s_add_u32 s55, s68, 98304\n"
"s_add_u32 s79, s68, 131072\n"
"s_and_b32 s79, s79, 0x3fffff\n"
"s_xor_b32 s69, s79, s68\n"
"s_lshr_b32 s69, s69, 8\n"
"s_and_b32 s69, s69, 8192\n"
"s_xor_b32 s69, s69, s79\n"
"s_xor_b32 s56, s69, 16384\n"
"v_mov_b32 v24, 0\n"
"v_mov_b32 v25, 0\n"
"v_mov_b32 v26, 0\n"
"v_mov_b32 v27, 0\n"
"s_mov_b32 s70, 3\n"
"s_barrier\n"
"buffer_load_dwordx4 v[32:35], v3, s[36:39], s52 offen offset:0 sc1\n"
"ds_read_b128 a[0:3], v10 offset:0\n"
"ds_read_b128 a[4:7], v10 offset:1024\n"
"buffer_load_dwordx4 v[36:39], v3, s[36:39], s52 offen offset:1280 sc1\n"
"ds_read_b128 a[8:11], v10 offset:2048\n"
"ds_read_b128 a[12:15], v10 offset:3072\n"
"buffer_load_dwordx4 v[40:43], v3, s[36:39], s52 offen offset:2560 sc1\n"
"ds_read_b128 a[16:19], v10 offset:4096\n"
"ds_read_b128 a[20:23], v10 offset:5120\n"
"buffer_load_dwordx4 v[44:47], v3, s[36:39], s52 offen offset:3840 sc1\n"
"ds_read_b128 a[24:27], v10 offset:6144\n"
"ds_read_b128 a[28:31], v10 offset:7168\n"
"buffer_load_dwordx4 v[48:51], v3, s[36:39], s53 offen offset:0 sc1\n"
"ds_read_b128 a[32:35], v10 offset:8192\n"
"ds_read_b128 a[36:39], v10 offset:9216\n"
"buffer_load_dwordx4 v[52:55], v3, s[36:39], s53 offen offset:1280 sc1\n"
"ds_read_b128 a[40:43], v10 offset:10240\n"
"ds_read_b128 a[44:47], v10 offset:11264\n"
"buffer_load_dwordx4 v[56:59], v3, s[36:39], s53 offen offset:2560 sc1\n"
"ds_read_b128 a[48:51], v10 offset:12288\n"
"ds_read_b128 a[52:55], v10 offset:13312\n"
"buffer_load_dwordx4 v[60:63], v3, s[36:39], s53 offen offset:3840 sc1\n"
"ds_read_b128 a[56:59], v10 offset:14336\n"
"ds_read_b128 a[60:63], v10 offset:15360\n"
"buffer_load_dwordx4 v[64:67], v3, s[36:39], s54 offen offset:0 sc1\n"
"ds_read_b128 a[64:67], v10 offset:16384\n"
"ds_read_b128 a[68:71], v10 offset:17408\n"
"buffer_load_dwordx4 v[68:71], v3, s[36:39], s54 offen offset:1280 sc1\n"
"ds_read_b128 a[72:75], v10 offset:18432\n"
"ds_read_b128 a[76:79], v10 offset:19456\n"
"buffer_load_dwordx4 v[72:75], v3, s[36:39], s54 offen offset:2560 sc1\n"
"ds_read_b128 a[80:83], v10 offset:20480\n"
"ds_read_b128 a[84:87], v10 offset:21504\n"
"buffer_load_dwordx4 v[76:79], v3, s[36:39], s54 offen offset:3840 sc1\n"
"ds_read_b128 a[88:91], v10 offset:22528\n"
"ds_read_b128 a[92:95], v10 offset:23552\n"
"buffer_load_dwordx4 v[80:83], v3, s[36:39], s55 offen offset:0 sc1\n"
"ds_read_b128 a[96:99], v10 offset:24576\n"
"ds_read_b128 a[100:103], v10 offset:25600\n"
"buffer_load_dwordx4 v[84:87], v3, s[36:39], s55 offen offset:1280 sc1\n"
"ds_read_b128 a[104:107], v10 offset:26624\n"
"ds_read_b128 a[108:111], v10 offset:27648\n"
"buffer_load_dwordx4 v[88:91], v3, s[36:39], s55 offen offset:2560 sc1\n"
"ds_read_b128 a[112:115], v10 offset:28672\n"
"ds_read_b128 a[116:119], v10 offset:29696\n"
"buffer_load_dwordx4 v[92:95], v3, s[36:39], s55 offen offset:3840 sc1\n"
"ds_read_b128 a[120:123], v10 offset:30720\n"
"ds_read_b128 a[124:127], v10 offset:31744\n"
"v_xor_b32 v10, 0x18000, v10\n"
"s_waitcnt lgkmcnt(0)\n"
"C11_%=:\n"
"s_waitcnt vmcnt(15)\n"
"v_smfmac_f32_16x16x64_bf16 v[24:27], v[32:35], a[0:7], v2\n"
"s_waitcnt vmcnt(14)\n"
"v_smfmac_f32_16x16x64_bf16 v[24:27], v[36:39], a[8:15], v2\n"
"s_waitcnt vmcnt(13)\n"
"v_smfmac_f32_16x16x64_bf16 v[24:27], v[40:43], a[16:23], v2\n"
"s_waitcnt vmcnt(12)\n"
"v_smfmac_f32_16x16x64_bf16 v[24:27], v[44:47], a[24:31], v2\n"
"s_waitcnt vmcnt(11)\n"
"v_smfmac_f32_16x16x64_bf16 v[24:27], v[48:51], a[32:39], v2\n"
"s_waitcnt vmcnt(10)\n"
"v_smfmac_f32_16x16x64_bf16 v[24:27], v[52:55], a[40:47], v2\n"
"s_waitcnt vmcnt(9)\n"
"v_smfmac_f32_16x16x64_bf16 v[24:27], v[56:59], a[48:55], v2\n"
"s_waitcnt vmcnt(8)\n"
"v_smfmac_f32_16x16x64_bf16 v[24:27], v[60:63], a[56:63], v2\n"
"s_waitcnt vmcnt(7)\n"
"v_smfmac_f32_16x16x64_bf16 v[24:27], v[64:67], a[64:71], v2\n"
"s_waitcnt vmcnt(6)\n"
"v_smfmac_f32_16x16x64_bf16 v[24:27], v[68:71], a[72:79], v2\n"
"s_waitcnt vmcnt(5)\n"
"v_smfmac_f32_16x16x64_bf16 v[24:27], v[72:75], a[80:87], v2\n"
"s_waitcnt vmcnt(4)\n"
"v_smfmac_f32_16x16x64_bf16 v[24:27], v[76:79], a[88:95], v2\n"
"s_waitcnt vmcnt(3)\n"
"v_smfmac_f32_16x16x64_bf16 v[24:27], v[80:83], a[96:103], v2\n"
"s_waitcnt vmcnt(2)\n"
"v_smfmac_f32_16x16x64_bf16 v[24:27], v[84:87], a[104:111], v2\n"
"s_waitcnt vmcnt(1)\n"
"v_smfmac_f32_16x16x64_bf16 v[24:27], v[88:91], a[112:119], v2\n"
"ds_read_b64 v[20:21], v8\n"
"s_waitcnt vmcnt(0)\n"
"v_smfmac_f32_16x16x64_bf16 v[24:27], v[92:95], a[120:127], v2\n"
"s_waitcnt lgkmcnt(0)\n"
"s_nop 6\n"
"v_add_f32 v24, v24, v26\n"
"v_add_f32 v25, v25, v27\n"
"v_add_f32 v24, v24, v20\n"
"v_add_f32 v25, v25, v21\n"
"v_cmp_u_f32 vcc, v24, v25\n"
"v_cvt_pk_bf16_f32 v22, v24, v25\n"
"s_nop 1\n"
"v_mov_b32_dpp v23, v22 quad_perm:[1,0,3,2] row_mask:0xf bank_mask:0xf\n"
"v_perm_b32 v22, v23, v22, v9\n"
"v_min_u32 v22, -2, v22\n"
"s_cbranch_vccnz C8_%=\n"
"buffer_store_dword v22, v7, s[36:39], s56 offen sc1\n"
"buffer_store_dword v22, v7, s[36:39], s69 offen sc1\n"
"s_barrier\n"
"C9_%=:\n"
"v_xor_b32 v8, 512, v8\n"
"s_add_u32 s73, s73, 1\n"
"s_add_u32 s79, s68, 131072\n"
"s_and_b32 s79, s79, 0x3fffff\n"
"s_xor_b32 s68, s79, s68\n"
"s_lshr_b32 s68, s68, 8\n"
"s_and_b32 s68, s68, 8192\n"
"s_xor_b32 s68, s68, s79\n"
"s_cmp_lt_u32 s73, 63\n"
"s_cbranch_scc1 C6_%=\n"
"s_branch C12_%=\n"
"C8_%=:\n"
"v_mov_b32 v24, 0\n"
"v_mov_b32 v25, 0\n"
"v_mov_b32 v26, 0\n"
"v_mov_b32 v27, 0\n"
"s_sub_u32 s70, s70, 1\n"
"s_cbranch_scc1 C10_%=\n"
"buffer_load_dwordx4 v[32:35], v3, s[36:39], s52 offen offset:0 sc1\n"
"buffer_load_dwordx4 v[36:39], v3, s[36:39], s52 offen offset:1280 sc1\n"
"buffer_load_dwordx4 v[40:43], v3, s[36:39], s52 offen offset:2560 sc1\n"
"buffer_load_dwordx4 v[44:47], v3, s[36:39], s52 offen offset:3840 sc1\n"
"buffer_load_dwordx4 v[48:51], v3, s[36:39], s53 offen offset:0 sc1\n"
"buffer_load_dwordx4 v[52:55], v3, s[36:39], s53 offen offset:1280 sc1\n"
"buffer_load_dwordx4 v[56:59], v3, s[36:39], s53 offen offset:2560 sc1\n"
"buffer_load_dwordx4 v[60:63], v3, s[36:39], s53 offen offset:3840 sc1\n"
"buffer_load_dwordx4 v[64:67], v3, s[36:39], s54 offen offset:0 sc1\n"
"buffer_load_dwordx4 v[68:71], v3, s[36:39], s54 offen offset:1280 sc1\n"
"buffer_load_dwordx4 v[72:75], v3, s[36:39], s54 offen offset:2560 sc1\n"
"buffer_load_dwordx4 v[76:79], v3, s[36:39], s54 offen offset:3840 sc1\n"
"buffer_load_dwordx4 v[80:83], v3, s[36:39], s55 offen offset:0 sc1\n"
"buffer_load_dwordx4 v[84:87], v3, s[36:39], s55 offen offset:1280 sc1\n"
"buffer_load_dwordx4 v[88:91], v3, s[36:39], s55 offen offset:2560 sc1\n"
"buffer_load_dwordx4 v[92:95], v3, s[36:39], s55 offen offset:3840 sc1\n"
"s_branch C11_%=\n"
"C10_%=:\n"
"s_mov_b32 s76, 131072\n"
"C13_%=:\n"
"s_waitcnt vmcnt(12)\n"
"v_max3_u32 v29, v32, v33, 0\n"
"v_max3_u32 v29, v34, v35, v29\n"
"v_max3_u32 v29, v36, v37, v29\n"
"v_max3_u32 v29, v38, v39, v29\n"
"v_max3_u32 v29, v40, v41, v29\n"
"v_max3_u32 v29, v42, v43, v29\n"
"v_max3_u32 v29, v44, v45, v29\n"
"v_max3_u32 v29, v46, v47, v29\n"
"v_cmp_eq_u32 vcc, -1, v29\n"
"s_nop 0\n"
"s_cbranch_vccz C14_%=\n"
"buffer_load_dwordx4 v[32:35], v3, s[36:39], s52 offen offset:0 sc1\n"
"buffer_load_dwordx4 v[36:39], v3, s[36:39], s52 offen offset:1280 sc1\n"
"buffer_load_dwordx4 v[40:43], v3, s[36:39], s52 offen offset:2560 sc1\n"
"buffer_load_dwordx4 v[44:47], v3, s[36:39], s52 offen offset:3840 sc1\n"
"buffer_load_dwordx4 v[48:51], v3, s[36:39], s53 offen offset:0 sc1\n"
"buffer_load_dwordx4 v[52:55], v3, s[36:39], s53 offen offset:1280 sc1\n"
"buffer_load_dwordx4 v[56:59], v3, s[36:39], s53 offen offset:2560 sc1\n"
"buffer_load_dwordx4 v[60:63], v3, s[36:39], s53 offen offset:3840 sc1\n"
"buffer_load_dwordx4 v[64:67], v3, s[36:39], s54 offen offset:0 sc1\n"
"buffer_load_dwordx4 v[68:71], v3, s[36:39], s54 offen offset:1280 sc1\n"
"buffer_load_dwordx4 v[72:75], v3, s[36:39], s54 offen offset:2560 sc1\n"
"buffer_load_dwordx4 v[76:79], v3, s[36:39], s54 offen offset:3840 sc1\n"
"buffer_load_dwordx4 v[80:83], v3, s[36:39], s55 offen offset:0 sc1\n"
"buffer_load_dwordx4 v[84:87], v3, s[36:39], s55 offen offset:1280 sc1\n"
"buffer_load_dwordx4 v[88:91], v3, s[36:39], s55 offen offset:2560 sc1\n"
"buffer_load_dwordx4 v[92:95], v3, s[36:39], s55 offen offset:3840 sc1\n"
"s_sub_u32 s76, s76, 1\n"
"s_cbranch_scc0 C13_%=\n"
"s_branch CABORT_%=\n"
"C14_%=:\n"
"v_smfmac_f32_16x16x64_bf16 v[24:27], v[32:35], a[0:7], v2\n"
"v_smfmac_f32_16x16x64_bf16 v[24:27], v[36:39], a[8:15], v2\n"
"v_smfmac_f32_16x16x64_bf16 v[24:27], v[40:43], a[16:23], v2\n"
"v_smfmac_f32_16x16x64_bf16 v[24:27], v[44:47], a[24:31], v2\n"
"C15_%=:\n"
"s_waitcnt vmcnt(8)\n"
"v_max3_u32 v29, v48, v49, 0\n"
"v_max3_u32 v29, v50, v51, v29\n"
"v_max3_u32 v29, v52, v53, v29\n"
"v_max3_u32 v29, v54, v55, v29\n"
"v_max3_u32 v29, v56, v57, v29\n"
"v_max3_u32 v29, v58, v59, v29\n"
"v_max3_u32 v29, v60, v61, v29\n"
"v_max3_u32 v29, v62, v63, v29\n"
"v_cmp_eq_u32 vcc, -1, v29\n"
"s_nop 0\n"
"s_cbranch_vccz C16_%=\n"
"buffer_load_dwordx4 v[48:51], v3, s[36:39], s53 offen offset:0 sc1\n"
"buffer_load_dwordx4 v[52:55], v3, s[36:39], s53 offen offset:1280 sc1\n"
"buffer_load_dwordx4 v[56:59], v3, s[36:39], s53 offen offset:2560 sc1\n"
"buffer_load_dwordx4 v[60:63], v3, s[36:39], s53 offen offset:3840 sc1\n"
"buffer_load_dwordx4 v[64:67], v3, s[36:39], s54 offen offset:0 sc1\n"
"buffer_load_dwordx4 v[68:71], v3, s[36:39], s54 offen offset:1280 sc1\n"
"buffer_load_dwordx4 v[72:75], v3, s[36:39], s54 offen offset:2560 sc1\n"
"buffer_load_dwordx4 v[76:79], v3, s[36:39], s54 offen offset:3840 sc1\n"
"buffer_load_dwordx4 v[80:83], v3, s[36:39], s55 offen offset:0 sc1\n"
"buffer_load_dwordx4 v[84:87], v3, s[36:39], s55 offen offset:1280 sc1\n"
"buffer_load_dwordx4 v[88:91], v3, s[36:39], s55 offen offset:2560 sc1\n"
"buffer_load_dwordx4 v[92:95], v3, s[36:39], s55 offen offset:3840 sc1\n"
"s_sub_u32 s76, s76, 1\n"
"s_cbranch_scc0 C15_%=\n"
"s_branch CABORT_%=\n"
"C16_%=:\n"
"v_smfmac_f32_16x16x64_bf16 v[24:27], v[48:51], a[32:39], v2\n"
"v_smfmac_f32_16x16x64_bf16 v[24:27], v[52:55], a[40:47], v2\n"
"v_smfmac_f32_16x16x64_bf16 v[24:27], v[56:59], a[48:55], v2\n"
"v_smfmac_f32_16x16x64_bf16 v[24:27], v[60:63], a[56:63], v2\n"
"C17_%=:\n"
"s_waitcnt vmcnt(4)\n"
"v_max3_u32 v29, v64, v65, 0\n"
"v_max3_u32 v29, v66, v67, v29\n"
"v_max3_u32 v29, v68, v69, v29\n"
"v_max3_u32 v29, v70, v71, v29\n"
"v_max3_u32 v29, v72, v73, v29\n"
"v_max3_u32 v29, v74, v75, v29\n"
"v_max3_u32 v29, v76, v77, v29\n"
"v_max3_u32 v29, v78, v79, v29\n"
"v_cmp_eq_u32 vcc, -1, v29\n"
"s_nop 0\n"
"s_cbranch_vccz C18_%=\n"
"buffer_load_dwordx4 v[64:67], v3, s[36:39], s54 offen offset:0 sc1\n"
"buffer_load_dwordx4 v[68:71], v3, s[36:39], s54 offen offset:1280 sc1\n"
"buffer_load_dwordx4 v[72:75], v3, s[36:39], s54 offen offset:2560 sc1\n"
"buffer_load_dwordx4 v[76:79], v3, s[36:39], s54 offen offset:3840 sc1\n"
"buffer_load_dwordx4 v[80:83], v3, s[36:39], s55 offen offset:0 sc1\n"
"buffer_load_dwordx4 v[84:87], v3, s[36:39], s55 offen offset:1280 sc1\n"
"buffer_load_dwordx4 v[88:91], v3, s[36:39], s55 offen offset:2560 sc1\n"
"buffer_load_dwordx4 v[92:95], v3, s[36:39], s55 offen offset:3840 sc1\n"
"s_sub_u32 s76, s76, 1\n"
"s_cbranch_scc0 C17_%=\n"
"s_branch CABORT_%=\n"
"C18_%=:\n"
"v_smfmac_f32_16x16x64_bf16 v[24:27], v[64:67], a[64:71], v2\n"
"v_smfmac_f32_16x16x64_bf16 v[24:27], v[68:71], a[72:79], v2\n"
"v_smfmac_f32_16x16x64_bf16 v[24:27], v[72:75], a[80:87], v2\n"
"v_smfmac_f32_16x16x64_bf16 v[24:27], v[76:79], a[88:95], v2\n"
"C19_%=:\n"
"s_waitcnt vmcnt(0)\n"
"v_max3_u32 v29, v80, v81, 0\n"
"v_max3_u32 v29, v82, v83, v29\n"
"v_max3_u32 v29, v84, v85, v29\n"
"v_max3_u32 v29, v86, v87, v29\n"
"v_max3_u32 v29, v88, v89, v29\n"
"v_max3_u32 v29, v90, v91, v29\n"
"v_max3_u32 v29, v92, v93, v29\n"
"v_max3_u32 v29, v94, v95, v29\n"
"v_cmp_eq_u32 vcc, -1, v29\n"
"s_nop 0\n"
"s_cbranch_vccz C20_%=\n"
"buffer_load_dwordx4 v[80:83], v3, s[36:39], s55 offen offset:0 sc1\n"
"buffer_load_dwordx4 v[84:87], v3, s[36:39], s55 offen offset:1280 sc1\n"
"buffer_load_dwordx4 v[88:91], v3, s[36:39], s55 offen offset:2560 sc1\n"
"buffer_load_dwordx4 v[92:95], v3, s[36:39], s55 offen offset:3840 sc1\n"
"s_sub_u32 s76, s76, 1\n"
"s_cbranch_scc0 C19_%=\n"
"s_branch CABORT_%=\n"
"C20_%=:\n"
"v_smfmac_f32_16x16x64_bf16 v[24:27], v[80:83], a[96:103], v2\n"
"v_smfmac_f32_16x16x64_bf16 v[24:27], v[84:87], a[104:111], v2\n"
"v_smfmac_f32_16x16x64_bf16 v[24:27], v[88:91], a[112:119], v2\n"
"v_smfmac_f32_16x16x64_bf16 v[24:27], v[92:95], a[120:127], v2\n"
"s_barrier\n"
"ds_read_b64 v[20:21], v8\n"
"s_waitcnt lgkmcnt(0)\n"
"s_nop 4\n"
"v_add_f32 v24, v24, v26\n"
"v_add_f32 v25, v25, v27\n"
"v_add_f32 v24, v24, v20\n"
"v_add_f32 v25, v25, v21\n"
"v_cvt_pk_bf16_f32 v22, v24, v25\n"
"s_nop 1\n"
"v_mov_b32_dpp v23, v22 quad_perm:[1,0,3,2] row_mask:0xf bank_mask:0xf\n"
"v_perm_b32 v22, v23, v22, v9\n"
"v_min_u32 v22, -2, v22\n"
"buffer_store_dword v22, v7, s[36:39], s56 offen sc1\n"
"buffer_store_dword v22, v7, s[36:39], s69 offen sc1\n"
"s_branch C9_%=\n"
"C12_%=:\n"
"s_branch C5_%=\n"
"C2_%=:\n"
"s_mov_b32 s43, 430\n"
"s_mov_b32 s64, 0\n"
"v_mov_b32 v12, -1\n"
"v_mov_b32 v13, -1\n"
"s_nop 4\n"
"C21_%=:\n"
"s_add_u32 s77, s73, 0\n"
"s_and_b32 s77, s77, 63\n"
"s_mul_i32 s77, s77, 40\n"
"s_add_u32 s77, s77, s45\n"
"s_lshr_b32 s78, s77, 7\n"
"s_xor_b32 s78, s78, s46\n"
"s_and_b32 s78, s78, 1\n"
"s_lshl_b32 s78, s78, 13\n"
"s_lshl_b32 s77, s77, 14\n"
"s_add_u32 s77, s77, s78\n"
"s_add_u32 s69, s77, 4194304\n"
"s_add_u32 s70, s69, 4096\n"
"s_add_u32 s52, s68, 0\n"
"s_add_u32 s53, s68, 32768\n"
"s_add_u32 s54, s68, 65536\n"
"s_add_u32 s55, s68, 98304\n"
"v_mov_b32 v24, 0\n"
"v_mov_b32 v25, 0\n"
"v_mov_b32 v26, 0\n"
"v_mov_b32 v27, 0\n"
"s_lshl_b32 s71, s73, 14\n"
"s_add_u32 s71, s71, s20\n"
"v_lshlrev_b32 v10, 2, v0\n"
"s_mov_b32 s76, 2000\n"
"C24_%=:\n"
"s_sleep 2\n"
"buffer_load_dword v11, v10, s[36:39], s71 offen sc1\n"
"s_sub_u32 s76, s76, 1\n"
"s_cbranch_scc1 C25_%=\n"
"s_waitcnt vmcnt(0)\n"
"v_cmp_eq_u32 vcc, -1, v11\n"
"s_nop 0\n"
"s_cbranch_vccnz C24_%=\n"
"v_readfirstlane_b32 s64, v11\n"
"s_nop 1\n"
"v_subrev_u32 v11, s64, v11\n"
"s_nop 1\n"
"v_max_i32_dpp v11, v11, v11 row_ror:1 row_mask:0xf bank_mask:0xf\n"
"s_nop 1\n"
"v_max_i32_dpp v11, v11, v11 row_ror:2 row_mask:0xf bank_mask:0xf\n"
"s_nop 1\n"
"v_max_i32_dpp v11, v11, v11 row_ror:4 row_mask:0xf bank_mask:0xf\n"
"s_nop 1\n"
"v_max_i32_dpp v11, v11, v11 row_ror:8 row_mask:0xf bank_mask:0xf\n"
"s_nop 0\n"
"v_readlane_b32 s72, v11, 0\n"
"v_readlane_b32 s77, v11, 16\n"
"v_readlane_b32 s78, v11, 32\n"
"v_readlane_b32 s79, v11, 48\n"
"s_max_i32 s72, s72, s77\n"
"s_max_i32 s78, s78, s79\n"
"s_max_i32 s72, s72, s78\n"
"s_add_u32 s64, s64, s72\n"
"s_bfe_i32 s72, s63, 524288\n"
"s_lshl_b32 s72, s72, 2\n"
"s_add_u32 s64, s64, s72\n"
"C25_%=:\n"
"s_waitcnt vmcnt(0)\n"
"ds_write_b64 v8, v[12:13]\n"
"s_mov_b32 s74, 6\n"
"s_sub_u32 s64, s64, 85\n"
"s_sub_u32 s65, s64, 260\n"
"s_add_u32 s72, s71, 4096\n"
"s_mov_b32 s76, 100\n"
"C28_%=:\n"
"s_memrealtime s[4:5]\n"
"s_nop 1\n"
"s_memrealtime s[6:7]\n"
"s_nop 1\n"
"s_memrealtime s[8:9]\n"
"s_nop 1\n"
"s_memrealtime s[10:11]\n"
"s_nop 1\n"
"s_memrealtime s[12:13]\n"
"s_nop 1\n"
"s_memrealtime s[14:15]\n"
"s_nop 1\n"
"s_memrealtime s[16:17]\n"
"s_nop 1\n"
"s_memrealtime s[18:19]\n"
"s_waitcnt lgkmcnt(0)\n"
"s_add_u32 s80, s4, s6\n"
"s_add_u32 s80, s80, s8\n"
"s_add_u32 s80, s80, s10\n"
"s_add_u32 s80, s80, s12\n"
"s_add_u32 s80, s80, s14\n"
"s_add_u32 s80, s80, s16\n"
"s_add_u32 s80, s80, s18\n"
"s_sub_u32 s76, s76, 1\n"
"s_cselect_b32 s79, 0, -1\n"
"s_sub_u32 s78, s65, s80\n"
"s_and_b32 s78, s78, s79\n"
"s_cmp_gt_i32 s78, 80\n"
"s_cbranch_scc1 C28_%=\n"
"s_max_i32 s78, s78, 0\n"
"s_add_u32 s80, s80, s78\n"
"s_mul_i32 s78, s78, 3\n"
"C27_%=:\n"
"s_sub_u32 s78, s78, 28\n"
"s_cbranch_scc0 C27_%=\n"
"buffer_load_dword v11, v10, s[36:39], s72 offen sc1\n"
"s_mov_b32 s76, 100\n"
"C30_%=:\n"
"s_memrealtime s[4:5]\n"
"s_nop 1\n"
"s_memrealtime s[6:7]\n"
"s_nop 1\n"
"s_memrealtime s[8:9]\n"
"s_nop 1\n"
"s_memrealtime s[10:11]\n"
"s_nop 1\n"
"s_memrealtime s[12:13]\n"
"s_nop 1\n"
"s_memrealtime s[14:15]\n"
"s_nop 1\n"
"s_memrealtime s[16:17]\n"
"s_nop 1\n"
"s_memrealtime s[18:19]\n"
"s_waitcnt lgkmcnt(0)\n"
"s_add_u32 s80, s4, s6\n"
"s_add_u32 s80, s80, s8\n"
"s_add_u32 s80, s80, s10\n"
"s_add_u32 s80, s80, s12\n"
"s_add_u32 s80, s80, s14\n"
"s_add_u32 s80, s80, s16\n"
"s_add_u32 s80, s80, s18\n"
"s_sub_u32 s76, s76, 1\n"
"s_cselect_b32 s79, 0, -1\n"
"s_sub_u32 s78, s64, s80\n"
"s_and_b32 s78, s78, s79\n"
"s_cmp_gt_i32 s78, 80\n"
"s_cbranch_scc1 C30_%=\n"
"s_max_i32 s78, s78, 0\n"
"s_add_u32 s80, s80, s78\n"
"s_mul_i32 s78, s78, 3\n"
"C29_%=:\n"
"s_sub_u32 s78, s78, 28\n"
"s_cbranch_scc0 C29_%=\n"
"s_waitcnt vmcnt(0)\n"
"v_cmp_ne_u32 vcc, -1, v11\n"
"s_nop 0\n"
"s_cbranch_vccz C26_%=\n"
"v_subrev_u32 v11, s64, v11\n"
"v_cndmask_b32 v11, 0, v11, vcc\n"
"s_nop 1\n"
"v_max_i32_dpp v11, v11, v11 row_ror:1 row_mask:0xf bank_mask:0xf\n"
"s_nop 1\n"
"v_max_i32_dpp v11, v11, v11 row_ror:2 row_mask:0xf bank_mask:0xf\n"
"s_nop 1\n"
"v_max_i32_dpp v11, v11, v11 row_ror:4 row_mask:0xf bank_mask:0xf\n"
"s_nop 1\n"
"v_max_i32_dpp v11, v11, v11 row_ror:8 row_mask:0xf bank_mask:0xf\n"
"s_nop 0\n"
"v_readlane_b32 s72, v11, 0\n"
"v_readlane_b32 s77, v11, 16\n"
"v_readlane_b32 s78, v11, 32\n"
"v_readlane_b32 s79, v11, 48\n"
"s_max_i32 s72, s72, s77\n"
"s_max_i32 s78, s78, s79\n"
"s_max_i32 s72, s72, s78\n"
"s_bfe_i32 s65, s63, 524288\n"
"s_lshl_b32 s65, s65, 2\n"
"s_add_u32 s72, s72, s65\n"
"s_sub_u32 s72, s72, 85\n"
"s_max_i32 s72, s72, 0\n"
"s_add_u32 s64, s64, s72\n"
"s_mov_b32 s76, 100\n"
"C32_%=:\n"
"s_memrealtime s[4:5]\n"
"s_nop 1\n"
"s_memrealtime s[6:7]\n"
"s_nop 1\n"
"s_memrealtime s[8:9]\n"
"s_nop 1\n"
"s_memrealtime s[10:11]\n"
"s_nop 1\n"
"s_memrealtime s[12:13]\n"
"s_nop 1\n"
"s_memrealtime s[14:15]\n"
"s_nop 1\n"
"s_memrealtime s[16:17]\n"
"s_nop 1\n"
"s_memrealtime s[18:19]\n"
"s_waitcnt lgkmcnt(0)\n"
"s_add_u32 s80, s4, s6\n"
"s_add_u32 s80, s80, s8\n"
"s_add_u32 s80, s80, s10\n"
"s_add_u32 s80, s80, s12\n"
"s_add_u32 s80, s80, s14\n"
"s_add_u32 s80, s80, s16\n"
"s_add_u32 s80, s80, s18\n"
"s_sub_u32 s76, s76, 1\n"
"s_cselect_b32 s79, 0, -1\n"
"s_sub_u32 s78, s64, s80\n"
"s_and_b32 s78, s78, s79\n"
"s_cmp_gt_i32 s78, 80\n"
"s_cbranch_scc1 C32_%=\n"
"s_max_i32 s78, s78, 0\n"
"s_add_u32 s80, s80, s78\n"
"s_mul_i32 s78, s78, 3\n"
"C31_%=:\n"
"s_sub_u32 s78, s78, 28\n"
"s_cbranch_scc0 C31_%=\n"
"C26_%=:\n"
"buffer_load_dwordx4 v[64:67], v0, s[36:39], s69 offen offset:0 sc1\n"
"s_nop 2\n"
"buffer_load_dwordx4 v[68:71], v0, s[36:39], s69 offen offset:1024 sc1\n"
"s_nop 2\n"
"buffer_load_dwordx4 v[72:75], v0, s[36:39], s69 offen offset:2048 sc1\n"
"s_nop 2\n"
"buffer_load_dwordx4 v[76:79], v0, s[36:39], s69 offen offset:3072 sc1\n"
"s_nop 2\n"
"buffer_load_dwordx4 v[80:83], v0, s[36:39], s70 offen offset:0 sc1\n"
"s_nop 2\n"
"buffer_load_dwordx4 v[84:87], v0, s[36:39], s70 offen offset:1024 sc1\n"
"s_nop 2\n"
"buffer_load_dwordx4 v[88:91], v0, s[36:39], s70 offen offset:2048 sc1\n"
"s_nop 2\n"
"buffer_load_dwordx4 v[92:95], v0, s[36:39], s70 offen offset:3072 sc1\n"
"s_nop 2\n"
"s_nop 15\n"
"s_nop 15\n"
"buffer_load_dwordx4 v[32:35], v4, s[36:39], s52 offen offset:0 sc1\n"
"buffer_load_dwordx4 v[36:39], v4, s[36:39], s53 offen offset:0 sc1\n"
"buffer_load_dwordx4 v[40:43], v4, s[36:39], s54 offen offset:0 sc1\n"
"buffer_load_dwordx4 v[44:47], v4, s[36:39], s55 offen offset:0 sc1\n"
"s_barrier\n"
"C33_%=:\n"
"s_waitcnt vmcnt(11)\n"
"v_permlane16_swap_b32 v64, v66\n"
"v_permlane16_swap_b32 v65, v67\n"
"v_swap_b32 v65, v66\n"
"s_waitcnt vmcnt(10)\n"
"v_permlane16_swap_b32 v68, v70\n"
"v_permlane16_swap_b32 v69, v71\n"
"v_swap_b32 v69, v70\n"
"s_waitcnt vmcnt(9)\n"
"v_permlane16_swap_b32 v72, v74\n"
"v_permlane16_swap_b32 v73, v75\n"
"v_swap_b32 v73, v74\n"
"s_waitcnt vmcnt(8)\n"
"v_permlane16_swap_b32 v76, v78\n"
"v_permlane16_swap_b32 v77, v79\n"
"v_swap_b32 v77, v78\n"
"s_waitcnt vmcnt(7)\n"
"v_permlane16_swap_b32 v80, v82\n"
"v_permlane16_swap_b32 v81, v83\n"
"v_swap_b32 v81, v82\n"
"s_waitcnt vmcnt(6)\n"
"v_permlane16_swap_b32 v84, v86\n"
"v_permlane16_swap_b32 v85, v87\n"
"v_swap_b32 v85, v86\n"
"s_waitcnt vmcnt(5)\n"
"v_permlane16_swap_b32 v88, v90\n"
"v_permlane16_swap_b32 v89, v91\n"
"v_swap_b32 v89, v90\n"
"s_waitcnt vmcnt(4)\n"
"v_permlane16_swap_b32 v92, v94\n"
"v_permlane16_swap_b32 v93, v95\n"
"v_swap_b32 v93, v94\n"
"C34_%=:\n"
"s_waitcnt vmcnt(3)\n"
"v_smfmac_f32_16x16x64_bf16 v[24:27], v[32:35], v[64:71], v2\n"
"s_waitcnt vmcnt(2)\n"
"v_smfmac_f32_16x16x64_bf16 v[24:27], v[36:39], v[72:79], v2\n"
"s_waitcnt vmcnt(1)\n"
"v_smfmac_f32_16x16x64_bf16 v[24:27], v[40:43], v[80:87], v2\n"
"s_waitcnt vmcnt(0)\n"
"v_smfmac_f32_16x16x64_bf16 v[24:27], v[44:47], v[88:95], v2\n"
"s_nop 7\n"
"v_permlane32_swap_b32 v24, v26\n"
"v_permlane32_swap_b32 v25, v27\n"
"v_add_f32 v24, v24, v26\n"
"v_add_f32 v25, v25, v27\n"
"v_cmp_u_f32 vcc, v24, v25\n"
"ds_write_b64 v8, v[24:25]\n"
"s_cbranch_vccnz C22_%=\n"
"C23_%=:\n"
"s_waitcnt lgkmcnt(0)\n"
"s_barrier\n"
"s_memrealtime s[4:5]\n"
"s_nop 1\n"
"s_memrealtime s[6:7]\n"
"s_nop 1\n"
"s_memrealtime s[8:9]\n"
"s_nop 1\n"
"s_memrealtime s[10:11]\n"
"s_nop 1\n"
"s_memrealtime s[12:13]\n"
"s_nop 1\n"
"s_memrealtime s[14:15]\n"
"s_nop 1\n"
"s_memrealtime s[16:17]\n"
"s_nop 1\n"
"s_memrealtime s[18:19]\n"
"s_waitcnt lgkmcnt(0)\n"
"s_add_u32 s42, s4, s6\n"
"s_add_u32 s42, s42, s8\n"
"s_add_u32 s42, s42, s10\n"
"s_add_u32 s42, s42, s12\n"
"s_add_u32 s42, s42, s14\n"
"s_add_u32 s42, s42, s16\n"
"s_add_u32 s42, s42, s18\n"
"s_sub_u32 s42, s42, s80\n"
"s_sub_u32 s42, s42, 85\n"
"s_sub_u32 s75, s42, s43\n"
"s_min_i32 s75, s75, 48\n"
"s_cmp_lt_i32 s75, -64\n"
"s_cselect_b32 s76, 0, 2\n"
"s_ashr_i32 s75, s75, s76\n"
"s_add_u32 s43, s43, s75\n"
"s_lshl_b32 s78, s45, 6\n"
"s_add_u32 s77, s71, s78\n"
"s_xor_b32 s77, s77, 8192\n"
"v_mov_b32 v29, s43\n"
"v_lshrrev_b32 v30, 2, v0\n"
"s_mov_b64 exec, 0xffff\n"
"buffer_store_dword v29, v30, s[36:39], s77 offen sc1\n"
"s_mov_b64 exec, -1\n"
"v_xor_b32 v8, 512, v8\n"
"s_add_u32 s73, s73, 1\n"
"s_add_u32 s79, s68, 131072\n"
"s_and_b32 s79, s79, 0x3fffff\n"
"s_xor_b32 s68, s79, s68\n"
"s_lshr_b32 s68, s68, 8\n"
"s_and_b32 s68, s68, 8192\n"
"s_xor_b32 s68, s68, s79\n"
"s_cmp_lt_u32 s73, 63\n"
"s_cbranch_scc1 C21_%=\n"
"s_branch C35_%=\n"
"C22_%=:\n"
"s_waitcnt lgkmcnt(0)\n"
"v_mov_b32 v24, 0\n"
"v_mov_b32 v25, 0\n"
"v_mov_b32 v26, 0\n"
"v_mov_b32 v27, 0\n"
"s_sub_u32 s74, s74, 1\n"
"s_cbranch_scc1 C37_%=\n"
"buffer_load_dwordx4 v[32:35], v4, s[36:39], s52 offen offset:0 sc1\n"
"buffer_load_dwordx4 v[36:39], v4, s[36:39], s53 offen offset:0 sc1\n"
"buffer_load_dwordx4 v[40:43], v4, s[36:39], s54 offen offset:0 sc1\n"
"buffer_load_dwordx4 v[44:47], v4, s[36:39], s55 offen offset:0 sc1\n"
"v_max3_u32 v29, v64, v65, 0\n"
"v_max3_u32 v29, v66, v67, v29\n"
"v_max3_u32 v29, v68, v69, v29\n"
"v_max3_u32 v29, v70, v71, v29\n"
"v_max3_u32 v29, v72, v73, v29\n"
"v_max3_u32 v29, v74, v75, v29\n"
"v_max3_u32 v29, v76, v77, v29\n"
"v_max3_u32 v29, v78, v79, v29\n"
"v_max3_u32 v29, v80, v81, v29\n"
"v_max3_u32 v29, v82, v83, v29\n"
"v_max3_u32 v29, v84, v85, v29\n"
"v_max3_u32 v29, v86, v87, v29\n"
"v_max3_u32 v29, v88, v89, v29\n"
"v_max3_u32 v29, v90, v91, v29\n"
"v_max3_u32 v29, v92, v93, v29\n"
"v_max3_u32 v29, v94, v95, v29\n"
"v_cmp_eq_u32 vcc, -1, v29\n"
"s_nop 0\n"
"s_cbranch_vccz C34_%=\n"
"buffer_load_dwordx4 v[64:67], v0, s[36:39], s69 offen offset:0 sc1\n"
"buffer_load_dwordx4 v[68:71], v0, s[36:39], s69 offen offset:1024 sc1\n"
"buffer_load_dwordx4 v[72:75], v0, s[36:39], s69 offen offset:2048 sc1\n"
"buffer_load_dwordx4 v[76:79], v0, s[36:39], s69 offen offset:3072 sc1\n"
"buffer_load_dwordx4 v[80:83], v0, s[36:39], s70 offen offset:0 sc1\n"
"buffer_load_dwordx4 v[84:87], v0, s[36:39], s70 offen offset:1024 sc1\n"
"buffer_load_dwordx4 v[88:91], v0, s[36:39], s70 offen offset:2048 sc1\n"
"buffer_load_dwordx4 v[92:95], v0, s[36:39], s70 offen offset:3072 sc1\n"
"buffer_load_dwordx4 v[32:35], v4, s[36:39], s52 offen offset:0 sc1\n"
"buffer_load_dwordx4 v[36:39], v4, s[36:39], s53 offen offset:0 sc1\n"
"buffer_load_dwordx4 v[40:43], v4, s[36:39], s54 offen offset:0 sc1\n"
"buffer_load_dwordx4 v[44:47], v4, s[36:39], s55 offen offset:0 sc1\n"
"s_branch C33_%=\n"
"C37_%=:\n"
"v_max3_u32 v29, v64, v65, 0\n"
"v_max3_u32 v29, v66, v67, v29\n"
"v_max3_u32 v29, v68, v69, v29\n"
"v_max3_u32 v29, v70, v71, v29\n"
"v_max3_u32 v29, v72, v73, v29\n"
"v_max3_u32 v29, v74, v75, v29\n"
"v_max3_u32 v29, v76, v77, v29\n"
"v_max3_u32 v29, v78, v79, v29\n"
"v_max3_u32 v29, v80, v81, v29\n"
"v_max3_u32 v29, v82, v83, v29\n"
"v_max3_u32 v29, v84, v85, v29\n"
"v_max3_u32 v29, v86, v87, v29\n"
"v_max3_u32 v29, v88, v89, v29\n"
"v_max3_u32 v29, v90, v91, v29\n"
"v_max3_u32 v29, v92, v93, v29\n"
"v_max3_u32 v29, v94, v95, v29\n"
"v_cmp_eq_u32 vcc, -1, v29\n"
"s_nop 0\n"
"s_cbranch_vccz C36_%=\n"
"s_mov_b32 s76, 131072\n"
"C38_%=:\n"
"buffer_load_dwordx4 v[64:67], v0, s[36:39], s69 offen offset:0 sc1\n"
"buffer_load_dwordx4 v[68:71], v0, s[36:39], s69 offen offset:1024 sc1\n"
"buffer_load_dwordx4 v[72:75], v0, s[36:39], s69 offen offset:2048 sc1\n"
"buffer_load_dwordx4 v[76:79], v0, s[36:39], s69 offen offset:3072 sc1\n"
"buffer_load_dwordx4 v[80:83], v0, s[36:39], s70 offen offset:0 sc1\n"
"buffer_load_dwordx4 v[84:87], v0, s[36:39], s70 offen offset:1024 sc1\n"
"buffer_load_dwordx4 v[88:91], v0, s[36:39], s70 offen offset:2048 sc1\n"
"buffer_load_dwordx4 v[92:95], v0, s[36:39], s70 offen offset:3072 sc1\n"
"s_sub_u32 s76, s76, 1\n"
"s_cbranch_scc1 CABORT_%=\n"
"s_waitcnt vmcnt(7)\n"
"v_max3_u32 v29, v64, v65, 0\n"
"v_max3_u32 v29, v66, v67, v29\n"
"s_waitcnt vmcnt(6)\n"
"v_max3_u32 v29, v68, v69, v29\n"
"v_max3_u32 v29, v70, v71, v29\n"
"s_waitcnt vmcnt(5)\n"
"v_max3_u32 v29, v72, v73, v29\n"
"v_max3_u32 v29, v74, v75, v29\n"
"s_waitcnt vmcnt(4)\n"
"v_max3_u32 v29, v76, v77, v29\n"
"v_max3_u32 v29, v78, v79, v29\n"
"s_waitcnt vmcnt(3)\n"
"v_max3_u32 v29, v80, v81, v29\n"
"v_max3_u32 v29, v82, v83, v29\n"
"s_waitcnt vmcnt(2)\n"
"v_max3_u32 v29, v84, v85, v29\n"
"v_max3_u32 v29, v86, v87, v29\n"
"s_waitcnt vmcnt(1)\n"
"v_max3_u32 v29, v88, v89, v29\n"
"v_max3_u32 v29, v90, v91, v29\n"
"s_waitcnt vmcnt(0)\n"
"v_max3_u32 v29, v92, v93, v29\n"
"v_max3_u32 v29, v94, v95, v29\n"
"v_cmp_eq_u32 vcc, -1, v29\n"
"s_nop 0\n"
"s_cbranch_vccnz C38_%=\n"
"v_permlane16_swap_b32 v64, v66\n"
"v_permlane16_swap_b32 v65, v67\n"
"v_swap_b32 v65, v66\n"
"v_permlane16_swap_b32 v68, v70\n"
"v_permlane16_swap_b32 v69, v71\n"
"v_swap_b32 v69, v70\n"
"v_permlane16_swap_b32 v72, v74\n"
"v_permlane16_swap_b32 v73, v75\n"
"v_swap_b32 v73, v74\n"
"v_permlane16_swap_b32 v76, v78\n"
"v_permlane16_swap_b32 v77, v79\n"
"v_swap_b32 v77, v78\n"
"v_permlane16_swap_b32 v80, v82\n"
"v_permlane16_swap_b32 v81, v83\n"
"v_swap_b32 v81, v82\n"
"v_permlane16_swap_b32 v84, v86\n"
"v_permlane16_swap_b32 v85, v87\n"
"v_swap_b32 v85, v86\n"
"v_permlane16_swap_b32 v88, v90\n"
"v_permlane16_swap_b32 v89, v91\n"
"v_swap_b32 v89, v90\n"
"v_permlane16_swap_b32 v92, v94\n"
"v_permlane16_swap_b32 v93, v95\n"
"v_swap_b32 v93, v94\n"
"C36_%=:\n"
"s_mov_b32 s76, 131072\n"
"C39_%=:\n"
"buffer_load_dwordx4 v[32:35], v4, s[36:39], s52 offen offset:0 sc1\n"
"buffer_load_dwordx4 v[36:39], v4, s[36:39], s53 offen offset:0 sc1\n"
"buffer_load_dwordx4 v[40:43], v4, s[36:39], s54 offen offset:0 sc1\n"
"buffer_load_dwordx4 v[44:47], v4, s[36:39], s55 offen offset:0 sc1\n"
"s_sub_u32 s76, s76, 1\n"
"s_cbranch_scc1 CABORT_%=\n"
"s_waitcnt vmcnt(3)\n"
"v_max3_u32 v29, v32, v33, 0\n"
"v_max3_u32 v29, v34, v35, v29\n"
"s_waitcnt vmcnt(2)\n"
"v_max3_u32 v29, v36, v37, v29\n"
"v_max3_u32 v29, v38, v39, v29\n"
"s_waitcnt vmcnt(1)\n"
"v_max3_u32 v29, v40, v41, v29\n"
"v_max3_u32 v29, v42, v43, v29\n"
"s_waitcnt vmcnt(0)\n"
"v_max3_u32 v29, v44, v45, v29\n"
"v_max3_u32 v29, v46, v47, v29\n"
"v_cmp_eq_u32 vcc, -1, v29\n"
"s_nop 0\n"
"s_cbranch_vccnz C39_%=\n"
"v_smfmac_f32_16x16x64_bf16 v[24:27], v[32:35], v[64:71], v2\n"
"v_smfmac_f32_16x16x64_bf16 v[24:27], v[36:39], v[72:79], v2\n"
"v_smfmac_f32_16x16x64_bf16 v[24:27], v[40:43], v[80:87], v2\n"
"v_smfmac_f32_16x16x64_bf16 v[24:27], v[44:47], v[88:95], v2\n"
"s_nop 7\n"
"v_permlane32_swap_b32 v24, v26\n"
"v_permlane32_swap_b32 v25, v27\n"
"v_add_f32 v24, v24, v26\n"
"v_add_f32 v25, v25, v27\n"
"ds_write_b64 v8, v[24:25]\n"
"s_branch C23_%=\n"
"C35_%=:\n"
"s_branch C5_%=\n"
"C3_%=:\n"
"v_mov_b32 v12, -1\n"
"v_mov_b32 v13, -1\n"
"v_mov_b32 v14, -1\n"
"v_mov_b32 v15, -1\n"
"s_nop 4\n"
"C40_%=:\n"
"s_cmp_gt_u32 s73, 61\n"
"s_cbranch_scc1 C41_%=\n"
"s_sleep 12\n"
"s_add_u32 s77, s73, 1\n"
"s_and_b32 s77, s77, 63\n"
"s_mul_i32 s77, s77, 40\n"
"s_add_u32 s77, s77, s45\n"
"s_lshr_b32 s78, s77, 7\n"
"s_xor_b32 s78, s78, s46\n"
"s_and_b32 s78, s78, 1\n"
"s_lshl_b32 s78, s78, 13\n"
"s_lshl_b32 s77, s77, 14\n"
"s_add_u32 s77, s77, s78\n"
"s_add_u32 s69, s77, 4194304\n"
"s_add_u32 s70, s69, 4096\n"
"buffer_store_dwordx4 v[12:15], v0, s[36:39], s69 offen offset:0 sc1\n"
"buffer_store_dwordx4 v[12:15], v0, s[36:39], s69 offen offset:1024 sc1\n"
"buffer_store_dwordx4 v[12:15], v0, s[36:39], s69 offen offset:2048 sc1\n"
"buffer_store_dwordx4 v[12:15], v0, s[36:39], s69 offen offset:3072 sc1\n"
"buffer_store_dwordx4 v[12:15], v0, s[36:39], s70 offen offset:0 sc1\n"
"buffer_store_dwordx4 v[12:15], v0, s[36:39], s70 offen offset:1024 sc1\n"
"buffer_store_dwordx4 v[12:15], v0, s[36:39], s70 offen offset:2048 sc1\n"
"buffer_store_dwordx4 v[12:15], v0, s[36:39], s70 offen offset:3072 sc1\n"
"s_add_u32 s79, s68, 262144\n"
"s_and_b32 s79, s79, 0x3fffff\n"
"s_xor_b32 s71, s79, s68\n"
"s_lshr_b32 s71, s71, 8\n"
"s_and_b32 s71, s71, 8192\n"
"s_xor_b32 s71, s71, s79\n"
"s_lshl_b32 s72, s73, 14\n"
"s_add_u32 s72, s72, s20\n"
"s_sub_u32 s72, s72, 98304\n"
"s_cmp_lt_u32 s45, 32\n"
"s_cselect_b32 s71, s71, s72\n"
"s_cselect_b32 s74, 0, 234881024\n"
"s_lshr_b32 s72, s45, 3\n"
"s_mul_i32 s72, s72, 32768\n"
"s_add_u32 s71, s71, s72\n"
"s_and_b32 s72, s45, 7\n"
"s_lshl_b32 s72, s72, 10\n"
"s_add_u32 s71, s71, s72\n"
"buffer_store_dwordx4 v[12:15], v0, s[36:39], s71 offen sc1\n"
"s_cmp_lt_u32 s45, 32\n"
"s_cselect_b32 s72, 14, 13\n"
"s_lshl_b32 s72, 1, s72\n"
"s_xor_b32 s72, s72, s71\n"
"s_cmp_lt_u32 s45, 36\n"
"s_cselect_b32 s72, s72, s38\n"
"buffer_store_dwordx4 v[12:15], v0, s[36:39], s72 offen sc1\n"
"C41_%=:\n"
"s_waitcnt vmcnt(0)\n"
"s_barrier\n"
"s_barrier\n"
"v_xor_b32 v8, 512, v8\n"
"s_add_u32 s73, s73, 1\n"
"s_add_u32 s79, s68, 131072\n"
"s_and_b32 s79, s79, 0x3fffff\n"
"s_xor_b32 s68, s79, s68\n"
"s_lshr_b32 s68, s68, 8\n"
"s_and_b32 s68, s68, 8192\n"
"s_xor_b32 s68, s68, s79\n"
"s_cmp_lt_u32 s73, 63\n"
"s_cbranch_scc1 C40_%=\n"
"s_branch C42_%=\n"
"C42_%=:\n"
"s_branch C5_%=\n"
"C4_%=:\n"
"s_mov_b32 s47, 0x8000\n"
"s_mov_b32 s75, 1\n"
"v_mov_b32 v28, -1\n"
"s_mov_b32 s76, 131072\n"
"C43_%=:\n"
"s_sleep 0\n"
"buffer_load_dword v28, v5, s[36:39], 0 offen sc1\n"
"s_sub_u32 s76, s76, 1\n"
"s_cbranch_scc1 CABORT_%=\n"
"s_waitcnt vmcnt(0)\n"
"v_cmp_gt_u32 vcc, s75, v28\n"
"s_nop 0\n"
"s_cbranch_vccnz C43_%=\n"
"s_add_u32 m0, s47, 0\n"
"s_add_u32 s72, s44, 0\n"
"s_add_u32 s74, s44, 1024\n"
"buffer_load_dwordx4 v1, s[36:39], s72 offen sc1 lds\n"
"buffer_load_dwordx4 v1, s[36:39], s74 offen offset:1024 sc1 lds\n"
"s_add_u32 m0, s47, 2048\n"
"s_add_u32 s72, s44, 4096\n"
"s_add_u32 s74, s44, 5120\n"
"buffer_load_dwordx4 v1, s[36:39], s72 offen sc1 lds\n"
"buffer_load_dwordx4 v1, s[36:39], s74 offen offset:1024 sc1 lds\n"
"s_add_u32 m0, s47, 4096\n"
"s_add_u32 s72, s44, 8192\n"
"s_add_u32 s74, s44, 9216\n"
"buffer_load_dwordx4 v1, s[36:39], s72 offen sc1 lds\n"
"buffer_load_dwordx4 v1, s[36:39], s74 offen offset:1024 sc1 lds\n"
"s_add_u32 m0, s47, 6144\n"
"s_add_u32 s72, s44, 12288\n"
"s_add_u32 s74, s44, 13312\n"
"buffer_load_dwordx4 v1, s[36:39], s72 offen sc1 lds\n"
"buffer_load_dwordx4 v1, s[36:39], s74 offen offset:1024 sc1 lds\n"
"s_add_u32 m0, s47, 8192\n"
"s_add_u32 s72, s44, 16384\n"
"s_add_u32 s74, s44, 17408\n"
"buffer_load_dwordx4 v1, s[36:39], s72 offen sc1 lds\n"
"buffer_load_dwordx4 v1, s[36:39], s74 offen offset:1024 sc1 lds\n"
"s_add_u32 m0, s47, 10240\n"
"s_add_u32 s72, s44, 20480\n"
"s_add_u32 s74, s44, 21504\n"
"buffer_load_dwordx4 v1, s[36:39], s72 offen sc1 lds\n"
"buffer_load_dwordx4 v1, s[36:39], s74 offen offset:1024 sc1 lds\n"
"s_add_u32 m0, s47, 12288\n"
"s_add_u32 s72, s44, 24576\n"
"s_add_u32 s74, s44, 25600\n"
"buffer_load_dwordx4 v1, s[36:39], s72 offen sc1 lds\n"
"buffer_load_dwordx4 v1, s[36:39], s74 offen offset:1024 sc1 lds\n"
"s_add_u32 m0, s47, 14336\n"
"s_add_u32 s72, s44, 28672\n"
"s_add_u32 s74, s44, 29696\n"
"buffer_load_dwordx4 v1, s[36:39], s72 offen sc1 lds\n"
"buffer_load_dwordx4 v1, s[36:39], s74 offen offset:1024 sc1 lds\n"
"s_add_u32 m0, s47, 16384\n"
"s_add_u32 s72, s44, 32768\n"
"s_add_u32 s74, s44, 33792\n"
"buffer_load_dwordx4 v1, s[36:39], s72 offen sc1 lds\n"
"buffer_load_dwordx4 v1, s[36:39], s74 offen offset:1024 sc1 lds\n"
"s_add_u32 m0, s47, 18432\n"
"s_add_u32 s72, s44, 36864\n"
"s_add_u32 s74, s44, 37888\n"
"buffer_load_dwordx4 v1, s[36:39], s72 offen sc1 lds\n"
"buffer_load_dwordx4 v1, s[36:39], s74 offen offset:1024 sc1 lds\n"
"s_add_u32 m0, s47, 20480\n"
"s_add_u32 s72, s44, 40960\n"
"s_add_u32 s74, s44, 41984\n"
"buffer_load_dwordx4 v1, s[36:39], s72 offen sc1 lds\n"
"buffer_load_dwordx4 v1, s[36:39], s74 offen offset:1024 sc1 lds\n"
"s_add_u32 m0, s47, 22528\n"
"s_add_u32 s72, s44, 45056\n"
"s_add_u32 s74, s44, 46080\n"
"buffer_load_dwordx4 v1, s[36:39], s72 offen sc1 lds\n"
"buffer_load_dwordx4 v1, s[36:39], s74 offen offset:1024 sc1 lds\n"
"s_add_u32 m0, s47, 24576\n"
"s_add_u32 s72, s44, 49152\n"
"s_add_u32 s74, s44, 50176\n"
"buffer_load_dwordx4 v1, s[36:39], s72 offen sc1 lds\n"
"buffer_load_dwordx4 v1, s[36:39], s74 offen offset:1024 sc1 lds\n"
"s_add_u32 m0, s47, 26624\n"
"s_add_u32 s72, s44, 53248\n"
"s_add_u32 s74, s44, 54272\n"
"buffer_load_dwordx4 v1, s[36:39], s72 offen sc1 lds\n"
"buffer_load_dwordx4 v1, s[36:39], s74 offen offset:1024 sc1 lds\n"
"s_add_u32 m0, s47, 28672\n"
"s_add_u32 s72, s44, 57344\n"
"s_add_u32 s74, s44, 58368\n"
"buffer_load_dwordx4 v1, s[36:39], s72 offen sc1 lds\n"
"buffer_load_dwordx4 v1, s[36:39], s74 offen offset:1024 sc1 lds\n"
"s_add_u32 m0, s47, 30720\n"
"s_add_u32 s72, s44, 61440\n"
"s_add_u32 s74, s44, 62464\n"
"buffer_load_dwordx4 v1, s[36:39], s72 offen sc1 lds\n"
"buffer_load_dwordx4 v1, s[36:39], s74 offen offset:1024 sc1 lds\n"
"s_add_u32 s44, s44, 1310720\n"
"s_xor_b32 s47, s47, 0x18000\n"
"s_nop 0\n"
"C44_%=:\n"
"s_add_u32 s75, s73, 2\n"
"v_cmp_gt_u32 vcc, s75, v28\n"
"s_nop 0\n"
"s_cbranch_vccz C45_%=\n"
"buffer_load_dword v28, v5, s[36:39], 0 offen sc1\n"
"C45_%=:\n"
"s_waitcnt vmcnt(0)\n"
"v_cmp_gt_u32 vcc, s75, v28\n"
"s_barrier\n"
"s_cbranch_vccnz C46_%=\n"
"s_sleep 5\n"
"s_add_u32 m0, s47, 0\n"
"s_add_u32 s72, s44, 0\n"
"s_add_u32 s74, s44, 1024\n"
"buffer_load_dwordx4 v1, s[36:39], s72 offen sc1 lds\n"
"buffer_load_dwordx4 v1, s[36:39], s74 offen offset:1024 sc1 lds\n"
"s_add_u32 m0, s47, 2048\n"
"s_add_u32 s72, s44, 4096\n"
"s_add_u32 s74, s44, 5120\n"
"buffer_load_dwordx4 v1, s[36:39], s72 offen sc1 lds\n"
"buffer_load_dwordx4 v1, s[36:39], s74 offen offset:1024 sc1 lds\n"
"s_add_u32 m0, s47, 4096\n"
"s_add_u32 s72, s44, 8192\n"
"s_add_u32 s74, s44, 9216\n"
"buffer_load_dwordx4 v1, s[36:39], s72 offen sc1 lds\n"
"buffer_load_dwordx4 v1, s[36:39], s74 offen offset:1024 sc1 lds\n"
"s_add_u32 m0, s47, 6144\n"
"s_add_u32 s72, s44, 12288\n"
"s_add_u32 s74, s44, 13312\n"
"buffer_load_dwordx4 v1, s[36:39], s72 offen sc1 lds\n"
"buffer_load_dwordx4 v1, s[36:39], s74 offen offset:1024 sc1 lds\n"
"s_add_u32 m0, s47, 8192\n"
"s_add_u32 s72, s44, 16384\n"
"s_add_u32 s74, s44, 17408\n"
"buffer_load_dwordx4 v1, s[36:39], s72 offen sc1 lds\n"
"buffer_load_dwordx4 v1, s[36:39], s74 offen offset:1024 sc1 lds\n"
"s_barrier\n"
"s_sleep 9\n"
"s_add_u32 m0, s47, 10240\n"
"s_add_u32 s72, s44, 20480\n"
"s_add_u32 s74, s44, 21504\n"
"buffer_load_dwordx4 v1, s[36:39], s72 offen sc1 lds\n"
"buffer_load_dwordx4 v1, s[36:39], s74 offen offset:1024 sc1 lds\n"
"s_add_u32 m0, s47, 12288\n"
"s_add_u32 s72, s44, 24576\n"
"s_add_u32 s74, s44, 25600\n"
"buffer_load_dwordx4 v1, s[36:39], s72 offen sc1 lds\n"
"buffer_load_dwordx4 v1, s[36:39], s74 offen offset:1024 sc1 lds\n"
"s_add_u32 m0, s47, 14336\n"
"s_add_u32 s72, s44, 28672\n"
"s_add_u32 s74, s44, 29696\n"
"buffer_load_dwordx4 v1, s[36:39], s72 offen sc1 lds\n"
"buffer_load_dwordx4 v1, s[36:39], s74 offen offset:1024 sc1 lds\n"
"s_add_u32 m0, s47, 16384\n"
"s_add_u32 s72, s44, 32768\n"
"s_add_u32 s74, s44, 33792\n"
"buffer_load_dwordx4 v1, s[36:39], s72 offen sc1 lds\n"
"buffer_load_dwordx4 v1, s[36:39], s74 offen offset:1024 sc1 lds\n"
"s_add_u32 m0, s47, 18432\n"
"s_add_u32 s72, s44, 36864\n"
"s_add_u32 s74, s44, 37888\n"
"buffer_load_dwordx4 v1, s[36:39], s72 offen sc1 lds\n"
"buffer_load_dwordx4 v1, s[36:39], s74 offen offset:1024 sc1 lds\n"
"s_add_u32 m0, s47, 20480\n"
"s_add_u32 s72, s44, 40960\n"
"s_add_u32 s74, s44, 41984\n"
"buffer_load_dwordx4 v1, s[36:39], s72 offen sc1 lds\n"
"buffer_load_dwordx4 v1, s[36:39], s74 offen offset:1024 sc1 lds\n"
"s_add_u32 m0, s47, 22528\n"
"s_add_u32 s72, s44, 45056\n"
"s_add_u32 s74, s44, 46080\n"
"buffer_load_dwordx4 v1, s[36:39], s72 offen sc1 lds\n"
"buffer_load_dwordx4 v1, s[36:39], s74 offen offset:1024 sc1 lds\n"
"s_add_u32 m0, s47, 24576\n"
"s_add_u32 s72, s44, 49152\n"
"s_add_u32 s74, s44, 50176\n"
"buffer_load_dwordx4 v1, s[36:39], s72 offen sc1 lds\n"
"buffer_load_dwordx4 v1, s[36:39], s74 offen offset:1024 sc1 lds\n"
"s_add_u32 m0, s47, 26624\n"
"s_add_u32 s72, s44, 53248\n"
"s_add_u32 s74, s44, 54272\n"
"buffer_load_dwordx4 v1, s[36:39], s72 offen sc1 lds\n"
"buffer_load_dwordx4 v1, s[36:39], s74 offen offset:1024 sc1 lds\n"
"s_add_u32 m0, s47, 28672\n"
"s_add_u32 s72, s44, 57344\n"
"s_add_u32 s74, s44, 58368\n"
"buffer_load_dwordx4 v1, s[36:39], s72 offen sc1 lds\n"
"buffer_load_dwordx4 v1, s[36:39], s74 offen offset:1024 sc1 lds\n"
"s_add_u32 m0, s47, 30720\n"
"s_add_u32 s72, s44, 61440\n"
"s_add_u32 s74, s44, 62464\n"
"buffer_load_dwordx4 v1, s[36:39], s72 offen sc1 lds\n"
"buffer_load_dwordx4 v1, s[36:39], s74 offen offset:1024 sc1 lds\n"
"s_add_u32 s44, s44, 1310720\n"
"s_xor_b32 s47, s47, 0x18000\n"
"s_branch C47_%=\n"
"C46_%=:\n"
"s_sleep 6\n"
"buffer_load_dword v28, v5, s[36:39], 0 offen sc1\n"
"s_barrier\n"
"s_waitcnt vmcnt(0)\n"
"v_cmp_gt_u32 vcc, s75, v28\n"
"s_nop 0\n"
"s_cbranch_vccz C48_%=\n"
"s_mov_b32 s76, 131072\n"
"C49_%=:\n"
"s_sleep 0\n"
"buffer_load_dword v28, v5, s[36:39], 0 offen sc1\n"
"s_sub_u32 s76, s76, 1\n"
"s_cbranch_scc1 CABORT_%=\n"
"s_waitcnt vmcnt(0)\n"
"v_cmp_gt_u32 vcc, s75, v28\n"
"s_nop 0\n"
"s_cbranch_vccnz C49_%=\n"
"C48_%=:\n"
"s_add_u32 m0, s47, 0\n"
"s_add_u32 s72, s44, 0\n"
"s_add_u32 s74, s44, 1024\n"
"buffer_load_dwordx4 v1, s[36:39], s72 offen sc1 lds\n"
"buffer_load_dwordx4 v1, s[36:39], s74 offen offset:1024 sc1 lds\n"
"s_add_u32 m0, s47, 2048\n"
"s_add_u32 s72, s44, 4096\n"
"s_add_u32 s74, s44, 5120\n"
"buffer_load_dwordx4 v1, s[36:39], s72 offen sc1 lds\n"
"buffer_load_dwordx4 v1, s[36:39], s74 offen offset:1024 sc1 lds\n"
"s_add_u32 m0, s47, 4096\n"
"s_add_u32 s72, s44, 8192\n"
"s_add_u32 s74, s44, 9216\n"
"buffer_load_dwordx4 v1, s[36:39], s72 offen sc1 lds\n"
"buffer_load_dwordx4 v1, s[36:39], s74 offen offset:1024 sc1 lds\n"
"s_add_u32 m0, s47, 6144\n"
"s_add_u32 s72, s44, 12288\n"
"s_add_u32 s74, s44, 13312\n"
"buffer_load_dwordx4 v1, s[36:39], s72 offen sc1 lds\n"
"buffer_load_dwordx4 v1, s[36:39], s74 offen offset:1024 sc1 lds\n"
"s_add_u32 m0, s47, 8192\n"
"s_add_u32 s72, s44, 16384\n"
"s_add_u32 s74, s44, 17408\n"
"buffer_load_dwordx4 v1, s[36:39], s72 offen sc1 lds\n"
"buffer_load_dwordx4 v1, s[36:39], s74 offen offset:1024 sc1 lds\n"
"s_add_u32 m0, s47, 10240\n"
"s_add_u32 s72, s44, 20480\n"
"s_add_u32 s74, s44, 21504\n"
"buffer_load_dwordx4 v1, s[36:39], s72 offen sc1 lds\n"
"buffer_load_dwordx4 v1, s[36:39], s74 offen offset:1024 sc1 lds\n"
"s_add_u32 m0, s47, 12288\n"
"s_add_u32 s72, s44, 24576\n"
"s_add_u32 s74, s44, 25600\n"
"buffer_load_dwordx4 v1, s[36:39], s72 offen sc1 lds\n"
"buffer_load_dwordx4 v1, s[36:39], s74 offen offset:1024 sc1 lds\n"
"s_add_u32 m0, s47, 14336\n"
"s_add_u32 s72, s44, 28672\n"
"s_add_u32 s74, s44, 29696\n"
"buffer_load_dwordx4 v1, s[36:39], s72 offen sc1 lds\n"
"buffer_load_dwordx4 v1, s[36:39], s74 offen offset:1024 sc1 lds\n"
"s_add_u32 m0, s47, 16384\n"
"s_add_u32 s72, s44, 32768\n"
"s_add_u32 s74, s44, 33792\n"
"buffer_load_dwordx4 v1, s[36:39], s72 offen sc1 lds\n"
"buffer_load_dwordx4 v1, s[36:39], s74 offen offset:1024 sc1 lds\n"
"s_add_u32 m0, s47, 18432\n"
"s_add_u32 s72, s44, 36864\n"
"s_add_u32 s74, s44, 37888\n"
"buffer_load_dwordx4 v1, s[36:39], s72 offen sc1 lds\n"
"buffer_load_dwordx4 v1, s[36:39], s74 offen offset:1024 sc1 lds\n"
"s_add_u32 m0, s47, 20480\n"
"s_add_u32 s72, s44, 40960\n"
"s_add_u32 s74, s44, 41984\n"
"buffer_load_dwordx4 v1, s[36:39], s72 offen sc1 lds\n"
"buffer_load_dwordx4 v1, s[36:39], s74 offen offset:1024 sc1 lds\n"
"s_add_u32 m0, s47, 22528\n"
"s_add_u32 s72, s44, 45056\n"
"s_add_u32 s74, s44, 46080\n"
"buffer_load_dwordx4 v1, s[36:39], s72 offen sc1 lds\n"
"buffer_load_dwordx4 v1, s[36:39], s74 offen offset:1024 sc1 lds\n"
"s_add_u32 m0, s47, 24576\n"
"s_add_u32 s72, s44, 49152\n"
"s_add_u32 s74, s44, 50176\n"
"buffer_load_dwordx4 v1, s[36:39], s72 offen sc1 lds\n"
"buffer_load_dwordx4 v1, s[36:39], s74 offen offset:1024 sc1 lds\n"
"s_add_u32 m0, s47, 26624\n"
"s_add_u32 s72, s44, 53248\n"
"s_add_u32 s74, s44, 54272\n"
"buffer_load_dwordx4 v1, s[36:39], s72 offen sc1 lds\n"
"buffer_load_dwordx4 v1, s[36:39], s74 offen offset:1024 sc1 lds\n"
"s_add_u32 m0, s47, 28672\n"
"s_add_u32 s72, s44, 57344\n"
"s_add_u32 s74, s44, 58368\n"
"buffer_load_dwordx4 v1, s[36:39], s72 offen sc1 lds\n"
"buffer_load_dwordx4 v1, s[36:39], s74 offen offset:1024 sc1 lds\n"
"s_add_u32 m0, s47, 30720\n"
"s_add_u32 s72, s44, 61440\n"
"s_add_u32 s74, s44, 62464\n"
"buffer_load_dwordx4 v1, s[36:39], s72 offen sc1 lds\n"
"buffer_load_dwordx4 v1, s[36:39], s74 offen offset:1024 sc1 lds\n"
"s_add_u32 s44, s44, 1310720\n"
"s_xor_b32 s47, s47, 0x18000\n"
"C47_%=:\n"
"v_xor_b32 v8, 512, v8\n"
"s_add_u32 s73, s73, 1\n"
"s_add_u32 s79, s68, 131072\n"
"s_and_b32 s79, s79, 0x3fffff\n"
"s_xor_b32 s68, s79, s68\n"
"s_lshr_b32 s68, s68, 8\n"
"s_and_b32 s68, s68, 8192\n"
"s_xor_b32 s68, s68, s79\n"
"s_cmp_lt_u32 s73, 63\n"
"s_cbranch_scc1 C44_%=\n"
"s_branch C50_%=\n"
"C50_%=:\n"
"s_branch C5_%=\n"
"C5_%=:\n"
"s_endpgm\n"
"CABORT_%=:\n"
"s_mov_b64 exec, -1\n"
"s_nop 7\n"
"s_nop 7\n"
"v_mov_b32 v0, 46268544\n"
"v_mov_b32 v1, 1\n"
"buffer_store_dword v1, v0, s[36:39], 0 offen sc1\n"
"s_endpgm\n");
}

DI unsigned peek(rsrc_t r, unsigned v) { return (unsigned)__builtin_amdgcn_raw_ptr_buffer_atomic_add_i32(0, r, v, 0, 16); }
DI unsigned peek1(rsrc_t r, int lane, unsigned v) { unsigned x = 0; if (lane == 0) x = peek(r, v); return __builtin_amdgcn_readfirstlane(x); }
DI void drain() { asm volatile("s_waitcnt vmcnt(0)" ::: "memory"); }
#define PST(k)
DI void norm_rows(rsrc_t ws, const u32x4 (&xv)[10], int tok, int l) {
    float v[80], sum = 0;
#pragma unroll
    for (int i = 0; i < 10; ++i)
#pragma unroll
        for (int k = 0; k < 4; ++k) { v[8 * i + 2 * k] = bflo(xv[i][k]); v[8 * i + 2 * k + 1] = bfhi(xv[i][k]); sum += v[8 * i + 2 * k] + v[8 * i + 2 * k + 1]; }
#pragma unroll
    for (int m = 4; m >= 1; m >>= 1) sum += __shfl_xor(sum, m);
    const float mean = sum * (1.f / DM);
    float var = 0;
#pragma unroll
    for (int i = 0; i < 80; ++i) { v[i] -= mean; var += v[i] * v[i]; }
#pragma unroll
    for (int m = 4; m >= 1; m >>= 1) var += __shfl_xor(var, m);
    const float rs = 1.f / sqrtf(var * (1.f / DM) + 1e-5f);
#pragma unroll
    for (int i = 0; i < 10; ++i)
        st16((u32x4){pk(v[8 * i] * rs, v[8 * i + 1] * rs), pk(v[8 * i + 2] * rs, v[8 * i + 3] * rs), pk(v[8 * i + 4] * rs, v[8 * i + 5] * rs), pk(v[8 * i + 6] * rs, v[8 * i + 7] * rs)},
             ws, (l + 8 * i) * 512 + (tok & 31) * 16, O_XN + (tok >> 5) * 40960);
}
DI int prepare(int id) {
    const kparams_t P = kparams();
    const rsrc_t ws = mkrsrc(ws_base(P->ws), WS_SPAN), rx = mkrsrc(P->x, NSEG * 32 * DM * 2), rm = mkrsrc(P->mem0, 8 * DM * 2);
    const int tid = threadIdx.x, lane = tid & 63, w = __builtin_amdgcn_readfirstlane(tid >> 6), l = tid & 7;
    volatile __attribute__((address_space(3))) unsigned* sh = (volatile __attribute__((address_space(3))) unsigned*)(uintptr_t)163824;
    PST(0);
    const int tok = w == 0 ? id * 8 + (lane >> 3) : 512 + id * 24 + (tid >> 3) - 8;
    const unsigned cal = lane < 16 ? O_COMM + 31 * 65536 + w * 16384 + ((id >> 2) & 1) * 8192 + (id >> 3) * 1024 + (id & 3) * 128 + lane * 4 : 0x7ffffff0u;
    u32x4 xv[10];
    unsigned want = 0, ch0 = 0;
    if (w == 0) {
#pragma unroll
        for (int i = 0; i < 10; ++i) xv[i] = __builtin_amdgcn_raw_buffer_load_b128(rx, tok * (DM * 2) + l * 16 + i * 128, 0, 0);
        if (lane == 0) want = peek(ws, H_ACK + blockIdx.x * 4);
        st4(SENT, ws, cal, 0);
        norm_rows(ws, xv, tok, l);
        want = (__builtin_amdgcn_readfirstlane(want) | 1u) + 1u;
        st4(want, ws, lane ? 0x7ffffff0u : H_MYC + blockIdx.x * 4, 0);
    } else {
        const u32x4 sent = {SENT, SENT, SENT, SENT};
        const int t = tid - 64, e = id * 128 + (t & 127), o = (e * 16) & 8191, ch = ((e >> 10) & 3) * 3 + ((o - 5120) >> 10), ln = e & 63, ec = (e >> 10) * 1024 * NREP + (e & 1023);
        const bool m0 = e < 4096 && o >= 5120 && ch < 10;
        const u32x4 mv = __builtin_amdgcn_raw_buffer_load_b128(rm, m0 ? ((2 * ((ln >> 2) & 3) + (ln & 1)) * DM + ch * 64 + (ln >> 4) * 16 + ((ln >> 1) & 1) * 8) * 2 : 0x7ffffff0u, 0, 0);
        st4(SENT, ws, cal, 0);
        for (int i = t; i < 640; i += 192) st16(sent, ws, (id * 640 + i) * 16, O_U);
        if (t < 128) { st16((u32x4){0u, 0u, 0u, 0u}, ws, e * 16, O_FLAG); for (int k = 0; k < NREP; ++k) st16(m0 ? unsent(mv) : sent, ws, ec * 16, O_COMM + k * 16384); }
        else if (t < 160) st16(sent, ws, (id * 32 + t - 128) * 16, O_QX);
    }
    PST(1);
    drain();
    __syncthreads();
    PST(2);
    const unsigned long long t0 = __builtin_amdgcn_s_memtime();
    asm volatile("s_waitcnt lgkmcnt(0)" ::: "memory");
    if (w) { st4(SENT, ws, cal, 0); drain(); sh[w] = (unsigned)(__builtin_amdgcn_s_memtime() - t0); }
    else ch0 = peek1(ws, lane, H_CHAL + id * 4);
    __syncthreads();
    if (w == 0) {
        PST(3);
        const unsigned best = min(min(65535u, sh[1]), min(sh[2], sh[3]));
        st4(ch0 << 16 | best, ws, one(lane, H_REQ + id * 64), 0);
        unsigned par = 0, last = ch0;
#pragma unroll 1
        for (unsigned spin = 0;; ++spin) {
            __builtin_amdgcn_s_sleep(2);
            FENCE;
            unsigned two = 0;
            if (lane < 2) two = peek(ws, lane == 0 ? H_CHAL + id * 4 : H_ACK + blockIdx.x * 4);
            const unsigned ch = __builtin_amdgcn_readlane(two, 0), a = __builtin_amdgcn_readlane(two, 1);
            if ((a & ~1u) == want) { par = a & 1u; break; }
            if (ch != last) st4(ch << 16 | best, ws, one(lane, H_REQ + id * 64), 0);
            last = ch;
            if (spin > SPIN_CAP) { st4(1u, ws, F_ABORT, 0); par = 2; break; }
        }
        PST(4);
        if (tid == 0) { st4(1u, ws, H_DONE + blockIdx.x * 4, 0); *sh = par; }
    } else {
        unsigned acc = 0;
        const int wv = (id >> 2) * 3 + w - 1, part = wv / 24;
        const rsrc_t rw = mkrsrc(part ? P->Wk : P->Wq, DM * NH * HD * 2);
#pragma unroll 1
        for (int k = 0; k < HELP; ++k) {
            u32x4 v[8];
#pragma unroll
            for (int u = 0; u < 8; ++u) { const int q = (wv % 24) * 8 + u; v[u] = __builtin_amdgcn_raw_buffer_load_b128(rw, ((10 - HELP + k) * 64 + q / 3) * (NH * HD * 2) + (id & 3) * 3072 + (q % 3) * 1024 + lane * 16, 0, 0); }
            __builtin_amdgcn_sched_barrier(0);
#pragma unroll
            for (int u = 0; u < 8; ++u) acc ^= v[u].x;
        }
        asm volatile("" ::"v"(acc));
        if (!HELP) __builtin_amdgcn_s_sleep(LDEL);
#pragma unroll
        for (int i = 0; i < 10; ++i) xv[i] = __builtin_amdgcn_raw_buffer_load_b128(rx, tok * (DM * 2) + l * 16 + i * 128, 0, 0);
        norm_rows(ws, xv, tok, l);
        const u32x4 sent = {SENT, SENT, SENT, SENT};
        const int t = tid - 64;
        if (t < 128) st16(sent, ws, (id * 128 + t) * 16, O_PTX + 62 * 65536); else if (t < 160) st16(sent, ws, (id * 32 + t - 128) * 16, O_PTM + 62 * 16384);
        if (t < 96) st16(sent, ws, (id * 96 + t) * 16, O_KM + 63 * 98304);
        drain();
    }
    __syncthreads();
    const int par = __builtin_amdgcn_readfirstlane(*sh);
    __syncthreads();
    if (par > 1) __builtin_amdgcn_endpgm();
    if (w == 0) st4(1u, ws, one(lane, F_X + fw(id)), 0);
    return par;
}
DI void release() {
    const rsrc_t ws = mkrsrc(ws_base(kparams()->ws), WS_SPAN);
    const int tid = threadIdx.x;
    volatile __attribute__((address_space(3))) unsigned* sh = (volatile __attribute__((address_space(3))) unsigned*)(uintptr_t)0;
    unsigned want = 0;
    if (tid < 64) { want = ((peek(ws, H_REQ + tid * 64) >> 16) + 1u) & 0xffffu; st4(want, ws, H_CHAL + tid * 4, 0); }
    st4(0u, ws, H_DONE + tid * 4, 0);
    if (tid == 0) st4(0u, ws, F_ABORT, 0);
    __builtin_amdgcn_s_sleep(MDEL);
    FENCE;
    unsigned m = peek(ws, H_MYC + tid * 4);
    drain();
    PST(1);
    if (tid < 64) {
        unsigned par = 2;
#pragma unroll 1
        for (unsigned spin = 0; spin < SPIN_CAP; ++spin) {
            FENCE;
            const unsigned q = peek(ws, H_REQ + tid * 64);
            if (__builtin_amdgcn_ballot_w64(q >> 16 != want) != 0) continue;
            unsigned lat = q & 0xffffu;
#pragma unroll
            for (int k = 32; k >= 1; k >>= 1) if (k != 4) lat = min(lat, (unsigned)__shfl_xor((int)lat, k));
            par = __builtin_amdgcn_readlane(lat, 4) < __builtin_amdgcn_readlane(lat, 0);
            break;
        }
        if (tid == 0) *sh = par;
    }
    __syncthreads();
    PST(2);
    const unsigned par = *sh;
    const bool idle = (tid & 7) >= 4 && (tid >> 3) == 31;
    unsigned last = ~m;
#pragma unroll 1
    for (unsigned spin = 0; spin < SPIN_CAP && par < 2; ++spin) {
        if (__builtin_amdgcn_ballot_w64(m != last) != 0) st4((m & ~1u) | par, ws, H_ACK + tid * 4, 0);
        last = m;
        FENCE;
        m = peek(ws, H_MYC + tid * 4);
        const unsigned d = peek(ws, H_DONE + tid * 4);
        if (__builtin_amdgcn_ballot_w64(!idle && d == 0) == 0) { if (tid == 0) st4(par, ws, F_PAR, 0); return; }
        if (__builtin_amdgcn_ballot_w64(m != last) == 0) __builtin_amdgcn_s_sleep(8);
    }
    st4(1u, ws, F_ABORT, 0);
}

__global__ void __launch_bounds__(256, 1) main_kernel(Params P) {
    Ctx c;
    c.w = __builtin_amdgcn_readfirstlane(threadIdx.x >> 6);
    c.par = 0;
    const int x = blockIdx.x & 7, r = blockIdx.x >> 3;
    int seg, t0, nt;
    if (x < 4) {
        if (r < 8) role_s(c, x * 8 + r);
        if (r < 16) role_v(c, x * 8 + r - 8);
        c.par = prepare((r - 16) * 4 + x);
        if (r < 26) role_c(c, x * 10 + r - 16);
        seg = (r - 26) >> 1; t0 = x * 5 + (r & 1) * 3; nt = 3 - (r & 1);
    } else {
        if (r == 31) { if (x == 4) release(); return; }
        if (r >= 23) role_g<2>(c, 2, (x - 4) * 8 + r - 23);
        if (r >= 15) role_w(c, (x - 4) * 8 + r - 15);
        if (r >= 12) role_g<3>(c, 3, (x - 4) * 3 + r - 12);
        const int kind = r >= 6 ? 1 : 0;
        role_g<0>(c, kind, (x - 4) * 6 + r - 6 * kind);
        if (r < 10) { seg = 62 + (x & 1); t0 = ((x >> 1) & 1) * 10 + r; nt = 1; }
        else { const int k = (x - 4) * 2 + r - 10; if (k == 7) return; seg = 61; t0 = 3 * k; nt = k < 6 ? 3 : 2; }
    }
    role_o(c, seg, t0, nt);
}

static void launch(void* const* d_in, void* d_out, void* d_ws, hipStream_t stream, unsigned mask, bool prep, unsigned tune) {
    Params P;
    P.x = (const bf16*)d_in[0]; P.mem0 = (const bf16*)d_in[1]; P.Wq = (const bf16*)d_in[2]; P.Wk = (const bf16*)d_in[3];
    P.Wv = (const bf16*)d_in[4]; P.Wo = (const bf16*)d_in[5]; P.wg = (const bf16*)d_in[8]; P.wu = (const bf16*)d_in[9]; P.wd = (const bf16*)d_in[10];
    P.out = (bf16*)d_out; P.ws = (unsigned char*)d_ws; P.mask = mask; P.tune = tune;
    (void)hipFuncSetAttribute((const void*)main_kernel, hipFuncAttributeMaxDynamicSharedMemorySize, LDS_BYTES);
    main_kernel<<<256, 256, LDS_BYTES, stream>>>(P);
}

extern "C" void kernel_launch(void* const* d_in, const int* in_sizes, int n_in, void* d_out, int out_size, void* d_ws, size_t ws_size, hipStream_t stream) {
    launch(d_in, d_out, d_ws, stream, 63u, true, 370739209u);
}
```
